```python
import torch

def _softmax(x, axis=-1):
    return torch.softmax(x, dim=axis)
def _gelu(x):
    return torch.nn.functional.gelu(x, approximate="tanh")
def _silu(x):
    return torch.nn.functional.silu(x)
def _sigmoid(x):
    return torch.sigmoid(x)
def _layernorm(x, axis=-1, eps=1e-5):
    m = x.mean(dim=axis, keepdim=True)
    v = x.var(dim=axis, keepdim=True, unbiased=False)
    return (x - m) / torch.sqrt(v + eps)
def _scatter_add(tgt, idx, src, axis=0):
    return tgt.clone().index_add_(axis, idx, src)
def _segment_sum(src, seg, n):
    out = torch.zeros((n,) + src.shape[1:], dtype=src.dtype)
    return out.index_add_(0, seg, src)
def _pad(x, pw):
    flat = tuple(v for pair in reversed(pw) for v in pair)
    return torch.nn.functional.pad(x, flat)

kernel_name = 'gated_mix__sliding_local_dilated__swiglu_ffn__prompt'

def setup_inputs():

    _w = (lambda a: (lambda t: t.to(torch.bfloat16) if t.is_floating_point() else t)(torch.as_tensor(a)))
    def _A_setup():
        B, S, D = 1, 2048, 640
        return {
            "x": _w((lambda *s: torch.randn(*s, dtype=torch.bfloat16) * 0.05)(B, S, D)),
            "wqkv": _w((lambda *s: torch.randn(*s, dtype=torch.bfloat16) * 0.8)(D, 3 * D)),
            "wo": _w((lambda *s: torch.randn(*s, dtype=torch.bfloat16) * 0.05)(D, D)),
        }
    _A_in = _A_setup()

    def _B_setup():
        B, S, D, F = 1, 2048, 640, 1536
        return {
            "x": _w((lambda *s: torch.randn(*s, dtype=torch.bfloat16) * 0.05)(B, S, D)),
            "w_gate": _w((lambda *s: torch.randn(*s, dtype=torch.bfloat16) * 0.2)(D, F)),
            "w_up": _w((lambda *s: torch.randn(*s, dtype=torch.bfloat16) * 0.2)(D, F)),
            "w_down": _w((lambda *s: torch.randn(*s, dtype=torch.bfloat16) * 0.2)(F, D)),
        }
    _B_in = _B_setup()

    return {
        "A_x": _A_in["x"],
        "A_wqkv": _A_in["wqkv"],
        "A_wo": _A_in["wo"],
        "B_x": _B_in["x"],
        "B_w_gate": _B_in["w_gate"],
        "B_w_up": _B_in["w_up"],
        "B_w_down": _B_in["w_down"],
        "gate_w": _w((lambda *s: torch.randn(*s, dtype=torch.bfloat16) * 0.05)(640) * 16.0),
    }

def reference(A_x, A_wqkv, A_wo, B_x, B_w_gate, B_w_up, B_w_down, gate_w):
    def _A_ref(x, wqkv, wo):
        B, S, D = x.shape
        H = 32; Dh = D // H
        W, dil = 8, 2
        qkv = (lambda x, *d: x.permute(*d))((x @ wqkv).reshape(B, S, 3, H, Dh), 2, 0, 3, 1, 4)
        q, k, v = qkv[0], qkv[1], qkv[2]
        scale = Dh ** -0.5
        pos = torch.arange(S)
        off = torch.arange(W) * dil
        idx = pos[:, None] - off[None, :]
        mask = idx >= 0
        idx_c = torch.where(mask, idx, 0).reshape(-1)
        kg = k[:, :, idx_c, :].reshape(B, H, S, W, Dh)
        vg = v[:, :, idx_c, :].reshape(B, H, S, W, Dh)
        logits = torch.einsum("bhsd,bhswd->bhsw", q, kg) * scale
        logits = torch.where(mask[None, None, :, :], logits, -1e9)
        attn = _softmax(logits, axis=-1)
        o = torch.einsum("bhsw,bhswd->bhsd", attn, vg)
        return (lambda x, *d: x.permute(*d))(o, 0, 2, 1, 3).reshape(B, S, D) @ wo

    def _B_ref(x, w_gate, w_up, w_down):
        g = _silu(x @ w_gate)
        u = x @ w_up
        return (g * u) @ w_down

    x = A_x
    g = _sigmoid((x * gate_w).sum(-1, keepdims=True))
    a = _A_ref(x, A_wqkv, A_wo)
    b = _B_ref(x, B_w_gate, B_w_up, B_w_down)
    return g * a + (1 - g) * b
```

```cpp
#include <hip/hip_runtime.h>
#include <hip/hip_bf16.h>
typedef unsigned short u16;
__device__ __forceinline__ float bf2f(u16 b){ unsigned u=((unsigned)b)<<16; return __builtin_bit_cast(float,u);}
__device__ __forceinline__ u16 f2bf(float f){ unsigned u=__builtin_bit_cast(unsigned,f); u+=0x7FFFu+((u>>16)&1u); return (u16)(u>>16);}
__global__ void gemm_naive(const u16* A,const u16* B,u16* C,int M,int N,int K){
  __shared__ float As[16][17], Bs[16][17];
  int tx=threadIdx.x&15, ty=threadIdx.x>>4;
  int r=blockIdx.y*16+ty, c=blockIdx.x*16+tx;
  float acc=0;
  for(int k0=0;k0<K;k0+=16){
    As[ty][tx]=bf2f(A[(size_t)r*K+k0+tx]);
    Bs[ty][tx]=bf2f(B[(size_t)(k0+ty)*N+c]);
    __syncthreads();
    for(int k=0;k<16;k++) acc+=As[ty][k]*Bs[k][tx];
    __syncthreads();
  }
  C[(size_t)r*N+c]=f2bf(acc);
}
__global__ void attn_naive(const u16* qkv,u16* o){
  int idx=blockIdx.x*blockDim.x+threadIdx.x;
  int s=idx>>5,h=idx&31;
  const u16* q=qkv+(size_t)s*1920+h*20;
  float l[8]; float m=-1e30f;
  for(int w=0;w<8;w++){ int t=s-2*w; if(t>=0){ const u16* k=qkv+(size_t)t*1920+640+h*20; float a=0; for(int d=0;d<20;d++) a+=bf2f(q[d])*bf2f(k[d]); l[w]=a*0.22360679775f;} else l[w]=-1e9f; m=fmaxf(m,l[w]); }
  float sum=0; for(int w=0;w<8;w++){ l[w]=__expf(l[w]-m); sum+=l[w]; }
  float inv=1.f/sum;
  for(int d=0;d<20;d++){ float a=0; for(int w=0;w<8;w++){ int t=s-2*w; if(t<0)t=0; a+=l[w]*inv*bf2f(qkv[(size_t)t*1920+1280+h*20+d]); } o[(size_t)s*640+h*20+d]=f2bf(a);}
}
__global__ void silu_mul(const u16* g,const u16* u,u16* gu,int n){
  int i=blockIdx.x*blockDim.x+threadIdx.x; if(i<n){ float x=bf2f(g[i]); float s=x/(1.f+__expf(-x)); gu[i]=f2bf(bf2f(f2bf(s))*bf2f(u[i])); }
}
__global__ void mix(const u16* x,const u16* gw,const u16* a,const u16* b,u16* out){
  int s=blockIdx.x; __shared__ float red[64];
  float acc=0; for(int d=threadIdx.x;d<640;d+=64) acc+=bf2f(f2bf(bf2f(x[s*640+d])*bf2f(gw[d])));
  red[threadIdx.x]=acc; __syncthreads();
  float t=0; for(int i=0;i<64;i++) t+=red[i];
  float g=1.f/(1.f+__expf(-bf2f(f2bf(t))));
  for(int d=threadIdx.x;d<640;d+=64) out[s*640+d]=f2bf(g*bf2f(a[s*640+d])+(1.f-g)*bf2f(b[s*640+d]));
}
extern "C" void kernel_launch(void* const* d_in, const int* in_sizes, int n_in,
                              void* d_out, int out_size, void* d_ws, size_t ws_size,
                              hipStream_t stream) {
  const u16* x=(const u16*)d_in[0]; const u16* wqkv=(const u16*)d_in[1]; const u16* wo=(const u16*)d_in[2];
  const u16* wg=(const u16*)d_in[4]; const u16* wu=(const u16*)d_in[5]; const u16* wd=(const u16*)d_in[6]; const u16* gw=(const u16*)d_in[7];
  u16* ws=(u16*)d_ws;
  u16* qkv=ws; u16* o=qkv+2048*1920; u16* g=o+2048*640; u16* u=g+2048*1536; u16* gu=u+2048*1536; u16* a=gu+2048*1536; u16* b=a+2048*640;
  gemm_naive<<<dim3(1920/16,128),256,0,stream>>>(x,wqkv,qkv,2048,1920,640);
  gemm_naive<<<dim3(1536/16,128),256,0,stream>>>(x,wg,g,2048,1536,640);
  gemm_naive<<<dim3(1536/16,128),256,0,stream>>>(x,wu,u,2048,1536,640);
  attn_naive<<<2048*32/256,256,0,stream>>>(qkv,o);
  silu_mul<<<2048*1536/256,256,0,stream>>>(g,u,gu,2048*1536);
  gemm_naive<<<dim3(640/16,128),256,0,stream>>>(o,wo,a,2048,640,640);
  gemm_naive<<<dim3(640/16,128),256,0,stream>>>(gu,wd,b,2048,640,1536);
  mix<<<2048,64,0,stream>>>(x,gw,a,b,(u16*)d_out);
}
```

```cpp
#include <hip/hip_runtime.h>
#include <hip/hip_bf16.h>
#include <stdint.h>
#include <type_traits>

#define SPLIT 0
#define STAMPS 0

#define ABL 0
#define LAS __attribute__((address_space(3)))
#define GAS __attribute__((address_space(1)))
typedef __bf16 bf16x8 __attribute__((ext_vector_type(8)));
typedef __bf16 bf16x2 __attribute__((ext_vector_type(2)));
typedef float f32x4 __attribute__((ext_vector_type(4)));
typedef float f32x2 __attribute__((ext_vector_type(2)));
typedef unsigned u32x4 __attribute__((ext_vector_type(4)));
typedef unsigned u32x2 __attribute__((ext_vector_type(2)));
typedef short s16x4 __attribute__((ext_vector_type(4)));
typedef unsigned long long u64;

namespace {
constexpr int LDS_BYTES = 163840;
constexpr unsigned WSLOT = 16384, XBASE = 81920, XSLOT = 22528, GATE_OFF = 149504, SCR_OFF = 154624;
constexpr unsigned OOB = 0x40000000u;
constexpr unsigned WS_FLAG = 0;
constexpr unsigned WS_DONE = 16384;
constexpr unsigned WS_EPOCH = 20480;
constexpr unsigned WS_G = 24576;
constexpr unsigned WS_O = 65536;
constexpr unsigned WS_GU = WS_O + 128 * 40 * 512;
constexpr unsigned WS_END = WS_GU + 128 * 48 * 1024;
constexpr unsigned WS_STAMP = 16u << 20;
constexpr int AUX_SC1 = 16;

__device__ __forceinline__ u32x4 mk_srd(const void* p, unsigned bytes) {
  const u64 a = (u64)p;
  u32x4 r; r.x = (unsigned)a; r.y = (unsigned)(a >> 32) & 0xffffu; r.z = bytes; r.w = 0x00020000u; return r;
}
__device__ __forceinline__ void dma16(unsigned lds, unsigned imm, unsigned voff, u32x4 srd, unsigned soff) {
  asm volatile("s_add_u32 m0, %0, %4\n\ts_nop 0\n\tbuffer_load_dwordx4 %1, %2, %3 offen lds"
               :: "s"(lds), "v"(voff), "s"(srd), "s"(soff), "i"(imm) : "memory", "scc");
}
__device__ __forceinline__ u32x4 lds128(unsigned a) { return *(const LAS u32x4*)a; }
__device__ __forceinline__ u32x2 ldstr(unsigned a) {
  return __builtin_bit_cast(u32x2, __builtin_amdgcn_ds_read_tr16_b64_v4i16((LAS s16x4*)a));
}
__device__ __forceinline__ u32x4 cat(u32x2 a, u32x2 b) { u32x4 r; r.x = a.x; r.y = a.y; r.z = b.x; r.w = b.y; return r; }
__device__ __forceinline__ f32x4 mfma(u32x4 a, u32x4 b, f32x4 c) {
  return __builtin_amdgcn_mfma_f32_16x16x32_bf16(__builtin_bit_cast(bf16x8, a), __builtin_bit_cast(bf16x8, b), c, 0, 0, 0);
}
__device__ __forceinline__ unsigned pk2(float a, float b) {
  f32x2 t = {a, b};
  return __builtin_bit_cast(unsigned, __builtin_convertvector(t, bf16x2));
}
__device__ __forceinline__ u32x2 pk4(f32x4 v) { u32x2 r; r.x = pk2(v.x, v.y); r.y = pk2(v.z, v.w); return r; }
#define SB() __builtin_amdgcn_sched_barrier(0)
#define WAITV(n) asm volatile("s_waitcnt vmcnt(" #n ")" ::: "memory")
#define WAITVL(n) asm volatile("s_waitcnt vmcnt(" #n ") lgkmcnt(0)" ::: "memory")
#define BAR() do { asm volatile("s_barrier" ::: "memory"); } while (0)
__device__ __forceinline__ unsigned rfl(unsigned v) { return __builtin_amdgcn_readfirstlane(v); }

#define STAMP(i) do {} while (0)

template <bool QKV>
struct P1 {
  static constexpr int NM = QKV ? 5 : 6;
  f32x4 acc[NM][4];
  u32x4 srdx, srdw;
  unsigned xv[3], wv[2];
  unsigned xm0[3];
  unsigned wm0;
  unsigned xa0, xa1;
  unsigned wa0, wa1, wa2;
  bool isgate;
  int nm;

  __device__ __forceinline__ void dma_w(int t, unsigned slot) {
    const unsigned so = (unsigned)t * (QKV ? 32u * 3840u : 32u * 3072u);
    dma16(slot + wm0, 0, wv[0], srdw, so);
    dma16(slot + wm0, 1024, wv[QKV ? 1 : 0], srdw, QKV ? so : so + 128);
  }
  __device__ __forceinline__ void dma_x1(int i, int j, unsigned slot) {
    const unsigned so = j < 10 ? (unsigned)j * 128u : OOB;
    const unsigned d = xm0[i] < XSLOT ? XBASE + slot + xm0[i] : SCR_OFF;
    dma16(d, 0, xv[i], srdx, so);
  }
  __device__ __forceinline__ void rd_w(u32x4 (&wf)[4], unsigned slot, int t) {
    if (QKV) {
      const unsigned a = wa0 + slot;
      const unsigned s2 = isgate ? (unsigned)t * 256u : slot;
      wf[0] = cat(ldstr(a), ldstr(a + 1920));
      wf[1] = cat(ldstr(a + 160), ldstr(a + 160 + 1920));
      wf[2] = cat(ldstr(wa1 + s2), ldstr(wa2 + s2));
      wf[3] = cat(ldstr(a + 320), ldstr(a + 320 + 1920));
    } else {
      const unsigned a = wa0 + slot, b = wa1 + slot;
      wf[0] = cat(ldstr(a), ldstr(a + 512));
      wf[1] = cat(ldstr(b), ldstr(b + 512));
      wf[2] = cat(ldstr(a + 2048), ldstr(a + 2048 + 512));
      wf[3] = cat(ldstr(b + 2048), ldstr(b + 2048 + 512));
    }
  }
  __device__ __forceinline__ void mm(int m, const u32x4 (&xf)[NM], const u32x4 (&wf)[4]) {
    if (ABL & 2) { asm volatile("" :: "v"(xf[m]), "v"(wf[0]), "v"(wf[1]), "v"(wf[2]), "v"(wf[3])); return; }
    acc[m][0] = mfma(wf[0], xf[m], acc[m][0]);
    acc[m][1] = mfma(wf[1], xf[m], acc[m][1]);
    acc[m][2] = mfma(wf[2], xf[m], acc[m][2]);
    if (QKV) acc[m][3] = mfma(xf[m], wf[3], acc[m][3]);
    else     acc[m][3] = mfma(wf[3], xf[m], acc[m][3]);
  }
  template <bool ODD>
  __device__ __forceinline__ void step(int t, unsigned wrd, unsigned wwr, unsigned xrd, unsigned xwr,
                                       const u32x4 (&xc)[NM], const u32x4 (&wc)[4], u32x4 (&xn)[NM], u32x4 (&wn)[4]) {
    if (ODD) WAITVL(9); else WAITVL(12);
    if (!(ABL & 8)) BAR();
    SB();
    mm(0, xc, wc);
    SB();
    if (!(ABL & 4)) rd_w(wn, wrd, t + 1);
    else { for (int n = 0; n < 4; ++n) { wn[n] = wc[n]; asm volatile("" : "+v"(wn[n])); } }
    SB();
    mm(1, xc, wc);
    SB();
    const unsigned xa = (ODD ? xa0 : xa1) + xrd;
    if (!(ABL & 4)) {
#pragma unroll
    for (int m = 0; m < 5; ++m) xn[m] = lds128(xa + m * 2048);
    if (!QKV) xn[NM - 1] = lds128(xa + (NM - 1) * 2048);
    } else { for (int m = 0; m < NM; ++m) { xn[m] = xc[m]; asm volatile("" : "+v"(xn[m])); } }
    SB();
    mm(2, xc, wc);
    SB();
    if (!(ABL & 1)) dma_w(t + 5, wwr);
    SB();
    mm(3, xc, wc);
    SB();
    if (ODD && !(ABL & 1)) { dma_x1(0, (t + 5) >> 1, xwr); dma_x1(1, (t + 5) >> 1, xwr); dma_x1(2, (t + 5) >> 1, xwr); }
    SB();
    mm(4, xc, wc);
    if (!QKV) { if (nm == 6) mm(NM - 1, xc, wc); }
    SB();
  }
  __device__ __forceinline__ void prologue_dma() {
    dma_w(0, 0);
    dma_x1(0, 0, 0); dma_x1(1, 0, 0); dma_x1(2, 0, 0);
    dma_w(1, WSLOT);
    dma_w(2, 2 * WSLOT);
    dma_x1(0, 1, XSLOT); dma_x1(1, 1, XSLOT); dma_x1(2, 1, XSLOT);
    dma_w(3, 3 * WSLOT);
    dma_w(4, 4 * WSLOT);
    dma_x1(0, 2, 2 * XSLOT); dma_x1(1, 2, 2 * XSLOT); dma_x1(2, 2, 2 * XSLOT);
  }
  __device__ __forceinline__ void zero() {
#pragma unroll
    for (int m = 0; m < NM; ++m)
#pragma unroll
      for (int n = 0; n < 4; ++n) { acc[m][n] = f32x4{0.f, 0.f, 0.f, 0.f}; asm volatile("" : "+v"(acc[m][n])); }
  }
  __device__ __forceinline__ void run() {
    u32x4 xA[NM], wA[4], xB[NM], wB[4];
    rd_w(wA, 0, 0);
#pragma unroll
    for (int m = 0; m < NM; ++m) xA[m] = lds128(xa0 + m * 2048);
    unsigned w0 = 0, w1 = WSLOT;
    unsigned x0 = 0, x1 = XSLOT;
#pragma unroll 1
    for (int t = 0; t < 20; t += 2) {
      unsigned w2 = w1 + WSLOT; if (w2 == 5 * WSLOT) w2 = 0;
      step<false>(t, w1, w0, x0, 0, xA, wA, xB, wB);
      step<true>(t + 1, w2, w1, x1, x0, xB, wB, xA, wA);
      w0 = w2; w1 = w2 + WSLOT; if (w1 == 5 * WSLOT) w1 = 0;
      x0 = x1; x1 += XSLOT; if (x1 == 3 * XSLOT) x1 = 0;
    }
    WAITVL(0);
    BAR();
  }
};

struct P2W {
  u32x4 srd;
  unsigned va[5];
  bool ow; unsigned ctoff;
  unsigned sa, sast, sb, sbst;
  unsigned cell0;
  __device__ __forceinline__ void dma(int s, int cell) {
    const unsigned so = s < 8 ? sa + s * sast : sb + (s - 8) * sbst;
#pragma unroll
    for (int j = 0; j < 5; ++j) dma16(cell0, cell * 5120 + j * 1024, va[j], srd, so);
  }
  __device__ __forceinline__ void pattern(bool tail) {
    unsigned ln = threadIdx.x & 63;
    asm volatile("" : "+v"(ln));
#pragma unroll
    for (int j = 0; j < 5; ++j) {
      const unsigned i = 64 * j + ln, r = (i * 6554u) >> 16, ch = i - r * 10;
      const unsigned ra = !ow ? r : tail ? 80 * (r >> 4) + 20 * ((r >> 2) & 3) + 16 + (r & 3) : 20 * (r >> 4) + (r & 15);
      va[j] = ra * 1280 + ctoff + ch * 16;
    }
  }
};

__device__ __forceinline__ float u2f(unsigned u) { return __builtin_bit_cast(float, u); }
__device__ __forceinline__ unsigned f2u(float f) { return __builtin_bit_cast(unsigned, f); }
__device__ __forceinline__ float xmax(float v) {
  auto r = __builtin_amdgcn_permlane16_swap(f2u(v), f2u(v), false, false);
  v = fmaxf(u2f(r[0]), u2f(r[1]));
  auto s = __builtin_amdgcn_permlane32_swap(f2u(v), f2u(v), false, false);
  return fmaxf(u2f(s[0]), u2f(s[1]));
}
__device__ __forceinline__ float xsum(float v) {
  auto r = __builtin_amdgcn_permlane16_swap(f2u(v), f2u(v), false, false);
  v = u2f(r[0]) + u2f(r[1]);
  auto s = __builtin_amdgcn_permlane32_swap(f2u(v), f2u(v), false, false);
  return u2f(s[0]) + u2f(s[1]);
}

template <int PH>
__global__ __launch_bounds__(512, 2) void fused(const void* __restrict__ x, const void* __restrict__ wqkv,
                                                const void* __restrict__ wo, const void* __restrict__ wg,
                                                const void* __restrict__ wu, const void* __restrict__ wd,
                                                const void* __restrict__ gw, void* out, void* ws) {
  __shared__ __attribute__((aligned(16))) char smem[LDS_BYTES];
  asm volatile("" :: "s"((unsigned)(size_t)(LAS char*)smem));
  const int tid = threadIdx.x, lane = tid & 63;
  const int w = rfl(tid >> 6);
  const int h = lane >> 4, c = lane & 15, q4 = (lane & 15) >> 2, p4 = lane & 3;
  const int b = blockIdx.x, xcd = b & 7, idx = b >> 3;
  const int rh = xcd & 1, cq = xcd >> 1;
  const bool isq = idx < 14;
  const int rb = 4 * xcd + (idx >> 3), ct = idx & 7;
  const __amdgpu_buffer_rsrc_t rws = __builtin_amdgcn_make_buffer_rsrc(ws, 0, WS_END, 0x00020000);
  STAMP(0);

  asm volatile("" :: "s"(x), "s"(wqkv), "s"(wo), "s"(wg), "s"(wu), "s"(wd), "s"(gw), "s"(out), "s"(ws));
  u64 ev = 0, tag = 0;
  if (PH == 3) asm volatile("global_load_dwordx2 %0, %1, %2 sc1" : "=v"(ev) : "v"(WS_EPOCH), "s"(ws) : "memory");
  auto make_tag = [&]() {
    asm volatile("" : "+v"(ev));
    const u64 e = ((u64)rfl((unsigned)(ev >> 32)) << 32) | rfl((unsigned)ev);
    tag = (e ^ 0x9E3779B97F4A7C15ull) * 0xBF58476D1CE4E5B9ull + 0x632BE59BD9B4E019ull;
    if (tag == e) tag += 1;
  };

  P2W pw;
  int nst, g0;
  {
    const bool ow = w < 2;
    g0 = (int)((0x271E171008000000ull >> (8 * w)) & 0xff);
    nst = (0x99778800 >> (4 * w)) & 15;
    pw.srd = ow ? mk_srd(wo, 640 * 1280) : mk_srd(wd, 1536 * 1280);
    pw.cell0 = w * 20480;
    pw.ow = ow; pw.ctoff = ct * 160;
    pw.sa = ow ? w * 320 * 1280 : g0 * 32 * 1280;
    pw.sast = ow ? 40 * 1280 : 32 * 1280;
    pw.sb = ow ? w * 320 * 1280 : (g0 + 8) * 32 * 1280;
    pw.sbst = ow ? 160 * 1280 : 32 * 1280;
  }

  if (PH & 1) {
    if (isq) {
      const int a = 7 * rh + (idx >= 7 ? idx - 7 : idx), g = 2 * cq + (idx >= 7 ? 1 : 0);
      const int hd = w & 3, par = w >> 2;
      P1<true> k;
      k.nm = 5;
      k.srdx = mk_srd(x, 2048 * 1280);
      k.srdw = mk_srd(wqkv, 640 * 3840);
      unsigned gv0, gv1;
      {
        const u32x4 sg = mk_srd(gw, 1280);
        asm volatile("buffer_load_ushort %0, %2, %3, 0 offen\n\tbuffer_load_ushort %1, %2, %3, 0 offen offset:1024"
                     : "=&v"(gv0), "=&v"(gv1) : "v"(tid * 2), "s"(sg) : "memory");
      }
#pragma unroll
      for (int i = 0; i < 3; ++i) {
        const int q = w * 3 + i;
        const unsigned rho = 8 * q + (lane >> 3);
        const unsigned pp = rho >= 80 ? 1 : 0, u = rho - 80 * pp;
        const unsigned tok = 146 * a + 2 * u + pp;
        k.xv[i] = q < 20 ? tok * 1280 + (((lane & 7) ^ ((rho >> 1) & 7)) << 4) : OOB;
        k.xm0[i] = q < 20 ? q * 1024 : XSLOT;
      }
#pragma unroll
      for (int j = 0; j < 2; ++j) {
        const int I = (2 * w + j) * 64 + lane;
        const int kb = I >= 736 ? 3 : I >= 488 ? 2 : I >= 248 ? 1 : 0;
        const int i = I - (kb == 3 ? 736 : kb == 2 ? 488 : kb == 1 ? 248 : 0);
        const unsigned r = ((unsigned)i * 2185u) >> 16, rem = i - r * 30;
        const unsigned seg = (rem * 6554u) >> 16, ch = rem - seg * 10;
        k.wv[j] = i < 240 ? (8 * kb + r) * 3840 + seg * 1280 + g * 160 + ch * 16 : OOB;
      }
      k.wm0 = w * 2048;
      k.prologue_dma();
      k.xa0 = XBASE + par * 5 * 2048 + c * 128 + ((h ^ (c >> 1)) << 4);
      k.xa1 = k.xa0 ^ 64;
      const unsigned kbo = h == 3 ? 736 * 16 : h == 2 ? 488 * 16 : h == 1 ? 248 * 16 : 0;
      k.wa0 = kbo + q4 * 480 + (20 * hd + 4 * p4) * 2;
      k.isgate = p4 == 3;
      k.wa1 = k.isgate ? GATE_OFF + (8 * h + q4) * 8 : kbo + q4 * 480 + p4 * 160 + (20 * hd + 16) * 2;
      k.wa2 = k.wa1 + (k.isgate ? 32 : 1920);
      k.zero();
      asm volatile("s_waitcnt vmcnt(14)" : "+v"(gv0), "+v"(gv1) :: "memory");
      {
        u32x2 v; v.x = gv0; v.y = 0;
        *(LAS u32x2*)(GATE_OFF + tid * 8) = v;
        if (tid < 128) { v.x = gv1; *(LAS u32x2*)(GATE_OFF + 4096 + tid * 8) = v; }
      }
      asm volatile("s_waitcnt lgkmcnt(0)" ::: "memory");
      BAR();
      STAMP(1);
      k.run();
      STAMP(2);
      if (PH & 2) { pw.pattern(false); pw.dma(0, 0); pw.dma(1, 1); pw.dma(2, 2); }
      u32x2 ql[5], kl[5], t2[5], vl[5], tt[5];
      const unsigned scr = pw.cell0 + 15360;
#pragma unroll
      for (int i = 0; i < 5; ++i) {
        ql[i] = pk4(k.acc[i][0]); kl[i] = pk4(k.acc[i][1]); t2[i] = pk4(k.acc[i][2]); vl[i] = pk4(k.acc[i][3]);
        *(LAS u32x2*)(scr + i * 512 + c * 32 + h * 8) = t2[i];
      }
      asm volatile("s_waitcnt lgkmcnt(0)" ::: "memory");
#pragma unroll
      for (int i = 0; i < 5; ++i) tt[i] = ldstr(scr + i * 512 + (4 * h + q4) * 32 + p4 * 8);
      asm volatile("s_waitcnt lgkmcnt(0)" : "+v"(tt[0]), "+v"(tt[1]), "+v"(tt[2]), "+v"(tt[3]), "+v"(tt[4]) :: "memory");
      if (PH & 2) pw.dma(3, 3);
      f32x4 ba, bb;
#pragma unroll
      for (int r = 0; r < 4; ++r) {
        const int kap = 4 * h + r;
        ba[r] = kap >= c + 9 ? 0.f : -1e30f;
        bb[r] = (kap <= c && kap >= c - 7) ? 0.f : -1e30f;
      }
      const unsigned m0mask = h == 0 ? 0xffffffffu : 0u;
      u32x4 kf[5];
#pragma unroll
      for (int i = 0; i < 5; ++i) {
        auto s0 = __builtin_amdgcn_permlane16_swap(t2[i].x, t2[i].x, false, false);
        auto s1 = __builtin_amdgcn_permlane16_swap(t2[i].y, t2[i].y, false, false);
        kf[i].x = kl[i].x; kf[i].y = kl[i].y; kf[i].z = s0[1]; kf[i].w = s1[1];
      }
      constexpr float SC = 0.22360679774997896f * 1.4426950408889634f;
#pragma unroll
      for (int i = 0; i < 5; ++i) {
        u32x4 qf; qf.x = ql[i].x; qf.y = ql[i].y; qf.z = t2[i].x & m0mask; qf.w = t2[i].y & m0mask;
        f32x4 sb = mfma(kf[i], qf, bb);
        f32x4 sa = f32x4{-1e30f, -1e30f, -1e30f, -1e30f};
        if (i > 0) sa = mfma(kf[i - 1], qf, ba);
        float m = fmaxf(fmaxf(fmaxf(sb.x, sb.y), fmaxf(sb.z, sb.w)), fmaxf(fmaxf(sa.x, sa.y), fmaxf(sa.z, sa.w)));
        m = xmax(m);
        const float mc = m * SC;
        f32x4 pa, pb;
#pragma unroll
        for (int r = 0; r < 4; ++r) {
          pb[r] = __builtin_amdgcn_exp2f(sb[r] * SC - mc);
          pa[r] = i > 0 ? __builtin_amdgcn_exp2f(sa[r] * SC - mc) : 0.f;
        }
        float l = ((pb.x + pb.y) + (pb.z + pb.w)) + ((pa.x + pa.y) + (pa.z + pa.w));
        l = xsum(l);
        const float rl = __builtin_amdgcn_rcpf(l);
        const u32x4 pf = cat(pk4(pa * rl), pk4(pb * rl));
        const f32x4 z = {0.f, 0.f, 0.f, 0.f};
        const f32x4 oa = mfma(cat(vl[i > 0 ? i - 1 : 0], vl[i]), pf, z);
        const f32x4 ob = mfma(cat(tt[i > 0 ? i - 1 : 0], tt[i]), pf, z);
        const int u = 16 * i + c, U = 73 * a + u;
        const bool ok = (a == 0 || u >= 7) && U < 1024;
        const unsigned mt = 2 * (U >> 4) + par, cc = U & 15;
        const unsigned oo = WS_O + mt * (40 * 512) + cc * 8;
        __builtin_amdgcn_raw_buffer_store_b64(pk4(oa), rws, ok ? oo + (4 * g + hd) * 512 + h * 128 : OOB, 0, AUX_SC1);
        __builtin_amdgcn_raw_buffer_store_b64(pk4(ob), rws, (ok && h == 2) ? oo + (32 + g) * 512 + hd * 128 : OOB, 0, AUX_SC1);
        if (g == 0 && hd == 0)
          __builtin_amdgcn_raw_buffer_store_b32(f2u(k.acc[i][2].x), rws,
                                                (ok && h == 3) ? WS_G + (mt * 16 + cc) * 4 : OOB, 0, AUX_SC1);
      }
      STAMP(3);
      if (PH == 3) {
        WAITV(0);
        BAR();
        make_tag();
        if (w == 0 && lane < 5) {
          const int U0 = a ? 73 * a + 7 : 0, U1 = min(73 * a + 80, 1024);
          const int r = (U0 >> 5) + lane;
          const int a0 = r * 32 < 80 ? 0 : (r * 32 - 7) / 73;
          GAS u64* f = (GAS u64*)((char*)ws + (lane == 4 ? WS_DONE + b * 8 : WS_FLAG + (r * 64 + 24 + (a - a0) * 8 + g) * 8));
          if (lane == 4 || r * 32 < U1) __hip_atomic_store(f, tag, __ATOMIC_RELAXED, __HIP_MEMORY_SCOPE_AGENT);
        }
      }
    } else {
      const int f = idx - 14;
      const int fr = f >= 12 ? f - 12 : f >= 6 ? f - 6 : f, fc = f >= 12 ? 2 : f >= 6 ? 1 : 0;
      const int ri = 6 * rh + fr, cj = 3 * cq + fc;
      const int nq = w & 3, mh = w >> 2;
      P1<false> k;
      k.nm = mh ? 5 : 6;
      k.isgate = false;
      k.srdx = mk_srd(x, 2048 * 1280);
      k.srdw = mk_srd((w & 1) ? wu : wg, 640 * 3072);
#pragma unroll
      for (int i = 0; i < 3; ++i) {
        const int q = w * 3 + i;
        const unsigned rho = 8 * q + (lane >> 3);
        const unsigned mt = 11 * ri + (rho >> 4);
        const unsigned tok = 32 * (mt >> 1) + 2 * (rho & 15) + (mt & 1);
        k.xv[i] = q < 22 ? tok * 1280 + (((lane & 7) ^ ((rho >> 1) & 7)) << 4) : OOB;
        k.xm0[i] = q < 22 ? q * 1024 : XSLOT;
      }
      {
        const int kb = w >> 1, r = lane >> 3, p8 = lane & 7;
        const int s = ((r >> 1) & 1) + 2 * (kb & 1);
        k.wv[0] = (8 * kb + r) * 3072 + cj * 256 + (((((p8 >> 1) ^ s) << 1) | (p8 & 1)) << 4);
        k.wv[1] = 0;
      }
      k.wm0 = w * 2048;
      k.prologue_dma();
      k.xa0 = XBASE + mh * 6 * 2048 + c * 128 + ((h ^ (c >> 1)) << 4);
      k.xa1 = k.xa0 ^ 64;
      {
        const int s = ((q4 >> 1) & 1) + 2 * (h & 1);
        k.wa0 = (h * 4 + (nq >> 1)) * 1024 + q4 * 128 + (((2 * (nq & 1)) ^ s) << 5) + p4 * 8;
        k.wa1 = k.wa0 ^ 32;
        k.wa2 = 0;
      }
      k.zero();
      WAITV(14);
      BAR();
      STAMP(1);
      k.run();
      STAMP(2);
      if (PH & 2) { pw.pattern(false); pw.dma(0, 0); pw.dma(1, 1); pw.dma(2, 2); pw.dma(3, 3); }
#pragma unroll
      for (int m = 0; m < 6; ++m) {
        if (m < k.nm) {
          float v[8];
#pragma unroll
          for (int j = 0; j < 8; ++j) {
            const float gt = k.acc[m][j >> 2][j & 3], up = k.acc[m][2 + (j >> 2)][j & 3];
            const float e = __builtin_amdgcn_exp2f(gt * -1.4426950408889634f);
            v[j] = gt * up * __builtin_amdgcn_rcpf(1.f + e);
          }
          u32x4 o; o.x = pk2(v[0], v[1]); o.y = pk2(v[2], v[3]); o.z = pk2(v[4], v[5]); o.w = pk2(v[6], v[7]);
          const unsigned mt = 11 * ri + 6 * mh + m;
          __builtin_amdgcn_raw_buffer_store_b128(o, rws, mt < 128 ? WS_GU + (mt * 48 + 4 * cj + nq) * 1024 + lane * 16 : OOB, 0, AUX_SC1);
        }
      }
      STAMP(3);
      if (PH == 3) {
        WAITV(0);
        BAR();
        make_tag();
        if (w == 0 && lane < 5) {
          const int m0_ = 11 * ri, m1 = min(m0_ + 11, 128);
          const int r = (m0_ >> 2) + lane;
          const int r0 = (4 * r) / 11;
          GAS u64* fp = (GAS u64*)((char*)ws + (lane == 4 ? WS_DONE + b * 8 : WS_FLAG + (r * 64 + (ri - r0) * 12 + cj) * 8));
          if (lane == 4 || r * 4 < m1) __hip_atomic_store(fp, tag, __ATOMIC_RELAXED, __HIP_MEMORY_SCOPE_AGENT);
        }
      }
    }
  }
  STAMP(4);

  if (PH & 2) {
    if (PH == 2) { pw.pattern(false); pw.dma(0, 0); pw.dma(1, 1); pw.dma(2, 2); pw.dma(3, 3); WAITV(0); }
    auto body = [&](auto OWT) {
      constexpr bool OW = decltype(OWT)::value;
      constexpr unsigned AMT = OW ? 40 * 512 : 48 * 1024;
      constexpr int NS = OW ? 10 : 9;
      const unsigned wra = pw.cell0 + (4 * h + q4) * 160 + p4 * 8;
      u32x4 wreg[4][5];
      auto rdw = [&](int cell) {
#pragma unroll
        for (int n = 0; n < 5; ++n) wreg[cell][n] = cat(ldstr(wra + cell * 5120 + n * 32), ldstr(wra + cell * 5120 + n * 32 + 2560));
      };
      rdw(0); rdw(1); rdw(2); rdw(3);
      asm volatile("s_waitcnt lgkmcnt(0)" ::: "memory");
      SB();
      pw.dma(4, 0); pw.dma(5, 1); pw.dma(6, 2);
      if (OW || nst > 7) pw.dma(7, 3);
      if (OW) pw.pattern(true);
      if (PH == 3) {
        const bool two_f = (4 * rb) / 11 != (4 * rb + 3) / 11;
        const int a0 = rb * 32 < 80 ? 0 : (rb * 32 - 7) / 73, a1 = rb * 32 + 31 < 80 ? 0 : (rb * 32 + 31 - 7) / 73;
        bool need;
        if (lane < 24) {
          const int cjj = lane >= 12 ? lane - 12 : lane;
          need = !OW && (lane < 12 || two_f) && cjj >= (g0 >> 2) && cjj <= ((g0 + nst - 1) >> 2);
        } else {
          const int gg = (lane - 24) & 7;
          need = lane < 40 && (lane < 32 || a1 != a0) && (gg == 0 || (OW && (gg >> 2) == w));
        }
        GAS u64* fp = (GAS u64*)((char*)ws + WS_FLAG + (rb * 64 + lane) * 8);
        for (unsigned spin = 0; spin < (1u << 18); ++spin) {
          const u64 v = __hip_atomic_load(fp, __ATOMIC_RELAXED, __HIP_MEMORY_SCOPE_AGENT);
          if (__all(!need || v == tag)) break;
          __builtin_amdgcn_s_sleep(2);
        }
        __builtin_amdgcn_fence(__ATOMIC_ACQUIRE, "wavefront");
        asm volatile("" ::: "memory");
      }
      STAMP(5);
      const unsigned av = OW ? lane * 8 : lane * 16;
      const unsigned as0 = (OW ? WS_O + 8 * w * 1024 : WS_GU + g0 * 1024) + 4 * rb * AMT;
      const unsigned as1 = WS_O + (16 + 2 * w) * 1024 + 4 * rb * AMT;
      u32x4 af[4][4];
      f32x4 acc[4][5];
      float sg[4];
      auto lda = [&](int s) {
        const unsigned so = (OW && s >= 8) ? as1 + (s - 8) * 1024 : as0 + s * 1024;
#pragma unroll
        for (int m = 0; m < 4; ++m) {
          if (OW) af[s & 3][m] = cat(__builtin_amdgcn_raw_buffer_load_b64(rws, av, so + m * AMT, AUX_SC1),
                                     __builtin_amdgcn_raw_buffer_load_b64(rws, av + 512, so + m * AMT, AUX_SC1));
          else    af[s & 3][m] = __builtin_amdgcn_raw_buffer_load_b128(rws, av, so + m * AMT, AUX_SC1);
        }
      };
      lda(0); lda(1); lda(2); lda(3);
      SB();
#pragma unroll
      for (int m = 0; m < 4; ++m)
#pragma unroll
        for (int n = 0; n < 5; ++n) acc[m][n] = f32x4{0.f, 0.f, 0.f, 0.f};
#pragma unroll
      for (int s = 0; s < NS; ++s) {
        if (OW || s < 7 || s < nst) {
#pragma unroll
          for (int m = 0; m < 4; ++m)
#pragma unroll
            for (int n = 0; n < 5; ++n) acc[m][n] = mfma(wreg[s & 3][n], af[s & 3][m], acc[m][n]);
          SB();
          asm volatile("" ::: "memory");
          if (s + 4 < NS && (OW || s + 4 < 7 || s + 4 < nst)) {
            rdw(s & 3);
            if (s + 8 < NS) {
              if (OW || s + 8 < nst) { asm volatile("s_waitcnt lgkmcnt(0)" ::: "memory"); SB(); pw.dma(s + 8, s & 3); }
            }
            lda(s + 4);
          }
          if (s == 6) {
#pragma unroll
            for (int m = 0; m < 4; ++m)
              sg[m] = __builtin_bit_cast(float, __builtin_amdgcn_raw_buffer_load_b32(rws, c * 4, WS_G + (4 * rb + m) * 64, AUX_SC1));
          }
          SB();
        }
      }
      STAMP(6);
#pragma unroll
      for (int m = 0; m < 4; ++m) {
        const float e = __builtin_amdgcn_exp2f(sg[m] * (OW ? -1.4426950408889634f : 1.4426950408889634f));
        const float f = __builtin_amdgcn_rcpf(1.f + e);
#pragma unroll
        for (int n = 0; n < 5; ++n) *(LAS f32x4*)(pw.cell0 + (m * 5 + n) * 1024 + lane * 16) = acc[m][n] * f;
      }
    };
    if (w < 2) body(std::true_type{}); else body(std::false_type{});
    if (PH == 3 && b == 0 && w == 4) {
      GAS u64* dp = (GAS u64*)((char*)ws + WS_DONE + lane * 8);
      for (unsigned spin = 0; spin < (1u << 18); ++spin) {
        bool ok = true;
#pragma unroll
        for (int j = 0; j < 4; ++j) ok &= __hip_atomic_load(dp + j * 64, __ATOMIC_RELAXED, __HIP_MEMORY_SCOPE_AGENT) == tag;
        if (__all(ok)) break;
        __builtin_amdgcn_s_sleep(2);
      }
      if (lane == 0) __hip_atomic_store((GAS u64*)((char*)ws + WS_EPOCH), tag, __ATOMIC_RELAXED, __HIP_MEMORY_SCOPE_AGENT);
    }
    asm volatile("s_waitcnt lgkmcnt(0)" ::: "memory");
    BAR();
    const int t0 = w < 4 ? 3 * w : 12 + 2 * (w - 4);
#pragma unroll
    for (int j = 0; j < 3; ++j) {
      if (j < 2 || w < 4) {
        const int tl = t0 + j;
        f32x4 s = *(const LAS f32x4*)(tl * 1024 + lane * 16);
#pragma unroll
        for (int v = 1; v < 8; ++v) s += *(const LAS f32x4*)(v * 20480 + tl * 1024 + lane * 16);
        const int m = tl / 5, n = tl - 5 * m;
        const int tok = 64 * rb + 32 * (m >> 1) + 2 * c + (m & 1);
        *(GAS u32x2*)((char*)out + tok * 1280 + ct * 160 + n * 32 + h * 8) = pk4(s);
      }
    }
    STAMP(7);
  }
}
}

extern "C" void kernel_launch(void* const* d_in, const int* in_sizes, int n_in, void* d_out, int out_size, void* d_ws,
                              size_t ws_size, hipStream_t stream) {
  fused<3><<<256, 512, 0, stream>>>(d_in[0], d_in[1], d_in[2], d_in[4], d_in[5], d_in[6], d_in[7], d_out, d_ws);
}
```

```cpp
#include <hip/hip_runtime.h>
#include <hip/hip_bf16.h>
#include <stdint.h>
#include <type_traits>

#define SPLIT 0
#define STAMPS 0

#define ABL 0
#define LAS __attribute__((address_space(3)))
#define GAS __attribute__((address_space(1)))
typedef __bf16 bf16x8 __attribute__((ext_vector_type(8)));
typedef __bf16 bf16x2 __attribute__((ext_vector_type(2)));
typedef float f32x4 __attribute__((ext_vector_type(4)));
typedef float f32x2 __attribute__((ext_vector_type(2)));
typedef unsigned u32x4 __attribute__((ext_vector_type(4)));
typedef unsigned u32x2 __attribute__((ext_vector_type(2)));
typedef short s16x4 __attribute__((ext_vector_type(4)));
typedef unsigned long long u64;

namespace {
constexpr int LDS_BYTES = 163840;
constexpr unsigned WSLOT = 16384, XBASE = 81920, XSLOT = 22528, GATE_OFF = 149504, SCR_OFF = 154624;
constexpr unsigned OOB = 0x40000000u;
constexpr unsigned WS_FLAG = 0;
constexpr unsigned WS_DONE = 16384;
constexpr unsigned WS_EPOCH = 20480;
constexpr unsigned WS_G = 24576;
constexpr unsigned WS_O = 65536;
constexpr unsigned WS_GU = WS_O + 128 * 40 * 512;
constexpr unsigned WS_END = WS_GU + 128 * 48 * 1024;
constexpr unsigned WS_STAMP = 16u << 20;
constexpr int AUX_SC1 = 16;

__device__ __forceinline__ u32x4 mk_srd(const void* p, unsigned bytes) {
  const u64 a = (u64)p;
  u32x4 r; r.x = (unsigned)a; r.y = (unsigned)(a >> 32) & 0xffffu; r.z = bytes; r.w = 0x00020000u; return r;
}
__device__ __forceinline__ void dma16(unsigned lds, unsigned imm, unsigned voff, u32x4 srd, unsigned soff) {
  asm volatile("s_add_u32 m0, %0, %4\n\ts_nop 0\n\tbuffer_load_dwordx4 %1, %2, %3 offen lds"
               :: "s"(lds), "v"(voff), "s"(srd), "s"(soff), "i"(imm) : "memory", "scc");
}
__device__ __forceinline__ u32x4 lds128(unsigned a) { return *(const LAS u32x4*)a; }
__device__ __forceinline__ u32x2 ldstr(unsigned a) {
  return __builtin_bit_cast(u32x2, __builtin_amdgcn_ds_read_tr16_b64_v4i16((LAS s16x4*)a));
}
__device__ __forceinline__ u32x4 cat(u32x2 a, u32x2 b) { u32x4 r; r.x = a.x; r.y = a.y; r.z = b.x; r.w = b.y; return r; }
__device__ __forceinline__ f32x4 mfma(u32x4 a, u32x4 b, f32x4 c) {
  return __builtin_amdgcn_mfma_f32_16x16x32_bf16(__builtin_bit_cast(bf16x8, a), __builtin_bit_cast(bf16x8, b), c, 0, 0, 0);
}
__device__ __forceinline__ unsigned pk2(float a, float b) {
  f32x2 t = {a, b};
  return __builtin_bit_cast(unsigned, __builtin_convertvector(t, bf16x2));
}
__device__ __forceinline__ u32x2 pk4(f32x4 v) { u32x2 r; r.x = pk2(v.x, v.y); r.y = pk2(v.z, v.w); return r; }
#define SB() __builtin_amdgcn_sched_barrier(0)
#define WAITV(n) asm volatile("s_waitcnt vmcnt(" #n ")" ::: "memory")
#define WAITVL(n) asm volatile("s_waitcnt vmcnt(" #n ") lgkmcnt(0)" ::: "memory")
#define BAR() do { asm volatile("s_barrier" ::: "memory"); } while (0)
__device__ __forceinline__ unsigned rfl(unsigned v) { return __builtin_amdgcn_readfirstlane(v); }

#define AMP0 -1
#define AMP1 -1
#define STAMP(i) do {} while (0)

template <bool QKV>
struct P1 {
  static constexpr int NM = QKV ? 5 : 6;
  f32x4 acc[NM][4];
  u32x4 srdx, srdw;
  unsigned xv[3], wv[2];
  unsigned xm0[3];
  unsigned wm0;
  unsigned xa0, xa1;
  unsigned wa0, wa1, wa2;
  bool isgate;
  int nm;

  __device__ __forceinline__ void dma_w(int t, unsigned slot) {
    const unsigned so = (unsigned)t * (QKV ? 32u * 3840u : 32u * 3072u);
    dma16(slot + wm0, 0, wv[0], srdw, so);
    dma16(slot + wm0, 1024, wv[QKV ? 1 : 0], srdw, QKV ? so : so + 128);
  }
  __device__ __forceinline__ void dma_x1(int i, int j, unsigned slot) {
    const unsigned so = j < 10 ? (unsigned)j * 128u : OOB;
    const unsigned d = xm0[i] < XSLOT ? XBASE + slot + xm0[i] : SCR_OFF;
    dma16(d, 0, xv[i], srdx, so);
  }
  __device__ __forceinline__ void rd_w(u32x4 (&wf)[4], unsigned slot, int t) {
    if (QKV) {
      const unsigned a = wa0 + slot;
      const unsigned s2 = isgate ? (unsigned)t * 256u : slot;
      wf[0] = cat(ldstr(a), ldstr(a + 1920));
      wf[1] = cat(ldstr(a + 160), ldstr(a + 160 + 1920));
      wf[2] = cat(ldstr(wa1 + s2), ldstr(wa2 + s2));
      wf[3] = cat(ldstr(a + 320), ldstr(a + 320 + 1920));
    } else {
      const unsigned a = wa0 + slot, b = wa1 + slot;
      wf[0] = cat(ldstr(a), ldstr(a + 512));
      wf[1] = cat(ldstr(b), ldstr(b + 512));
      wf[2] = cat(ldstr(a + 2048), ldstr(a + 2048 + 512));
      wf[3] = cat(ldstr(b + 2048), ldstr(b + 2048 + 512));
    }
  }
  __device__ __forceinline__ void mm(int m, const u32x4 (&xf)[NM], const u32x4 (&wf)[4]) {
    if (ABL & 2) { asm volatile("" :: "v"(xf[m]), "v"(wf[0]), "v"(wf[1]), "v"(wf[2]), "v"(wf[3])); return; }
    acc[m][0] = mfma(wf[0], xf[m], acc[m][0]);
    acc[m][1] = mfma(wf[1], xf[m], acc[m][1]);
    acc[m][2] = mfma(wf[2], xf[m], acc[m][2]);
    if (QKV) acc[m][3] = mfma(xf[m], wf[3], acc[m][3]);
    else     acc[m][3] = mfma(wf[3], xf[m], acc[m][3]);
  }
  template <bool ODD>
  __device__ __forceinline__ void step(int t, unsigned wrd, unsigned wwr, unsigned xrd, unsigned xwr,
                                       const u32x4 (&xc)[NM], const u32x4 (&wc)[4], u32x4 (&xn)[NM], u32x4 (&wn)[4]) {
    if (ODD) WAITVL(9); else WAITVL(12);
    if (!(ABL & 8)) BAR();
    SB();
    mm(0, xc, wc);
    SB();
    if (!(ABL & 4)) rd_w(wn, wrd, t + 1);
    else { for (int n = 0; n < 4; ++n) { wn[n] = wc[n]; asm volatile("" : "+v"(wn[n])); } }
    SB();
    mm(1, xc, wc);
    SB();
    const unsigned xa = (ODD ? xa0 : xa1) + xrd;
    if (!(ABL & 4)) {
#pragma unroll
    for (int m = 0; m < 5; ++m) xn[m] = lds128(xa + m * 2048);
    if (!QKV) xn[NM - 1] = lds128(xa + (NM - 1) * 2048);
    } else { for (int m = 0; m < NM; ++m) { xn[m] = xc[m]; asm volatile("" : "+v"(xn[m])); } }
    SB();
    mm(2, xc, wc);
    SB();
    if (!(ABL & 1)) dma_w(t + 5, wwr);
    SB();
    mm(3, xc, wc);
    SB();
    if (ODD && !(ABL & 1)) { dma_x1(0, (t + 5) >> 1, xwr); dma_x1(1, (t + 5) >> 1, xwr); dma_x1(2, (t + 5) >> 1, xwr); }
    SB();
    mm(4, xc, wc);
    if (!QKV) { if (nm == 6) mm(NM - 1, xc, wc); }
    SB();
  }
  __device__ __forceinline__ void prologue_dma() {
    dma_w(0, 0);
    dma_x1(0, 0, 0); dma_x1(1, 0, 0); dma_x1(2, 0, 0);
    dma_w(1, WSLOT);
    dma_w(2, 2 * WSLOT);
    dma_x1(0, 1, XSLOT); dma_x1(1, 1, XSLOT); dma_x1(2, 1, XSLOT);
    dma_w(3, 3 * WSLOT);
    dma_w(4, 4 * WSLOT);
    dma_x1(0, 2, 2 * XSLOT); dma_x1(1, 2, 2 * XSLOT); dma_x1(2, 2, 2 * XSLOT);
  }
  __device__ __forceinline__ void zero() {
#pragma unroll
    for (int m = 0; m < NM; ++m)
#pragma unroll
      for (int n = 0; n < 4; ++n) { acc[m][n] = f32x4{0.f, 0.f, 0.f, 0.f}; asm volatile("" : "+v"(acc[m][n])); }
  }
  __device__ __forceinline__ void run() {
    u32x4 xA[NM], wA[4], xB[NM], wB[4];
    rd_w(wA, 0, 0);
#pragma unroll
    for (int m = 0; m < NM; ++m) xA[m] = lds128(xa0 + m * 2048);
    unsigned w0 = 0, w1 = WSLOT;
    unsigned x0 = 0, x1 = XSLOT;
#pragma unroll 1
    for (int t = 0; t < 20; t += 2) {
      unsigned w2 = w1 + WSLOT; if (w2 == 5 * WSLOT) w2 = 0;
      step<false>(t, w1, w0, x0, 0, xA, wA, xB, wB);
      step<true>(t + 1, w2, w1, x1, x0, xB, wB, xA, wA);
      w0 = w2; w1 = w2 + WSLOT; if (w1 == 5 * WSLOT) w1 = 0;
      x0 = x1; x1 += XSLOT; if (x1 == 3 * XSLOT) x1 = 0;
    }
    WAITVL(0);
    BAR();
  }
};

struct P2W {
  u32x4 srd;
  unsigned va[5];
  bool ow; unsigned ctoff;
  unsigned sa, sast, sb, sbst;
  unsigned cell0;
  __device__ __forceinline__ void dma(int s, int cell) {
    const unsigned so = s < 8 ? sa + s * sast : sb + (s - 8) * sbst;
#pragma unroll
    for (int j = 0; j < 5; ++j) dma16(cell0, cell * 5120 + j * 1024, va[j], srd, so);
  }
  __device__ __forceinline__ void pattern(bool tail) {
    unsigned ln = threadIdx.x & 63;
    asm volatile("" : "+v"(ln));
#pragma unroll
    for (int j = 0; j < 5; ++j) {
      const unsigned i = 64 * j + ln, r = (i * 6554u) >> 16, ch = i - r * 10;
      const unsigned ra = !ow ? r : tail ? 80 * (r >> 4) + 20 * ((r >> 2) & 3) + 16 + (r & 3) : 20 * (r >> 4) + (r & 15);
      va[j] = ra * 1280 + ctoff + ch * 16;
    }
  }
};

__device__ __forceinline__ float u2f(unsigned u) { return __builtin_bit_cast(float, u); }
__device__ __forceinline__ unsigned f2u(float f) { return __builtin_bit_cast(unsigned, f); }
__device__ __forceinline__ float xmax(float v) {
  auto r = __builtin_amdgcn_permlane16_swap(f2u(v), f2u(v), false, false);
  v = fmaxf(u2f(r[0]), u2f(r[1]));
  auto s = __builtin_amdgcn_permlane32_swap(f2u(v), f2u(v), false, false);
  return fmaxf(u2f(s[0]), u2f(s[1]));
}
__device__ __forceinline__ float xsum(float v) {
  auto r = __builtin_amdgcn_permlane16_swap(f2u(v), f2u(v), false, false);
  v = u2f(r[0]) + u2f(r[1]);
  auto s = __builtin_amdgcn_permlane32_swap(f2u(v), f2u(v), false, false);
  return u2f(s[0]) + u2f(s[1]);
}

template <int PH>
__global__ __launch_bounds__(512, 2) void fused(const void* __restrict__ x, const void* __restrict__ wqkv,
                                                const void* __restrict__ wo, const void* __restrict__ wg,
                                                const void* __restrict__ wu, const void* __restrict__ wd,
                                                const void* __restrict__ gw, void* out, void* ws) {
  __shared__ __attribute__((aligned(16))) char smem[LDS_BYTES];
  asm volatile("" :: "s"((unsigned)(size_t)(LAS char*)smem));
  const int tid = threadIdx.x, lane = tid & 63;
  const int w = rfl(tid >> 6);
  const int h = lane >> 4, c = lane & 15, q4 = (lane & 15) >> 2, p4 = lane & 3;
  const int b = blockIdx.x, xcd = b & 7, idx = b >> 3;
  const int rh = xcd & 1, cq = xcd >> 1;
  const bool isq = idx < 14;
  const int rb = 4 * xcd + (idx >> 3), ct = idx & 7;
  const __amdgpu_buffer_rsrc_t rws = __builtin_amdgcn_make_buffer_rsrc(ws, 0, WS_END, 0x00020000);
  u64 amp_t0 = 0, amp_t1 = 0;
  STAMP(0);

  asm volatile("" :: "s"(x), "s"(wqkv), "s"(wo), "s"(wg), "s"(wu), "s"(wd), "s"(gw), "s"(out), "s"(ws));
  u64 ev = 0, tag = 0;
  if (PH == 3) asm volatile("global_load_dwordx2 %0, %1, %2 sc1" : "=v"(ev) : "v"(WS_EPOCH), "s"(ws) : "memory");
  auto make_tag = [&]() {
    asm volatile("" : "+v"(ev));
    const u64 e = ((u64)rfl((unsigned)(ev >> 32)) << 32) | rfl((unsigned)ev);
    tag = (e ^ 0x9E3779B97F4A7C15ull) * 0xBF58476D1CE4E5B9ull + 0x632BE59BD9B4E019ull;
    if (tag == e) tag += 1;
  };

  P2W pw;
  int nst, g0;
  {
    const bool ow = w < 2;
    g0 = (int)((0x271E171008000000ull >> (8 * w)) & 0xff);
    nst = (0x99778800 >> (4 * w)) & 15;
    pw.srd = ow ? mk_srd(wo, 640 * 1280) : mk_srd(wd, 1536 * 1280);
    pw.cell0 = w * 20480;
    pw.ow = ow; pw.ctoff = ct * 160;
    pw.sa = ow ? w * 320 * 1280 : g0 * 32 * 1280;
    pw.sast = ow ? 40 * 1280 : 32 * 1280;
    pw.sb = ow ? w * 320 * 1280 : (g0 + 8) * 32 * 1280;
    pw.sbst = ow ? 160 * 1280 : 32 * 1280;
  }

  if (PH & 1) {
    if (isq) {
      const int a = 7 * rh + (idx >= 7 ? idx - 7 : idx), g = 2 * cq + (idx >= 7 ? 1 : 0);
      const int hd = w & 3, par = w >> 2;
      P1<true> k;
      k.nm = 5;
      k.srdx = mk_srd(x, 2048 * 1280);
      k.srdw = mk_srd(wqkv, 640 * 3840);
      unsigned gv0, gv1;
      {
        const u32x4 sg = mk_srd(gw, 1280);
        asm volatile("buffer_load_ushort %0, %2, %3, 0 offen\n\tbuffer_load_ushort %1, %2, %3, 0 offen offset:1024"
                     : "=&v"(gv0), "=&v"(gv1) : "v"(tid * 2), "s"(sg) : "memory");
      }
#pragma unroll
      for (int i = 0; i < 3; ++i) {
        const int q = w * 3 + i;
        const unsigned rho = 8 * q + (lane >> 3);
        const unsigned pp = rho >= 80 ? 1 : 0, u = rho - 80 * pp;
        const unsigned tok = 146 * a + 2 * u + pp;
        k.xv[i] = q < 20 ? tok * 1280 + (((lane & 7) ^ ((rho >> 1) & 7)) << 4) : OOB;
        k.xm0[i] = q < 20 ? q * 1024 : XSLOT;
      }
#pragma unroll
      for (int j = 0; j < 2; ++j) {
        const int I = (2 * w + j) * 64 + lane;
        const int kb = I >= 736 ? 3 : I >= 488 ? 2 : I >= 248 ? 1 : 0;
        const int i = I - (kb == 3 ? 736 : kb == 2 ? 488 : kb == 1 ? 248 : 0);
        const unsigned r = ((unsigned)i * 2185u) >> 16, rem = i - r * 30;
        const unsigned seg = (rem * 6554u) >> 16, ch = rem - seg * 10;
        k.wv[j] = i < 240 ? (8 * kb + r) * 3840 + seg * 1280 + g * 160 + ch * 16 : OOB;
      }
      k.wm0 = w * 2048;
      k.prologue_dma();
      k.xa0 = XBASE + par * 5 * 2048 + c * 128 + ((h ^ (c >> 1)) << 4);
      k.xa1 = k.xa0 ^ 64;
      const unsigned kbo = h == 3 ? 736 * 16 : h == 2 ? 488 * 16 : h == 1 ? 248 * 16 : 0;
      k.wa0 = kbo + q4 * 480 + (20 * hd + 4 * p4) * 2;
      k.isgate = p4 == 3;
      k.wa1 = k.isgate ? GATE_OFF + (8 * h + q4) * 8 : kbo + q4 * 480 + p4 * 160 + (20 * hd + 16) * 2;
      k.wa2 = k.wa1 + (k.isgate ? 32 : 1920);
      k.zero();
      asm volatile("s_waitcnt vmcnt(14)" : "+v"(gv0), "+v"(gv1) :: "memory");
      {
        u32x2 v; v.x = gv0; v.y = 0;
        *(LAS u32x2*)(GATE_OFF + tid * 8) = v;
        if (tid < 128) { v.x = gv1; *(LAS u32x2*)(GATE_OFF + 4096 + tid * 8) = v; }
      }
      asm volatile("s_waitcnt lgkmcnt(0)" ::: "memory");
      BAR();
      STAMP(1);
      k.run();
      STAMP(2);
      if (PH & 2) { pw.pattern(false); pw.dma(0, 0); pw.dma(1, 1); pw.dma(2, 2); }
      u32x2 ql[5], kl[5], t2[5], vl[5], tt[5];
      const unsigned scr = pw.cell0 + 15360;
#pragma unroll
      for (int i = 0; i < 5; ++i) {
        ql[i] = pk4(k.acc[i][0]); kl[i] = pk4(k.acc[i][1]); t2[i] = pk4(k.acc[i][2]); vl[i] = pk4(k.acc[i][3]);
        *(LAS u32x2*)(scr + i * 512 + c * 32 + h * 8) = t2[i];
      }
      asm volatile("s_waitcnt lgkmcnt(0)" ::: "memory");
#pragma unroll
      for (int i = 0; i < 5; ++i) tt[i] = ldstr(scr + i * 512 + (4 * h + q4) * 32 + p4 * 8);
      asm volatile("s_waitcnt lgkmcnt(0)" : "+v"(tt[0]), "+v"(tt[1]), "+v"(tt[2]), "+v"(tt[3]), "+v"(tt[4]) :: "memory");
      if (PH & 2) pw.dma(3, 3);
      f32x4 ba, bb;
#pragma unroll
      for (int r = 0; r < 4; ++r) {
        const int kap = 4 * h + r;
        ba[r] = kap >= c + 9 ? 0.f : -1e30f;
        bb[r] = (kap <= c && kap >= c - 7) ? 0.f : -1e30f;
      }
      const unsigned m0mask = h == 0 ? 0xffffffffu : 0u;
      u32x4 kf[5];
#pragma unroll
      for (int i = 0; i < 5; ++i) {
        auto s0 = __builtin_amdgcn_permlane16_swap(t2[i].x, t2[i].x, false, false);
        auto s1 = __builtin_amdgcn_permlane16_swap(t2[i].y, t2[i].y, false, false);
        kf[i].x = kl[i].x; kf[i].y = kl[i].y; kf[i].z = s0[1]; kf[i].w = s1[1];
      }
      constexpr float SC = 0.22360679774997896f * 1.4426950408889634f;
#pragma unroll
      for (int i = 0; i < 5; ++i) {
        u32x4 qf; qf.x = ql[i].x; qf.y = ql[i].y; qf.z = t2[i].x & m0mask; qf.w = t2[i].y & m0mask;
        f32x4 sb = mfma(kf[i], qf, bb);
        f32x4 sa = f32x4{-1e30f, -1e30f, -1e30f, -1e30f};
        if (i > 0) sa = mfma(kf[i - 1], qf, ba);
        float m = fmaxf(fmaxf(fmaxf(sb.x, sb.y), fmaxf(sb.z, sb.w)), fmaxf(fmaxf(sa.x, sa.y), fmaxf(sa.z, sa.w)));
        m = xmax(m);
        const float mc = m * SC;
        f32x4 pa, pb;
#pragma unroll
        for (int r = 0; r < 4; ++r) {
          pb[r] = __builtin_amdgcn_exp2f(sb[r] * SC - mc);
          pa[r] = i > 0 ? __builtin_amdgcn_exp2f(sa[r] * SC - mc) : 0.f;
        }
        float l = ((pb.x + pb.y) + (pb.z + pb.w)) + ((pa.x + pa.y) + (pa.z + pa.w));
        l = xsum(l);
        const float rl = __builtin_amdgcn_rcpf(l);
        const u32x4 pf = cat(pk4(pa * rl), pk4(pb * rl));
        const f32x4 z = {0.f, 0.f, 0.f, 0.f};
        const f32x4 oa = mfma(cat(vl[i > 0 ? i - 1 : 0], vl[i]), pf, z);
        const f32x4 ob = mfma(cat(tt[i > 0 ? i - 1 : 0], tt[i]), pf, z);
        const int u = 16 * i + c, U = 73 * a + u;
        const bool ok = (a == 0 || u >= 7) && U < 1024;
        const unsigned mt = 2 * (U >> 4) + par, cc = U & 15;
        const unsigned oo = WS_O + mt * (40 * 512) + cc * 8;
        __builtin_amdgcn_raw_buffer_store_b64(pk4(oa), rws, ok ? oo + (4 * g + hd) * 512 + h * 128 : OOB, 0, AUX_SC1);
        __builtin_amdgcn_raw_buffer_store_b64(pk4(ob), rws, (ok && h == 2) ? oo + (32 + g) * 512 + hd * 128 : OOB, 0, AUX_SC1);
        if (g == 0 && hd == 0)
          __builtin_amdgcn_raw_buffer_store_b32(f2u(k.acc[i][2].x), rws,
                                                (ok && h == 3) ? WS_G + (mt * 16 + cc) * 4 : OOB, 0, AUX_SC1);
      }
      STAMP(3);
      if (PH == 3) {
        WAITV(0);
        BAR();
        make_tag();
        if (w == 0 && lane < 5) {
          const int U0 = a ? 73 * a + 7 : 0, U1 = min(73 * a + 80, 1024);
          const int r = (U0 >> 5) + lane;
          const int a0 = r * 32 < 80 ? 0 : (r * 32 - 7) / 73;
          GAS u64* f = (GAS u64*)((char*)ws + (lane == 4 ? WS_DONE + b * 8 : WS_FLAG + (r * 64 + 24 + (a - a0) * 8 + g) * 8));
          if (lane == 4 || r * 32 < U1) __hip_atomic_store(f, tag, __ATOMIC_RELAXED, __HIP_MEMORY_SCOPE_AGENT);
        }
      }
    } else {
      const int f = idx - 14;
      const int fr = f >= 12 ? f - 12 : f >= 6 ? f - 6 : f, fc = f >= 12 ? 2 : f >= 6 ? 1 : 0;
      const int ri = 6 * rh + fr, cj = 3 * cq + fc;
      const int nq = w & 3, mh = w >> 2;
      P1<false> k;
      k.nm = mh ? 5 : 6;
      k.isgate = false;
      k.srdx = mk_srd(x, 2048 * 1280);
      k.srdw = mk_srd((w & 1) ? wu : wg, 640 * 3072);
#pragma unroll
      for (int i = 0; i < 3; ++i) {
        const int q = w * 3 + i;
        const unsigned rho = 8 * q + (lane >> 3);
        const unsigned mt = 11 * ri + (rho >> 4);
        const unsigned tok = 32 * (mt >> 1) + 2 * (rho & 15) + (mt & 1);
        k.xv[i] = q < 22 ? tok * 1280 + (((lane & 7) ^ ((rho >> 1) & 7)) << 4) : OOB;
        k.xm0[i] = q < 22 ? q * 1024 : XSLOT;
      }
      {
        const int kb = w >> 1, r = lane >> 3, p8 = lane & 7;
        const int s = ((r >> 1) & 1) + 2 * (kb & 1);
        k.wv[0] = (8 * kb + r) * 3072 + cj * 256 + (((((p8 >> 1) ^ s) << 1) | (p8 & 1)) << 4);
        k.wv[1] = 0;
      }
      k.wm0 = w * 2048;
      k.prologue_dma();
      k.xa0 = XBASE + mh * 6 * 2048 + c * 128 + ((h ^ (c >> 1)) << 4);
      k.xa1 = k.xa0 ^ 64;
      {
        const int s = ((q4 >> 1) & 1) + 2 * (h & 1);
        k.wa0 = (h * 4 + (nq >> 1)) * 1024 + q4 * 128 + (((2 * (nq & 1)) ^ s) << 5) + p4 * 8;
        k.wa1 = k.wa0 ^ 32;
        k.wa2 = 0;
      }
      k.zero();
      WAITV(14);
      BAR();
      STAMP(1);
      k.run();
      STAMP(2);
      if (PH & 2) { pw.pattern(false); pw.dma(0, 0); pw.dma(1, 1); pw.dma(2, 2); pw.dma(3, 3); }
#pragma unroll
      for (int m = 0; m < 6; ++m) {
        if (m < k.nm) {
          float v[8];
#pragma unroll
          for (int j = 0; j < 8; ++j) {
            const float gt = k.acc[m][j >> 2][j & 3], up = k.acc[m][2 + (j >> 2)][j & 3];
            const float e = __builtin_amdgcn_exp2f(gt * -1.4426950408889634f);
            v[j] = gt * up * __builtin_amdgcn_rcpf(1.f + e);
          }
          u32x4 o; o.x = pk2(v[0], v[1]); o.y = pk2(v[2], v[3]); o.z = pk2(v[4], v[5]); o.w = pk2(v[6], v[7]);
          const unsigned mt = 11 * ri + 6 * mh + m;
          __builtin_amdgcn_raw_buffer_store_b128(o, rws, mt < 128 ? WS_GU + (mt * 48 + 4 * cj + nq) * 1024 + lane * 16 : OOB, 0, AUX_SC1);
        }
      }
      STAMP(3);
      if (PH == 3) {
        WAITV(0);
        BAR();
        make_tag();
        if (w == 0 && lane < 5) {
          const int m0_ = 11 * ri, m1 = min(m0_ + 11, 128);
          const int r = (m0_ >> 2) + lane;
          const int r0 = (4 * r) / 11;
          GAS u64* fp = (GAS u64*)((char*)ws + (lane == 4 ? WS_DONE + b * 8 : WS_FLAG + (r * 64 + (ri - r0) * 12 + cj) * 8));
          if (lane == 4 || r * 4 < m1) __hip_atomic_store(fp, tag, __ATOMIC_RELAXED, __HIP_MEMORY_SCOPE_AGENT);
        }
      }
    }
  }
  STAMP(4);

  if (PH & 2) {
    if (PH == 2) { pw.pattern(false); pw.dma(0, 0); pw.dma(1, 1); pw.dma(2, 2); pw.dma(3, 3); WAITV(0); }
    auto body = [&](auto OWT) {
      constexpr bool OW = decltype(OWT)::value;
      constexpr unsigned AMT = OW ? 40 * 512 : 48 * 1024;
      constexpr int NS = OW ? 10 : 9;
      const unsigned wra = pw.cell0 + (4 * h + q4) * 160 + p4 * 8;
      u32x4 wreg[4][5];
      auto rdw = [&](int cell) {
#pragma unroll
        for (int n = 0; n < 5; ++n) wreg[cell][n] = cat(ldstr(wra + cell * 5120 + n * 32), ldstr(wra + cell * 5120 + n * 32 + 2560));
      };
      rdw(0); rdw(1); rdw(2); rdw(3);
      asm volatile("s_waitcnt lgkmcnt(0)" ::: "memory");
      SB();
      pw.dma(4, 0); pw.dma(5, 1); pw.dma(6, 2);
      if (OW || nst > 7) pw.dma(7, 3);
      if (OW) pw.pattern(true);
      if (PH == 3) {
        const bool two_f = (4 * rb) / 11 != (4 * rb + 3) / 11;
        const int a0 = rb * 32 < 80 ? 0 : (rb * 32 - 7) / 73, a1 = rb * 32 + 31 < 80 ? 0 : (rb * 32 + 31 - 7) / 73;
        bool need;
        if (lane < 24) {
          const int cjj = lane >= 12 ? lane - 12 : lane;
          need = !OW && (lane < 12 || two_f) && cjj >= (g0 >> 2) && cjj <= ((g0 + nst - 1) >> 2);
        } else {
          const int gg = (lane - 24) & 7;
          need = lane < 40 && (lane < 32 || a1 != a0) && (gg == 0 || (OW && (gg >> 2) == w));
        }
        GAS u64* fp = (GAS u64*)((char*)ws + WS_FLAG + (rb * 64 + lane) * 8);
        for (unsigned spin = 0; spin < (1u << 18); ++spin) {
          const u64 v = __hip_atomic_load(fp, __ATOMIC_RELAXED, __HIP_MEMORY_SCOPE_AGENT);
          if (__all(!need || v == tag)) break;
          __builtin_amdgcn_s_sleep(2);
        }
        __builtin_amdgcn_fence(__ATOMIC_ACQUIRE, "wavefront");
        asm volatile("" ::: "memory");
      }
      STAMP(5);
      const unsigned av = OW ? lane * 8 : lane * 16;
      const unsigned as0 = (OW ? WS_O + 8 * w * 1024 : WS_GU + g0 * 1024) + 4 * rb * AMT;
      const unsigned as1 = WS_O + (16 + 2 * w) * 1024 + 4 * rb * AMT;
      u32x4 af[4][4];
      f32x4 acc[4][5];
      float sg[4];
      auto lda = [&](int s) {
        const unsigned so = (OW && s >= 8) ? as1 + (s - 8) * 1024 : as0 + s * 1024;
#pragma unroll
        for (int m = 0; m < 4; ++m) {
          if (OW) af[s & 3][m] = cat(__builtin_amdgcn_raw_buffer_load_b64(rws, av, so + m * AMT, AUX_SC1),
                                     __builtin_amdgcn_raw_buffer_load_b64(rws, av + 512, so + m * AMT, AUX_SC1));
          else    af[s & 3][m] = __builtin_amdgcn_raw_buffer_load_b128(rws, av, so + m * AMT, AUX_SC1);
        }
      };
      lda(0); lda(1); lda(2); lda(3);
      SB();
#pragma unroll
      for (int m = 0; m < 4; ++m)
#pragma unroll
        for (int n = 0; n < 5; ++n) acc[m][n] = f32x4{0.f, 0.f, 0.f, 0.f};
#pragma unroll
      for (int s = 0; s < NS; ++s) {
        if (OW || s < 7 || s < nst) {
#pragma unroll
          for (int m = 0; m < 4; ++m)
#pragma unroll
            for (int n = 0; n < 5; ++n) acc[m][n] = mfma(wreg[s & 3][n], af[s & 3][m], acc[m][n]);
          SB();
          asm volatile("" ::: "memory");
          if (s + 4 < NS && (OW || s + 4 < 7 || s + 4 < nst)) {
            rdw(s & 3);
            if (s + 8 < NS) {
              if (OW || s + 8 < nst) { asm volatile("s_waitcnt lgkmcnt(0)" ::: "memory"); SB(); pw.dma(s + 8, s & 3); }
            }
            lda(s + 4);
          }
          if (s == 6) {
#pragma unroll
            for (int m = 0; m < 4; ++m)
              sg[m] = __builtin_bit_cast(float, __builtin_amdgcn_raw_buffer_load_b32(rws, c * 4, WS_G + (4 * rb + m) * 64, AUX_SC1));
          }
          SB();
        }
      }
      STAMP(6);
#pragma unroll
      for (int m = 0; m < 4; ++m) {
        const float e = __builtin_amdgcn_exp2f(sg[m] * (OW ? -1.4426950408889634f : 1.4426950408889634f));
        const float f = __builtin_amdgcn_rcpf(1.f + e);
#pragma unroll
        for (int n = 0; n < 5; ++n) *(LAS f32x4*)(pw.cell0 + (m * 5 + n) * 1024 + lane * 16) = acc[m][n] * f;
      }
    };
    if (w < 2) body(std::true_type{}); else body(std::false_type{});
    if (PH == 3 && b == 0 && w == 4) {
      GAS u64* dp = (GAS u64*)((char*)ws + WS_DONE + lane * 8);
      for (unsigned spin = 0; spin < (1u << 18); ++spin) {
        bool ok = true;
#pragma unroll
        for (int j = 0; j < 4; ++j) ok &= __hip_atomic_load(dp + j * 64, __ATOMIC_RELAXED, __HIP_MEMORY_SCOPE_AGENT) == tag;
        if (__all(ok)) break;
        __builtin_amdgcn_s_sleep(2);
      }
      if (lane == 0) __hip_atomic_store((GAS u64*)((char*)ws + WS_EPOCH), tag, __ATOMIC_RELAXED, __HIP_MEMORY_SCOPE_AGENT);
    }
    asm volatile("s_waitcnt lgkmcnt(0)" ::: "memory");
    BAR();
    const int t0 = w < 4 ? 3 * w : 12 + 2 * (w - 4);
#pragma unroll
    for (int j = 0; j < 3; ++j) {
      if (j < 2 || w < 4) {
        const int tl = t0 + j;
        f32x4 s = *(const LAS f32x4*)(tl * 1024 + lane * 16);
#pragma unroll
        for (int v = 1; v < 8; ++v) s += *(const LAS f32x4*)(v * 20480 + tl * 1024 + lane * 16);
        const int m = tl / 5, n = tl - 5 * m;
        const int tok = 64 * rb + 32 * (m >> 1) + 2 * c + (m & 1);
        *(GAS u32x2*)((char*)out + tok * 1280 + ct * 160 + n * 32 + h * 8) = pk4(s);
      }
    }
    STAMP(7);
    if (AMP0 >= 0) {
      const u64 d = amp_t1 - amp_t0, t = __builtin_amdgcn_s_memrealtime();
      while (__builtin_amdgcn_s_memrealtime() - t < d) __builtin_amdgcn_s_sleep(1);
    }
  }
}
}

extern "C" void kernel_launch(void* const* d_in, const int* in_sizes, int n_in, void* d_out, int out_size, void* d_ws,
                              size_t ws_size, hipStream_t stream) {
  fused<3><<<256, 512, 0, stream>>>(d_in[0], d_in[1], d_in[2], d_in[4], d_in[5], d_in[6], d_in[7], d_out, d_ws);
}
```

```cpp
#include <hip/hip_runtime.h>
#include <hip/hip_bf16.h>
#include <stdint.h>
#include <type_traits>

#define SPLIT 0
#define STAMPS 0

#define ABL 0
#define LAS __attribute__((address_space(3)))
#define GAS __attribute__((address_space(1)))
typedef __bf16 bf16x8 __attribute__((ext_vector_type(8)));
typedef __bf16 bf16x2 __attribute__((ext_vector_type(2)));
typedef float f32x4 __attribute__((ext_vector_type(4)));
typedef float f32x2 __attribute__((ext_vector_type(2)));
typedef unsigned u32x4 __attribute__((ext_vector_type(4)));
typedef unsigned u32x2 __attribute__((ext_vector_type(2)));
typedef short s16x4 __attribute__((ext_vector_type(4)));
typedef unsigned long long u64;

namespace {
constexpr int LDS_BYTES = 163840;
constexpr unsigned WSLOT = 16384, XBASE = 81920, XSLOT = 22528, GATE_OFF = 149504, SCR_OFF = 154624;
constexpr unsigned OOB = 0x40000000u;
constexpr unsigned WS_FLAG = 0;
constexpr unsigned WS_DONE = 16384;
constexpr unsigned WS_EPOCH = 20480;
constexpr unsigned WS_G = 24576;
constexpr unsigned WS_O = 65536;
constexpr unsigned WS_GU = WS_O + 128 * 40 * 512;
constexpr unsigned WS_END = WS_GU + 128 * 48 * 1024;
constexpr unsigned WS_STAMP = 16u << 20;
constexpr int AUX_SC1 = 16;

__device__ __forceinline__ u32x4 mk_srd(const void* p, unsigned bytes) {
  const u64 a = (u64)p;
  u32x4 r; r.x = (unsigned)a; r.y = (unsigned)(a >> 32) & 0xffffu; r.z = bytes; r.w = 0x00020000u; return r;
}
__device__ __forceinline__ void dma16(unsigned lds, unsigned imm, unsigned voff, u32x4 srd, unsigned soff) {
  asm volatile("s_add_u32 m0, %0, %4\n\ts_nop 0\n\tbuffer_load_dwordx4 %1, %2, %3 offen lds"
               :: "s"(lds), "v"(voff), "s"(srd), "s"(soff), "i"(imm) : "memory", "scc");
}
__device__ __forceinline__ u32x4 lds128(unsigned a) { return *(const LAS u32x4*)a; }
__device__ __forceinline__ u32x2 ldstr(unsigned a) {
  return __builtin_bit_cast(u32x2, __builtin_amdgcn_ds_read_tr16_b64_v4i16((LAS s16x4*)a));
}
__device__ __forceinline__ u32x4 cat(u32x2 a, u32x2 b) { u32x4 r; r.x = a.x; r.y = a.y; r.z = b.x; r.w = b.y; return r; }
__device__ __forceinline__ f32x4 mfma(u32x4 a, u32x4 b, f32x4 c) {
  return __builtin_amdgcn_mfma_f32_16x16x32_bf16(__builtin_bit_cast(bf16x8, a), __builtin_bit_cast(bf16x8, b), c, 0, 0, 0);
}
__device__ __forceinline__ unsigned pk2(float a, float b) {
  f32x2 t = {a, b};
  return __builtin_bit_cast(unsigned, __builtin_convertvector(t, bf16x2));
}
__device__ __forceinline__ u32x2 pk4(f32x4 v) { u32x2 r; r.x = pk2(v.x, v.y); r.y = pk2(v.z, v.w); return r; }
#define SB() __builtin_amdgcn_sched_barrier(0)
#define WAITV(n) asm volatile("s_waitcnt vmcnt(" #n ")" ::: "memory")
#define WAITVL(n) asm volatile("s_waitcnt vmcnt(" #n ") lgkmcnt(0)" ::: "memory")
#define BAR() do { asm volatile("s_barrier" ::: "memory"); } while (0)
__device__ __forceinline__ unsigned rfl(unsigned v) { return __builtin_amdgcn_readfirstlane(v); }

#define AMP0 -1
#define AMP1 -1
#define STAMP(i) do {} while (0)

template <bool QKV>
struct P1 {
  static constexpr int NM = QKV ? 5 : 6;
  f32x4 acc[NM][4];
  u32x4 srdx, srdw;
  unsigned xv[3], wv[2];
  unsigned xm0[3];
  unsigned wm0;
  unsigned xa0, xa1;
  unsigned wa0, wa1, wa2;
  bool isgate;
  int nm;

  __device__ __forceinline__ void dma_w(int t, unsigned slot) {
    const unsigned so = (unsigned)t * (QKV ? 32u * 3840u : 32u * 3072u);
    dma16(slot + wm0, 0, wv[0], srdw, so);
    dma16(slot + wm0, 1024, wv[QKV ? 1 : 0], srdw, QKV ? so : so + 128);
  }
  __device__ __forceinline__ void dma_x1(int i, int j, unsigned slot) {
    const unsigned so = j < 10 ? (unsigned)j * 128u : OOB;
    const unsigned d = xm0[i] < XSLOT ? XBASE + slot + xm0[i] : SCR_OFF;
    dma16(d, 0, xv[i], srdx, so);
  }
  __device__ __forceinline__ void rd_w(u32x4 (&wf)[4], unsigned slot, int t) {
    if (QKV) {
      const unsigned a = wa0 + slot;
      const unsigned s2 = isgate ? (unsigned)t * 256u : slot;
      wf[0] = cat(ldstr(a), ldstr(a + 1920));
      wf[1] = cat(ldstr(a + 160), ldstr(a + 160 + 1920));
      wf[2] = cat(ldstr(wa1 + s2), ldstr(wa2 + s2));
      wf[3] = cat(ldstr(a + 320), ldstr(a + 320 + 1920));
    } else {
      const unsigned a = wa0 + slot, b = wa1 + slot;
      wf[0] = cat(ldstr(a), ldstr(a + 512));
      wf[1] = cat(ldstr(b), ldstr(b + 512));
      wf[2] = cat(ldstr(a + 2048), ldstr(a + 2048 + 512));
      wf[3] = cat(ldstr(b + 2048), ldstr(b + 2048 + 512));
    }
  }
  __device__ __forceinline__ void mm(int m, const u32x4 (&xf)[NM], const u32x4 (&wf)[4]) {
    if (ABL & 2) { asm volatile("" :: "v"(xf[m]), "v"(wf[0]), "v"(wf[1]), "v"(wf[2]), "v"(wf[3])); return; }
    acc[m][0] = mfma(wf[0], xf[m], acc[m][0]);
    acc[m][1] = mfma(wf[1], xf[m], acc[m][1]);
    acc[m][2] = mfma(wf[2], xf[m], acc[m][2]);
    if (QKV) acc[m][3] = mfma(xf[m], wf[3], acc[m][3]);
    else     acc[m][3] = mfma(wf[3], xf[m], acc[m][3]);
  }
  template <bool ODD>
  __device__ __forceinline__ void step(int t, unsigned wrd, unsigned wwr, unsigned xrd, unsigned xwr,
                                       const u32x4 (&xc)[NM], const u32x4 (&wc)[4], u32x4 (&xn)[NM], u32x4 (&wn)[4]) {
    if (ODD) WAITVL(9); else WAITVL(12);
    if (!(ABL & 8)) BAR();
    SB();
    mm(0, xc, wc);
    SB();
    if (!(ABL & 4)) rd_w(wn, wrd, t + 1);
    else { for (int n = 0; n < 4; ++n) { wn[n] = wc[n]; asm volatile("" : "+v"(wn[n])); } }
    SB();
    mm(1, xc, wc);
    SB();
    const unsigned xa = (ODD ? xa0 : xa1) + xrd;
    if (!(ABL & 4)) {
#pragma unroll
    for (int m = 0; m < 5; ++m) xn[m] = lds128(xa + m * 2048);
    if (!QKV) xn[NM - 1] = lds128(xa + (NM - 1) * 2048);
    } else { for (int m = 0; m < NM; ++m) { xn[m] = xc[m]; asm volatile("" : "+v"(xn[m])); } }
    SB();
    mm(2, xc, wc);
    SB();
    if (!(ABL & 1)) dma_w(t + 5, wwr);
    SB();
    mm(3, xc, wc);
    SB();
    if (ODD && !(ABL & 1)) { dma_x1(0, (t + 5) >> 1, xwr); dma_x1(1, (t + 5) >> 1, xwr); dma_x1(2, (t + 5) >> 1, xwr); }
    SB();
    mm(4, xc, wc);
    if (!QKV) { if (nm == 6) mm(NM - 1, xc, wc); }
    SB();
  }
  __device__ __forceinline__ void prologue_dma() {
    dma_w(0, 0);
    dma_x1(0, 0, 0); dma_x1(1, 0, 0); dma_x1(2, 0, 0);
    dma_w(1, WSLOT);
    dma_w(2, 2 * WSLOT);
    dma_x1(0, 1, XSLOT); dma_x1(1, 1, XSLOT); dma_x1(2, 1, XSLOT);
    dma_w(3, 3 * WSLOT);
    dma_w(4, 4 * WSLOT);
    dma_x1(0, 2, 2 * XSLOT); dma_x1(1, 2, 2 * XSLOT); dma_x1(2, 2, 2 * XSLOT);
  }
  __device__ __forceinline__ void zero() {
#pragma unroll
    for (int m = 0; m < NM; ++m)
#pragma unroll
      for (int n = 0; n < 4; ++n) { acc[m][n] = f32x4{0.f, 0.f, 0.f, 0.f}; asm volatile("" : "+v"(acc[m][n])); }
  }
  __device__ __forceinline__ void run() {
    u32x4 xA[NM], wA[4], xB[NM], wB[4];
    rd_w(wA, 0, 0);
#pragma unroll
    for (int m = 0; m < NM; ++m) xA[m] = lds128(xa0 + m * 2048);
    unsigned w0 = 0, w1 = WSLOT;
    unsigned x0 = 0, x1 = XSLOT;
#pragma unroll 1
    for (int t = 0; t < 20; t += 2) {
      unsigned w2 = w1 + WSLOT; if (w2 == 5 * WSLOT) w2 = 0;
      step<false>(t, w1, w0, x0, 0, xA, wA, xB, wB);
      step<true>(t + 1, w2, w1, x1, x0, xB, wB, xA, wA);
      w0 = w2; w1 = w2 + WSLOT; if (w1 == 5 * WSLOT) w1 = 0;
      x0 = x1; x1 += XSLOT; if (x1 == 3 * XSLOT) x1 = 0;
    }
    WAITVL(0);
    BAR();
  }
};

struct P2W {
  u32x4 srd;
  unsigned va[5];
  bool ow; unsigned ctoff;
  unsigned sa, sast, sb, sbst;
  unsigned cell0;
  __device__ __forceinline__ void dma(int s, int cell) {
    const unsigned so = s < 8 ? sa + s * sast : sb + (s - 8) * sbst;
#pragma unroll
    for (int j = 0; j < 5; ++j) dma16(cell0, cell * 5120 + j * 1024, va[j], srd, so);
  }
  __device__ __forceinline__ void pattern(bool tail) {
    unsigned ln = threadIdx.x & 63;
    asm volatile("" : "+v"(ln));
#pragma unroll
    for (int j = 0; j < 5; ++j) {
      const unsigned i = 64 * j + ln, r = (i * 6554u) >> 16, ch = i - r * 10;
      const unsigned ra = !ow ? r : tail ? 80 * (r >> 4) + 20 * ((r >> 2) & 3) + 16 + (r & 3) : 20 * (r >> 4) + (r & 15);
      va[j] = ra * 1280 + ctoff + ch * 16;
    }
  }
};

__device__ __forceinline__ float u2f(unsigned u) { return __builtin_bit_cast(float, u); }
__device__ __forceinline__ unsigned f2u(float f) { return __builtin_bit_cast(unsigned, f); }
__device__ __forceinline__ float xmax(float v) {
  auto r = __builtin_amdgcn_permlane16_swap(f2u(v), f2u(v), false, false);
  v = fmaxf(u2f(r[0]), u2f(r[1]));
  auto s = __builtin_amdgcn_permlane32_swap(f2u(v), f2u(v), false, false);
  return fmaxf(u2f(s[0]), u2f(s[1]));
}
__device__ __forceinline__ float xsum(float v) {
  auto r = __builtin_amdgcn_permlane16_swap(f2u(v), f2u(v), false, false);
  v = u2f(r[0]) + u2f(r[1]);
  auto s = __builtin_amdgcn_permlane32_swap(f2u(v), f2u(v), false, false);
  return u2f(s[0]) + u2f(s[1]);
}

template <int PH>
__global__ __launch_bounds__(512, 2) void fused(const void* __restrict__ x, const void* __restrict__ wqkv,
                                                const void* __restrict__ wo, const void* __restrict__ wg,
                                                const void* __restrict__ wu, const void* __restrict__ wd,
                                                const void* __restrict__ gw, void* out, void* ws) {
  __shared__ __attribute__((aligned(16))) char smem[LDS_BYTES];
  u64 tfirst, tsecond;
  asm volatile("s_memrealtime %0\n\ts_waitcnt lgkmcnt(0)" : "=s"(tfirst));
  __builtin_amdgcn_sched_barrier(0);
  asm volatile("s_memrealtime %0\n\ts_waitcnt lgkmcnt(0)" : "=s"(tsecond) : "s"(x), "s"(ws));
  asm volatile("" :: "s"((unsigned)(size_t)(LAS char*)smem));
  const int tid = threadIdx.x, lane = tid & 63;
  const int w = rfl(tid >> 6);
  const int h = lane >> 4, c = lane & 15, q4 = (lane & 15) >> 2, p4 = lane & 3;
  const int b = blockIdx.x, xcd = b & 7, idx = b >> 3;
  const int rh = xcd & 1, cq = xcd >> 1;
  const bool isq = idx < 14;
  const int rb = 4 * xcd + (idx >> 3), ct = idx & 7;
  const __amdgpu_buffer_rsrc_t rws = __builtin_amdgcn_make_buffer_rsrc(ws, 0, WS_END, 0x00020000);
  u64 amp_t0 = 0, amp_t1 = 0;
  STAMP(0);

  asm volatile("" :: "s"(x), "s"(wqkv), "s"(wo), "s"(wg), "s"(wu), "s"(wd), "s"(gw), "s"(out), "s"(ws));
  u64 ev = 0, tag = 0;
  if (PH == 3) asm volatile("global_load_dwordx2 %0, %1, %2 sc1" : "=v"(ev) : "v"(WS_EPOCH), "s"(ws) : "memory");
  auto make_tag = [&]() {
    asm volatile("" : "+v"(ev));
    const u64 e = ((u64)rfl((unsigned)(ev >> 32)) << 32) | rfl((unsigned)ev);
    tag = (e ^ 0x9E3779B97F4A7C15ull) * 0xBF58476D1CE4E5B9ull + 0x632BE59BD9B4E019ull;
    if (tag == e) tag += 1;
  };

  P2W pw;
  int nst, g0;
  {
    const bool ow = w < 2;
    g0 = (int)((0x271E171008000000ull >> (8 * w)) & 0xff);
    nst = (0x99778800 >> (4 * w)) & 15;
    pw.srd = ow ? mk_srd(wo, 640 * 1280) : mk_srd(wd, 1536 * 1280);
    pw.cell0 = w * 20480;
    pw.ow = ow; pw.ctoff = ct * 160;
    pw.sa = ow ? w * 320 * 1280 : g0 * 32 * 1280;
    pw.sast = ow ? 40 * 1280 : 32 * 1280;
    pw.sb = ow ? w * 320 * 1280 : (g0 + 8) * 32 * 1280;
    pw.sbst = ow ? 160 * 1280 : 32 * 1280;
  }

  if (PH & 1) {
    if (isq) {
      const int a = 7 * rh + (idx >= 7 ? idx - 7 : idx), g = 2 * cq + (idx >= 7 ? 1 : 0);
      const int hd = w & 3, par = w >> 2;
      P1<true> k;
      k.nm = 5;
      k.srdx = mk_srd(x, 2048 * 1280);
      k.srdw = mk_srd(wqkv, 640 * 3840);
      unsigned gv0, gv1;
      {
        const u32x4 sg = mk_srd(gw, 1280);
        asm volatile("buffer_load_ushort %0, %2, %3, 0 offen\n\tbuffer_load_ushort %1, %2, %3, 0 offen offset:1024"
                     : "=&v"(gv0), "=&v"(gv1) : "v"(tid * 2), "s"(sg) : "memory");
      }
#pragma unroll
      for (int i = 0; i < 3; ++i) {
        const int q = w * 3 + i;
        const unsigned rho = 8 * q + (lane >> 3);
        const unsigned pp = rho >= 80 ? 1 : 0, u = rho - 80 * pp;
        const unsigned tok = 146 * a + 2 * u + pp;
        k.xv[i] = q < 20 ? tok * 1280 + (((lane & 7) ^ ((rho >> 1) & 7)) << 4) : OOB;
        k.xm0[i] = q < 20 ? q * 1024 : XSLOT;
      }
#pragma unroll
      for (int j = 0; j < 2; ++j) {
        const int I = (2 * w + j) * 64 + lane;
        const int kb = I >= 736 ? 3 : I >= 488 ? 2 : I >= 248 ? 1 : 0;
        const int i = I - (kb == 3 ? 736 : kb == 2 ? 488 : kb == 1 ? 248 : 0);
        const unsigned r = ((unsigned)i * 2185u) >> 16, rem = i - r * 30;
        const unsigned seg = (rem * 6554u) >> 16, ch = rem - seg * 10;
        k.wv[j] = i < 240 ? (8 * kb + r) * 3840 + seg * 1280 + g * 160 + ch * 16 : OOB;
      }
      k.wm0 = w * 2048;
      k.prologue_dma();
      k.xa0 = XBASE + par * 5 * 2048 + c * 128 + ((h ^ (c >> 1)) << 4);
      k.xa1 = k.xa0 ^ 64;
      const unsigned kbo = h == 3 ? 736 * 16 : h == 2 ? 488 * 16 : h == 1 ? 248 * 16 : 0;
      k.wa0 = kbo + q4 * 480 + (20 * hd + 4 * p4) * 2;
      k.isgate = p4 == 3;
      k.wa1 = k.isgate ? GATE_OFF + (8 * h + q4) * 8 : kbo + q4 * 480 + p4 * 160 + (20 * hd + 16) * 2;
      k.wa2 = k.wa1 + (k.isgate ? 32 : 1920);
      k.zero();
      asm volatile("s_waitcnt vmcnt(14)" : "+v"(gv0), "+v"(gv1) :: "memory");
      {
        u32x2 v; v.x = gv0; v.y = 0;
        *(LAS u32x2*)(GATE_OFF + tid * 8) = v;
        if (tid < 128) { v.x = gv1; *(LAS u32x2*)(GATE_OFF + 4096 + tid * 8) = v; }
      }
      asm volatile("s_waitcnt lgkmcnt(0)" ::: "memory");
      BAR();
      STAMP(1);
      k.run();
      STAMP(2);
      if (PH & 2) { pw.pattern(false); pw.dma(0, 0); pw.dma(1, 1); pw.dma(2, 2); }
      u32x2 ql[5], kl[5], t2[5], vl[5], tt[5];
      const unsigned scr = pw.cell0 + 15360;
#pragma unroll
      for (int i = 0; i < 5; ++i) {
        ql[i] = pk4(k.acc[i][0]); kl[i] = pk4(k.acc[i][1]); t2[i] = pk4(k.acc[i][2]); vl[i] = pk4(k.acc[i][3]);
        *(LAS u32x2*)(scr + i * 512 + c * 32 + h * 8) = t2[i];
      }
      asm volatile("s_waitcnt lgkmcnt(0)" ::: "memory");
#pragma unroll
      for (int i = 0; i < 5; ++i) tt[i] = ldstr(scr + i * 512 + (4 * h + q4) * 32 + p4 * 8);
      asm volatile("s_waitcnt lgkmcnt(0)" : "+v"(tt[0]), "+v"(tt[1]), "+v"(tt[2]), "+v"(tt[3]), "+v"(tt[4]) :: "memory");
      if (PH & 2) pw.dma(3, 3);
      f32x4 ba, bb;
#pragma unroll
      for (int r = 0; r < 4; ++r) {
        const int kap = 4 * h + r;
        ba[r] = kap >= c + 9 ? 0.f : -1e30f;
        bb[r] = (kap <= c && kap >= c - 7) ? 0.f : -1e30f;
      }
      const unsigned m0mask = h == 0 ? 0xffffffffu : 0u;
      u32x4 kf[5];
#pragma unroll
      for (int i = 0; i < 5; ++i) {
        auto s0 = __builtin_amdgcn_permlane16_swap(t2[i].x, t2[i].x, false, false);
        auto s1 = __builtin_amdgcn_permlane16_swap(t2[i].y, t2[i].y, false, false);
        kf[i].x = kl[i].x; kf[i].y = kl[i].y; kf[i].z = s0[1]; kf[i].w = s1[1];
      }
      constexpr float SC = 0.22360679774997896f * 1.4426950408889634f;
#pragma unroll
      for (int i = 0; i < 5; ++i) {
        u32x4 qf; qf.x = ql[i].x; qf.y = ql[i].y; qf.z = t2[i].x & m0mask; qf.w = t2[i].y & m0mask;
        f32x4 sb = mfma(kf[i], qf, bb);
        f32x4 sa = f32x4{-1e30f, -1e30f, -1e30f, -1e30f};
        if (i > 0) sa = mfma(kf[i - 1], qf, ba);
        float m = fmaxf(fmaxf(fmaxf(sb.x, sb.y), fmaxf(sb.z, sb.w)), fmaxf(fmaxf(sa.x, sa.y), fmaxf(sa.z, sa.w)));
        m = xmax(m);
        const float mc = m * SC;
        f32x4 pa, pb;
#pragma unroll
        for (int r = 0; r < 4; ++r) {
          pb[r] = __builtin_amdgcn_exp2f(sb[r] * SC - mc);
          pa[r] = i > 0 ? __builtin_amdgcn_exp2f(sa[r] * SC - mc) : 0.f;
        }
        float l = ((pb.x + pb.y) + (pb.z + pb.w)) + ((pa.x + pa.y) + (pa.z + pa.w));
        l = xsum(l);
        const float rl = __builtin_amdgcn_rcpf(l);
        const u32x4 pf = cat(pk4(pa * rl), pk4(pb * rl));
        const f32x4 z = {0.f, 0.f, 0.f, 0.f};
        const f32x4 oa = mfma(cat(vl[i > 0 ? i - 1 : 0], vl[i]), pf, z);
        const f32x4 ob = mfma(cat(tt[i > 0 ? i - 1 : 0], tt[i]), pf, z);
        const int u = 16 * i + c, U = 73 * a + u;
        const bool ok = (a == 0 || u >= 7) && U < 1024;
        const unsigned mt = 2 * (U >> 4) + par, cc = U & 15;
        const unsigned oo = WS_O + mt * (40 * 512) + cc * 8;
        __builtin_amdgcn_raw_buffer_store_b64(pk4(oa), rws, ok ? oo + (4 * g + hd) * 512 + h * 128 : OOB, 0, AUX_SC1);
        __builtin_amdgcn_raw_buffer_store_b64(pk4(ob), rws, (ok && h == 2) ? oo + (32 + g) * 512 + hd * 128 : OOB, 0, AUX_SC1);
        if (g == 0 && hd == 0)
          __builtin_amdgcn_raw_buffer_store_b32(f2u(k.acc[i][2].x), rws,
                                                (ok && h == 3) ? WS_G + (mt * 16 + cc) * 4 : OOB, 0, AUX_SC1);
      }
      STAMP(3);
      if (PH == 3) {
        WAITV(0);
        BAR();
        make_tag();
        if (w == 0 && lane < 5) {
          const int U0 = a ? 73 * a + 7 : 0, U1 = min(73 * a + 80, 1024);
          const int r = (U0 >> 5) + lane;
          const int a0 = r * 32 < 80 ? 0 : (r * 32 - 7) / 73;
          GAS u64* f = (GAS u64*)((char*)ws + (lane == 4 ? WS_DONE + b * 8 : WS_FLAG + (r * 64 + 24 + (a - a0) * 8 + g) * 8));
          if (lane == 4 || r * 32 < U1) __hip_atomic_store(f, tag, __ATOMIC_RELAXED, __HIP_MEMORY_SCOPE_AGENT);
        }
      }
    } else {
      const int f = idx - 14;
      const int fr = f >= 12 ? f - 12 : f >= 6 ? f - 6 : f, fc = f >= 12 ? 2 : f >= 6 ? 1 : 0;
      const int ri = 6 * rh + fr, cj = 3 * cq + fc;
      const int nq = w & 3, mh = w >> 2;
      P1<false> k;
      k.nm = mh ? 5 : 6;
      k.isgate = false;
      k.srdx = mk_srd(x, 2048 * 1280);
      k.srdw = mk_srd((w & 1) ? wu : wg, 640 * 3072);
#pragma unroll
      for (int i = 0; i < 3; ++i) {
        const int q = w * 3 + i;
        const unsigned rho = 8 * q + (lane >> 3);
        const unsigned mt = 11 * ri + (rho >> 4);
        const unsigned tok = 32 * (mt >> 1) + 2 * (rho & 15) + (mt & 1);
        k.xv[i] = q < 22 ? tok * 1280 + (((lane & 7) ^ ((rho >> 1) & 7)) << 4) : OOB;
        k.xm0[i] = q < 22 ? q * 1024 : XSLOT;
      }
      {
        const int kb = w >> 1, r = lane >> 3, p8 = lane & 7;
        const int s = ((r >> 1) & 1) + 2 * (kb & 1);
        k.wv[0] = (8 * kb + r) * 3072 + cj * 256 + (((((p8 >> 1) ^ s) << 1) | (p8 & 1)) << 4);
        k.wv[1] = 0;
      }
      k.wm0 = w * 2048;
      k.prologue_dma();
      k.xa0 = XBASE + mh * 6 * 2048 + c * 128 + ((h ^ (c >> 1)) << 4);
      k.xa1 = k.xa0 ^ 64;
      {
        const int s = ((q4 >> 1) & 1) + 2 * (h & 1);
        k.wa0 = (h * 4 + (nq >> 1)) * 1024 + q4 * 128 + (((2 * (nq & 1)) ^ s) << 5) + p4 * 8;
        k.wa1 = k.wa0 ^ 32;
        k.wa2 = 0;
      }
      k.zero();
      WAITV(14);
      BAR();
      STAMP(1);
      k.run();
      STAMP(2);
      if (PH & 2) { pw.pattern(false); pw.dma(0, 0); pw.dma(1, 1); pw.dma(2, 2); pw.dma(3, 3); }
#pragma unroll
      for (int m = 0; m < 6; ++m) {
        if (m < k.nm) {
          float v[8];
#pragma unroll
          for (int j = 0; j < 8; ++j) {
            const float gt = k.acc[m][j >> 2][j & 3], up = k.acc[m][2 + (j >> 2)][j & 3];
            const float e = __builtin_amdgcn_exp2f(gt * -1.4426950408889634f);
            v[j] = gt * up * __builtin_amdgcn_rcpf(1.f + e);
          }
          u32x4 o; o.x = pk2(v[0], v[1]); o.y = pk2(v[2], v[3]); o.z = pk2(v[4], v[5]); o.w = pk2(v[6], v[7]);
          const unsigned mt = 11 * ri + 6 * mh + m;
          __builtin_amdgcn_raw_buffer_store_b128(o, rws, mt < 128 ? WS_GU + (mt * 48 + 4 * cj + nq) * 1024 + lane * 16 : OOB, 0, AUX_SC1);
        }
      }
      STAMP(3);
      if (PH == 3) {
        WAITV(0);
        BAR();
        make_tag();
        if (w == 0 && lane < 5) {
          const int m0_ = 11 * ri, m1 = min(m0_ + 11, 128);
          const int r = (m0_ >> 2) + lane;
          const int r0 = (4 * r) / 11;
          GAS u64* fp = (GAS u64*)((char*)ws + (lane == 4 ? WS_DONE + b * 8 : WS_FLAG + (r * 64 + (ri - r0) * 12 + cj) * 8));
          if (lane == 4 || r * 4 < m1) __hip_atomic_store(fp, tag, __ATOMIC_RELAXED, __HIP_MEMORY_SCOPE_AGENT);
        }
      }
    }
  }
  STAMP(4);

  if (PH & 2) {
    if (PH == 2) { pw.pattern(false); pw.dma(0, 0); pw.dma(1, 1); pw.dma(2, 2); pw.dma(3, 3); WAITV(0); }
    auto body = [&](auto OWT) {
      constexpr bool OW = decltype(OWT)::value;
      constexpr unsigned AMT = OW ? 40 * 512 : 48 * 1024;
      constexpr int NS = OW ? 10 : 9;
      const unsigned wra = pw.cell0 + (4 * h + q4) * 160 + p4 * 8;
      u32x4 wreg[4][5];
      auto rdw = [&](int cell) {
#pragma unroll
        for (int n = 0; n < 5; ++n) wreg[cell][n] = cat(ldstr(wra + cell * 5120 + n * 32), ldstr(wra + cell * 5120 + n * 32 + 2560));
      };
      rdw(0); rdw(1); rdw(2); rdw(3);
      asm volatile("s_waitcnt lgkmcnt(0)" ::: "memory");
      SB();
      pw.dma(4, 0); pw.dma(5, 1); pw.dma(6, 2);
      if (OW || nst > 7) pw.dma(7, 3);
      if (OW) pw.pattern(true);
      if (PH == 3) {
        const bool two_f = (4 * rb) / 11 != (4 * rb + 3) / 11;
        const int a0 = rb * 32 < 80 ? 0 : (rb * 32 - 7) / 73, a1 = rb * 32 + 31 < 80 ? 0 : (rb * 32 + 31 - 7) / 73;
        bool need;
        if (lane < 24) {
          const int cjj = lane >= 12 ? lane - 12 : lane;
          need = !OW && (lane < 12 || two_f) && cjj >= (g0 >> 2) && cjj <= ((g0 + nst - 1) >> 2);
        } else {
          const int gg = (lane - 24) & 7;
          need = lane < 40 && (lane < 32 || a1 != a0) && (gg == 0 || (OW && (gg >> 2) == w));
        }
        GAS u64* fp = (GAS u64*)((char*)ws + WS_FLAG + (rb * 64 + lane) * 8);
        for (unsigned spin = 0; spin < (1u << 18); ++spin) {
          const u64 v = __hip_atomic_load(fp, __ATOMIC_RELAXED, __HIP_MEMORY_SCOPE_AGENT);
          if (__all(!need || v == tag)) break;
          __builtin_amdgcn_s_sleep(2);
        }
        __builtin_amdgcn_fence(__ATOMIC_ACQUIRE, "wavefront");
        asm volatile("" ::: "memory");
      }
      STAMP(5);
      const unsigned av = OW ? lane * 8 : lane * 16;
      const unsigned as0 = (OW ? WS_O + 8 * w * 1024 : WS_GU + g0 * 1024) + 4 * rb * AMT;
      const unsigned as1 = WS_O + (16 + 2 * w) * 1024 + 4 * rb * AMT;
      u32x4 af[4][4];
      f32x4 acc[4][5];
      float sg[4];
      auto lda = [&](int s) {
        const unsigned so = (OW && s >= 8) ? as1 + (s - 8) * 1024 : as0 + s * 1024;
#pragma unroll
        for (int m = 0; m < 4; ++m) {
          if (OW) af[s & 3][m] = cat(__builtin_amdgcn_raw_buffer_load_b64(rws, av, so + m * AMT, AUX_SC1),
                                     __builtin_amdgcn_raw_buffer_load_b64(rws, av + 512, so + m * AMT, AUX_SC1));
          else    af[s & 3][m] = __builtin_amdgcn_raw_buffer_load_b128(rws, av, so + m * AMT, AUX_SC1);
        }
      };
      lda(0); lda(1); lda(2); lda(3);
      SB();
#pragma unroll
      for (int m = 0; m < 4; ++m)
#pragma unroll
        for (int n = 0; n < 5; ++n) acc[m][n] = f32x4{0.f, 0.f, 0.f, 0.f};
#pragma unroll
      for (int s = 0; s < NS; ++s) {
        if (OW || s < 7 || s < nst) {
#pragma unroll
          for (int m = 0; m < 4; ++m)
#pragma unroll
            for (int n = 0; n < 5; ++n) acc[m][n] = mfma(wreg[s & 3][n], af[s & 3][m], acc[m][n]);
          SB();
          asm volatile("" ::: "memory");
          if (s + 4 < NS && (OW || s + 4 < 7 || s + 4 < nst)) {
            rdw(s & 3);
            if (s + 8 < NS) {
              if (OW || s + 8 < nst) { asm volatile("s_waitcnt lgkmcnt(0)" ::: "memory"); SB(); pw.dma(s + 8, s & 3); }
            }
            lda(s + 4);
          }
          if (s == 6) {
#pragma unroll
            for (int m = 0; m < 4; ++m)
              sg[m] = __builtin_bit_cast(float, __builtin_amdgcn_raw_buffer_load_b32(rws, c * 4, WS_G + (4 * rb + m) * 64, AUX_SC1));
          }
          SB();
        }
      }
      STAMP(6);
#pragma unroll
      for (int m = 0; m < 4; ++m) {
        const float e = __builtin_amdgcn_exp2f(sg[m] * (OW ? -1.4426950408889634f : 1.4426950408889634f));
        const float f = __builtin_amdgcn_rcpf(1.f + e);
#pragma unroll
        for (int n = 0; n < 5; ++n) *(LAS f32x4*)(pw.cell0 + (m * 5 + n) * 1024 + lane * 16) = acc[m][n] * f;
      }
    };
    if (w < 2) body(std::true_type{}); else body(std::false_type{});
    if (PH == 3 && b == 0 && w == 4) {
      GAS u64* dp = (GAS u64*)((char*)ws + WS_DONE + lane * 8);
      for (unsigned spin = 0; spin < (1u << 18); ++spin) {
        bool ok = true;
#pragma unroll
        for (int j = 0; j < 4; ++j) ok &= __hip_atomic_load(dp + j * 64, __ATOMIC_RELAXED, __HIP_MEMORY_SCOPE_AGENT) == tag;
        if (__all(ok)) break;
        __builtin_amdgcn_s_sleep(2);
      }
      if (lane == 0) __hip_atomic_store((GAS u64*)((char*)ws + WS_EPOCH), tag, __ATOMIC_RELAXED, __HIP_MEMORY_SCOPE_AGENT);
    }
    asm volatile("s_waitcnt lgkmcnt(0)" ::: "memory");
    BAR();
    const int t0 = w < 4 ? 3 * w : 12 + 2 * (w - 4);
#pragma unroll
    for (int j = 0; j < 3; ++j) {
      if (j < 2 || w < 4) {
        const int tl = t0 + j;
        f32x4 s = *(const LAS f32x4*)(tl * 1024 + lane * 16);
#pragma unroll
        for (int v = 1; v < 8; ++v) s += *(const LAS f32x4*)(v * 20480 + tl * 1024 + lane * 16);
        const int m = tl / 5, n = tl - 5 * m;
        const int tok = 64 * rb + 32 * (m >> 1) + 2 * c + (m & 1);
        *(GAS u32x2*)((char*)out + tok * 1280 + ct * 160 + n * 32 + h * 8) = pk4(s);
      }
    }
    STAMP(7);
    { const u64 t = __builtin_amdgcn_s_memrealtime(); while (__builtin_amdgcn_s_memrealtime() - t < 4 * (tsecond - tfirst)) __builtin_amdgcn_s_sleep(1); }
    if (AMP0 >= 0) {
      const u64 d = amp_t1 - amp_t0, t = __builtin_amdgcn_s_memrealtime();
      while (__builtin_amdgcn_s_memrealtime() - t < d) __builtin_amdgcn_s_sleep(1);
    }
  }
}
}

extern "C" void kernel_launch(void* const* d_in, const int* in_sizes, int n_in, void* d_out, int out_size, void* d_ws,
                              size_t ws_size, hipStream_t stream) {
  fused<3><<<256, 512, 0, stream>>>(d_in[0], d_in[1], d_in[2], d_in[4], d_in[5], d_in[6], d_in[7], d_out, d_ws);
}
```

```cpp
#include <hip/hip_runtime.h>
#include <hip/hip_bf16.h>
#include <stdint.h>
#include <type_traits>

#define SPLIT 0
#define STAMPS 0

#define ABL 0
#define LAS __attribute__((address_space(3)))
#define GAS __attribute__((address_space(1)))
typedef __bf16 bf16x8 __attribute__((ext_vector_type(8)));
typedef __bf16 bf16x2 __attribute__((ext_vector_type(2)));
typedef float f32x4 __attribute__((ext_vector_type(4)));
typedef float f32x2 __attribute__((ext_vector_type(2)));
typedef unsigned u32x4 __attribute__((ext_vector_type(4)));
typedef unsigned u32x2 __attribute__((ext_vector_type(2)));
typedef short s16x4 __attribute__((ext_vector_type(4)));
typedef unsigned long long u64;

namespace {
constexpr int LDS_BYTES = 163840;
constexpr unsigned WSLOT = 16384, XBASE = 81920, XSLOT = 22528, GATE_OFF = 149504, SCR_OFF = 154624;
constexpr unsigned OOB = 0x40000000u;
constexpr unsigned WS_FLAG = 0;
constexpr unsigned WS_DONE = 16384;
constexpr unsigned WS_EPOCH = 20480;
constexpr unsigned WS_G = 24576;
constexpr unsigned WS_O = 65536;
constexpr unsigned WS_GU = WS_O + 128 * 40 * 512;
constexpr unsigned WS_END = WS_GU + 128 * 48 * 1024;
constexpr unsigned WS_STAMP = 16u << 20;
constexpr int AUX_SC1 = 16;

__device__ __forceinline__ u32x4 mk_srd(const void* p, unsigned bytes) {
  const u64 a = (u64)p;
  u32x4 r; r.x = (unsigned)a; r.y = (unsigned)(a >> 32) & 0xffffu; r.z = bytes; r.w = 0x00020000u; return r;
}
__device__ __forceinline__ void dma16(unsigned lds, unsigned imm, unsigned voff, u32x4 srd, unsigned soff) {
  asm volatile("s_add_u32 m0, %0, %4\n\ts_nop 0\n\tbuffer_load_dwordx4 %1, %2, %3 offen lds"
               :: "s"(lds), "v"(voff), "s"(srd), "s"(soff), "i"(imm) : "memory", "scc");
}
__device__ __forceinline__ u32x4 lds128(unsigned a) { return *(const LAS u32x4*)a; }
__device__ __forceinline__ u32x2 ldstr(unsigned a) {
  return __builtin_bit_cast(u32x2, __builtin_amdgcn_ds_read_tr16_b64_v4i16((LAS s16x4*)a));
}
__device__ __forceinline__ u32x4 cat(u32x2 a, u32x2 b) { u32x4 r; r.x = a.x; r.y = a.y; r.z = b.x; r.w = b.y; return r; }
__device__ __forceinline__ f32x4 mfma(u32x4 a, u32x4 b, f32x4 c) {
  return __builtin_amdgcn_mfma_f32_16x16x32_bf16(__builtin_bit_cast(bf16x8, a), __builtin_bit_cast(bf16x8, b), c, 0, 0, 0);
}
__device__ __forceinline__ unsigned pk2(float a, float b) {
  f32x2 t = {a, b};
  return __builtin_bit_cast(unsigned, __builtin_convertvector(t, bf16x2));
}
__device__ __forceinline__ u32x2 pk4(f32x4 v) { u32x2 r; r.x = pk2(v.x, v.y); r.y = pk2(v.z, v.w); return r; }
#define SB() __builtin_amdgcn_sched_barrier(0)
#define WAITV(n) asm volatile("s_waitcnt vmcnt(" #n ")" ::: "memory")
#define WAITVL(n) asm volatile("s_waitcnt vmcnt(" #n ") lgkmcnt(0)" ::: "memory")
#define BAR() do { asm volatile("s_barrier" ::: "memory"); } while (0)
__device__ __forceinline__ unsigned rfl(unsigned v) { return __builtin_amdgcn_readfirstlane(v); }

#define AMP0 -1
#define AMP1 -1
#define STAMP(i) do {} while (0)

template <bool QKV>
struct P1 {
  static constexpr int NM = QKV ? 5 : 6;
  f32x4 acc[NM][4];
  u32x4 srdx, srdw;
  unsigned xv[3], wv[2];
  unsigned xm0[3];
  unsigned wm0;
  unsigned xa0, xa1;
  unsigned wa0, wa1, wa2;
  bool isgate;
  int nm;

  __device__ __forceinline__ void dma_w(int t, unsigned slot) {
    const unsigned so = (unsigned)t * (QKV ? 32u * 3840u : 32u * 3072u);
    dma16(slot + wm0, 0, wv[0], srdw, so);
    dma16(slot + wm0, 1024, wv[QKV ? 1 : 0], srdw, QKV ? so : so + 128);
  }
  __device__ __forceinline__ void dma_x1(int i, int j, unsigned slot) {
    const unsigned so = j < 10 ? (unsigned)j * 128u : OOB;
    const unsigned d = xm0[i] < XSLOT ? XBASE + slot + xm0[i] : SCR_OFF;
    dma16(d, 0, xv[i], srdx, so);
  }
  __device__ __forceinline__ void rd_w(u32x4 (&wf)[4], unsigned slot, int t) {
    if (QKV) {
      const unsigned a = wa0 + slot;
      const unsigned s2 = isgate ? (unsigned)t * 256u : slot;
      wf[0] = cat(ldstr(a), ldstr(a + 1920));
      wf[1] = cat(ldstr(a + 160), ldstr(a + 160 + 1920));
      wf[2] = cat(ldstr(wa1 + s2), ldstr(wa2 + s2));
      wf[3] = cat(ldstr(a + 320), ldstr(a + 320 + 1920));
    } else {
      const unsigned a = wa0 + slot, b = wa1 + slot;
      wf[0] = cat(ldstr(a), ldstr(a + 512));
      wf[1] = cat(ldstr(b), ldstr(b + 512));
      wf[2] = cat(ldstr(a + 2048), ldstr(a + 2048 + 512));
      wf[3] = cat(ldstr(b + 2048), ldstr(b + 2048 + 512));
    }
  }
  __device__ __forceinline__ void mm(int m, const u32x4 (&xf)[NM], const u32x4 (&wf)[4]) {
    if (ABL & 2) { asm volatile("" :: "v"(xf[m]), "v"(wf[0]), "v"(wf[1]), "v"(wf[2]), "v"(wf[3])); return; }
    acc[m][0] = mfma(wf[0], xf[m], acc[m][0]);
    acc[m][1] = mfma(wf[1], xf[m], acc[m][1]);
    acc[m][2] = mfma(wf[2], xf[m], acc[m][2]);
    if (QKV) acc[m][3] = mfma(xf[m], wf[3], acc[m][3]);
    else     acc[m][3] = mfma(wf[3], xf[m], acc[m][3]);
  }
  template <bool ODD>
  __device__ __forceinline__ void step(int t, unsigned wrd, unsigned wwr, unsigned xrd, unsigned xwr,
                                       const u32x4 (&xc)[NM], const u32x4 (&wc)[4], u32x4 (&xn)[NM], u32x4 (&wn)[4]) {
    if (ODD) WAITVL(9); else WAITVL(12);
    if (!(ABL & 8)) BAR();
    SB();
    mm(0, xc, wc);
    SB();
    if (!(ABL & 4)) rd_w(wn, wrd, t + 1);
    else { for (int n = 0; n < 4; ++n) { wn[n] = wc[n]; asm volatile("" : "+v"(wn[n])); } }
    SB();
    mm(1, xc, wc);
    SB();
    const unsigned xa = (ODD ? xa0 : xa1) + xrd;
    if (!(ABL & 4)) {
#pragma unroll
    for (int m = 0; m < 5; ++m) xn[m] = lds128(xa + m * 2048);
    if (!QKV) xn[NM - 1] = lds128(xa + (NM - 1) * 2048);
    } else { for (int m = 0; m < NM; ++m) { xn[m] = xc[m]; asm volatile("" : "+v"(xn[m])); } }
    SB();
    mm(2, xc, wc);
    SB();
    if (!(ABL & 1)) dma_w(t + 5, wwr);
    SB();
    mm(3, xc, wc);
    SB();
    if (ODD && !(ABL & 1)) { dma_x1(0, (t + 5) >> 1, xwr); dma_x1(1, (t + 5) >> 1, xwr); dma_x1(2, (t + 5) >> 1, xwr); }
    SB();
    mm(4, xc, wc);
    if (!QKV) { if (nm == 6) mm(NM - 1, xc, wc); }
    SB();
  }
  __device__ __forceinline__ void prologue_dma() {
    dma_w(0, 0);
    dma_x1(0, 0, 0); dma_x1(1, 0, 0); dma_x1(2, 0, 0);
    dma_w(1, WSLOT);
    dma_w(2, 2 * WSLOT);
    dma_x1(0, 1, XSLOT); dma_x1(1, 1, XSLOT); dma_x1(2, 1, XSLOT);
    dma_w(3, 3 * WSLOT);
    dma_w(4, 4 * WSLOT);
    dma_x1(0, 2, 2 * XSLOT); dma_x1(1, 2, 2 * XSLOT); dma_x1(2, 2, 2 * XSLOT);
  }
  __device__ __forceinline__ void zero() {
#pragma unroll
    for (int m = 0; m < NM; ++m)
#pragma unroll
      for (int n = 0; n < 4; ++n) { acc[m][n] = f32x4{0.f, 0.f, 0.f, 0.f}; asm volatile("" : "+v"(acc[m][n])); }
  }
  __device__ __forceinline__ void run() {
    u32x4 xA[NM], wA[4], xB[NM], wB[4];
    rd_w(wA, 0, 0);
#pragma unroll
    for (int m = 0; m < NM; ++m) xA[m] = lds128(xa0 + m * 2048);
    unsigned w0 = 0, w1 = WSLOT;
    unsigned x0 = 0, x1 = XSLOT;
#pragma unroll 1
    for (int t = 0; t < 20; t += 2) {
      unsigned w2 = w1 + WSLOT; if (w2 == 5 * WSLOT) w2 = 0;
      step<false>(t, w1, w0, x0, 0, xA, wA, xB, wB);
      step<true>(t + 1, w2, w1, x1, x0, xB, wB, xA, wA);
      w0 = w2; w1 = w2 + WSLOT; if (w1 == 5 * WSLOT) w1 = 0;
      x0 = x1; x1 += XSLOT; if (x1 == 3 * XSLOT) x1 = 0;
    }
    WAITVL(0);
    BAR();
  }
};

struct P2W {
  u32x4 srd;
  unsigned va[5];
  bool ow; unsigned ctoff;
  unsigned sa, sast, sb, sbst;
  unsigned cell0;
  __device__ __forceinline__ void dma(int s, int cell) {
    const unsigned so = s < 8 ? sa + s * sast : sb + (s - 8) * sbst;
#pragma unroll
    for (int j = 0; j < 5; ++j) dma16(cell0, cell * 5120 + j * 1024, va[j], srd, so);
  }
  __device__ __forceinline__ void pattern(bool tail) {
    unsigned ln = threadIdx.x & 63;
    asm volatile("" : "+v"(ln));
#pragma unroll
    for (int j = 0; j < 5; ++j) {
      const unsigned i = 64 * j + ln, r = (i * 6554u) >> 16, ch = i - r * 10;
      const unsigned ra = !ow ? r : tail ? 80 * (r >> 4) + 20 * ((r >> 2) & 3) + 16 + (r & 3) : 20 * (r >> 4) + (r & 15);
      va[j] = ra * 1280 + ctoff + ch * 16;
    }
  }
};

__device__ __forceinline__ float u2f(unsigned u) { return __builtin_bit_cast(float, u); }
__device__ __forceinline__ unsigned f2u(float f) { return __builtin_bit_cast(unsigned, f); }
__device__ __forceinline__ float xmax(float v) {
  auto r = __builtin_amdgcn_permlane16_swap(f2u(v), f2u(v), false, false);
  v = fmaxf(u2f(r[0]), u2f(r[1]));
  auto s = __builtin_amdgcn_permlane32_swap(f2u(v), f2u(v), false, false);
  return fmaxf(u2f(s[0]), u2f(s[1]));
}
__device__ __forceinline__ float xsum(float v) {
  auto r = __builtin_amdgcn_permlane16_swap(f2u(v), f2u(v), false, false);
  v = u2f(r[0]) + u2f(r[1]);
  auto s = __builtin_amdgcn_permlane32_swap(f2u(v), f2u(v), false, false);
  return u2f(s[0]) + u2f(s[1]);
}

template <int PH>
__global__ __launch_bounds__(512, 2) void fused(const void* __restrict__ x, const void* __restrict__ wqkv,
                                                const void* __restrict__ wo, const void* __restrict__ wg,
                                                const void* __restrict__ wu, const void* __restrict__ wd,
                                                const void* __restrict__ gw, void* out, void* ws) {
  __shared__ __attribute__((aligned(16))) char smem[LDS_BYTES];
  asm volatile("" :: "s"((unsigned)(size_t)(LAS char*)smem));
  const int tid = threadIdx.x, lane = tid & 63;
  const int w = rfl(tid >> 6);
  const int h = lane >> 4, c = lane & 15, q4 = (lane & 15) >> 2, p4 = lane & 3;
  const int b = blockIdx.x, xcd = b & 7, idx = b >> 3;
  const int rh = xcd & 1, cq = xcd >> 1;
  const bool isq = idx < 14;
  const int rb = 4 * xcd + (idx >> 3), ct = idx & 7;
  const __amdgpu_buffer_rsrc_t rws = __builtin_amdgcn_make_buffer_rsrc(ws, 0, WS_END, 0x00020000);
  u64 amp_t0 = 0, amp_t1 = 0;
  STAMP(0);

  asm volatile("" :: "s"(x), "s"(wqkv), "s"(wo), "s"(wg), "s"(wu), "s"(wd), "s"(gw), "s"(out), "s"(ws));
  u64 ev = 0, tag = 0;
  if (PH == 3) asm volatile("global_load_dwordx2 %0, %1, %2 sc1" : "=v"(ev) : "v"(WS_EPOCH), "s"(ws) : "memory");
  auto make_tag = [&]() {
    asm volatile("" : "+v"(ev));
    const u64 e = ((u64)rfl((unsigned)(ev >> 32)) << 32) | rfl((unsigned)ev);
    tag = (e ^ 0x9E3779B97F4A7C15ull) * 0xBF58476D1CE4E5B9ull + 0x632BE59BD9B4E019ull;
    if (tag == e) tag += 1;
  };

  P2W pw;
  int nst, g0;
  {
    const bool ow = w < 2;
    g0 = (int)((0x271E171008000000ull >> (8 * w)) & 0xff);
    nst = (0x99778800 >> (4 * w)) & 15;
    pw.srd = ow ? mk_srd(wo, 640 * 1280) : mk_srd(wd, 1536 * 1280);
    pw.cell0 = w * 20480;
    pw.ow = ow; pw.ctoff = ct * 160;
    pw.sa = ow ? w * 320 * 1280 : g0 * 32 * 1280;
    pw.sast = ow ? 40 * 1280 : 32 * 1280;
    pw.sb = ow ? w * 320 * 1280 : (g0 + 8) * 32 * 1280;
    pw.sbst = ow ? 160 * 1280 : 32 * 1280;
  }

  if (PH & 1) {
    if (isq) {
      const int a = 7 * rh + (idx >= 7 ? idx - 7 : idx), g = 2 * cq + (idx >= 7 ? 1 : 0);
      const int hd = w & 3, par = w >> 2;
      P1<true> k;
      k.nm = 5;
      k.srdx = mk_srd(x, 2048 * 1280);
      k.srdw = mk_srd(wqkv, 640 * 3840);
      unsigned gv0, gv1;
      {
        const u32x4 sg = mk_srd(gw, 1280);
        asm volatile("buffer_load_ushort %0, %2, %3, 0 offen\n\tbuffer_load_ushort %1, %2, %3, 0 offen offset:1024"
                     : "=&v"(gv0), "=&v"(gv1) : "v"(tid * 2), "s"(sg) : "memory");
      }
#pragma unroll
      for (int i = 0; i < 3; ++i) {
        const int q = w * 3 + i;
        const unsigned rho = 8 * q + (lane >> 3);
        const unsigned pp = rho >= 80 ? 1 : 0, u = rho - 80 * pp;
        const unsigned tok = 146 * a + 2 * u + pp;
        k.xv[i] = q < 20 ? tok * 1280 + (((lane & 7) ^ ((rho >> 1) & 7)) << 4) : OOB;
        k.xm0[i] = q < 20 ? q * 1024 : XSLOT;
      }
#pragma unroll
      for (int j = 0; j < 2; ++j) {
        const int I = (2 * w + j) * 64 + lane;
        const int kb = I >= 736 ? 3 : I >= 488 ? 2 : I >= 248 ? 1 : 0;
        const int i = I - (kb == 3 ? 736 : kb == 2 ? 488 : kb == 1 ? 248 : 0);
        const unsigned r = ((unsigned)i * 2185u) >> 16, rem = i - r * 30;
        const unsigned seg = (rem * 6554u) >> 16, ch = rem - seg * 10;
        k.wv[j] = i < 240 ? (8 * kb + r) * 3840 + seg * 1280 + g * 160 + ch * 16 : OOB;
      }
      k.wm0 = w * 2048;
      k.prologue_dma();
      k.xa0 = XBASE + par * 5 * 2048 + c * 128 + ((h ^ (c >> 1)) << 4);
      k.xa1 = k.xa0 ^ 64;
      const unsigned kbo = h == 3 ? 736 * 16 : h == 2 ? 488 * 16 : h == 1 ? 248 * 16 : 0;
      k.wa0 = kbo + q4 * 480 + (20 * hd + 4 * p4) * 2;
      k.isgate = p4 == 3;
      k.wa1 = k.isgate ? GATE_OFF + (8 * h + q4) * 8 : kbo + q4 * 480 + p4 * 160 + (20 * hd + 16) * 2;
      k.wa2 = k.wa1 + (k.isgate ? 32 : 1920);
      k.zero();
      asm volatile("s_waitcnt vmcnt(14)" : "+v"(gv0), "+v"(gv1) :: "memory");
      {
        u32x2 v; v.x = gv0; v.y = 0;
        *(LAS u32x2*)(GATE_OFF + tid * 8) = v;
        if (tid < 128) { v.x = gv1; *(LAS u32x2*)(GATE_OFF + 4096 + tid * 8) = v; }
      }
      asm volatile("s_waitcnt lgkmcnt(0)" ::: "memory");
      BAR();
      STAMP(1);
      k.run();
      STAMP(2);
      if (PH & 2) { pw.pattern(false); pw.dma(0, 0); pw.dma(1, 1); pw.dma(2, 2); }
      u32x2 ql[5], kl[5], t2[5], vl[5], tt[5];
      const unsigned scr = pw.cell0 + 15360;
#pragma unroll
      for (int i = 0; i < 5; ++i) {
        ql[i] = pk4(k.acc[i][0]); kl[i] = pk4(k.acc[i][1]); t2[i] = pk4(k.acc[i][2]); vl[i] = pk4(k.acc[i][3]);
        *(LAS u32x2*)(scr + i * 512 + c * 32 + h * 8) = t2[i];
      }
      asm volatile("s_waitcnt lgkmcnt(0)" ::: "memory");
#pragma unroll
      for (int i = 0; i < 5; ++i) tt[i] = ldstr(scr + i * 512 + (4 * h + q4) * 32 + p4 * 8);
      asm volatile("s_waitcnt lgkmcnt(0)" : "+v"(tt[0]), "+v"(tt[1]), "+v"(tt[2]), "+v"(tt[3]), "+v"(tt[4]) :: "memory");
      if (PH & 2) pw.dma(3, 3);
      f32x4 ba, bb;
#pragma unroll
      for (int r = 0; r < 4; ++r) {
        const int kap = 4 * h + r;
        ba[r] = kap >= c + 9 ? 0.f : -1e30f;
        bb[r] = (kap <= c && kap >= c - 7) ? 0.f : -1e30f;
      }
      const unsigned m0mask = h == 0 ? 0xffffffffu : 0u;
      u32x4 kf[5];
#pragma unroll
      for (int i = 0; i < 5; ++i) {
        auto s0 = __builtin_amdgcn_permlane16_swap(t2[i].x, t2[i].x, false, false);
        auto s1 = __builtin_amdgcn_permlane16_swap(t2[i].y, t2[i].y, false, false);
        kf[i].x = kl[i].x; kf[i].y = kl[i].y; kf[i].z = s0[1]; kf[i].w = s1[1];
      }
      constexpr float SC = 0.22360679774997896f * 1.4426950408889634f;
#pragma unroll
      for (int i = 0; i < 5; ++i) {
        u32x4 qf; qf.x = ql[i].x; qf.y = ql[i].y; qf.z = t2[i].x & m0mask; qf.w = t2[i].y & m0mask;
        f32x4 sb = mfma(kf[i], qf, bb);
        f32x4 sa = f32x4{-1e30f, -1e30f, -1e30f, -1e30f};
        if (i > 0) sa = mfma(kf[i - 1], qf, ba);
        float m = fmaxf(fmaxf(fmaxf(sb.x, sb.y), fmaxf(sb.z, sb.w)), fmaxf(fmaxf(sa.x, sa.y), fmaxf(sa.z, sa.w)));
        m = xmax(m);
        const float mc = m * SC;
        f32x4 pa, pb;
#pragma unroll
        for (int r = 0; r < 4; ++r) {
          pb[r] = __builtin_amdgcn_exp2f(sb[r] * SC - mc);
          pa[r] = i > 0 ? __builtin_amdgcn_exp2f(sa[r] * SC - mc) : 0.f;
        }
        float l = ((pb.x + pb.y) + (pb.z + pb.w)) + ((pa.x + pa.y) + (pa.z + pa.w));
        l = xsum(l);
        const float rl = __builtin_amdgcn_rcpf(l);
        const u32x4 pf = cat(pk4(pa * rl), pk4(pb * rl));
        const f32x4 z = {0.f, 0.f, 0.f, 0.f};
        const f32x4 oa = mfma(cat(vl[i > 0 ? i - 1 : 0], vl[i]), pf, z);
        const f32x4 ob = mfma(cat(tt[i > 0 ? i - 1 : 0], tt[i]), pf, z);
        const int u = 16 * i + c, U = 73 * a + u;
        const bool ok = (a == 0 || u >= 7) && U < 1024;
        const unsigned mt = 2 * (U >> 4) + par, cc = U & 15;
        const unsigned oo = WS_O + mt * (40 * 512) + cc * 8;
        __builtin_amdgcn_raw_buffer_store_b64(pk4(oa), rws, ok ? oo + (4 * g + hd) * 512 + h * 128 : OOB, 0, AUX_SC1);
        __builtin_amdgcn_raw_buffer_store_b64(pk4(ob), rws, (ok && h == 2) ? oo + (32 + g) * 512 + hd * 128 : OOB, 0, AUX_SC1);
        if (g == 0 && hd == 0)
          __builtin_amdgcn_raw_buffer_store_b32(f2u(k.acc[i][2].x), rws,
                                                (ok && h == 3) ? WS_G + (mt * 16 + cc) * 4 : OOB, 0, AUX_SC1);
      }
      STAMP(3);
      if (PH == 3) {
        WAITV(0);
        BAR();
        make_tag();
        if (w == 0 && lane < 5) {
          const int U0 = a ? 73 * a + 7 : 0, U1 = min(73 * a + 80, 1024);
          const int r = (U0 >> 5) + lane;
          const int a0 = r * 32 < 80 ? 0 : (r * 32 - 7) / 73;
          GAS u64* f = (GAS u64*)((char*)ws + (lane == 4 ? WS_DONE + b * 8 : WS_FLAG + (r * 64 + 24 + (a - a0) * 8 + g) * 8));
          if (lane == 4 || r * 32 < U1) __hip_atomic_store(f, tag, __ATOMIC_RELAXED, __HIP_MEMORY_SCOPE_AGENT);
        }
      }
    } else {
      const int f = idx - 14;
      const int fr = f >= 12 ? f - 12 : f >= 6 ? f - 6 : f, fc = f >= 12 ? 2 : f >= 6 ? 1 : 0;
      const int ri = 6 * rh + fr, cj = 3 * cq + fc;
      const int nq = w & 3, mh = w >> 2;
      P1<false> k;
      k.nm = mh ? 5 : 6;
      k.isgate = false;
      k.srdx = mk_srd(x, 2048 * 1280);
      k.srdw = mk_srd((w & 1) ? wu : wg, 640 * 3072);
#pragma unroll
      for (int i = 0; i < 3; ++i) {
        const int q = w * 3 + i;
        const unsigned rho = 8 * q + (lane >> 3);
        const unsigned mt = 11 * ri + (rho >> 4);
        const unsigned tok = 32 * (mt >> 1) + 2 * (rho & 15) + (mt & 1);
        k.xv[i] = q < 22 ? tok * 1280 + (((lane & 7) ^ ((rho >> 1) & 7)) << 4) : OOB;
        k.xm0[i] = q < 22 ? q * 1024 : XSLOT;
      }
      {
        const int kb = w >> 1, r = lane >> 3, p8 = lane & 7;
        const int s = ((r >> 1) & 1) + 2 * (kb & 1);
        k.wv[0] = (8 * kb + r) * 3072 + cj * 256 + (((((p8 >> 1) ^ s) << 1) | (p8 & 1)) << 4);
        k.wv[1] = 0;
      }
      k.wm0 = w * 2048;
      k.prologue_dma();
      k.xa0 = XBASE + mh * 6 * 2048 + c * 128 + ((h ^ (c >> 1)) << 4);
      k.xa1 = k.xa0 ^ 64;
      {
        const int s = ((q4 >> 1) & 1) + 2 * (h & 1);
        k.wa0 = (h * 4 + (nq >> 1)) * 1024 + q4 * 128 + (((2 * (nq & 1)) ^ s) << 5) + p4 * 8;
        k.wa1 = k.wa0 ^ 32;
        k.wa2 = 0;
      }
      k.zero();
      WAITV(14);
      BAR();
      STAMP(1);
      k.run();
      STAMP(2);
      if (PH & 2) { pw.pattern(false); pw.dma(0, 0); pw.dma(1, 1); pw.dma(2, 2); pw.dma(3, 3); }
#pragma unroll
      for (int m = 0; m < 6; ++m) {
        if (m < k.nm) {
          float v[8];
#pragma unroll
          for (int j = 0; j < 8; ++j) {
            const float gt = k.acc[m][j >> 2][j & 3], up = k.acc[m][2 + (j >> 2)][j & 3];
            const float e = __builtin_amdgcn_exp2f(gt * -1.4426950408889634f);
            v[j] = gt * up * __builtin_amdgcn_rcpf(1.f + e);
          }
          u32x4 o; o.x = pk2(v[0], v[1]); o.y = pk2(v[2], v[3]); o.z = pk2(v[4], v[5]); o.w = pk2(v[6], v[7]);
          const unsigned mt = 11 * ri + 6 * mh + m;
          __builtin_amdgcn_raw_buffer_store_b128(o, rws, mt < 128 ? WS_GU + (mt * 48 + 4 * cj + nq) * 1024 + lane * 16 : OOB, 0, AUX_SC1);
        }
      }
      STAMP(3);
      if (PH == 3) {
        WAITV(0);
        BAR();
        make_tag();
        if (w == 0 && lane < 5) {
          const int m0_ = 11 * ri, m1 = min(m0_ + 11, 128);
          const int r = (m0_ >> 2) + lane;
          const int r0 = (4 * r) / 11;
          GAS u64* fp = (GAS u64*)((char*)ws + (lane == 4 ? WS_DONE + b * 8 : WS_FLAG + (r * 64 + (ri - r0) * 12 + cj) * 8));
          if (lane == 4 || r * 4 < m1) __hip_atomic_store(fp, tag, __ATOMIC_RELAXED, __HIP_MEMORY_SCOPE_AGENT);
        }
      }
    }
  }
  STAMP(4);

  if (PH & 2) {
    if (PH == 2) { pw.pattern(false); pw.dma(0, 0); pw.dma(1, 1); pw.dma(2, 2); pw.dma(3, 3); WAITV(0); }
    auto body = [&](auto OWT) {
      constexpr bool OW = decltype(OWT)::value;
      constexpr unsigned AMT = OW ? 40 * 512 : 48 * 1024;
      constexpr int NS = OW ? 10 : 9;
      const unsigned wra = pw.cell0 + (4 * h + q4) * 160 + p4 * 8;
      u32x4 wreg[4][5];
      auto rdw = [&](int cell) {
#pragma unroll
        for (int n = 0; n < 5; ++n) wreg[cell][n] = cat(ldstr(wra + cell * 5120 + n * 32), ldstr(wra + cell * 5120 + n * 32 + 2560));
      };
      rdw(0); rdw(1); rdw(2); rdw(3);
      asm volatile("s_waitcnt lgkmcnt(0)" ::: "memory");
      SB();
      pw.dma(4, 0); pw.dma(5, 1); pw.dma(6, 2);
      if (OW || nst > 7) pw.dma(7, 3);
      if (OW) pw.pattern(true);
      if (PH == 3) {
        const bool two_f = (4 * rb) / 11 != (4 * rb + 3) / 11;
        const int a0 = rb * 32 < 80 ? 0 : (rb * 32 - 7) / 73, a1 = rb * 32 + 31 < 80 ? 0 : (rb * 32 + 31 - 7) / 73;
        bool need;
        if (lane < 24) {
          const int cjj = lane >= 12 ? lane - 12 : lane;
          need = !OW && (lane < 12 || two_f) && cjj >= (g0 >> 2) && cjj <= ((g0 + nst - 1) >> 2);
        } else {
          const int gg = (lane - 24) & 7;
          need = lane < 40 && (lane < 32 || a1 != a0) && (gg == 0 || (OW && (gg >> 2) == w));
        }
        GAS u64* fp = (GAS u64*)((char*)ws + WS_FLAG + (rb * 64 + lane) * 8);
        for (unsigned spin = 0; spin < (1u << 18); ++spin) {
          const u64 v = __hip_atomic_load(fp, __ATOMIC_RELAXED, __HIP_MEMORY_SCOPE_AGENT);
          if (__all(!need || v == tag)) break;
          __builtin_amdgcn_s_sleep(2);
        }
        __builtin_amdgcn_fence(__ATOMIC_ACQUIRE, "wavefront");
        asm volatile("" ::: "memory");
      }
      STAMP(5);
      const unsigned av = OW ? lane * 8 : lane * 16;
      const unsigned as0 = (OW ? WS_O + 8 * w * 1024 : WS_GU + g0 * 1024) + 4 * rb * AMT;
      const unsigned as1 = WS_O + (16 + 2 * w) * 1024 + 4 * rb * AMT;
      u32x4 af[4][4];
      f32x4 acc[4][5];
      float sg[4];
      auto lda = [&](int s) {
        const unsigned so = (OW && s >= 8) ? as1 + (s - 8) * 1024 : as0 + s * 1024;
#pragma unroll
        for (int m = 0; m < 4; ++m) {
          if (OW) af[s & 3][m] = cat(__builtin_amdgcn_raw_buffer_load_b64(rws, av, so + m * AMT, AUX_SC1),
                                     __builtin_amdgcn_raw_buffer_load_b64(rws, av + 512, so + m * AMT, AUX_SC1));
          else    af[s & 3][m] = __builtin_amdgcn_raw_buffer_load_b128(rws, av, so + m * AMT, AUX_SC1);
        }
      };
      lda(0); lda(1); lda(2); lda(3);
      SB();
#pragma unroll
      for (int m = 0; m < 4; ++m)
#pragma unroll
        for (int n = 0; n < 5; ++n) acc[m][n] = f32x4{0.f, 0.f, 0.f, 0.f};
#pragma unroll
      for (int s = 0; s < NS; ++s) {
        if (OW || s < 7 || s < nst) {
#pragma unroll
          for (int m = 0; m < 4; ++m)
#pragma unroll
            for (int n = 0; n < 5; ++n) acc[m][n] = mfma(wreg[s & 3][n], af[s & 3][m], acc[m][n]);
          SB();
          asm volatile("" ::: "memory");
          if (s + 4 < NS && (OW || s + 4 < 7 || s + 4 < nst)) {
            rdw(s & 3);
            if (s + 8 < NS) {
              if (OW || s + 8 < nst) { asm volatile("s_waitcnt lgkmcnt(0)" ::: "memory"); SB(); pw.dma(s + 8, s & 3); }
            }
            lda(s + 4);
          }
          if (s == 6) {
#pragma unroll
            for (int m = 0; m < 4; ++m)
              sg[m] = __builtin_bit_cast(float, __builtin_amdgcn_raw_buffer_load_b32(rws, c * 4, WS_G + (4 * rb + m) * 64, AUX_SC1));
          }
          SB();
        }
      }
      STAMP(6);
#pragma unroll
      for (int m = 0; m < 4; ++m) {
        const float e = __builtin_amdgcn_exp2f(sg[m] * (OW ? -1.4426950408889634f : 1.4426950408889634f));
        const float f = __builtin_amdgcn_rcpf(1.f + e);
#pragma unroll
        for (int n = 0; n < 5; ++n) *(LAS f32x4*)(pw.cell0 + (m * 5 + n) * 1024 + lane * 16) = acc[m][n] * f;
      }
    };
    if (w < 2) body(std::true_type{}); else body(std::false_type{});
    if (PH == 3 && b == 0 && w == 4) {
      GAS u64* dp = (GAS u64*)((char*)ws + WS_DONE + lane * 8);
      for (unsigned spin = 0; spin < (1u << 18); ++spin) {
        bool ok = true;
#pragma unroll
        for (int j = 0; j < 4; ++j) ok &= __hip_atomic_load(dp + j * 64, __ATOMIC_RELAXED, __HIP_MEMORY_SCOPE_AGENT) == tag;
        if (__all(ok)) break;
        __builtin_amdgcn_s_sleep(2);
      }
      if (lane == 0) __hip_atomic_store((GAS u64*)((char*)ws + WS_EPOCH), tag, __ATOMIC_RELAXED, __HIP_MEMORY_SCOPE_AGENT);
    }
    asm volatile("s_waitcnt lgkmcnt(0)" ::: "memory");
    BAR();
    const int t0 = w < 4 ? 3 * w : 12 + 2 * (w - 4);
#pragma unroll
    for (int j = 0; j < 3; ++j) {
      if (j < 2 || w < 4) {
        const int tl = t0 + j;
        f32x4 s = *(const LAS f32x4*)(tl * 1024 + lane * 16);
#pragma unroll
        for (int v = 1; v < 8; ++v) s += *(const LAS f32x4*)(v * 20480 + tl * 1024 + lane * 16);
        const int m = tl / 5, n = tl - 5 * m;
        const int tok = 64 * rb + 32 * (m >> 1) + 2 * c + (m & 1);
        *(GAS u32x2*)((char*)out + tok * 1280 + ct * 160 + n * 32 + h * 8) = pk4(s);
      }
    }
    STAMP(7);
    if (AMP0 >= 0) {
      const u64 d = amp_t1 - amp_t0, t = __builtin_amdgcn_s_memrealtime();
      while (__builtin_amdgcn_s_memrealtime() - t < d) __builtin_amdgcn_s_sleep(1);
    }
  }
}
__global__ void k_lead(void* ws) { if (threadIdx.x == 0 && blockIdx.x == 0) ((u64*)((char*)ws + WS_STAMP))[0] = __builtin_amdgcn_s_memrealtime(); }
}

extern "C" void kernel_launch(void* const* d_in, const int* in_sizes, int n_in, void* d_out, int out_size, void* d_ws,
                              size_t ws_size, hipStream_t stream) {
  k_lead<<<256, 64, 0, stream>>>(d_ws);
  fused<3><<<256, 512, 0, stream>>>(d_in[0], d_in[1], d_in[2], d_in[4], d_in[5], d_in[6], d_in[7], d_out, d_ws);
}
```

```cpp
#include <hip/hip_runtime.h>
#include <hip/hip_bf16.h>
#include <stdint.h>
#include <type_traits>

#define SPLIT 0
#define STAMPS 0

#define ABL 0
#define LAS __attribute__((address_space(3)))
#define GAS __attribute__((address_space(1)))
typedef __bf16 bf16x8 __attribute__((ext_vector_type(8)));
typedef __bf16 bf16x2 __attribute__((ext_vector_type(2)));
typedef float f32x4 __attribute__((ext_vector_type(4)));
typedef float f32x2 __attribute__((ext_vector_type(2)));
typedef unsigned u32x4 __attribute__((ext_vector_type(4)));
typedef unsigned u32x2 __attribute__((ext_vector_type(2)));
typedef short s16x4 __attribute__((ext_vector_type(4)));
typedef unsigned long long u64;

namespace {
constexpr int LDS_BYTES = 163840;
constexpr unsigned WSLOT = 16384, XBASE = 81920, XSLOT = 22528, GATE_OFF = 149504, SCR_OFF = 154624;
constexpr unsigned OOB = 0x40000000u;
constexpr unsigned WS_FLAG = 0;
constexpr unsigned WS_DONE = 16384;
constexpr unsigned WS_EPOCH = 20480;
constexpr unsigned WS_G = 24576;
constexpr unsigned WS_O = 65536;
constexpr unsigned WS_GU = WS_O + 128 * 40 * 512;
constexpr unsigned WS_END = WS_GU + 128 * 48 * 1024;
constexpr unsigned WS_STAMP = 16u << 20;
constexpr int AUX_SC1 = 16;

__device__ __forceinline__ u32x4 mk_srd(const void* p, unsigned bytes) {
  const u64 a = (u64)p;
  u32x4 r; r.x = (unsigned)a; r.y = (unsigned)(a >> 32) & 0xffffu; r.z = bytes; r.w = 0x00020000u; return r;
}
__device__ __forceinline__ void dma16(unsigned lds, unsigned imm, unsigned voff, u32x4 srd, unsigned soff) {
  asm volatile("s_add_u32 m0, %0, %4\n\ts_nop 0\n\tbuffer_load_dwordx4 %1, %2, %3 offen lds"
               :: "s"(lds), "v"(voff), "s"(srd), "s"(soff), "i"(imm) : "memory", "scc");
}
__device__ __forceinline__ u32x4 lds128(unsigned a) { return *(const LAS u32x4*)a; }
__device__ __forceinline__ u32x2 ldstr(unsigned a) {
  return __builtin_bit_cast(u32x2, __builtin_amdgcn_ds_read_tr16_b64_v4i16((LAS s16x4*)a));
}
__device__ __forceinline__ u32x4 cat(u32x2 a, u32x2 b) { u32x4 r; r.x = a.x; r.y = a.y; r.z = b.x; r.w = b.y; return r; }
__device__ __forceinline__ f32x4 mfma(u32x4 a, u32x4 b, f32x4 c) {
  return __builtin_amdgcn_mfma_f32_16x16x32_bf16(__builtin_bit_cast(bf16x8, a), __builtin_bit_cast(bf16x8, b), c, 0, 0, 0);
}
__device__ __forceinline__ unsigned pk2(float a, float b) {
  f32x2 t = {a, b};
  return __builtin_bit_cast(unsigned, __builtin_convertvector(t, bf16x2));
}
__device__ __forceinline__ u32x2 pk4(f32x4 v) { u32x2 r; r.x = pk2(v.x, v.y); r.y = pk2(v.z, v.w); return r; }
#define SB() __builtin_amdgcn_sched_barrier(0)
#define WAITV(n) asm volatile("s_waitcnt vmcnt(" #n ")" ::: "memory")
#define WAITVL(n) asm volatile("s_waitcnt vmcnt(" #n ") lgkmcnt(0)" ::: "memory")
#define BAR() do { asm volatile("s_barrier" ::: "memory"); } while (0)
__device__ __forceinline__ unsigned rfl(unsigned v) { return __builtin_amdgcn_readfirstlane(v); }

#define AMP0 -1
#define AMP1 -1
#define STAMP(i) do {} while (0)

template <bool QKV>
struct P1 {
  static constexpr int NM = QKV ? 5 : 6;
  f32x4 acc[NM][4];
  u32x4 srdx, srdw;
  unsigned xv[3], wv[2];
  unsigned xm0[3];
  unsigned wm0;
  unsigned xa0, xa1;
  unsigned wa0, wa1, wa2;
  bool isgate;
  int nm;

  __device__ __forceinline__ void dma_w(int t, unsigned slot) {
    const unsigned so = (unsigned)t * (QKV ? 32u * 3840u : 32u * 3072u);
    dma16(slot + wm0, 0, wv[0], srdw, so);
    dma16(slot + wm0, 1024, wv[QKV ? 1 : 0], srdw, QKV ? so : so + 128);
  }
  __device__ __forceinline__ void dma_x1(int i, int j, unsigned slot) {
    const unsigned so = j < 10 ? (unsigned)j * 128u : OOB;
    const unsigned d = xm0[i] < XSLOT ? XBASE + slot + xm0[i] : SCR_OFF;
    dma16(d, 0, xv[i], srdx, so);
  }
  __device__ __forceinline__ void rd_w(u32x4 (&wf)[4], unsigned slot, int t) {
    if (QKV) {
      const unsigned a = wa0 + slot;
      const unsigned s2 = isgate ? (unsigned)t * 256u : slot;
      wf[0] = cat(ldstr(a), ldstr(a + 1920));
      wf[1] = cat(ldstr(a + 160), ldstr(a + 160 + 1920));
      wf[2] = cat(ldstr(wa1 + s2), ldstr(wa2 + s2));
      wf[3] = cat(ldstr(a + 320), ldstr(a + 320 + 1920));
    } else {
      const unsigned a = wa0 + slot, b = wa1 + slot;
      wf[0] = cat(ldstr(a), ldstr(a + 512));
      wf[1] = cat(ldstr(b), ldstr(b + 512));
      wf[2] = cat(ldstr(a + 2048), ldstr(a + 2048 + 512));
      wf[3] = cat(ldstr(b + 2048), ldstr(b + 2048 + 512));
    }
  }
  __device__ __forceinline__ void mm(int m, const u32x4 (&xf)[NM], const u32x4 (&wf)[4]) {
    if (ABL & 2) { asm volatile("" :: "v"(xf[m]), "v"(wf[0]), "v"(wf[1]), "v"(wf[2]), "v"(wf[3])); return; }
    acc[m][0] = mfma(wf[0], xf[m], acc[m][0]);
    acc[m][1] = mfma(wf[1], xf[m], acc[m][1]);
    acc[m][2] = mfma(wf[2], xf[m], acc[m][2]);
    if (QKV) acc[m][3] = mfma(xf[m], wf[3], acc[m][3]);
    else     acc[m][3] = mfma(wf[3], xf[m], acc[m][3]);
  }
  template <bool ODD>
  __device__ __forceinline__ void step(int t, unsigned wrd, unsigned wwr, unsigned xrd, unsigned xwr,
                                       const u32x4 (&xc)[NM], const u32x4 (&wc)[4], u32x4 (&xn)[NM], u32x4 (&wn)[4]) {
    if (ODD) WAITVL(9); else WAITVL(12);
    if (!(ABL & 8)) BAR();
    SB();
    mm(0, xc, wc);
    SB();
    if (!(ABL & 4)) rd_w(wn, wrd, t + 1);
    else { for (int n = 0; n < 4; ++n) { wn[n] = wc[n]; asm volatile("" : "+v"(wn[n])); } }
    SB();
    mm(1, xc, wc);
    SB();
    const unsigned xa = (ODD ? xa0 : xa1) + xrd;
    if (!(ABL & 4)) {
#pragma unroll
    for (int m = 0; m < 5; ++m) xn[m] = lds128(xa + m * 2048);
    if (!QKV) xn[NM - 1] = lds128(xa + (NM - 1) * 2048);
    } else { for (int m = 0; m < NM; ++m) { xn[m] = xc[m]; asm volatile("" : "+v"(xn[m])); } }
    SB();
    mm(2, xc, wc);
    SB();
    if (!(ABL & 1)) dma_w(t + 5, wwr);
    SB();
    mm(3, xc, wc);
    SB();
    if (ODD && !(ABL & 1)) { dma_x1(0, (t + 5) >> 1, xwr); dma_x1(1, (t + 5) >> 1, xwr); dma_x1(2, (t + 5) >> 1, xwr); }
    SB();
    mm(4, xc, wc);
    if (!QKV) { if (nm == 6) mm(NM - 1, xc, wc); }
    SB();
  }
  __device__ __forceinline__ void prologue_dma() {
    dma_w(0, 0);
    dma_x1(0, 0, 0); dma_x1(1, 0, 0); dma_x1(2, 0, 0);
    dma_w(1, WSLOT);
    dma_w(2, 2 * WSLOT);
    dma_x1(0, 1, XSLOT); dma_x1(1, 1, XSLOT); dma_x1(2, 1, XSLOT);
    dma_w(3, 3 * WSLOT);
    dma_w(4, 4 * WSLOT);
    dma_x1(0, 2, 2 * XSLOT); dma_x1(1, 2, 2 * XSLOT); dma_x1(2, 2, 2 * XSLOT);
  }
  __device__ __forceinline__ void zero() {
#pragma unroll
    for (int m = 0; m < NM; ++m)
#pragma unroll
      for (int n = 0; n < 4; ++n) { acc[m][n] = f32x4{0.f, 0.f, 0.f, 0.f}; asm volatile("" : "+v"(acc[m][n])); }
  }
  __device__ __forceinline__ void run() {
    u32x4 xA[NM], wA[4], xB[NM], wB[4];
    rd_w(wA, 0, 0);
#pragma unroll
    for (int m = 0; m < NM; ++m) xA[m] = lds128(xa0 + m * 2048);
    unsigned w0 = 0, w1 = WSLOT;
    unsigned x0 = 0, x1 = XSLOT;
#pragma unroll 1
    for (int t = 0; t < 20; t += 2) {
      unsigned w2 = w1 + WSLOT; if (w2 == 5 * WSLOT) w2 = 0;
      step<false>(t, w1, w0, x0, 0, xA, wA, xB, wB);
      step<true>(t + 1, w2, w1, x1, x0, xB, wB, xA, wA);
      w0 = w2; w1 = w2 + WSLOT; if (w1 == 5 * WSLOT) w1 = 0;
      x0 = x1; x1 += XSLOT; if (x1 == 3 * XSLOT) x1 = 0;
    }
    WAITVL(0);
    BAR();
  }
};

struct P2W {
  u32x4 srd;
  unsigned va[5];
  bool ow; unsigned ctoff;
  unsigned sa, sast, sb, sbst;
  unsigned cell0;
  __device__ __forceinline__ void dma(int s, int cell) {
    const unsigned so = s < 8 ? sa + s * sast : sb + (s - 8) * sbst;
#pragma unroll
    for (int j = 0; j < 5; ++j) dma16(cell0, cell * 5120 + j * 1024, va[j], srd, so);
  }
  __device__ __forceinline__ void pattern(bool tail) {
    unsigned ln = threadIdx.x & 63;
    asm volatile("" : "+v"(ln));
#pragma unroll
    for (int j = 0; j < 5; ++j) {
      const unsigned i = 64 * j + ln, r = (i * 6554u) >> 16, ch = i - r * 10;
      const unsigned ra = !ow ? r : tail ? 80 * (r >> 4) + 20 * ((r >> 2) & 3) + 16 + (r & 3) : 20 * (r >> 4) + (r & 15);
      va[j] = ra * 1280 + ctoff + ch * 16;
    }
  }
};

__device__ __forceinline__ float u2f(unsigned u) { return __builtin_bit_cast(float, u); }
__device__ __forceinline__ unsigned f2u(float f) { return __builtin_bit_cast(unsigned, f); }
__device__ __forceinline__ float xmax(float v) {
  auto r = __builtin_amdgcn_permlane16_swap(f2u(v), f2u(v), false, false);
  v = fmaxf(u2f(r[0]), u2f(r[1]));
  auto s = __builtin_amdgcn_permlane32_swap(f2u(v), f2u(v), false, false);
  return fmaxf(u2f(s[0]), u2f(s[1]));
}
__device__ __forceinline__ float xsum(float v) {
  auto r = __builtin_amdgcn_permlane16_swap(f2u(v), f2u(v), false, false);
  v = u2f(r[0]) + u2f(r[1]);
  auto s = __builtin_amdgcn_permlane32_swap(f2u(v), f2u(v), false, false);
  return u2f(s[0]) + u2f(s[1]);
}

template <int PH>
__global__ __launch_bounds__(512, 2) void fused(const void* x_, const void* wqkv_, const void* wo_, const void* wg_,
                                                const void* wu_, const void* wd_, const void* gw_, void* out_, void* ws_) {
  __shared__ __attribute__((aligned(16))) char smem[LDS_BYTES];
  asm volatile("" :: "s"((unsigned)(size_t)(LAS char*)smem));
  if (threadIdx.x < 64) {
    const GAS unsigned* ka = (const GAS unsigned*)__builtin_amdgcn_kernarg_segment_ptr();
    if (threadIdx.x < 18) *(LAS unsigned*)(SCR_OFF + 2048 + threadIdx.x * 4) = __builtin_nontemporal_load(ka + threadIdx.x);
  }
  asm volatile("s_waitcnt lgkmcnt(0)\n\ts_barrier" ::: "memory");
  const unsigned kav = *(const LAS unsigned*)(SCR_OFF + 2048 + (threadIdx.x & 31) * 4);
  auto karg = [&](int i) { return (char*)(((u64)(unsigned)__builtin_amdgcn_readlane(kav, 2 * i + 1) << 32) | (unsigned)__builtin_amdgcn_readlane(kav, 2 * i)); };
  const void *x = karg(0), *wqkv = karg(1), *wo = karg(2), *wg = karg(3), *wu = karg(4), *wd = karg(5), *gw = karg(6);
  void *out = karg(7), *ws = karg(8);
  const int tid = threadIdx.x, lane = tid & 63;
  const int w = rfl(tid >> 6);
  const int h = lane >> 4, c = lane & 15, q4 = (lane & 15) >> 2, p4 = lane & 3;
  const int b = blockIdx.x, xcd = b & 7, idx = b >> 3;
  const int rh = xcd & 1, cq = xcd >> 1;
  const bool isq = idx < 14;
  const int rb = 4 * xcd + (idx >> 3), ct = idx & 7;
  const __amdgpu_buffer_rsrc_t rws = __builtin_amdgcn_make_buffer_rsrc(ws, 0, WS_END, 0x00020000);
  u64 amp_t0 = 0, amp_t1 = 0;
  STAMP(0);

  asm volatile("" :: "s"(x), "s"(wqkv), "s"(wo), "s"(wg), "s"(wu), "s"(wd), "s"(gw), "s"(out), "s"(ws));
  u64 ev = 0, tag = 0;
  if (PH == 3) asm volatile("s_nop 4\n\tglobal_load_dwordx2 %0, %1, %2 sc1" : "=v"(ev) : "v"(WS_EPOCH), "s"(ws) : "memory");
  auto make_tag = [&]() {
    asm volatile("" : "+v"(ev));
    const u64 e = ((u64)rfl((unsigned)(ev >> 32)) << 32) | rfl((unsigned)ev);
    tag = (e ^ 0x9E3779B97F4A7C15ull) * 0xBF58476D1CE4E5B9ull + 0x632BE59BD9B4E019ull;
    if (tag == e) tag += 1;
  };

  P2W pw;
  int nst, g0;
  {
    const bool ow = w < 2;
    g0 = (int)((0x271E171008000000ull >> (8 * w)) & 0xff);
    nst = (0x99778800 >> (4 * w)) & 15;
    pw.srd = ow ? mk_srd(wo, 640 * 1280) : mk_srd(wd, 1536 * 1280);
    pw.cell0 = w * 20480;
    pw.ow = ow; pw.ctoff = ct * 160;
    pw.sa = ow ? w * 320 * 1280 : g0 * 32 * 1280;
    pw.sast = ow ? 40 * 1280 : 32 * 1280;
    pw.sb = ow ? w * 320 * 1280 : (g0 + 8) * 32 * 1280;
    pw.sbst = ow ? 160 * 1280 : 32 * 1280;
  }

  if (PH & 1) {
    if (isq) {
      const int a = 7 * rh + (idx >= 7 ? idx - 7 : idx), g = 2 * cq + (idx >= 7 ? 1 : 0);
      const int hd = w & 3, par = w >> 2;
      P1<true> k;
      k.nm = 5;
      k.srdx = mk_srd(x, 2048 * 1280);
      k.srdw = mk_srd(wqkv, 640 * 3840);
      unsigned gv0, gv1;
      {
        const u32x4 sg = mk_srd(gw, 1280);
        asm volatile("buffer_load_ushort %0, %2, %3, 0 offen\n\tbuffer_load_ushort %1, %2, %3, 0 offen offset:1024"
                     : "=&v"(gv0), "=&v"(gv1) : "v"(tid * 2), "s"(sg) : "memory");
      }
#pragma unroll
      for (int i = 0; i < 3; ++i) {
        const int q = w * 3 + i;
        const unsigned rho = 8 * q + (lane >> 3);
        const unsigned pp = rho >= 80 ? 1 : 0, u = rho - 80 * pp;
        const unsigned tok = 146 * a + 2 * u + pp;
        k.xv[i] = q < 20 ? tok * 1280 + (((lane & 7) ^ ((rho >> 1) & 7)) << 4) : OOB;
        k.xm0[i] = q < 20 ? q * 1024 : XSLOT;
      }
#pragma unroll
      for (int j = 0; j < 2; ++j) {
        const int I = (2 * w + j) * 64 + lane;
        const int kb = I >= 736 ? 3 : I >= 488 ? 2 : I >= 248 ? 1 : 0;
        const int i = I - (kb == 3 ? 736 : kb == 2 ? 488 : kb == 1 ? 248 : 0);
        const unsigned r = ((unsigned)i * 2185u) >> 16, rem = i - r * 30;
        const unsigned seg = (rem * 6554u) >> 16, ch = rem - seg * 10;
        k.wv[j] = i < 240 ? (8 * kb + r) * 3840 + seg * 1280 + g * 160 + ch * 16 : OOB;
      }
      k.wm0 = w * 2048;
      k.prologue_dma();
      k.xa0 = XBASE + par * 5 * 2048 + c * 128 + ((h ^ (c >> 1)) << 4);
      k.xa1 = k.xa0 ^ 64;
      const unsigned kbo = h == 3 ? 736 * 16 : h == 2 ? 488 * 16 : h == 1 ? 248 * 16 : 0;
      k.wa0 = kbo + q4 * 480 + (20 * hd + 4 * p4) * 2;
      k.isgate = p4 == 3;
      k.wa1 = k.isgate ? GATE_OFF + (8 * h + q4) * 8 : kbo + q4 * 480 + p4 * 160 + (20 * hd + 16) * 2;
      k.wa2 = k.wa1 + (k.isgate ? 32 : 1920);
      k.zero();
      asm volatile("s_waitcnt vmcnt(14)" : "+v"(gv0), "+v"(gv1) :: "memory");
      {
        u32x2 v; v.x = gv0; v.y = 0;
        *(LAS u32x2*)(GATE_OFF + tid * 8) = v;
        if (tid < 128) { v.x = gv1; *(LAS u32x2*)(GATE_OFF + 4096 + tid * 8) = v; }
      }
      asm volatile("s_waitcnt lgkmcnt(0)" ::: "memory");
      BAR();
      STAMP(1);
      k.run();
      STAMP(2);
      if (PH & 2) { pw.pattern(false); pw.dma(0, 0); pw.dma(1, 1); pw.dma(2, 2); }
      u32x2 ql[5], kl[5], t2[5], vl[5], tt[5];
      const unsigned scr = pw.cell0 + 15360;
#pragma unroll
      for (int i = 0; i < 5; ++i) {
        ql[i] = pk4(k.acc[i][0]); kl[i] = pk4(k.acc[i][1]); t2[i] = pk4(k.acc[i][2]); vl[i] = pk4(k.acc[i][3]);
        *(LAS u32x2*)(scr + i * 512 + c * 32 + h * 8) = t2[i];
      }
      asm volatile("s_waitcnt lgkmcnt(0)" ::: "memory");
#pragma unroll
      for (int i = 0; i < 5; ++i) tt[i] = ldstr(scr + i * 512 + (4 * h + q4) * 32 + p4 * 8);
      asm volatile("s_waitcnt lgkmcnt(0)" : "+v"(tt[0]), "+v"(tt[1]), "+v"(tt[2]), "+v"(tt[3]), "+v"(tt[4]) :: "memory");
      if (PH & 2) pw.dma(3, 3);
      f32x4 ba, bb;
#pragma unroll
      for (int r = 0; r < 4; ++r) {
        const int kap = 4 * h + r;
        ba[r] = kap >= c + 9 ? 0.f : -1e30f;
        bb[r] = (kap <= c && kap >= c - 7) ? 0.f : -1e30f;
      }
      const unsigned m0mask = h == 0 ? 0xffffffffu : 0u;
      u32x4 kf[5];
#pragma unroll
      for (int i = 0; i < 5; ++i) {
        auto s0 = __builtin_amdgcn_permlane16_swap(t2[i].x, t2[i].x, false, false);
        auto s1 = __builtin_amdgcn_permlane16_swap(t2[i].y, t2[i].y, false, false);
        kf[i].x = kl[i].x; kf[i].y = kl[i].y; kf[i].z = s0[1]; kf[i].w = s1[1];
      }
      constexpr float SC = 0.22360679774997896f * 1.4426950408889634f;
#pragma unroll
      for (int i = 0; i < 5; ++i) {
        u32x4 qf; qf.x = ql[i].x; qf.y = ql[i].y; qf.z = t2[i].x & m0mask; qf.w = t2[i].y & m0mask;
        f32x4 sb = mfma(kf[i], qf, bb);
        f32x4 sa = f32x4{-1e30f, -1e30f, -1e30f, -1e30f};
        if (i > 0) sa = mfma(kf[i - 1], qf, ba);
        float m = fmaxf(fmaxf(fmaxf(sb.x, sb.y), fmaxf(sb.z, sb.w)), fmaxf(fmaxf(sa.x, sa.y), fmaxf(sa.z, sa.w)));
        m = xmax(m);
        const float mc = m * SC;
        f32x4 pa, pb;
#pragma unroll
        for (int r = 0; r < 4; ++r) {
          pb[r] = __builtin_amdgcn_exp2f(sb[r] * SC - mc);
          pa[r] = i > 0 ? __builtin_amdgcn_exp2f(sa[r] * SC - mc) : 0.f;
        }
        float l = ((pb.x + pb.y) + (pb.z + pb.w)) + ((pa.x + pa.y) + (pa.z + pa.w));
        l = xsum(l);
        const float rl = __builtin_amdgcn_rcpf(l);
        const u32x4 pf = cat(pk4(pa * rl), pk4(pb * rl));
        const f32x4 z = {0.f, 0.f, 0.f, 0.f};
        const f32x4 oa = mfma(cat(vl[i > 0 ? i - 1 : 0], vl[i]), pf, z);
        const f32x4 ob = mfma(cat(tt[i > 0 ? i - 1 : 0], tt[i]), pf, z);
        const int u = 16 * i + c, U = 73 * a + u;
        const bool ok = (a == 0 || u >= 7) && U < 1024;
        const unsigned mt = 2 * (U >> 4) + par, cc = U & 15;
        const unsigned oo = WS_O + mt * (40 * 512) + cc * 8;
        __builtin_amdgcn_raw_buffer_store_b64(pk4(oa), rws, ok ? oo + (4 * g + hd) * 512 + h * 128 : OOB, 0, AUX_SC1);
        __builtin_amdgcn_raw_buffer_store_b64(pk4(ob), rws, (ok && h == 2) ? oo + (32 + g) * 512 + hd * 128 : OOB, 0, AUX_SC1);
        if (g == 0 && hd == 0)
          __builtin_amdgcn_raw_buffer_store_b32(f2u(k.acc[i][2].x), rws,
                                                (ok && h == 3) ? WS_G + (mt * 16 + cc) * 4 : OOB, 0, AUX_SC1);
      }
      STAMP(3);
      if (PH == 3) {
        WAITV(0);
        BAR();
        make_tag();
        if (w == 0 && lane < 5) {
          const int U0 = a ? 73 * a + 7 : 0, U1 = min(73 * a + 80, 1024);
          const int r = (U0 >> 5) + lane;
          const int a0 = r * 32 < 80 ? 0 : (r * 32 - 7) / 73;
          GAS u64* f = (GAS u64*)((char*)ws + (lane == 4 ? WS_DONE + b * 8 : WS_FLAG + (r * 64 + 24 + (a - a0) * 8 + g) * 8));
          if (lane == 4 || r * 32 < U1) __hip_atomic_store(f, tag, __ATOMIC_RELAXED, __HIP_MEMORY_SCOPE_AGENT);
        }
      }
    } else {
      const int f = idx - 14;
      const int fr = f >= 12 ? f - 12 : f >= 6 ? f - 6 : f, fc = f >= 12 ? 2 : f >= 6 ? 1 : 0;
      const int ri = 6 * rh + fr, cj = 3 * cq + fc;
      const int nq = w & 3, mh = w >> 2;
      P1<false> k;
      k.nm = mh ? 5 : 6;
      k.isgate = false;
      k.srdx = mk_srd(x, 2048 * 1280);
      k.srdw = mk_srd((w & 1) ? wu : wg, 640 * 3072);
#pragma unroll
      for (int i = 0; i < 3; ++i) {
        const int q = w * 3 + i;
        const unsigned rho = 8 * q + (lane >> 3);
        const unsigned mt = 11 * ri + (rho >> 4);
        const unsigned tok = 32 * (mt >> 1) + 2 * (rho & 15) + (mt & 1);
        k.xv[i] = q < 22 ? tok * 1280 + (((lane & 7) ^ ((rho >> 1) & 7)) << 4) : OOB;
        k.xm0[i] = q < 22 ? q * 1024 : XSLOT;
      }
      {
        const int kb = w >> 1, r = lane >> 3, p8 = lane & 7;
        const int s = ((r >> 1) & 1) + 2 * (kb & 1);
        k.wv[0] = (8 * kb + r) * 3072 + cj * 256 + (((((p8 >> 1) ^ s) << 1) | (p8 & 1)) << 4);
        k.wv[1] = 0;
      }
      k.wm0 = w * 2048;
      k.prologue_dma();
      k.xa0 = XBASE + mh * 6 * 2048 + c * 128 + ((h ^ (c >> 1)) << 4);
      k.xa1 = k.xa0 ^ 64;
      {
        const int s = ((q4 >> 1) & 1) + 2 * (h & 1);
        k.wa0 = (h * 4 + (nq >> 1)) * 1024 + q4 * 128 + (((2 * (nq & 1)) ^ s) << 5) + p4 * 8;
        k.wa1 = k.wa0 ^ 32;
        k.wa2 = 0;
      }
      k.zero();
      WAITV(14);
      BAR();
      STAMP(1);
      k.run();
      STAMP(2);
      if (PH & 2) { pw.pattern(false); pw.dma(0, 0); pw.dma(1, 1); pw.dma(2, 2); pw.dma(3, 3); }
#pragma unroll
      for (int m = 0; m < 6; ++m) {
        if (m < k.nm) {
          float v[8];
#pragma unroll
          for (int j = 0; j < 8; ++j) {
            const float gt = k.acc[m][j >> 2][j & 3], up = k.acc[m][2 + (j >> 2)][j & 3];
            const float e = __builtin_amdgcn_exp2f(gt * -1.4426950408889634f);
            v[j] = gt * up * __builtin_amdgcn_rcpf(1.f + e);
          }
          u32x4 o; o.x = pk2(v[0], v[1]); o.y = pk2(v[2], v[3]); o.z = pk2(v[4], v[5]); o.w = pk2(v[6], v[7]);
          const unsigned mt = 11 * ri + 6 * mh + m;
          __builtin_amdgcn_raw_buffer_store_b128(o, rws, mt < 128 ? WS_GU + (mt * 48 + 4 * cj + nq) * 1024 + lane * 16 : OOB, 0, AUX_SC1);
        }
      }
      STAMP(3);
      if (PH == 3) {
        WAITV(0);
        BAR();
        make_tag();
        if (w == 0 && lane < 5) {
          const int m0_ = 11 * ri, m1 = min(m0_ + 11, 128);
          const int r = (m0_ >> 2) + lane;
          const int r0 = (4 * r) / 11;
          GAS u64* fp = (GAS u64*)((char*)ws + (lane == 4 ? WS_DONE + b * 8 : WS_FLAG + (r * 64 + (ri - r0) * 12 + cj) * 8));
          if (lane == 4 || r * 4 < m1) __hip_atomic_store(fp, tag, __ATOMIC_RELAXED, __HIP_MEMORY_SCOPE_AGENT);
        }
      }
    }
  }
  STAMP(4);

  if (PH & 2) {
    if (PH == 2) { pw.pattern(false); pw.dma(0, 0); pw.dma(1, 1); pw.dma(2, 2); pw.dma(3, 3); WAITV(0); }
    auto body = [&](auto OWT) {
      constexpr bool OW = decltype(OWT)::value;
      constexpr unsigned AMT = OW ? 40 * 512 : 48 * 1024;
      constexpr int NS = OW ? 10 : 9;
      const unsigned wra = pw.cell0 + (4 * h + q4) * 160 + p4 * 8;
      u32x4 wreg[4][5];
      auto rdw = [&](int cell) {
#pragma unroll
        for (int n = 0; n < 5; ++n) wreg[cell][n] = cat(ldstr(wra + cell * 5120 + n * 32), ldstr(wra + cell * 5120 + n * 32 + 2560));
      };
      rdw(0); rdw(1); rdw(2); rdw(3);
      asm volatile("s_waitcnt lgkmcnt(0)" ::: "memory");
      SB();
      pw.dma(4, 0); pw.dma(5, 1); pw.dma(6, 2);
      if (OW || nst > 7) pw.dma(7, 3);
      if (OW) pw.pattern(true);
      if (PH == 3) {
        const bool two_f = (4 * rb) / 11 != (4 * rb + 3) / 11;
        const int a0 = rb * 32 < 80 ? 0 : (rb * 32 - 7) / 73, a1 = rb * 32 + 31 < 80 ? 0 : (rb * 32 + 31 - 7) / 73;
        bool need;
        if (lane < 24) {
          const int cjj = lane >= 12 ? lane - 12 : lane;
          need = !OW && (lane < 12 || two_f) && cjj >= (g0 >> 2) && cjj <= ((g0 + nst - 1) >> 2);
        } else {
          const int gg = (lane - 24) & 7;
          need = lane < 40 && (lane < 32 || a1 != a0) && (gg == 0 || (OW && (gg >> 2) == w));
        }
        GAS u64* fp = (GAS u64*)((char*)ws + WS_FLAG + (rb * 64 + lane) * 8);
        for (unsigned spin = 0; spin < (1u << 18); ++spin) {
          const u64 v = __hip_atomic_load(fp, __ATOMIC_RELAXED, __HIP_MEMORY_SCOPE_AGENT);
          if (__all(!need || v == tag)) break;
          __builtin_amdgcn_s_sleep(2);
        }
        __builtin_amdgcn_fence(__ATOMIC_ACQUIRE, "wavefront");
        asm volatile("" ::: "memory");
      }
      STAMP(5);
      const unsigned av = OW ? lane * 8 : lane * 16;
      const unsigned as0 = (OW ? WS_O + 8 * w * 1024 : WS_GU + g0 * 1024) + 4 * rb * AMT;
      const unsigned as1 = WS_O + (16 + 2 * w) * 1024 + 4 * rb * AMT;
      u32x4 af[4][4];
      f32x4 acc[4][5];
      float sg[4];
      auto lda = [&](int s) {
        const unsigned so = (OW && s >= 8) ? as1 + (s - 8) * 1024 : as0 + s * 1024;
#pragma unroll
        for (int m = 0; m < 4; ++m) {
          if (OW) af[s & 3][m] = cat(__builtin_amdgcn_raw_buffer_load_b64(rws, av, so + m * AMT, AUX_SC1),
                                     __builtin_amdgcn_raw_buffer_load_b64(rws, av + 512, so + m * AMT, AUX_SC1));
          else    af[s & 3][m] = __builtin_amdgcn_raw_buffer_load_b128(rws, av, so + m * AMT, AUX_SC1);
        }
      };
      lda(0); lda(1); lda(2); lda(3);
      SB();
#pragma unroll
      for (int m = 0; m < 4; ++m)
#pragma unroll
        for (int n = 0; n < 5; ++n) acc[m][n] = f32x4{0.f, 0.f, 0.f, 0.f};
#pragma unroll
      for (int s = 0; s < NS; ++s) {
        if (OW || s < 7 || s < nst) {
#pragma unroll
          for (int m = 0; m < 4; ++m)
#pragma unroll
            for (int n = 0; n < 5; ++n) acc[m][n] = mfma(wreg[s & 3][n], af[s & 3][m], acc[m][n]);
          SB();
          asm volatile("" ::: "memory");
          if (s + 4 < NS && (OW || s + 4 < 7 || s + 4 < nst)) {
            rdw(s & 3);
            if (s + 8 < NS) {
              if (OW || s + 8 < nst) { asm volatile("s_waitcnt lgkmcnt(0)" ::: "memory"); SB(); pw.dma(s + 8, s & 3); }
            }
            lda(s + 4);
          }
          if (s == 6) {
#pragma unroll
            for (int m = 0; m < 4; ++m)
              sg[m] = __builtin_bit_cast(float, __builtin_amdgcn_raw_buffer_load_b32(rws, c * 4, WS_G + (4 * rb + m) * 64, AUX_SC1));
          }
          SB();
        }
      }
      STAMP(6);
#pragma unroll
      for (int m = 0; m < 4; ++m) {
        const float e = __builtin_amdgcn_exp2f(sg[m] * (OW ? -1.4426950408889634f : 1.4426950408889634f));
        const float f = __builtin_amdgcn_rcpf(1.f + e);
#pragma unroll
        for (int n = 0; n < 5; ++n) *(LAS f32x4*)(pw.cell0 + (m * 5 + n) * 1024 + lane * 16) = acc[m][n] * f;
      }
    };
    if (w < 2) body(std::true_type{}); else body(std::false_type{});
    if (PH == 3 && b == 0 && w == 4) {
      GAS u64* dp = (GAS u64*)((char*)ws + WS_DONE + lane * 8);
      for (unsigned spin = 0; spin < (1u << 18); ++spin) {
        bool ok = true;
#pragma unroll
        for (int j = 0; j < 4; ++j) ok &= __hip_atomic_load(dp + j * 64, __ATOMIC_RELAXED, __HIP_MEMORY_SCOPE_AGENT) == tag;
        if (__all(ok)) break;
        __builtin_amdgcn_s_sleep(2);
      }
      if (lane == 0) __hip_atomic_store((GAS u64*)((char*)ws + WS_EPOCH), tag, __ATOMIC_RELAXED, __HIP_MEMORY_SCOPE_AGENT);
    }
    asm volatile("s_waitcnt lgkmcnt(0)" ::: "memory");
    BAR();
    const int t0 = w < 4 ? 3 * w : 12 + 2 * (w - 4);
#pragma unroll
    for (int j = 0; j < 3; ++j) {
      if (j < 2 || w < 4) {
        const int tl = t0 + j;
        f32x4 s = *(const LAS f32x4*)(tl * 1024 + lane * 16);
#pragma unroll
        for (int v = 1; v < 8; ++v) s += *(const LAS f32x4*)(v * 20480 + tl * 1024 + lane * 16);
        const int m = tl / 5, n = tl - 5 * m;
        const int tok = 64 * rb + 32 * (m >> 1) + 2 * c + (m & 1);
        *(GAS u32x2*)((char*)out + tok * 1280 + ct * 160 + n * 32 + h * 8) = pk4(s);
      }
    }
    STAMP(7);
    if (AMP0 >= 0) {
      const u64 d = amp_t1 - amp_t0, t = __builtin_amdgcn_s_memrealtime();
      while (__builtin_amdgcn_s_memrealtime() - t < d) __builtin_amdgcn_s_sleep(1);
    }
  }
}
}

extern "C" void kernel_launch(void* const* d_in, const int* in_sizes, int n_in, void* d_out, int out_size, void* d_ws,
                              size_t ws_size, hipStream_t stream) {
  fused<3><<<256, 512, 0, stream>>>(d_in[0], d_in[1], d_in[2], d_in[4], d_in[5], d_in[6], d_in[7], d_out, d_ws);
}
```

```cpp
#include <hip/hip_runtime.h>
#include <hip/hip_bf16.h>
#include <stdint.h>
#include <type_traits>

#define SPLIT 0
#define STAMPS 0

#define ABL 0
#define LAS __attribute__((address_space(3)))
#define GAS __attribute__((address_space(1)))
typedef __bf16 bf16x8 __attribute__((ext_vector_type(8)));
typedef __bf16 bf16x2 __attribute__((ext_vector_type(2)));
typedef float f32x4 __attribute__((ext_vector_type(4)));
typedef float f32x2 __attribute__((ext_vector_type(2)));
typedef unsigned u32x4 __attribute__((ext_vector_type(4)));
typedef unsigned u32x2 __attribute__((ext_vector_type(2)));
typedef short s16x4 __attribute__((ext_vector_type(4)));
typedef unsigned long long u64;

namespace {
constexpr int LDS_BYTES = 163840;
constexpr unsigned WSLOT = 16384, XBASE = 81920, XSLOT = 22528, GATE_OFF = 149504, SCR_OFF = 154624;
constexpr unsigned OOB = 0x40000000u;
constexpr unsigned WS_FLAG = 0;
constexpr unsigned WS_DONE = 16384;
constexpr unsigned WS_EPOCH = 20480;
constexpr unsigned WS_G = 24576;
constexpr unsigned WS_O = 65536;
constexpr unsigned WS_GU = WS_O + 128 * 40 * 512;
constexpr unsigned WS_END = WS_GU + 128 * 48 * 1024;
constexpr unsigned WS_STAMP = 16u << 20;
constexpr int AUX_SC1 = 16;

__device__ __forceinline__ u32x4 mk_srd(const void* p, unsigned bytes) {
  const u64 a = (u64)p;
  u32x4 r; r.x = (unsigned)a; r.y = (unsigned)(a >> 32) & 0xffffu; r.z = bytes; r.w = 0x00020000u; return r;
}
__device__ __forceinline__ void dma16(unsigned lds, unsigned imm, unsigned voff, u32x4 srd, unsigned soff) {
  asm volatile("s_add_u32 m0, %0, %4\n\ts_nop 0\n\tbuffer_load_dwordx4 %1, %2, %3 offen lds"
               :: "s"(lds), "v"(voff), "s"(srd), "s"(soff), "i"(imm) : "memory", "scc");
}
__device__ __forceinline__ u32x4 lds128(unsigned a) { return *(const LAS u32x4*)a; }
__device__ __forceinline__ u32x2 ldstr(unsigned a) {
  return __builtin_bit_cast(u32x2, __builtin_amdgcn_ds_read_tr16_b64_v4i16((LAS s16x4*)a));
}
__device__ __forceinline__ u32x4 cat(u32x2 a, u32x2 b) { u32x4 r; r.x = a.x; r.y = a.y; r.z = b.x; r.w = b.y; return r; }
__device__ __forceinline__ f32x4 mfma(u32x4 a, u32x4 b, f32x4 c) {
  return __builtin_amdgcn_mfma_f32_16x16x32_bf16(__builtin_bit_cast(bf16x8, a), __builtin_bit_cast(bf16x8, b), c, 0, 0, 0);
}
__device__ __forceinline__ unsigned pk2(float a, float b) {
  f32x2 t = {a, b};
  return __builtin_bit_cast(unsigned, __builtin_convertvector(t, bf16x2));
}
__device__ __forceinline__ u32x2 pk4(f32x4 v) { u32x2 r; r.x = pk2(v.x, v.y); r.y = pk2(v.z, v.w); return r; }
#define SB() __builtin_amdgcn_sched_barrier(0)
#define WAITV(n) asm volatile("s_waitcnt vmcnt(" #n ")" ::: "memory")
#define WAITVL(n) asm volatile("s_waitcnt vmcnt(" #n ") lgkmcnt(0)" ::: "memory")
#define BAR() do { asm volatile("s_barrier" ::: "memory"); } while (0)
__device__ __forceinline__ unsigned rfl(unsigned v) { return __builtin_amdgcn_readfirstlane(v); }

#define AMP0 -1
#define AMP1 -1
#define STAMP(i) do {} while (0)

template <bool QKV>
struct P1 {
  static constexpr int NM = QKV ? 5 : 6;
  f32x4 acc[NM][4];
  u32x4 srdx, srdw;
  unsigned xv[3], wv[2];
  unsigned xm0[3];
  unsigned wm0;
  unsigned xa0, xa1;
  unsigned wa0, wa1, wa2;
  bool isgate;
  int nm;

  __device__ __forceinline__ void dma_w(int t, unsigned slot) {
    const unsigned so = (unsigned)t * (QKV ? 32u * 3840u : 32u * 3072u);
    dma16(slot + wm0, 0, wv[0], srdw, so);
    dma16(slot + wm0, 1024, wv[QKV ? 1 : 0], srdw, QKV ? so : so + 128);
  }
  __device__ __forceinline__ void dma_x1(int i, int j, unsigned slot) {
    const unsigned so = j < 10 ? (unsigned)j * 128u : OOB;
    const unsigned d = xm0[i] < XSLOT ? XBASE + slot + xm0[i] : SCR_OFF;
    dma16(d, 0, xv[i], srdx, so);
  }
  __device__ __forceinline__ void rd_w(u32x4 (&wf)[4], unsigned slot, int t) {
    if (QKV) {
      const unsigned a = wa0 + slot;
      const unsigned s2 = isgate ? (unsigned)t * 256u : slot;
      wf[0] = cat(ldstr(a), ldstr(a + 1920));
      wf[1] = cat(ldstr(a + 160), ldstr(a + 160 + 1920));
      wf[2] = cat(ldstr(wa1 + s2), ldstr(wa2 + s2));
      wf[3] = cat(ldstr(a + 320), ldstr(a + 320 + 1920));
    } else {
      const unsigned a = wa0 + slot, b = wa1 + slot;
      wf[0] = cat(ldstr(a), ldstr(a + 512));
      wf[1] = cat(ldstr(b), ldstr(b + 512));
      wf[2] = cat(ldstr(a + 2048), ldstr(a + 2048 + 512));
      wf[3] = cat(ldstr(b + 2048), ldstr(b + 2048 + 512));
    }
  }
  __device__ __forceinline__ void mm(int m, const u32x4 (&xf)[NM], const u32x4 (&wf)[4]) {
    if (ABL & 2) { asm volatile("" :: "v"(xf[m]), "v"(wf[0]), "v"(wf[1]), "v"(wf[2]), "v"(wf[3])); return; }
    acc[m][0] = mfma(wf[0], xf[m], acc[m][0]);
    acc[m][1] = mfma(wf[1], xf[m], acc[m][1]);
    acc[m][2] = mfma(wf[2], xf[m], acc[m][2]);
    if (QKV) acc[m][3] = mfma(xf[m], wf[3], acc[m][3]);
    else     acc[m][3] = mfma(wf[3], xf[m], acc[m][3]);
  }
  template <bool ODD>
  __device__ __forceinline__ void step(int t, unsigned wrd, unsigned wwr, unsigned xrd, unsigned xwr,
                                       const u32x4 (&xc)[NM], const u32x4 (&wc)[4], u32x4 (&xn)[NM], u32x4 (&wn)[4]) {
    if (ODD) WAITVL(9); else WAITVL(12);
    if (!(ABL & 8)) BAR();
    SB();
    mm(0, xc, wc);
    SB();
    if (!(ABL & 4)) rd_w(wn, wrd, t + 1);
    else { for (int n = 0; n < 4; ++n) { wn[n] = wc[n]; asm volatile("" : "+v"(wn[n])); } }
    SB();
    mm(1, xc, wc);
    SB();
    const unsigned xa = (ODD ? xa0 : xa1) + xrd;
    if (!(ABL & 4)) {
#pragma unroll
    for (int m = 0; m < 5; ++m) xn[m] = lds128(xa + m * 2048);
    if (!QKV) xn[NM - 1] = lds128(xa + (NM - 1) * 2048);
    } else { for (int m = 0; m < NM; ++m) { xn[m] = xc[m]; asm volatile("" : "+v"(xn[m])); } }
    SB();
    mm(2, xc, wc);
    SB();
    if (!(ABL & 1)) dma_w(t + 5, wwr);
    SB();
    mm(3, xc, wc);
    SB();
    if (ODD && !(ABL & 1)) { dma_x1(0, (t + 5) >> 1, xwr); dma_x1(1, (t + 5) >> 1, xwr); dma_x1(2, (t + 5) >> 1, xwr); }
    SB();
    mm(4, xc, wc);
    if (!QKV) { if (nm == 6) mm(NM - 1, xc, wc); }
    SB();
  }
  __device__ __forceinline__ void prologue_dma() {
    dma_w(0, 0);
    dma_x1(0, 0, 0); dma_x1(1, 0, 0); dma_x1(2, 0, 0);
    dma_w(1, WSLOT);
    dma_w(2, 2 * WSLOT);
    dma_x1(0, 1, XSLOT); dma_x1(1, 1, XSLOT); dma_x1(2, 1, XSLOT);
    dma_w(3, 3 * WSLOT);
    dma_w(4, 4 * WSLOT);
    dma_x1(0, 2, 2 * XSLOT); dma_x1(1, 2, 2 * XSLOT); dma_x1(2, 2, 2 * XSLOT);
  }
  __device__ __forceinline__ void zero() {
#pragma unroll
    for (int m = 0; m < NM; ++m)
#pragma unroll
      for (int n = 0; n < 4; ++n) { acc[m][n] = f32x4{0.f, 0.f, 0.f, 0.f}; asm volatile("" : "+v"(acc[m][n])); }
  }
  __device__ __forceinline__ void run() {
    u32x4 xA[NM], wA[4], xB[NM], wB[4];
    rd_w(wA, 0, 0);
#pragma unroll
    for (int m = 0; m < NM; ++m) xA[m] = lds128(xa0 + m * 2048);
    unsigned w0 = 0, w1 = WSLOT;
    unsigned x0 = 0, x1 = XSLOT;
#pragma unroll 1
    for (int t = 0; t < 20; t += 2) {
      unsigned w2 = w1 + WSLOT; if (w2 == 5 * WSLOT) w2 = 0;
      step<false>(t, w1, w0, x0, 0, xA, wA, xB, wB);
      step<true>(t + 1, w2, w1, x1, x0, xB, wB, xA, wA);
      w0 = w2; w1 = w2 + WSLOT; if (w1 == 5 * WSLOT) w1 = 0;
      x0 = x1; x1 += XSLOT; if (x1 == 3 * XSLOT) x1 = 0;
    }
    WAITVL(0);
    BAR();
  }
};

struct P2W {
  u32x4 srd;
  unsigned va[5];
  bool ow; unsigned ctoff;
  unsigned sa, sast, sb, sbst;
  unsigned cell0;
  __device__ __forceinline__ void dma(int s, int cell) {
    const unsigned so = s < 8 ? sa + s * sast : sb + (s - 8) * sbst;
#pragma unroll
    for (int j = 0; j < 5; ++j) dma16(cell0, cell * 5120 + j * 1024, va[j], srd, so);
  }
  __device__ __forceinline__ void pattern(bool tail) {
    unsigned ln = threadIdx.x & 63;
    asm volatile("" : "+v"(ln));
#pragma unroll
    for (int j = 0; j < 5; ++j) {
      const unsigned i = 64 * j + ln, r = (i * 6554u) >> 16, ch = i - r * 10;
      const unsigned ra = !ow ? r : tail ? 80 * (r >> 4) + 20 * ((r >> 2) & 3) + 16 + (r & 3) : 20 * (r >> 4) + (r & 15);
      va[j] = ra * 1280 + ctoff + ch * 16;
    }
  }
};

__device__ __forceinline__ float u2f(unsigned u) { return __builtin_bit_cast(float, u); }
__device__ __forceinline__ unsigned f2u(float f) { return __builtin_bit_cast(unsigned, f); }
__device__ __forceinline__ float xmax(float v) {
  auto r = __builtin_amdgcn_permlane16_swap(f2u(v), f2u(v), false, false);
  v = fmaxf(u2f(r[0]), u2f(r[1]));
  auto s = __builtin_amdgcn_permlane32_swap(f2u(v), f2u(v), false, false);
  return fmaxf(u2f(s[0]), u2f(s[1]));
}
__device__ __forceinline__ float xsum(float v) {
  auto r = __builtin_amdgcn_permlane16_swap(f2u(v), f2u(v), false, false);
  v = u2f(r[0]) + u2f(r[1]);
  auto s = __builtin_amdgcn_permlane32_swap(f2u(v), f2u(v), false, false);
  return u2f(s[0]) + u2f(s[1]);
}

template <int PH>
__global__ __launch_bounds__(512, 2) void fused(const void* __restrict__ x, const void* __restrict__ wqkv,
                                                const void* __restrict__ wo, const void* __restrict__ wg,
                                                const void* __restrict__ wu, const void* __restrict__ wd,
                                                const void* __restrict__ gw, void* out, void* ws) {
  __shared__ __attribute__((aligned(16))) char smem[LDS_BYTES];
  asm volatile("" :: "s"((unsigned)(size_t)(LAS char*)smem));
  const int tid = threadIdx.x, lane = tid & 63;
  const int w = rfl(tid >> 6);
  const int h = lane >> 4, c = lane & 15, q4 = (lane & 15) >> 2, p4 = lane & 3;
  const int b = blockIdx.x, xcd = b & 7, idx = b >> 3;
  const int rh = xcd & 1, cq = xcd >> 1;
  const bool isq = idx < 14;
  const int rb = 4 * xcd + (idx >> 3), ct = idx & 7;
  const __amdgpu_buffer_rsrc_t rws = __builtin_amdgcn_make_buffer_rsrc(ws, 0, WS_END, 0x00020000);
  u64 amp_t0 = 0, amp_t1 = 0;
  STAMP(0);

  asm volatile("" :: "s"(x), "s"(wqkv), "s"(wo), "s"(wg), "s"(wu), "s"(wd), "s"(gw), "s"(out), "s"(ws));
  u64 ev = 0, tag = 0;
  if (PH == 3) asm volatile("s_nop 4\n\tglobal_load_dwordx2 %0, %1, %2 sc1" : "=v"(ev) : "v"(WS_EPOCH), "s"(ws) : "memory");
  auto make_tag = [&]() {
    asm volatile("" : "+v"(ev));
    const u64 e = ((u64)rfl((unsigned)(ev >> 32)) << 32) | rfl((unsigned)ev);
    tag = (e ^ 0x9E3779B97F4A7C15ull) * 0xBF58476D1CE4E5B9ull + 0x632BE59BD9B4E019ull;
    if (tag == e) tag += 1;
  };

  P2W pw;
  int nst, g0;
  {
    const bool ow = w < 2;
    g0 = (int)((0x271E171008000000ull >> (8 * w)) & 0xff);
    nst = (0x99778800 >> (4 * w)) & 15;
    pw.srd = ow ? mk_srd(wo, 640 * 1280) : mk_srd(wd, 1536 * 1280);
    pw.cell0 = w * 20480;
    pw.ow = ow; pw.ctoff = ct * 160;
    pw.sa = ow ? w * 320 * 1280 : g0 * 32 * 1280;
    pw.sast = ow ? 40 * 1280 : 32 * 1280;
    pw.sb = ow ? w * 320 * 1280 : (g0 + 8) * 32 * 1280;
    pw.sbst = ow ? 160 * 1280 : 32 * 1280;
  }

  if (PH & 1) {
    if (isq) {
      const int a = 7 * rh + (idx >= 7 ? idx - 7 : idx), g = 2 * cq + (idx >= 7 ? 1 : 0);
      const int hd = w & 3, par = w >> 2;
      P1<true> k;
      k.nm = 5;
      k.srdx = mk_srd(x, 2048 * 1280);
      k.srdw = mk_srd(wqkv, 640 * 3840);
      unsigned gv0, gv1;
      {
        const u32x4 sg = mk_srd(gw, 1280);
        asm volatile("buffer_load_ushort %0, %2, %3, 0 offen\n\tbuffer_load_ushort %1, %2, %3, 0 offen offset:1024"
                     : "=&v"(gv0), "=&v"(gv1) : "v"(tid * 2), "s"(sg) : "memory");
      }
#pragma unroll
      for (int i = 0; i < 3; ++i) {
        const int q = w * 3 + i;
        const unsigned rho = 8 * q + (lane >> 3);
        const unsigned pp = rho >= 80 ? 1 : 0, u = rho - 80 * pp;
        const unsigned tok = 146 * a + 2 * u + pp;
        k.xv[i] = q < 20 ? tok * 1280 + (((lane & 7) ^ ((rho >> 1) & 7)) << 4) : OOB;
        k.xm0[i] = q < 20 ? q * 1024 : XSLOT;
      }
#pragma unroll
      for (int j = 0; j < 2; ++j) {
        const int I = (2 * w + j) * 64 + lane;
        const int kb = I >= 736 ? 3 : I >= 488 ? 2 : I >= 248 ? 1 : 0;
        const int i = I - (kb == 3 ? 736 : kb == 2 ? 488 : kb == 1 ? 248 : 0);
        const unsigned r = ((unsigned)i * 2185u) >> 16, rem = i - r * 30;
        const unsigned seg = (rem * 6554u) >> 16, ch = rem - seg * 10;
        k.wv[j] = i < 240 ? (8 * kb + r) * 3840 + seg * 1280 + g * 160 + ch * 16 : OOB;
      }
      k.wm0 = w * 2048;
      k.prologue_dma();
      k.xa0 = XBASE + par * 5 * 2048 + c * 128 + ((h ^ (c >> 1)) << 4);
      k.xa1 = k.xa0 ^ 64;
      const unsigned kbo = h == 3 ? 736 * 16 : h == 2 ? 488 * 16 : h == 1 ? 248 * 16 : 0;
      k.wa0 = kbo + q4 * 480 + (20 * hd + 4 * p4) * 2;
      k.isgate = p4 == 3;
      k.wa1 = k.isgate ? GATE_OFF + (8 * h + q4) * 8 : kbo + q4 * 480 + p4 * 160 + (20 * hd + 16) * 2;
      k.wa2 = k.wa1 + (k.isgate ? 32 : 1920);
      k.zero();
      asm volatile("s_waitcnt vmcnt(14)" : "+v"(gv0), "+v"(gv1) :: "memory");
      {
        u32x2 v; v.x = gv0; v.y = 0;
        *(LAS u32x2*)(GATE_OFF + tid * 8) = v;
        if (tid < 128) { v.x = gv1; *(LAS u32x2*)(GATE_OFF + 4096 + tid * 8) = v; }
      }
      asm volatile("s_waitcnt lgkmcnt(0)" ::: "memory");
      BAR();
      STAMP(1);
      k.run();
      STAMP(2);
      if (PH & 2) { pw.pattern(false); pw.dma(0, 0); pw.dma(1, 1); pw.dma(2, 2); }
      u32x2 ql[5], kl[5], t2[5], vl[5], tt[5];
      const unsigned scr = pw.cell0 + 15360;
#pragma unroll
      for (int i = 0; i < 5; ++i) {
        ql[i] = pk4(k.acc[i][0]); kl[i] = pk4(k.acc[i][1]); t2[i] = pk4(k.acc[i][2]); vl[i] = pk4(k.acc[i][3]);
        *(LAS u32x2*)(scr + i * 512 + c * 32 + h * 8) = t2[i];
      }
      asm volatile("s_waitcnt lgkmcnt(0)" ::: "memory");
#pragma unroll
      for (int i = 0; i < 5; ++i) tt[i] = ldstr(scr + i * 512 + (4 * h + q4) * 32 + p4 * 8);
      asm volatile("s_waitcnt lgkmcnt(0)" : "+v"(tt[0]), "+v"(tt[1]), "+v"(tt[2]), "+v"(tt[3]), "+v"(tt[4]) :: "memory");
      if (PH & 2) pw.dma(3, 3);
      f32x4 ba, bb;
#pragma unroll
      for (int r = 0; r < 4; ++r) {
        const int kap = 4 * h + r;
        ba[r] = kap >= c + 9 ? 0.f : -1e30f;
        bb[r] = (kap <= c && kap >= c - 7) ? 0.f : -1e30f;
      }
      const unsigned m0mask = h == 0 ? 0xffffffffu : 0u;
      u32x4 kf[5];
#pragma unroll
      for (int i = 0; i < 5; ++i) {
        auto s0 = __builtin_amdgcn_permlane16_swap(t2[i].x, t2[i].x, false, false);
        auto s1 = __builtin_amdgcn_permlane16_swap(t2[i].y, t2[i].y, false, false);
        kf[i].x = kl[i].x; kf[i].y = kl[i].y; kf[i].z = s0[1]; kf[i].w = s1[1];
      }
      constexpr float SC = 0.22360679774997896f * 1.4426950408889634f;
#pragma unroll
      for (int i = 0; i < 5; ++i) {
        u32x4 qf; qf.x = ql[i].x; qf.y = ql[i].y; qf.z = t2[i].x & m0mask; qf.w = t2[i].y & m0mask;
        f32x4 sb = mfma(kf[i], qf, bb);
        f32x4 sa = f32x4{-1e30f, -1e30f, -1e30f, -1e30f};
        if (i > 0) sa = mfma(kf[i - 1], qf, ba);
        float m = fmaxf(fmaxf(fmaxf(sb.x, sb.y), fmaxf(sb.z, sb.w)), fmaxf(fmaxf(sa.x, sa.y), fmaxf(sa.z, sa.w)));
        m = xmax(m);
        const float mc = m * SC;
        f32x4 pa, pb;
#pragma unroll
        for (int r = 0; r < 4; ++r) {
          pb[r] = __builtin_amdgcn_exp2f(sb[r] * SC - mc);
          pa[r] = i > 0 ? __builtin_amdgcn_exp2f(sa[r] * SC - mc) : 0.f;
        }
        float l = ((pb.x + pb.y) + (pb.z + pb.w)) + ((pa.x + pa.y) + (pa.z + pa.w));
        l = xsum(l);
        const float rl = __builtin_amdgcn_rcpf(l);
        const u32x4 pf = cat(pk4(pa * rl), pk4(pb * rl));
        const f32x4 z = {0.f, 0.f, 0.f, 0.f};
        const f32x4 oa = mfma(cat(vl[i > 0 ? i - 1 : 0], vl[i]), pf, z);
        const f32x4 ob = mfma(cat(tt[i > 0 ? i - 1 : 0], tt[i]), pf, z);
        const int u = 16 * i + c, U = 73 * a + u;
        const bool ok = (a == 0 || u >= 7) && U < 1024;
        const unsigned mt = 2 * (U >> 4) + par, cc = U & 15;
        const unsigned oo = WS_O + mt * (40 * 512) + cc * 8;
        __builtin_amdgcn_raw_buffer_store_b64(pk4(oa), rws, ok ? oo + (4 * g + hd) * 512 + h * 128 : OOB, 0, AUX_SC1);
        __builtin_amdgcn_raw_buffer_store_b64(pk4(ob), rws, (ok && h == 2) ? oo + (32 + g) * 512 + hd * 128 : OOB, 0, AUX_SC1);
        if (g == 0 && hd == 0)
          __builtin_amdgcn_raw_buffer_store_b32(f2u(k.acc[i][2].x), rws,
                                                (ok && h == 3) ? WS_G + (mt * 16 + cc) * 4 : OOB, 0, AUX_SC1);
      }
      STAMP(3);
      if (PH == 3) {
        WAITV(0);
        BAR();
        make_tag();
        if (w == 0 && lane < 5) {
          const int U0 = a ? 73 * a + 7 : 0, U1 = min(73 * a + 80, 1024);
          const int r = (U0 >> 5) + lane;
          const int a0 = r * 32 < 80 ? 0 : (r * 32 - 7) / 73;
          GAS u64* f = (GAS u64*)((char*)ws + (lane == 4 ? WS_DONE + b * 8 : WS_FLAG + (r * 64 + 24 + (a - a0) * 8 + g) * 8));
          if (lane == 4 || r * 32 < U1) __hip_atomic_store(f, tag, __ATOMIC_RELAXED, __HIP_MEMORY_SCOPE_AGENT);
        }
      }
    } else {
      const int f = idx - 14;
      const int fr = f >= 12 ? f - 12 : f >= 6 ? f - 6 : f, fc = f >= 12 ? 2 : f >= 6 ? 1 : 0;
      const int ri = 6 * rh + fr, cj = 3 * cq + fc;
      const int nq = w & 3, mh = w >> 2;
      P1<false> k;
      k.nm = mh ? 5 : 6;
      k.isgate = false;
      k.srdx = mk_srd(x, 2048 * 1280);
      k.srdw = mk_srd((w & 1) ? wu : wg, 640 * 3072);
#pragma unroll
      for (int i = 0; i < 3; ++i) {
        const int q = w * 3 + i;
        const unsigned rho = 8 * q + (lane >> 3);
        const unsigned mt = 11 * ri + (rho >> 4);
        const unsigned tok = 32 * (mt >> 1) + 2 * (rho & 15) + (mt & 1);
        k.xv[i] = q < 22 ? tok * 1280 + (((lane & 7) ^ ((rho >> 1) & 7)) << 4) : OOB;
        k.xm0[i] = q < 22 ? q * 1024 : XSLOT;
      }
      {
        const int kb = w >> 1, r = lane >> 3, p8 = lane & 7;
        const int s = ((r >> 1) & 1) + 2 * (kb & 1);
        k.wv[0] = (8 * kb + r) * 3072 + cj * 256 + (((((p8 >> 1) ^ s) << 1) | (p8 & 1)) << 4);
        k.wv[1] = 0;
      }
      k.wm0 = w * 2048;
      k.prologue_dma();
      k.xa0 = XBASE + mh * 6 * 2048 + c * 128 + ((h ^ (c >> 1)) << 4);
      k.xa1 = k.xa0 ^ 64;
      {
        const int s = ((q4 >> 1) & 1) + 2 * (h & 1);
        k.wa0 = (h * 4 + (nq >> 1)) * 1024 + q4 * 128 + (((2 * (nq & 1)) ^ s) << 5) + p4 * 8;
        k.wa1 = k.wa0 ^ 32;
        k.wa2 = 0;
      }
      k.zero();
      WAITV(14);
      BAR();
      STAMP(1);
      k.run();
      STAMP(2);
      if (PH & 2) { pw.pattern(false); pw.dma(0, 0); pw.dma(1, 1); pw.dma(2, 2); pw.dma(3, 3); }
#pragma unroll
      for (int m = 0; m < 6; ++m) {
        if (m < k.nm) {
          float v[8];
#pragma unroll
          for (int j = 0; j < 8; ++j) {
            const float gt = k.acc[m][j >> 2][j & 3], up = k.acc[m][2 + (j >> 2)][j & 3];
            const float e = __builtin_amdgcn_exp2f(gt * -1.4426950408889634f);
            v[j] = gt * up * __builtin_amdgcn_rcpf(1.f + e);
          }
          u32x4 o; o.x = pk2(v[0], v[1]); o.y = pk2(v[2], v[3]); o.z = pk2(v[4], v[5]); o.w = pk2(v[6], v[7]);
          const unsigned mt = 11 * ri + 6 * mh + m;
          __builtin_amdgcn_raw_buffer_store_b128(o, rws, mt < 128 ? WS_GU + (mt * 48 + 4 * cj + nq) * 1024 + lane * 16 : OOB, 0, AUX_SC1);
        }
      }
      STAMP(3);
      if (PH == 3) {
        WAITV(0);
        BAR();
        make_tag();
        if (w == 0 && lane < 5) {
          const int m0_ = 11 * ri, m1 = min(m0_ + 11, 128);
          const int r = (m0_ >> 2) + lane;
          const int r0 = (4 * r) / 11;
          GAS u64* fp = (GAS u64*)((char*)ws + (lane == 4 ? WS_DONE + b * 8 : WS_FLAG + (r * 64 + (ri - r0) * 12 + cj) * 8));
          if (lane == 4 || r * 4 < m1) __hip_atomic_store(fp, tag, __ATOMIC_RELAXED, __HIP_MEMORY_SCOPE_AGENT);
        }
      }
    }
  }
  STAMP(4);

  if (PH & 2) {
    if (PH == 2) { pw.pattern(false); pw.dma(0, 0); pw.dma(1, 1); pw.dma(2, 2); pw.dma(3, 3); WAITV(0); }
    auto body = [&](auto OWT) {
      constexpr bool OW = decltype(OWT)::value;
      constexpr unsigned AMT = OW ? 40 * 512 : 48 * 1024;
      constexpr int NS = OW ? 10 : 9;
      const unsigned wra = pw.cell0 + (4 * h + q4) * 160 + p4 * 8;
      u32x4 wreg[4][5];
      auto rdw = [&](int cell) {
#pragma unroll
        for (int n = 0; n < 5; ++n) wreg[cell][n] = cat(ldstr(wra + cell * 5120 + n * 32), ldstr(wra + cell * 5120 + n * 32 + 2560));
      };
      rdw(0); rdw(1); rdw(2); rdw(3);
      asm volatile("s_waitcnt lgkmcnt(0)" ::: "memory");
      SB();
      pw.dma(4, 0); pw.dma(5, 1); pw.dma(6, 2);
      if (OW || nst > 7) pw.dma(7, 3);
      if (OW) pw.pattern(true);
      if (PH == 3) {
        const bool two_f = (4 * rb) / 11 != (4 * rb + 3) / 11;
        const int a0 = rb * 32 < 80 ? 0 : (rb * 32 - 7) / 73, a1 = rb * 32 + 31 < 80 ? 0 : (rb * 32 + 31 - 7) / 73;
        bool need;
        if (lane < 24) {
          const int cjj = lane >= 12 ? lane - 12 : lane;
          need = !OW && (lane < 12 || two_f) && cjj >= (g0 >> 2) && cjj <= ((g0 + nst - 1) >> 2);
        } else {
          const int gg = (lane - 24) & 7;
          need = lane < 40 && (lane < 32 || a1 != a0) && (gg == 0 || (OW && (gg >> 2) == w));
        }
        GAS u64* fp = (GAS u64*)((char*)ws + WS_FLAG + (rb * 64 + lane) * 8);
        for (unsigned spin = 0; spin < (1u << 18); ++spin) {
          const u64 v = __hip_atomic_load(fp, __ATOMIC_RELAXED, __HIP_MEMORY_SCOPE_AGENT);
          if (__all(!need || v == tag)) break;
          __builtin_amdgcn_s_sleep(2);
        }
        __builtin_amdgcn_fence(__ATOMIC_ACQUIRE, "wavefront");
        asm volatile("" ::: "memory");
      }
      STAMP(5);
      const unsigned av = OW ? lane * 8 : lane * 16;
      const unsigned as0 = (OW ? WS_O + 8 * w * 1024 : WS_GU + g0 * 1024) + 4 * rb * AMT;
      const unsigned as1 = WS_O + (16 + 2 * w) * 1024 + 4 * rb * AMT;
      u32x4 af[4][4];
      f32x4 acc[4][5];
      float sg[4];
      auto lda = [&](int s) {
        const unsigned so = (OW && s >= 8) ? as1 + (s - 8) * 1024 : as0 + s * 1024;
#pragma unroll
        for (int m = 0; m < 4; ++m) {
          if (OW) af[s & 3][m] = cat(__builtin_amdgcn_raw_buffer_load_b64(rws, av, so + m * AMT, AUX_SC1),
                                     __builtin_amdgcn_raw_buffer_load_b64(rws, av + 512, so + m * AMT, AUX_SC1));
          else    af[s & 3][m] = __builtin_amdgcn_raw_buffer_load_b128(rws, av, so + m * AMT, AUX_SC1);
        }
      };
      lda(0); lda(1); lda(2); lda(3);
      SB();
#pragma unroll
      for (int m = 0; m < 4; ++m)
#pragma unroll
        for (int n = 0; n < 5; ++n) acc[m][n] = f32x4{0.f, 0.f, 0.f, 0.f};
#pragma unroll
      for (int s = 0; s < NS; ++s) {
        if (OW || s < 7 || s < nst) {
#pragma unroll
          for (int m = 0; m < 4; ++m)
#pragma unroll
            for (int n = 0; n < 5; ++n) acc[m][n] = mfma(wreg[s & 3][n], af[s & 3][m], acc[m][n]);
          SB();
          asm volatile("" ::: "memory");
          if (s + 4 < NS && (OW || s + 4 < 7 || s + 4 < nst)) {
            rdw(s & 3);
            if (s + 8 < NS) {
              if (OW || s + 8 < nst) { asm volatile("s_waitcnt lgkmcnt(0)" ::: "memory"); SB(); pw.dma(s + 8, s & 3); }
            }
            lda(s + 4);
          }
          if (s == 6) {
#pragma unroll
            for (int m = 0; m < 4; ++m)
              sg[m] = __builtin_bit_cast(float, __builtin_amdgcn_raw_buffer_load_b32(rws, c * 4, WS_G + (4 * rb + m) * 64, AUX_SC1));
          }
          SB();
        }
      }
      STAMP(6);
#pragma unroll
      for (int m = 0; m < 4; ++m) {
        const float e = __builtin_amdgcn_exp2f(sg[m] * (OW ? -1.4426950408889634f : 1.4426950408889634f));
        const float f = __builtin_amdgcn_rcpf(1.f + e);
#pragma unroll
        for (int n = 0; n < 5; ++n) *(LAS f32x4*)(pw.cell0 + (m * 5 + n) * 1024 + lane * 16) = acc[m][n] * f;
      }
    };
    if (w < 2) body(std::true_type{}); else body(std::false_type{});
    if (PH == 3 && b == 0 && w == 4) {
      GAS u64* dp = (GAS u64*)((char*)ws + WS_DONE + lane * 8);
      for (unsigned spin = 0; spin < (1u << 18); ++spin) {
        bool ok = true;
#pragma unroll
        for (int j = 0; j < 4; ++j) ok &= __hip_atomic_load(dp + j * 64, __ATOMIC_RELAXED, __HIP_MEMORY_SCOPE_AGENT) == tag;
        if (__all(ok)) break;
        __builtin_amdgcn_s_sleep(2);
      }
      if (lane == 0) __hip_atomic_store((GAS u64*)((char*)ws + WS_EPOCH), tag, __ATOMIC_RELAXED, __HIP_MEMORY_SCOPE_AGENT);
    }
    asm volatile("s_waitcnt lgkmcnt(0)" ::: "memory");
    BAR();
    const int t0 = w < 4 ? 3 * w : 12 + 2 * (w - 4);
#pragma unroll
    for (int j = 0; j < 3; ++j) {
      if (j < 2 || w < 4) {
        const int tl = t0 + j;
        f32x4 s = *(const LAS f32x4*)(tl * 1024 + lane * 16);
#pragma unroll
        for (int v = 1; v < 8; ++v) s += *(const LAS f32x4*)(v * 20480 + tl * 1024 + lane * 16);
        const int m = tl / 5, n = tl - 5 * m;
        const int tok = 64 * rb + 32 * (m >> 1) + 2 * c + (m & 1);
        *(GAS u32x2*)((char*)out + tok * 1280 + ct * 160 + n * 32 + h * 8) = pk4(s);
      }
    }
    STAMP(7);
    if (AMP0 >= 0) {
      const u64 d = amp_t1 - amp_t0, t = __builtin_amdgcn_s_memrealtime();
      while (__builtin_amdgcn_s_memrealtime() - t < d) __builtin_amdgcn_s_sleep(1);
    }
  }
}
__global__ void k_lead(void* ws) { if (threadIdx.x == 0 && blockIdx.x == 0) ((u64*)((char*)ws + WS_STAMP))[0] = __builtin_amdgcn_s_memrealtime(); }
}

extern "C" void kernel_launch(void* const* d_in, const int* in_sizes, int n_in, void* d_out, int out_size, void* d_ws,
                              size_t ws_size, hipStream_t stream) {
  fused<3><<<256, 512, 0, stream>>>(d_in[0], d_in[1], d_in[2], d_in[4], d_in[5], d_in[6], d_in[7], d_out, d_ws);
  k_lead<<<256, 64, 0, stream>>>(d_ws);
}
```

```cpp
#define ROT 4
#include <hip/hip_runtime.h>
#include <hip/hip_bf16.h>
#include <stdint.h>
#include <type_traits>

#define STAMPS 0

#define LAS __attribute__((address_space(3)))
#define GAS __attribute__((address_space(1)))
typedef __bf16 bf16x8 __attribute__((ext_vector_type(8)));
typedef __bf16 bf16x2 __attribute__((ext_vector_type(2)));
typedef float f32x4 __attribute__((ext_vector_type(4)));
typedef float f32x2 __attribute__((ext_vector_type(2)));
typedef unsigned u32x4 __attribute__((ext_vector_type(4)));
typedef unsigned u32x2 __attribute__((ext_vector_type(2)));
typedef short s16x4 __attribute__((ext_vector_type(4)));
typedef unsigned long long u64;

namespace {
constexpr int LDS_BYTES = 163840;
constexpr unsigned GATE_OFF = 143360, PACK_OFF = 148480, PKF_OFF = PACK_OFF + 4096 * 3;
constexpr unsigned OOB = 0x40000000u;
constexpr unsigned WS_FLAG = 0;
constexpr unsigned WS_DONE = 4096;
constexpr unsigned WS_PKF = 8192;
constexpr unsigned WS_EPOCH = 12288;
constexpr unsigned WS_G = 16384;
constexpr unsigned WS_O = 131072;
constexpr unsigned WS_GU = WS_O + 128 * 40 * 512;
constexpr unsigned WS_PW = WS_GU + 128 * 48 * 1024;
constexpr unsigned WS_END = WS_PW + 8 * 68 * 5120;
constexpr unsigned WS_STAMP = 16u << 20;
constexpr int AUX_SC1 = 16;
constexpr float LOG2E = 1.4426950408889634f;

__device__ __forceinline__ u32x4 mk_srd(const void* p, unsigned bytes) {
  const u64 a = (u64)p;
  u32x4 r; r.x = (unsigned)a; r.y = (unsigned)(a >> 32) & 0xffffu; r.z = bytes; r.w = 0x00020000u; return r;
}
__device__ __forceinline__ void dma16(unsigned lds, unsigned imm, unsigned voff, u32x4 srd, unsigned soff) {
  asm volatile("s_add_u32 m0, %0, %4\n\ts_nop 0\n\tbuffer_load_dwordx4 %1, %2, %3 offen lds"
               :: "s"(lds), "v"(voff), "s"(srd), "s"(soff), "i"(imm) : "memory", "scc");
}
__device__ __forceinline__ void dma16c(unsigned lds, unsigned imm, unsigned voff, u32x4 srd, unsigned soff) {
  asm volatile("s_add_u32 m0, %0, %4\n\ts_nop 0\n\tbuffer_load_dwordx4 %1, %2, %3 offen sc1 lds"
               :: "s"(lds), "v"(voff), "s"(srd), "s"(soff), "i"(imm) : "memory", "scc");
}
__device__ __forceinline__ void ld128(u32x4& d, unsigned voff, u32x4 srd, unsigned soff) {
  asm volatile("buffer_load_dwordx4 %0, %1, %2, %3 offen sc1" : "=v"(d) : "v"(voff), "s"(srd), "s"(soff) : "memory");
}
__device__ __forceinline__ void ld64(u32x2& d, unsigned voff, u32x4 srd, unsigned soff) {
  asm volatile("buffer_load_dwordx2 %0, %1, %2, %3 offen sc1" : "=v"(d) : "v"(voff), "s"(srd), "s"(soff) : "memory");
}
__device__ __forceinline__ void ld32(float& d, unsigned voff, u32x4 srd, unsigned soff) {
  asm volatile("buffer_load_dword %0, %1, %2, %3 offen sc1" : "=v"(d) : "v"(voff), "s"(srd), "s"(soff) : "memory");
}
__device__ __forceinline__ u32x4 lds128(unsigned a) { return *(const LAS u32x4*)a; }
__device__ __forceinline__ u32x2 ldstr(unsigned a) {
  return __builtin_bit_cast(u32x2, __builtin_amdgcn_ds_read_tr16_b64_v4i16((LAS s16x4*)a));
}
__device__ __forceinline__ u32x4 cat(u32x2 a, u32x2 b) { u32x4 r; r.x = a.x; r.y = a.y; r.z = b.x; r.w = b.y; return r; }
__device__ __forceinline__ f32x4 mfma(u32x4 a, u32x4 b, f32x4 c) {
  return __builtin_amdgcn_mfma_f32_16x16x32_bf16(__builtin_bit_cast(bf16x8, a), __builtin_bit_cast(bf16x8, b), c, 0, 0, 0);
}
__device__ __forceinline__ void mac(f32x4& c, u32x4 a, u32x4 b) {
  asm volatile("v_mfma_f32_16x16x32_bf16 %0, %1, %2, %0" : "+v"(c) : "v"(a), "v"(b));
}
__device__ __forceinline__ void mul0(f32x4& c, u32x4 a, u32x4 b) {
  asm volatile("v_mfma_f32_16x16x32_bf16 %0, %1, %2, 0" : "=&v"(c) : "v"(a), "v"(b));
}
__device__ __forceinline__ unsigned pk2(float a, float b) {
  f32x2 t = {a, b};
  return __builtin_bit_cast(unsigned, __builtin_convertvector(t, bf16x2));
}
__device__ __forceinline__ u32x2 pk4(f32x4 v) { u32x2 r; r.x = pk2(v.x, v.y); r.y = pk2(v.z, v.w); return r; }
__device__ __forceinline__ float u2f(unsigned u) { return __builtin_bit_cast(float, u); }
__device__ __forceinline__ unsigned f2u(float f) { return __builtin_bit_cast(unsigned, f); }
#define SB() __builtin_amdgcn_sched_barrier(0)
#define WAITV(n) asm volatile("s_waitcnt vmcnt(%0)" :: "i"(n) : "memory")
#define WAITVL(n) asm volatile("s_waitcnt vmcnt(%0) lgkmcnt(0)" :: "i"(n) : "memory")
#define WAITL() asm volatile("s_waitcnt lgkmcnt(0)" ::: "memory")
#define BAR() asm volatile("s_barrier" ::: "memory")
#define STPAD() do { SB(); asm volatile("s_nop 1" ::: "memory"); SB(); } while (0)
#define MFMA_DONE() do { SB(); asm volatile("s_nop 7\n\ts_nop 1" ::: "memory"); SB(); } while (0)
#define RLX __ATOMIC_RELAXED, __HIP_MEMORY_SCOPE_AGENT
__device__ __forceinline__ unsigned rfl(unsigned v) { return __builtin_amdgcn_readfirstlane(v); }
__device__ __forceinline__ unsigned opaque_lane() { unsigned l = threadIdx.x & 63; asm volatile("" : "+v"(l)); return l; }

#define STAMP(i) do {} while (0)

__device__ __forceinline__ float xmax(float v) {
  auto r = __builtin_amdgcn_permlane16_swap(f2u(v), f2u(v), false, false);
  v = fmaxf(u2f(r[0]), u2f(r[1]));
  auto s = __builtin_amdgcn_permlane32_swap(f2u(v), f2u(v), false, false);
  return fmaxf(u2f(s[0]), u2f(s[1]));
}
__device__ __forceinline__ float xsum(float v) {
  auto r = __builtin_amdgcn_permlane16_swap(f2u(v), f2u(v), false, false);
  v = u2f(r[0]) + u2f(r[1]);
  auto s = __builtin_amdgcn_permlane32_swap(f2u(v), f2u(v), false, false);
  return u2f(s[0]) + u2f(s[1]);
}

template <bool QKV, bool RB>
struct P1 {
  static constexpr int NM = QKV ? 5 : 4;
  static constexpr int NN = QKV ? 4 : (RB ? 4 : 8);
  static constexpr int NWS = QKV ? 5 : 4;
  static constexpr unsigned WSL = QKV ? 16384 : 24576, XB = NWS * WSL, XSL = QKV ? 20480 : 16384;
  static constexpr unsigned WROW = QKV ? 3840 : 3072;
  static constexpr int NWP = QKV ? 2 : (RB ? 4 : 2);
  static constexpr int NXP = (QKV && RB) ? 3 : 2;
  static constexpr int VE = QKV ? (RB ? 11 : 9) : (RB ? 10 : 6), VO = QKV ? (RB ? 9 : 8) : VE;
  f32x4 acc[NM][NN];
  u32x4 srdx, srdw;
  unsigned xv[3], wv[4];
  unsigned xd;
  unsigned wd0, wd1;
  unsigned xa0, wa0, wa1, wa2;
  unsigned gmask;
  __device__ __forceinline__ void tick(int) {}

  unsigned sow, sox, gof;
  unsigned pdx, psx, pdw, pdw1, psw;
  template <bool ODD>
  __device__ __forceinline__ void prep(bool wreal, bool xreal, unsigned wwr, unsigned xwr) {
    pdx = xwr + xd; psx = xreal ? sox : OOB;
    if (!ODD) { sox += 128; if (sox == 1280) sox = 0; }
    pdw = wwr + wd0; psw = wreal ? sow : OOB;
    if (!QKV && RB) pdw1 = wwr + wd1;
    sow += 32 * WROW; if (sow == 640 * WROW) sow = 0;
    if (!QKV && RB) asm volatile("" : "+s"(pdw1));
    asm volatile("" : "+s"(pdx), "+s"(psx), "+s"(pdw), "+s"(psw), "+s"(sow), "+s"(sox));
  }
  template <bool ODD>
  __device__ __forceinline__ void issue() {
    if (ODD) {
      dma16(pdx, 0, xv[0], srdx, psx);
      if (NXP == 3) dma16(pdx, 1024, xv[1], srdx, psx);
    } else {
      dma16(pdx, (NXP - 1) * 1024, xv[NXP - 1], srdx, psx);
    }
    dma16(pdw, 0, wv[0], srdw, psw);
    dma16(pdw, 1024, wv[1], srdw, psw);
    if (!QKV && RB) {
      dma16(pdw, 2048, wv[2], srdw, psw);
      dma16(pdw1, 0, wv[3], srdw, psw);
    }
  }
  template <bool ODD>
  __device__ __forceinline__ void loads(bool wreal, bool xreal, unsigned wwr, unsigned xwr) { prep<ODD>(wreal, xreal, wwr, xwr); issue<ODD>(); }
  __device__ __forceinline__ void prologue(int rot) {
    sow = rot * (32 * WROW); sox = rot * 64; gof = rot * 256;
    if (QKV) {
      loads<true>(true, true, 0, 0); loads<false>(true, true, WSL, 0); loads<true>(true, true, 2 * WSL, XSL);
      loads<false>(true, true, 3 * WSL, XSL); loads<true>(true, true, 4 * WSL, 2 * XSL);
    } else {
      dma16(xd, 0, xv[0], srdx, sox);
      loads<false>(true, true, 0, 0); loads<true>(true, true, WSL, XSL); loads<false>(true, true, 2 * WSL, XSL);
      loads<true>(true, true, 3 * WSL, 2 * XSL);
    }
  }
  static constexpr int VPRO = QKV ? (RB ? 13 : 11) : (RB ? 15 : 9);
  struct Ad { unsigned x, w[4]; };
  __device__ __forceinline__ void addr(Ad& a, unsigned xb, unsigned wrd) {
    a.x = xb; a.w[0] = wa0 + wrd;
    if (QKV) {
      const unsigned s2 = (gmask & (gof - wrd)) + wrd;
      gof += 256; if (gof == 5120) gof = 0;
      a.w[1] = wa1 + s2; a.w[2] = wa2 + s2;
      asm volatile("" : "+v"(a.x), "+v"(a.w[0]), "+v"(a.w[1]), "+v"(a.w[2]));
    } else if (RB) {
      a.w[1] = a.w[0] ^ 32;
      asm volatile("" : "+v"(a.x), "+v"(a.w[0]), "+v"(a.w[1]));
    } else {
      a.w[1] = a.w[0] ^ 32; a.w[2] = a.w[0] ^ 64; a.w[3] = a.w[0] ^ 96;
      asm volatile("" : "+v"(a.x), "+v"(a.w[0]), "+v"(a.w[1]), "+v"(a.w[2]), "+v"(a.w[3]));
    }
  }
  __device__ __forceinline__ unsigned wad(const Ad& a, int n, int hi) {
    if (QKV) return n == 2 ? a.w[1 + hi] : a.w[0] + (n == 0 ? 0 : n == 1 ? 160 : 320) + hi * 1920;
    if (RB) return a.w[n & 1] + (n >> 1) * 3072 + hi * 512;
    return a.w[n & 3] + (n >> 2) * 3072 + hi * 512;
  }
  __device__ __forceinline__ void rd_all(u32x4 (&xf)[NM], u32x4 (&wf)[NN], const Ad& a) {
#pragma unroll
    for (int n = 0; n < NN; ++n) wf[n] = cat(ldstr(wad(a, n, 0)), ldstr(wad(a, n, 1)));
#pragma unroll
    for (int m = 0; m < NM; ++m) xf[m] = lds128(a.x + m * 2048);
  }
  template <bool RD = true, class L>
  __device__ __forceinline__ void mm_rd(const u32x4 (&xc)[NM], const u32x4 (&wc)[NN], u32x4 (&xn)[NM], u32x4 (&wn)[NN], const Ad& a, L&& late) {
    u32x2 lo[NN];
    constexpr bool NMAJ = !QKV && !RB;
    int r = 0;
#pragma unroll
    for (int i = 0; i < NM * NN; ++i) {
      const int m = NMAJ ? i % NM : i / NN, n = NMAJ ? i / NM : i % NN;
      if (QKV && m == 0 && n == 0) continue;
      if (QKV && n == 3) mac(acc[m][3], xc[m], wc[3]);
      else               mac(acc[m][n], wc[n], xc[m]);
      SB();
      if (!RD) continue;
      if (NMAJ) {
        if (i < 4) xn[i] = lds128(a.x + i * 2048);
        else if ((i & 3) < 2) {
          const int k = (i >> 2) - 1;
          if (i & 1) wn[k] = cat(lo[k], ldstr(wad(a, k, 1))); else lo[k] = ldstr(wad(a, k, 0));
        }
      } else {
        if (r < 2 * NN) {
          const int k = r >> 1;
          if (r & 1) wn[k] = cat(lo[k], ldstr(wad(a, k, 1))); else lo[k] = ldstr(wad(a, k, 0));
        } else if (r < 2 * NN + NM) {
          xn[r - 2 * NN] = lds128(a.x + (r - 2 * NN) * 2048);
        }
        else late(r - 2 * NN - NM);
        ++r;
      }
      SB();
    }
    if (NMAJ && RD) { wn[NN - 1] = cat(ldstr(wad(a, NN - 1, 0)), ldstr(wad(a, NN - 1, 1))); SB(); }
  }
  __device__ __forceinline__ void pin(u32x4 (&xf)[NM], u32x4 (&wf)[NN]) {
    asm volatile("" : "+v"(xf[0]), "+v"(xf[1]), "+v"(xf[2]), "+v"(xf[3]), "+v"(wf[0]), "+v"(wf[1]), "+v"(wf[2]), "+v"(wf[3]));
    if (NM == 5) asm volatile("" : "+v"(xf[NM - 1]));
    if (NN == 8) asm volatile("" : "+v"(wf[NN - 4]), "+v"(wf[NN - 3]), "+v"(wf[NN - 2]), "+v"(wf[NN - 1]));
  }
  template <int TL1, int TL2, class F, class G, class H>
  __device__ __forceinline__ void run(F&& pre, G&& extra, H&& tail) {
#pragma unroll
    for (int m = 0; m < NM; ++m)
#pragma unroll
      for (int n = 0; n < NN; ++n) { acc[m][n] = f32x4{0.f, 0.f, 0.f, 0.f}; asm volatile("" : "+v"(acc[m][n])); }
    pre();
    u32x4 xA[NM], wA[NN], xB[NM], wB[NN];
    Ad a;
    addr(a, xa0, 0);
    rd_all(xA, wA, a);
    if (!RB) __builtin_amdgcn_s_setprio(1);
    unsigned w0 = 0, w1 = WSL, w2 = 2 * WSL;
    unsigned xp = 2 * XSL, x0 = 0, x1 = XSL;
    int t = 0;
    auto adv = [&]() {
      w0 = w2; w1 = w2 + WSL; if (w1 == NWS * WSL) w1 = 0;
      w2 = w1 + WSL; if (w2 == NWS * WSL) w2 = 0;
      xp = x0; x0 = x1; x1 += XSL; if (x1 == 3 * XSL) x1 = 0;
      t += 2;
      asm volatile("" : "+s"(w0), "+s"(w1), "+s"(w2), "+s"(xp), "+s"(x0), "+s"(x1), "+s"(t));
    };
    asm volatile("" : "+s"(w0), "+s"(w1), "+s"(w2), "+s"(xp), "+s"(x0), "+s"(x1), "+s"(t));
    asm volatile("" : "+s"(gof));
    auto none = [](int) {};
    addr(a, (xa0 ^ 64) + x0, w1);
    if (RB) prep<false>(true, true, w0, xp);
#pragma unroll 1
    do {
      pin(xA, wA);
      SB(); WAITVL(VE); tick(2 * t); BAR(); tick(2 * t + 1); SB();
      if (RB) {
        issue<false>(); SB();
        mm_rd(xA, wA, xB, wB, a, [&](int i) {
          if (i == 0) addr(a, xa0 + x1, w2);
          if (i == 1) prep<true>(!QKV || t < 14, t < 14, w1, x0);
        });
      } else {
        mm_rd(xA, wA, xB, wB, a, none);
        loads<false>(true, true, w0, xp); addr(a, xa0 + x1, w2); SB();
      }
      pin(xB, wB);
      SB(); WAITVL(VO); tick(2 * t + 2); BAR(); tick(2 * t + 3); SB();
      if (RB) {
        issue<true>(); extra(t + 1); SB();
        mm_rd(xB, wB, xA, wA, a, [&](int i) {
          if (i == 0) adv();
          if (i == 1) addr(a, (xa0 ^ 64) + x0, w1);
          if (i == 2) prep<false>(true, true, w0, xp);
        });
      } else {
        mm_rd(xB, wB, xA, wA, a, none);
        loads<true>(!QKV || t < 14, t < 14, w1, x0); adv(); addr(a, (xa0 ^ 64) + x0, w1); SB();
      }
      asm volatile("" : "+s"(gof));
    } while (t < 16);
    constexpr unsigned S18 = (18 % NWS) * WSL, S19 = (19 % NWS) * WSL, X9 = 0;
    constexpr int OO = NWP + NXP - 1, OE = NWP + 1;
    pin(xA, wA);
    SB(); WAITVL(VE); tick(32); BAR(); tick(33); SB();
    if (RB) { mm_rd(xA, wA, xB, wB, a, [&](int i) { if (i == 0) addr(a, xa0 + X9, S18); if (i == 1) tail(0); }); }
    else    { mm_rd(xA, wA, xB, wB, a, none); tail(0); addr(a, xa0 + X9, S18); SB(); }
    pin(xB, wB);
    SB(); WAITVL(QKV ? OE - 1 + OO : OO); tick(34); BAR(); tick(35); SB();
    if (RB) { tail(1); SB(); mm_rd(xB, wB, xA, wA, a, [&](int i) { if (i == 0) addr(a, (xa0 ^ 64) + X9, S19); }); }
    else    { mm_rd(xB, wB, xA, wA, a, none); tail(1); addr(a, (xa0 ^ 64) + X9, S19); SB(); }
    pin(xA, wA);
    SB(); WAITVL((QKV ? OO : 0) + TL1); tick(36); BAR(); tick(37); SB();
    if (RB) { tail(2); SB(); mm_rd(xA, wA, xB, wB, a, none); }
    else    { mm_rd(xA, wA, xB, wB, a, none); tail(2); SB(); }
    pin(xB, wB);
    SB(); WAITVL(TL1 + TL2); tick(38); BAR(); tick(39); SB();
    if (RB) { tail(3); SB(); mm_rd<false>(xB, wB, xA, wA, a, none); }
    else    { mm_rd<false>(xB, wB, xA, wA, a, none); tail(3); SB(); }
    if (!RB) __builtin_amdgcn_s_setprio(0);
    MFMA_DONE();
    tick(40);
  }
};

__global__ __launch_bounds__(512, 2) void fused(const void* __restrict__ x, const void* __restrict__ wqkv,
                                                const void* __restrict__ wo, const void* __restrict__ wg,
                                                const void* __restrict__ wu, const void* __restrict__ wd,
                                                const void* __restrict__ gw, void* out, void* ws) {
  __shared__ __attribute__((aligned(16))) char smem[LDS_BYTES];
  asm volatile("" :: "s"((unsigned)(size_t)(LAS char*)smem));
  asm volatile("" :: "s"(x), "s"(wqkv), "s"(wo), "s"(wg), "s"(wu), "s"(wd), "s"(gw), "s"(out), "s"(ws));
  const int tid = threadIdx.x, lane = tid & 63;
  const int w = rfl(tid >> 6);
  const int h = lane >> 4, c = lane & 15, q4 = (lane & 15) >> 2, p4 = lane & 3;
  const int b = blockIdx.x, xcd = b & 7, idx = b >> 3;
  const int rh = xcd & 1, cq = xcd >> 1;
  const int a = 8 * rh + (idx & 7);
  const int gc = 2 * cq + ((idx >> 3) & 1);
  const int rb = 4 * xcd + (idx >> 3), ct = idx & 7;
  const __amdgpu_buffer_rsrc_t rws = __builtin_amdgcn_make_buffer_rsrc(ws, 0, WS_END, 0x00020000);
  const u32x4 sws = mk_srd(ws, WS_END);
  STAMP(0);

  u32x2 ev;
  u64 tag = 0;
  asm volatile("global_load_dwordx2 %0, %1, %2 sc1" : "=v"(ev) : "v"(WS_EPOCH), "s"(ws) : "memory");
  auto make_tag = [&](auto NV) {
    unsigned lo, hi;
    asm volatile("s_waitcnt vmcnt(%4)\n\tv_readfirstlane_b32 %0, %2\n\tv_readfirstlane_b32 %1, %3"
                 : "=s"(lo), "=s"(hi) : "v"(ev.x), "v"(ev.y), "i"(NV.value) : "memory");
    const u64 e = ((u64)hi << 32) | lo;
    tag = (e ^ 0x9E3779B97F4A7C15ull) * 0xBF58476D1CE4E5B9ull + 0x632BE59BD9B4E019ull;
    if (tag == e) tag += 1;
  };
  const unsigned pwb = WS_PW + ct * (68 * 5120);
  const unsigned cell0 = w * 20480;
  const int grot = 3 * ((idx >> 3) & 1);
  auto gstep = [&](int s) { const int v = s + grot; return v >= 6 ? v - 6 : v; };
  auto cell_dma = [&](int v, int cell) {
    const unsigned so = pwb + (20 + 6 * v + gstep(2 + cell)) * 5120;
#pragma unroll
    for (int j = 0; j < 5; ++j) dma16c(v * 20480, cell * 5120 + j * 1024, lane * 16, sws, so + j * 1024);
  };
  auto ldw = [&](u32x4 (&d)[5], unsigned so) {
#pragma unroll
    for (int n = 0; n < 5; ++n) d[n] = __builtin_amdgcn_raw_buffer_load_b128(rws, lane * 16, so + n * 1024, AUX_SC1);
  };
  const unsigned pscr = PACK_OFF + (w == 7 ? 2 : (w & 3)) * 4096;
  auto pack_issue = [&]() {
    int u = 3 * b + (w & 3);
    asm volatile("" : "+s"(u));
    const int ks = u / 10, cg = u - 10 * ks;
    const unsigned ln = opaque_lane(), r8 = ln >> 3;
    const u32x4 srd = ks < 20 ? mk_srd(wo, 640 * 1280) : mk_srd(wd, 1536 * 1280);
    const unsigned base = ks < 16 ? 40 * ks : ks < 20 ? 160 * (ks - 16) + 16 : 32 * (ks - 20);
    const unsigned vr = (ks >= 16 && ks < 20) ? 20 * (r8 >> 2) + (r8 & 3) : r8;
    const unsigned vo = vr * 1280 + (ln & 7) * 16;
#pragma unroll
    for (int p = 0; p < 4; ++p) {
      const unsigned rp = ks < 16 ? 20 * (p >> 1) + 8 * (p & 1) : ks < 20 ? 40 * p : 8 * p;
      dma16(w < 7 ? pscr : PKF_OFF - p * 1024, p * 1024, vo, srd, (u < 680 && w < 7) ? (base + rp) * 1280 + cg * 128 : OOB);
    }
  };
  unsigned pra, prb, pst[4];
  auto pack_prep = [&]() {
    const int u = 3 * b + (w & 3);
    const int ks = u / 10, cg = u - 10 * ks;
    const unsigned hs = ks < 16 ? 8 : 4, ho = ks < 16 ? 512 : 2048;
    pra = pscr + (h * hs + q4) * 128 + p4 * 8; prb = pra + ho;
#pragma unroll
    for (int j = 0; j < 4; ++j) {
      const int nt = 4 * cg + j, ctt = nt / 5;
      pst[j] = ((u < 680 && w < 7) ? WS_PW + (ctt * 68 + ks) * 5120 + (nt - 5 * ctt) * 1024 : OOB) + lane * 16;
    }
    asm volatile("" : "+v"(pra), "+v"(prb), "+v"(pst[0]), "+v"(pst[1]), "+v"(pst[2]), "+v"(pst[3]));
  };
  auto side = [&](int t) {
    if ((t & 3) != 3) return;
    if (t == 3) {
      u32x4 fr[4];
#pragma unroll
      for (int j = 0; j < 4; ++j) fr[j] = cat(ldstr(pra + j * 32), ldstr(prb + j * 32));
#pragma unroll
      for (int j = 0; j < 4; ++j) __builtin_amdgcn_raw_buffer_store_b128(fr[j], rws, pst[j], 0, AUX_SC1);
    } else if (t == 7) {
      u32x2 tv; tv.x = (unsigned)tag; tv.y = (unsigned)(tag >> 32);
      __builtin_amdgcn_raw_buffer_store_b64(tv, rws, (w == 4 && opaque_lane() == 0) ? WS_PKF + b * 8 : OOB, 0, AUX_SC1);
    } else if (t == 11 && w == 7) {
      const unsigned vo = opaque_lane() * 16;
      dma16c(PKF_OFF, 0, vo, sws, WS_PKF);
      dma16c(PKF_OFF, 1024, vo, sws, WS_PKF + 1024);
    }
  };
  auto pk_check = [&]() {
    const u32x4 p0 = lds128(PKF_OFF + lane * 16), p1 = lds128(PKF_OFF + 1024 + lane * 16);
    const unsigned lo = (unsigned)tag, hi = (unsigned)(tag >> 32);
    if (!__all(p0.x == lo && p0.z == lo && p0.y == hi && p0.w == hi && p1.x == lo && p1.z == lo && p1.y == hi && p1.w == hi)) {
      GAS u64* fp = (GAS u64*)((char*)ws + WS_PKF + opaque_lane() * 8);
#pragma unroll 1
      for (unsigned spin = 0; spin < (1u << 18); ++spin) {
        bool ok = true;
#pragma unroll
        for (int j = 0; j < 4; ++j) ok &= __hip_atomic_load(fp + 64 * j, RLX) == tag;
        if (__all(ok)) break;
        __builtin_amdgcn_s_sleep(2);
      }
    }
    asm volatile("" ::: "memory");
  };
  const int rot = ROT == 4 ? (0xEA40 >> (4 * ((a + (idx >> 3)) & 3))) & 15 : ROT == 2 ? 10 * ((a + (idx >> 3)) & 1) : 0;

  auto role = [&](auto RBT) {
    constexpr bool RB = decltype(RBT)::value;
    u32x4 wo_[3][5], wg_[2][5];
    auto tail = [&](int i) {
      if (i == 0) pk_check();
      if (RB) {
        if (i == 1) ldw(wo_[0], pwb + 2 * w * 5120);
        if (i == 2) { ldw(wo_[1], pwb + (2 * w + 1) * 5120); }
        if (i == 3) { ldw(wg_[0], pwb + (20 + 6 * w + gstep(0)) * 5120); ldw(wg_[1], pwb + (20 + 6 * w + gstep(1)) * 5120); }
      } else {
        if (i == 2) ldw(wo_[0], pwb + 2 * w * 5120);
        if (i == 3) ldw(wo_[1], pwb + (2 * w + 1) * 5120);
      }
    };
    if (idx < 16) {
      const int g = gc, hd = w & 3;
      constexpr int par = RB ? 1 : 0;
      using K = P1<true, RB>;
      K k;
      k.srdx = mk_srd(x, 2048 * 1280);
      k.srdw = mk_srd(wqkv, 640 * 3840);
      unsigned gv0, gv1;
      asm volatile("buffer_load_ushort %0, %2, %3, 0 offen\n\tbuffer_load_ushort %1, %2, %3, 0 offen offset:1024"
                   : "=&v"(gv0), "=&v"(gv1) : "v"(tid * 2), "s"(mk_srd(gw, 1280)) : "memory");
      const int q0 = RB ? 8 + 3 * (w - 4) : 2 * w;
#pragma unroll
      for (int i = 0; i < K::NXP; ++i) {
        const int rho = 8 * (q0 + i) + (lane >> 3);
        const int pp = rho >= 80 ? 1 : 0, u = rho - 80 * pp;
        const int tok = 128 * a - 32 + 2 * u + pp;
        const unsigned v = tok * 1280 + (((lane & 7) ^ ((rho >> 1) & 7)) << 4);
        k.xv[i] = (u >= 9 && tok >= 0) ? v : OOB;
      }
      k.xd = K::XB + q0 * 1024;
#pragma unroll
      for (int j = 0; j < 2; ++j) {
        const unsigned I = (2 * w + j) * 64 + lane;
        const unsigned kb = (I >= 248) + (I >= 488) + (I >= 736);
        const unsigned i = I - 240 * kb - 8 * ((kb + 1) >> 1);
        const unsigned r = (i * 2185u) >> 16, rem = i - r * 30;
        const unsigned seg = (rem * 6554u) >> 16, ch = rem - seg * 10;
        const unsigned v = (8 * kb + r) * 3840 + seg * 1280 + g * 160 + ch * 16;
        k.wv[j] = i < 240 ? v : OOB;
      }
      k.wd0 = w * 2048;
      k.prologue(rot);
      if (RB) { pack_issue(); pack_prep(); }
      k.xa0 = K::XB + par * 5 * 2048 + c * 128 + ((h ^ (c >> 1)) << 4);
      const unsigned kbo = h * 3840 + ((h + 1) >> 1) * 128;
      k.wa0 = kbo + q4 * 480 + (20 * hd + 4 * p4) * 2;
      const bool isgate = p4 == 3;
      k.gmask = isgate ? ~0u : 0u;
      asm volatile("" : "+v"(k.gmask));
      k.wa1 = isgate ? GATE_OFF + (8 * h + q4) * 8 : kbo + q4 * 480 + p4 * 160 + (20 * hd + 16) * 2;
      k.wa2 = k.wa1 + (isgate ? 32 : 1920);
      k.template run<RB ? 5 : 0, 5>([&]() {
        asm volatile("s_waitcnt vmcnt(%2)" : "+v"(gv0), "+v"(gv1) : "i"(K::VPRO + (RB ? 4 : 0)) : "memory");
        u32x2 v; v.x = gv0; v.y = 0;
        *(LAS u32x2*)(GATE_OFF + tid * 8) = v;
        if (!RB && tid < 128) { v.x = gv1; *(LAS u32x2*)(GATE_OFF + 4096 + tid * 8) = v; }
        make_tag(std::integral_constant<int, K::VPRO + (RB ? 4 : 0)>{});
        WAITL();
        BAR();
        STAMP(1);
      }, side, tail);
      STAMP(2);
      u32x2 ql[5], kl[5], t2[5], vl[5], tt[5];
      const unsigned scr = cell0 + 15360;
#pragma unroll
      for (int i = 0; i < 5; ++i) {
        if (i) ql[i] = pk4(k.acc[i][0]);
        kl[i] = pk4(k.acc[i][1]); t2[i] = pk4(k.acc[i][2]); vl[i] = pk4(k.acc[i][3]);
        *(LAS u32x2*)(scr + i * 512 + c * 32 + h * 8) = t2[i];
      }
      float sgate[4];
#pragma unroll
      for (int i = 0; i < 4; ++i) sgate[i] = k.acc[i + 1][2].x;
      WAITL();
#pragma unroll
      for (int i = 0; i < 5; ++i) tt[i] = ldstr(scr + i * 512 + (4 * h + q4) * 32 + p4 * 8);
      f32x4 ba, bb;
#pragma unroll
      for (int r = 0; r < 4; ++r) {
        const int kap = 4 * h + r;
        ba[r] = kap >= c + 9 ? 0.f : -1e30f;
        bb[r] = (kap <= c && kap >= c - 7) ? 0.f : -1e30f;
      }
      const unsigned m0mask = h == 0 ? 0xffffffffu : 0u;
      u32x4 kf[5];
#pragma unroll
      for (int i = 0; i < 5; ++i) {
        auto s0 = __builtin_amdgcn_permlane16_swap(t2[i].x, t2[i].x, false, false);
        auto s1 = __builtin_amdgcn_permlane16_swap(t2[i].y, t2[i].y, false, false);
        kf[i].x = kl[i].x; kf[i].y = kl[i].y; kf[i].z = s0[1]; kf[i].w = s1[1];
      }
      constexpr float SC = 0.22360679774997896f * LOG2E;
      const unsigned ovo = WS_O + (h & 1) * (2 * 20480) + (2 * g + (hd >> 1)) * 1024 + (2 * (hd & 1) + (h >> 1)) * 256 + c * 16;
      const unsigned ovt = h == 2 ? WS_O + (32 + g) * 512 + hd * 128 + c * 8 : OOB;
      const unsigned ovg = (h == 3 && hd == 0) ? WS_G + g * 8192 + c * 4 : OOB;
      u32x2 oprev;
#pragma unroll
      for (int i = 1; i < 5; ++i) {
        u32x4 qf; qf.x = ql[i].x; qf.y = ql[i].y; qf.z = t2[i].x & m0mask; qf.w = t2[i].y & m0mask;
        f32x4 bam = ba;
        if (i == 1 && a == 0) bam = f32x4{-1e30f, -1e30f, -1e30f, -1e30f};
        const f32x4 sb = mfma(kf[i], qf, bb);
        const f32x4 sa = mfma(kf[i - 1], qf, bam);
        float m = fmaxf(fmaxf(fmaxf(sb.x, sb.y), fmaxf(sb.z, sb.w)), fmaxf(fmaxf(sa.x, sa.y), fmaxf(sa.z, sa.w)));
        m = xmax(m);
        const float mc = m * SC;
        f32x4 pa, pb;
#pragma unroll
        for (int r = 0; r < 4; ++r) {
          pb[r] = __builtin_amdgcn_exp2f(sb[r] * SC - mc);
          pa[r] = __builtin_amdgcn_exp2f(sa[r] * SC - mc);
        }
        float l = ((pb.x + pb.y) + (pb.z + pb.w)) + ((pa.x + pa.y) + (pa.z + pa.w));
        l = xsum(l);
        const float rl = __builtin_amdgcn_rcpf(l);
        const u32x4 pf = cat(pk4(pa * rl), pk4(pb * rl));
        const f32x4 z = {0.f, 0.f, 0.f, 0.f};
        const u32x2 oa = pk4(mfma(cat(vl[i - 1], vl[i]), pf, z));
        const f32x4 ob = mfma(cat(tt[i - 1], tt[i]), pf, z);
        const unsigned mt = 8 * a + 2 * (i - 1) + par;
        __builtin_amdgcn_raw_buffer_store_b64(pk4(ob), rws, ovt, mt * 20480, AUX_SC1);
        __builtin_amdgcn_raw_buffer_store_b32(f2u(sgate[i - 1]), rws, ovg, mt * 64, AUX_SC1);
        if (i & 1) {
          oprev = oa;
        } else {
          auto sx = __builtin_amdgcn_permlane16_swap(oprev.x, oa.x, false, false);
          auto sy = __builtin_amdgcn_permlane16_swap(oprev.y, oa.y, false, false);
          u32x4 o; o.x = sx[0]; o.y = sy[0]; o.z = sx[1]; o.w = sy[1];
          __builtin_amdgcn_raw_buffer_store_b128(o, rws, ovo, (mt - 2) * 20480, AUX_SC1);
          STPAD();
        }
      }
    } else {
      const int cj = gc;
      using K = P1<false, RB>;
      K k;
      const int mh = RB ? (w < 6 ? 1 : 0) : (w >> 1);
      const int up = (w >> 1) & 1, kp = w & 1;
      k.srdx = mk_srd(x, 2048 * 1280);
      k.srdw = mk_srd(up ? wu : wg, 640 * 3072);
#pragma unroll
      for (int i = 0; i < 2; ++i) {
        const int rho = 8 * (2 * w + i) + (lane >> 3);
        const int j = rho >> 4;
        const int tok = 128 * a + 32 * (j >> 1) + 2 * (rho & 15) + (j & 1);
        k.xv[i] = tok * 1280 + (((lane & 7) ^ ((rho >> 1) & 7)) << 4);
      }
      k.xd = K::XB + 2 * w * 1024;
      {
        const int r = lane >> 3, p8 = lane & 7;
        const int s = ((r >> 1) & 1) + 2 * kp;
        k.wv[0] = (8 * kp + r) * 3072 + cj * 384 + (((((p8 >> 1) ^ s) << 1) | (p8 & 1)) << 4) + (RB ? 0 : 16 * 3072 + 128);
        k.wv[1] = k.wv[0] + 128; k.wv[2] = k.wv[0] + 256; k.wv[3] = k.wv[0] + 16 * 3072;
      }
      k.wd0 = RB ? (kp * 6 + 3 * up) * 1024 : ((kp + 2) * 6 + 3 * up + 1) * 1024;
      k.wd1 = ((kp + 2) * 6 + 3 * up) * 1024;
      k.prologue(rot);
      if (RB) { pack_issue(); pack_prep(); }
      k.xa0 = K::XB + mh * 4 * 2048 + c * 128 + ((h ^ (c >> 1)) << 4);
      {
        const int s = ((q4 >> 1) & 1) + 2 * (h & 1);
        k.wa0 = (h * 6 + (RB ? 2 : (w & 1))) * 1024 + q4 * 128 + (((RB ? 2 * (w & 1) : 0) ^ s) << 5) + p4 * 8;
      }
      k.template run<RB ? 5 : 0, 5>([&]() {
        make_tag(std::integral_constant<int, K::VPRO + (RB ? 4 : 0)>{});
        BAR();
        STAMP(1);
      }, side, tail);
      STAMP(2);
      constexpr int NK = RB ? 1 : 2;
      const unsigned gso = WS_GU + ((8 * a + 4 * mh) * 48 + 6 * cj + (RB ? 4 + (w & 1) : 2 * (w & 1))) * 1024;
#pragma unroll
      for (int m = 0; m < 4; ++m) {
#pragma unroll
        for (int kk = 0; kk < NK; ++kk) {
          float v[8];
#pragma unroll
          for (int j = 0; j < 8; ++j) {
            const float gt = k.acc[m][2 * kk + (j >> 2)][j & 3], uu = k.acc[m][2 * NK + 2 * kk + (j >> 2)][j & 3];
            const float e = __builtin_amdgcn_exp2f(gt * -LOG2E);
            v[j] = gt * uu * __builtin_amdgcn_rcpf(1.f + e);
          }
          u32x4 o; o.x = pk2(v[0], v[1]); o.y = pk2(v[2], v[3]); o.z = pk2(v[4], v[5]); o.w = pk2(v[6], v[7]);
          __builtin_amdgcn_raw_buffer_store_b128(o, rws, lane * 16, gso + m * (48 * 1024) + kk * 1024, AUX_SC1);
          STPAD();
        }
      }
    }
    SB();
    asm volatile("" : "+v"(wo_[0][0]), "+v"(wo_[0][1]), "+v"(wo_[0][2]), "+v"(wo_[0][3]), "+v"(wo_[0][4]),
                      "+v"(wo_[1][0]), "+v"(wo_[1][1]), "+v"(wo_[1][2]), "+v"(wo_[1][3]), "+v"(wo_[1][4]));
    if (RB) asm volatile("" : "+v"(wg_[0][0]), "+v"(wg_[0][1]), "+v"(wg_[0][2]), "+v"(wg_[0][3]), "+v"(wg_[0][4]),
                              "+v"(wg_[1][0]), "+v"(wg_[1][1]), "+v"(wg_[1][2]), "+v"(wg_[1][3]), "+v"(wg_[1][4]));
    SB();
    if (RB) { cell_dma(w, 0); cell_dma(w - 4, 0); cell_dma(w, 1); cell_dma(w - 4, 1); WAITV(20); }
    else WAITV(0);
    STAMP(3);
    WAITL();
    BAR();
    if (!RB && w == 0 && lane < 2) {
      GAS u64* fp = (GAS u64*)((char*)ws + (lane ? WS_DONE + b * 8 : WS_FLAG + (a * 16 + (idx < 16 ? 8 : 0) + gc) * 8));
      __hip_atomic_store(fp, tag, RLX);
    }
    STAMP(4);

    SB();
    const unsigned fo = WS_FLAG + ((rb >> 1) * 16 + c) * 8;
    auto wait_flags = [&](u32x2 v, bool need) {
#pragma unroll 1
      for (unsigned spin = 0; spin < (1u << 18); ++spin) {
        if (__all(!need || (v.x == (unsigned)tag && v.y == (unsigned)(tag >> 32)))) break;
        __builtin_amdgcn_s_sleep(1);
        ld64(v, fo, sws, 0);
        asm volatile("s_waitcnt vmcnt(0)" : "+v"(v) :: "memory");
      }
      asm volatile("" ::: "memory");
    };
    u32x2 fv;
    ld64(fv, fo, sws, 0);
    if (RB) { cell_dma(w, 2); cell_dma(w - 4, 2); }
    else {
#pragma unroll
      for (int n = 0; n < 5; ++n) ld128(wo_[2][n], lane * 16, sws, pwb + (16 + w) * 5120 + n * 1024);
    }
    asm volatile("s_waitcnt vmcnt(%1)" : "+v"(fv) : "i"(RB ? 10 : 5) : "memory");
    wait_flags(fv, c == 8 + w || (!RB && (c >> 1) == 4 + w));
    STAMP(5);
    f32x4 acc[4][5];
    float sg[4];
    {
      u32x4 af[2][4];
      u32x2 tl[4], th[4];
      const unsigned ob = WS_O + 4 * rb * 20480;
#pragma unroll
      for (int s = 0; s < 2; ++s)
#pragma unroll
        for (int m = 0; m < 4; ++m) ld128(af[s][m], lane * 16, sws, ob + m * 20480 + (2 * w + s) * 1024);
      if (!RB) {
#pragma unroll
        for (int m = 0; m < 4; ++m) {
          ld64(tl[m], lane * 8, sws, ob + m * 20480 + (16 + w) * 1024);
          ld64(th[m], lane * 8, sws, ob + m * 20480 + (16 + w) * 1024 + 512);
        }
      }
#pragma unroll
      for (int m = 0; m < 4; ++m) ld32(sg[m], c * 4, sws, WS_G + w * 8192 + (4 * rb + m) * 64);
      if (RB) { cell_dma(w, 3); cell_dma(w - 4, 3); }
      else {
#pragma unroll
        for (int j = 0; j < 10; ++j) ld128(wg_[j / 5][j % 5], lane * 16, sws, pwb + (20 + 6 * w + gstep(j / 5)) * 5120 + (j % 5) * 1024);
      }
      ld64(fv, fo, sws, 0);
      constexpr int NB = (RB ? 0 : 8) + 4 + 10 + 1;
      asm volatile("s_waitcnt vmcnt(%4)" : "+v"(af[0][0]), "+v"(af[0][1]), "+v"(af[0][2]), "+v"(af[0][3]) : "i"(NB + 4) : "memory");
#pragma unroll
      for (int m = 0; m < 4; ++m)
#pragma unroll
        for (int n = 0; n < 5; ++n) mul0(acc[m][n], wo_[0][n], af[0][m]);
      asm volatile("s_waitcnt vmcnt(%4)" : "+v"(af[1][0]), "+v"(af[1][1]), "+v"(af[1][2]), "+v"(af[1][3]) : "i"(NB) : "memory");
#pragma unroll
      for (int m = 0; m < 4; ++m)
#pragma unroll
        for (int n = 0; n < 5; ++n) mac(acc[m][n], wo_[1][n], af[1][m]);
      if (!RB) {
        asm volatile("s_waitcnt vmcnt(15)" : "+v"(tl[0]), "+v"(tl[1]), "+v"(tl[2]), "+v"(tl[3]), "+v"(th[0]), "+v"(th[1]), "+v"(th[2]), "+v"(th[3]),
                     "+v"(wo_[2][0]), "+v"(wo_[2][1]), "+v"(wo_[2][2]), "+v"(wo_[2][3]), "+v"(wo_[2][4]) :: "memory");
        u32x4 at[4];
#pragma unroll
        for (int m = 0; m < 4; ++m) at[m] = cat(tl[m], th[m]);
        asm volatile("s_nop 1" : "+v"(at[0]), "+v"(at[1]), "+v"(at[2]), "+v"(at[3]));
#pragma unroll
        for (int m = 0; m < 4; ++m)
#pragma unroll
          for (int n = 0; n < 5; ++n) mac(acc[m][n], wo_[2][n], at[m]);
      }
      asm volatile("s_waitcnt vmcnt(11)" : "+v"(sg[0]), "+v"(sg[1]), "+v"(sg[2]), "+v"(sg[3]) :: "memory");
    }
    MFMA_DONE();
#pragma unroll
    for (int m = 0; m < 4; ++m) {
      sg[m] = __builtin_amdgcn_exp2f(__builtin_amdgcn_fmed3f(sg[m], -60.f, 60.f) * LOG2E);
#pragma unroll
      for (int n = 0; n < 5; ++n) acc[m][n] *= sg[m];
      asm volatile("" : "+v"(acc[m][0]), "+v"(acc[m][1]), "+v"(acc[m][2]), "+v"(acc[m][3]), "+v"(acc[m][4]));
    }
    if (RB) asm volatile("s_waitcnt vmcnt(0)" : "+v"(fv) :: "memory");
    else    asm volatile("s_waitcnt vmcnt(0)" : "+v"(fv), "+v"(wg_[0][0]), "+v"(wg_[0][1]), "+v"(wg_[0][2]), "+v"(wg_[0][3]), "+v"(wg_[0][4]),
                         "+v"(wg_[1][0]), "+v"(wg_[1][1]), "+v"(wg_[1][2]), "+v"(wg_[1][3]), "+v"(wg_[1][4]) :: "memory");
    BAR();
    STAMP(6);
    wait_flags(fv, c == w);
    STAMP(7);
    {
      u32x4 af[6][4];
      const unsigned gb = WS_GU + (4 * rb * 48 + 6 * w) * 1024;
#pragma unroll
      for (int s = 0; s < 6; ++s) {
        const unsigned so = gb + gstep(s) * 1024;
#pragma unroll
        for (int m = 0; m < 4; ++m) ld128(af[s][m], lane * 16, sws, so + m * (48 * 1024));
      }
      u32x4 wc[5];
#pragma unroll
      for (int s = 0; s < 6; ++s) {
        if (s >= 1 && s < 5) {
#pragma unroll
          for (int n = 0; n < 5; ++n) wc[n] = lds128(cell0 + (s - 1) * 5120 + n * 1024 + lane * 16);
        }
        SB();
#pragma unroll
        for (int m = 0; m < 4; ++m) {
          asm volatile("s_waitcnt vmcnt(%1)" : "+v"(af[s][m]) : "i"(23 - 4 * s - m) : "memory");
#pragma unroll
          for (int n = 0; n < 5; ++n) mac(acc[m][n], wg_[s & 1][n], af[s][m]);
        }
        SB();
        if (s >= 1 && s < 5) {
#pragma unroll
          for (int n = 0; n < 5; ++n) wg_[(s + 1) & 1][n] = wc[n];
        }
      }
    }
    MFMA_DONE();
    STAMP(8);
#pragma unroll
    for (int m = 0; m < 4; ++m) {
      const float f = __builtin_amdgcn_rcpf(1.f + sg[m]);
#pragma unroll
      for (int n = 0; n < 5; ++n) *(LAS f32x4*)(cell0 + (m * 5 + n) * 1024 + lane * 16) = acc[m][n] * f;
    }
  };
  if (w < 4) role(std::false_type{}); else role(std::true_type{});

  if (b == 0 && w == 4) {
    GAS u64* dp = (GAS u64*)((char*)ws + WS_DONE + lane * 8);
#pragma unroll 1
    for (unsigned spin = 0; spin < (1u << 18); ++spin) {
      bool ok = true;
#pragma unroll
      for (int j = 0; j < 4; ++j) ok &= __hip_atomic_load(dp + j * 64, RLX) == tag;
      if (__all(ok)) break;
      __builtin_amdgcn_s_sleep(2);
    }
    if (lane == 0) __hip_atomic_store((GAS u64*)((char*)ws + WS_EPOCH), tag, RLX);
  }
  WAITL();
  BAR();
  STAMP(9);
  const int t0 = w < 4 ? 3 * w : 12 + 2 * (w - 4);
#pragma unroll
  for (int j = 0; j < 3; ++j) {
    if (j < 2 || w < 4) {
      const int tl = t0 + j;
      f32x4 s = *(const LAS f32x4*)(tl * 1024 + lane * 16);
#pragma unroll
      for (int v = 1; v < 8; ++v) s += *(const LAS f32x4*)(v * 20480 + tl * 1024 + lane * 16);
      const int m = tl / 5, n = tl - 5 * m;
      const int tok = 64 * rb + 32 * (m >> 1) + 2 * c + (m & 1);
      *(GAS u32x2*)((char*)out + tok * 1280 + ct * 160 + n * 32 + h * 8) = pk4(s);
    }
  }
  STAMP(10);
}
}

extern "C" void kernel_launch(void* const* d_in, const int* in_sizes, int n_in, void* d_out, int out_size, void* d_ws,
                              size_t ws_size, hipStream_t stream) {
  fused<<<256, 512, 0, stream>>>(d_in[0], d_in[1], d_in[2], d_in[4], d_in[5], d_in[6], d_in[7], d_out, d_ws);
}
```

```cpp
#include <hip/hip_runtime.h>
#include <hip/hip_bf16.h>
#include <stdint.h>
#include <type_traits>

#define STAMPS 0
#define BEARLY 0
#define ROT 0

#define LAS __attribute__((address_space(3)))
#define GAS __attribute__((address_space(1)))
typedef __bf16 bf16x8 __attribute__((ext_vector_type(8)));
typedef __bf16 bf16x2 __attribute__((ext_vector_type(2)));
typedef float f32x4 __attribute__((ext_vector_type(4)));
typedef float f32x2 __attribute__((ext_vector_type(2)));
typedef unsigned u32x4 __attribute__((ext_vector_type(4)));
typedef unsigned u32x2 __attribute__((ext_vector_type(2)));
typedef short s16x4 __attribute__((ext_vector_type(4)));
typedef unsigned long long u64;

namespace {
constexpr int LDS_BYTES = 163840;
constexpr unsigned GATE_OFF = 143360, PACK_OFF = 148480, PKF_OFF = PACK_OFF + 4096 * 3;
constexpr unsigned OOB = 0x40000000u;
constexpr unsigned WS_FLAG = 0;
constexpr unsigned WS_DONE = 4096;
constexpr unsigned WS_PKF = 8192;
constexpr unsigned WS_EPOCH = 12288;
constexpr unsigned WS_G = 16384;
constexpr unsigned WS_O = 131072;
constexpr unsigned WS_GU = WS_O + 128 * 40 * 512;
constexpr unsigned WS_PW = WS_GU + 128 * 48 * 1024;
constexpr unsigned WS_END = WS_PW + 8 * 68 * 5120;
constexpr unsigned WS_STAMP = 16u << 20;
constexpr int AUX_SC1 = 16;
constexpr float LOG2E = 1.4426950408889634f;

__device__ __forceinline__ u32x4 mk_srd(const void* p, unsigned bytes) {
  const u64 a = (u64)p;
  u32x4 r; r.x = (unsigned)a; r.y = (unsigned)(a >> 32) & 0xffffu; r.z = bytes; r.w = 0x00020000u; return r;
}
__device__ __forceinline__ void dma16(unsigned lds, unsigned imm, unsigned voff, u32x4 srd, unsigned soff) {
  asm volatile("s_add_u32 m0, %0, %4\n\ts_nop 0\n\tbuffer_load_dwordx4 %1, %2, %3 offen lds"
               :: "s"(lds), "v"(voff), "s"(srd), "s"(soff), "i"(imm) : "memory", "scc");
}
__device__ __forceinline__ void dma16c(unsigned lds, unsigned imm, unsigned voff, u32x4 srd, unsigned soff) {
  asm volatile("s_add_u32 m0, %0, %4\n\ts_nop 0\n\tbuffer_load_dwordx4 %1, %2, %3 offen sc1 lds"
               :: "s"(lds), "v"(voff), "s"(srd), "s"(soff), "i"(imm) : "memory", "scc");
}
__device__ __forceinline__ void ld128(u32x4& d, unsigned voff, u32x4 srd, unsigned soff) {
  asm volatile("buffer_load_dwordx4 %0, %1, %2, %3 offen sc1" : "=v"(d) : "v"(voff), "s"(srd), "s"(soff) : "memory");
}
__device__ __forceinline__ void ld64(u32x2& d, unsigned voff, u32x4 srd, unsigned soff) {
  asm volatile("buffer_load_dwordx2 %0, %1, %2, %3 offen sc1" : "=v"(d) : "v"(voff), "s"(srd), "s"(soff) : "memory");
}
__device__ __forceinline__ void ld32(float& d, unsigned voff, u32x4 srd, unsigned soff) {
  asm volatile("buffer_load_dword %0, %1, %2, %3 offen sc1" : "=v"(d) : "v"(voff), "s"(srd), "s"(soff) : "memory");
}
__device__ __forceinline__ u32x4 lds128(unsigned a) { return *(const LAS u32x4*)a; }
__device__ __forceinline__ u32x2 ldstr(unsigned a) {
  return __builtin_bit_cast(u32x2, __builtin_amdgcn_ds_read_tr16_b64_v4i16((LAS s16x4*)a));
}
__device__ __forceinline__ u32x4 cat(u32x2 a, u32x2 b) { u32x4 r; r.x = a.x; r.y = a.y; r.z = b.x; r.w = b.y; return r; }
__device__ __forceinline__ f32x4 mfma(u32x4 a, u32x4 b, f32x4 c) {
  return __builtin_amdgcn_mfma_f32_16x16x32_bf16(__builtin_bit_cast(bf16x8, a), __builtin_bit_cast(bf16x8, b), c, 0, 0, 0);
}
__device__ __forceinline__ void mac(f32x4& c, u32x4 a, u32x4 b) {
  asm volatile("v_mfma_f32_16x16x32_bf16 %0, %1, %2, %0" : "+v"(c) : "v"(a), "v"(b));
}
__device__ __forceinline__ void mul0(f32x4& c, u32x4 a, u32x4 b) {
  asm volatile("v_mfma_f32_16x16x32_bf16 %0, %1, %2, 0" : "=&v"(c) : "v"(a), "v"(b));
}
__device__ __forceinline__ unsigned pk2(float a, float b) {
  f32x2 t = {a, b};
  return __builtin_bit_cast(unsigned, __builtin_convertvector(t, bf16x2));
}
__device__ __forceinline__ u32x2 pk4(f32x4 v) { u32x2 r; r.x = pk2(v.x, v.y); r.y = pk2(v.z, v.w); return r; }
__device__ __forceinline__ float u2f(unsigned u) { return __builtin_bit_cast(float, u); }
__device__ __forceinline__ unsigned f2u(float f) { return __builtin_bit_cast(unsigned, f); }
#define SB() __builtin_amdgcn_sched_barrier(0)
#define WAITV(n) asm volatile("s_waitcnt vmcnt(%0)" :: "i"(n) : "memory")
#define WAITVL(n) asm volatile("s_waitcnt vmcnt(%0) lgkmcnt(0)" :: "i"(n) : "memory")
#define WAITL() asm volatile("s_waitcnt lgkmcnt(0)" ::: "memory")
#define BAR() asm volatile("s_barrier" ::: "memory")
#define STPAD() do { SB(); asm volatile("s_nop 1" ::: "memory"); SB(); } while (0)
#define MFMA_DONE() do { SB(); asm volatile("s_nop 7\n\ts_nop 1" ::: "memory"); SB(); } while (0)
#define RLX __ATOMIC_RELAXED, __HIP_MEMORY_SCOPE_AGENT
__device__ __forceinline__ unsigned rfl(unsigned v) { return __builtin_amdgcn_readfirstlane(v); }
__device__ __forceinline__ unsigned opaque_lane() { unsigned l = threadIdx.x & 63; asm volatile("" : "+v"(l)); return l; }

#define AMP0 -1
#define AMP1 -1
#define AMPSEL 0
#define STAMP(i) do {} while (0)

__device__ __forceinline__ float xmax(float v) {
  auto r = __builtin_amdgcn_permlane16_swap(f2u(v), f2u(v), false, false);
  v = fmaxf(u2f(r[0]), u2f(r[1]));
  auto s = __builtin_amdgcn_permlane32_swap(f2u(v), f2u(v), false, false);
  return fmaxf(u2f(s[0]), u2f(s[1]));
}
__device__ __forceinline__ float xsum(float v) {
  auto r = __builtin_amdgcn_permlane16_swap(f2u(v), f2u(v), false, false);
  v = u2f(r[0]) + u2f(r[1]);
  auto s = __builtin_amdgcn_permlane32_swap(f2u(v), f2u(v), false, false);
  return u2f(s[0]) + u2f(s[1]);
}

template <bool QKV, bool RB>
struct P1 {
  static constexpr int NM = QKV ? 5 : 4;
  static constexpr int NN = QKV ? 4 : (RB ? 4 : 8);
  static constexpr int NWS = QKV ? 5 : 4;
  static constexpr unsigned WSL = QKV ? 16384 : 24576, XB = NWS * WSL, XSL = QKV ? 20480 : 16384;
  static constexpr unsigned WROW = QKV ? 3840 : 3072;
  static constexpr int NWP = QKV ? 2 : (RB ? 4 : 2);
  static constexpr int NXO = QKV ? 1 : (RB ? 2 : 0), NXE = QKV ? (RB ? 1 : 2) : NXO;
  static constexpr int NXP = NXO + NXE;
  static constexpr int OO = NWP + NXO, OE = NWP + NXE;
  static constexpr int VE = QKV ? 2 * OO + OE : OO + OE, VO = QKV ? NWP + OO + OE : OO + OE;
  f32x4 acc[NM][NN];
  u32x4 srdx, srdw;
  unsigned xv[4], wv[4];
  unsigned xd;
  unsigned wd0, wd1;
  unsigned xa0, wa0, wa1, wa2;
  unsigned gmask;
  __device__ __forceinline__ void tick(int) {}

  unsigned sow, sox, gof;
  unsigned pdx, psx, pdw, pdw1, psw;
  template <bool ODD>
  __device__ __forceinline__ void prep(bool wreal, bool xreal, unsigned wwr, unsigned xwr) {
    if (NXP) {
      pdx = xwr + xd; psx = xreal ? sox : OOB;
      if (!ODD) { sox += 128; if (sox == 1280) sox = 0; }
      asm volatile("" : "+s"(pdx), "+s"(psx), "+s"(sox));
    }
    pdw = wwr + wd0; psw = wreal ? sow : OOB;
    if (!QKV && RB) pdw1 = wwr + wd1;
    sow += 32 * WROW; if (sow == 640 * WROW) sow = 0;
    if (!QKV && RB) asm volatile("" : "+s"(pdw1));
    asm volatile("" : "+s"(pdw), "+s"(psw), "+s"(sow));
  }
  template <bool ODD>
  __device__ __forceinline__ void issue() {
#pragma unroll
    for (int i = ODD ? 0 : NXO; i < (ODD ? NXO : NXP); ++i) dma16(pdx, i * 1024, xv[i], srdx, psx);
    dma16(pdw, 0, wv[0], srdw, psw);
    dma16(pdw, 1024, wv[1], srdw, psw);
    if (!QKV && RB) {
      dma16(pdw, 2048, wv[2], srdw, psw);
      dma16(pdw1, 0, wv[3], srdw, psw);
    }
  }
  template <bool ODD>
  __device__ __forceinline__ void loads(bool wreal, bool xreal, unsigned wwr, unsigned xwr) { prep<ODD>(wreal, xreal, wwr, xwr); issue<ODD>(); }
  __device__ __forceinline__ void prologue(int rot) {
    sow = rot * (32 * WROW); sox = rot * 64; gof = rot * 256;
    if (QKV) {
      loads<true>(true, true, 0, 0); loads<false>(true, true, WSL, 0); loads<true>(true, true, 2 * WSL, XSL);
      loads<false>(true, true, 3 * WSL, XSL); loads<true>(true, true, 4 * WSL, 2 * XSL);
    } else {
#pragma unroll
      for (int i = 0; i < NXO; ++i) dma16(xd, i * 1024, xv[i], srdx, sox);
      loads<false>(true, true, 0, 0); loads<true>(true, true, WSL, XSL); loads<false>(true, true, 2 * WSL, XSL);
      loads<true>(true, true, 3 * WSL, 2 * XSL);
    }
  }
  static constexpr int VPRO = QKV ? NWP + 2 * OO + OE : 2 * OO + OE;
  struct Ad { unsigned x, w[4]; };
  __device__ __forceinline__ void addr(Ad& a, unsigned xb, unsigned wrd) {
    a.x = xb; a.w[0] = wa0 + wrd;
    if (QKV) {
      const unsigned s2 = (gmask & (gof - wrd)) + wrd;
      gof += 256; if (gof == 5120) gof = 0;
      a.w[1] = wa1 + s2; a.w[2] = wa2 + s2;
      asm volatile("" : "+v"(a.x), "+v"(a.w[0]), "+v"(a.w[1]), "+v"(a.w[2]));
    } else if (RB) {
      a.w[1] = a.w[0] ^ 32;
      asm volatile("" : "+v"(a.x), "+v"(a.w[0]), "+v"(a.w[1]));
    } else {
      a.w[1] = a.w[0] ^ 32; a.w[2] = a.w[0] ^ 64; a.w[3] = a.w[0] ^ 96;
      asm volatile("" : "+v"(a.x), "+v"(a.w[0]), "+v"(a.w[1]), "+v"(a.w[2]), "+v"(a.w[3]));
    }
  }
  __device__ __forceinline__ unsigned wad(const Ad& a, int n, int hi) {
    if (QKV) return n == 2 ? a.w[1 + hi] : a.w[0] + (n == 0 ? 0 : n == 1 ? 160 : 320) + hi * 1920;
    if (RB) return a.w[n & 1] + (n >> 1) * 3072 + hi * 512;
    return a.w[n & 3] + (n >> 2) * 3072 + hi * 512;
  }
  __device__ __forceinline__ void rd_all(u32x4 (&xf)[NM], u32x4 (&wf)[NN], const Ad& a) {
#pragma unroll
    for (int n = 0; n < NN; ++n) wf[n] = cat(ldstr(wad(a, n, 0)), ldstr(wad(a, n, 1)));
#pragma unroll
    for (int m = 0; m < NM; ++m) xf[m] = lds128(a.x + m * 2048);
  }
  template <bool RD = true, class L>
  __device__ __forceinline__ void mm_rd(const u32x4 (&xc)[NM], const u32x4 (&wc)[NN], u32x4 (&xn)[NM], u32x4 (&wn)[NN], const Ad& a, L&& late) {
    u32x2 lo[NN];
    constexpr bool NMAJ = !QKV && !RB;
    constexpr bool EARLY = !QKV && RB && BEARLY;
    if (EARLY && RD) { rd_all(xn, wn, a); SB(); }
    int r = 0;
#pragma unroll
    for (int i = 0; i < NM * NN; ++i) {
      const int m = NMAJ ? i % NM : i / NN, n = NMAJ ? i / NM : i % NN;
      if (QKV && m == 0 && n == 0) continue;
      if (QKV && n == 3) mac(acc[m][3], xc[m], wc[3]);
      else               mac(acc[m][n], wc[n], xc[m]);
      SB();
      if (!RD || EARLY) continue;
      if (NMAJ) {
        if (i < 4) xn[i] = lds128(a.x + i * 2048);
        else if ((i & 3) < 2) {
          const int k = (i >> 2) - 1;
          if (i & 1) wn[k] = cat(lo[k], ldstr(wad(a, k, 1))); else lo[k] = ldstr(wad(a, k, 0));
        }
      } else {
        if (r < 2 * NN) {
          const int k = r >> 1;
          if (r & 1) wn[k] = cat(lo[k], ldstr(wad(a, k, 1))); else lo[k] = ldstr(wad(a, k, 0));
        } else if (r < 2 * NN + NM) {
          xn[r - 2 * NN] = lds128(a.x + (r - 2 * NN) * 2048);
        }
        else late(r - 2 * NN - NM);
        ++r;
      }
      SB();
    }
    if (NMAJ && RD) { wn[NN - 1] = cat(ldstr(wad(a, NN - 1, 0)), ldstr(wad(a, NN - 1, 1))); SB(); }
  }
  __device__ __forceinline__ void pin(u32x4 (&xf)[NM], u32x4 (&wf)[NN]) {
    asm volatile("" : "+v"(xf[0]), "+v"(xf[1]), "+v"(xf[2]), "+v"(xf[3]), "+v"(wf[0]), "+v"(wf[1]), "+v"(wf[2]), "+v"(wf[3]));
    if (NM == 5) asm volatile("" : "+v"(xf[NM - 1]));
    if (NN == 8) asm volatile("" : "+v"(wf[NN - 4]), "+v"(wf[NN - 3]), "+v"(wf[NN - 2]), "+v"(wf[NN - 1]));
  }
  template <int TL0, int TL1, int TL2, class F, class G, class H>
  __device__ __forceinline__ void run(F&& pre, G&& extra, H&& tail) {
#pragma unroll
    for (int m = 0; m < NM; ++m)
#pragma unroll
      for (int n = 0; n < NN; ++n) { acc[m][n] = f32x4{0.f, 0.f, 0.f, 0.f}; asm volatile("" : "+v"(acc[m][n])); }
    pre();
    u32x4 xA[NM], wA[NN], xB[NM], wB[NN];
    Ad a;
    addr(a, xa0, 0);
    rd_all(xA, wA, a);
    if (!RB) __builtin_amdgcn_s_setprio(1);
    unsigned w0 = 0, w1 = WSL, w2 = 2 * WSL;
    unsigned xp = 2 * XSL, x0 = 0, x1 = XSL;
    int t = 0;
    unsigned nw1, nw2, nx1;
    auto adv0 = [&]() { nw1 = w2 + WSL; if (nw1 == NWS * WSL) nw1 = 0; asm volatile("" : "+s"(nw1)); };
    auto adv1 = [&]() { nw2 = nw1 + WSL; if (nw2 == NWS * WSL) nw2 = 0; asm volatile("" : "+s"(nw2)); };
    auto adv2 = [&]() { nx1 = x1 + XSL; if (nx1 == 3 * XSL) nx1 = 0; asm volatile("" : "+s"(nx1)); };
    auto adv3 = [&]() {
      w0 = w2; w1 = nw1; w2 = nw2; xp = x0; x0 = x1; x1 = nx1; t += 2;
      asm volatile("" : "+s"(w0), "+s"(w1), "+s"(w2), "+s"(xp), "+s"(x0), "+s"(x1), "+s"(t));
    };
    auto adv = [&]() { adv0(); adv1(); adv2(); adv3(); };
    asm volatile("" : "+s"(w0), "+s"(w1), "+s"(w2), "+s"(xp), "+s"(x0), "+s"(x1), "+s"(t));
    asm volatile("" : "+s"(gof));
    auto none = [](int) {};
    if (RB) {
      pin(xA, wA);
      SB(); WAITVL(VE); tick(0); BAR(); tick(1); SB();
      loads<false>(true, true, w0, xp); extra(0); addr(a, (xa0 ^ 64) + x0, w1); SB();
      auto half = [&](auto LAST) {
        mm_rd(xA, wA, xB, wB, a, [&](int i) {
          if (QKV && i == 0) adv0();
          if (QKV && i == 2) adv1();
          if (QKV && i == 4) adv2();
        });
        pin(xB, wB);
        SB(); WAITVL(VO); tick(2 * t + 2); BAR(); tick(2 * t + 3); SB();
        loads<true>(!QKV || !LAST.value, !LAST.value, w1, x0);
        if (!LAST.value) extra(t + 1);
        addr(a, xa0 + x1, w2);
        if (QKV) adv3(); else adv();
        SB();
        mm_rd(xB, wB, xA, wA, a, none);
      };
#pragma unroll 1
      do {
        half(std::false_type{});
        pin(xA, wA);
        SB(); WAITVL(VE); tick(2 * t); BAR(); tick(2 * t + 1); SB();
        loads<false>(true, true, w0, xp); extra(t); addr(a, (xa0 ^ 64) + x0, w1); SB();
        asm volatile("" : "+s"(gof));
      } while (t < 14);
      half(std::true_type{});
    } else {
      addr(a, (xa0 ^ 64) + x0, w1);
#pragma unroll 1
      do {
        pin(xA, wA);
        SB(); WAITVL(VE); tick(2 * t); BAR(); tick(2 * t + 1); SB();
        mm_rd(xA, wA, xB, wB, a, none);
        loads<false>(true, true, w0, xp); addr(a, xa0 + x1, w2); SB();
        pin(xB, wB);
        SB(); WAITVL(VO); tick(2 * t + 2); BAR(); tick(2 * t + 3); SB();
        mm_rd(xB, wB, xA, wA, a, none);
        loads<true>(!QKV || t < 14, t < 14, w1, x0); adv(); addr(a, (xa0 ^ 64) + x0, w1); SB();
        asm volatile("" : "+s"(gof));
      } while (t < 16);
    }
    constexpr unsigned S17 = (17 % NWS) * WSL, S18 = (18 % NWS) * WSL, S19 = (19 % NWS) * WSL, X8 = 2 * XSL, X9 = 0;
    pin(xA, wA);
    SB(); WAITVL(VE); tick(32); BAR(); tick(33); SB();
    if (RB) { tail(0); addr(a, (xa0 ^ 64) + X8, S17); SB(); mm_rd(xA, wA, xB, wB, a, none); }
    else    { mm_rd(xA, wA, xB, wB, a, none); tail(0); addr(a, xa0 + X9, S18); SB(); }
    pin(xB, wB);
    SB(); WAITVL((QKV ? NWP + OO : OO) + TL0); tick(34); BAR(); tick(35); SB();
    if (RB) { tail(1); addr(a, xa0 + X9, S18); SB(); mm_rd(xB, wB, xA, wA, a, none); }
    else    { mm_rd(xB, wB, xA, wA, a, none); tail(1); addr(a, (xa0 ^ 64) + X9, S19); SB(); }
    pin(xA, wA);
    SB(); WAITVL((QKV ? OO : 0) + TL0 + TL1); tick(36); BAR(); tick(37); SB();
    if (RB) { tail(2); addr(a, (xa0 ^ 64) + X9, S19); SB(); mm_rd(xA, wA, xB, wB, a, none); }
    else    { mm_rd(xA, wA, xB, wB, a, none); tail(2); SB(); }
    pin(xB, wB);
    SB(); WAITVL(TL0 + TL1 + TL2); tick(38); BAR(); tick(39); SB();
    if (RB) { tail(3); SB(); mm_rd<false>(xB, wB, xA, wA, a, none); }
    else    { mm_rd<false>(xB, wB, xA, wA, a, none); tail(3); SB(); }
    if (!RB) __builtin_amdgcn_s_setprio(0);
    MFMA_DONE();
    tick(40);
  }
};

__global__ __launch_bounds__(512, 2) void fused(const void* __restrict__ x, const void* __restrict__ wqkv,
                                                const void* __restrict__ wo, const void* __restrict__ wg,
                                                const void* __restrict__ wu, const void* __restrict__ wd,
                                                const void* __restrict__ gw, void* out, void* ws) {
  __shared__ __attribute__((aligned(16))) char smem[LDS_BYTES];
  asm volatile("" :: "s"((unsigned)(size_t)(LAS char*)smem));
  asm volatile("" :: "s"(x), "s"(wqkv), "s"(wo), "s"(wg), "s"(wu), "s"(wd), "s"(gw), "s"(out), "s"(ws));
  const int tid = threadIdx.x, lane = tid & 63;
  const int w = rfl(tid >> 6);
  const int h = lane >> 4, c = lane & 15, q4 = (lane & 15) >> 2, p4 = lane & 3;
  const int b = blockIdx.x, xcd = b & 7, idx = b >> 3;
  const int rh = xcd & 1, cq = xcd >> 1;
  const int a = 8 * rh + (idx & 7);
  const int gc = 2 * cq + ((idx >> 3) & 1);
  const int rb = 4 * xcd + (idx >> 3), ct = idx & 7;
  const __amdgpu_buffer_rsrc_t rws = __builtin_amdgcn_make_buffer_rsrc(ws, 0, WS_END, 0x00020000);
  const u32x4 sws = mk_srd(ws, WS_END);
  u64 amp_t0 = 0, amp_t1 = 0;
  STAMP(0);

  u32x2 ev;
  u64 tag = 0;
  asm volatile("global_load_dwordx2 %0, %1, %2 sc1" : "=v"(ev) : "v"(WS_EPOCH), "s"(ws) : "memory");
  auto make_tag = [&](auto NV) {
    unsigned lo, hi;
    asm volatile("s_waitcnt vmcnt(%4)\n\tv_readfirstlane_b32 %0, %2\n\tv_readfirstlane_b32 %1, %3"
                 : "=s"(lo), "=s"(hi) : "v"(ev.x), "v"(ev.y), "i"(NV.value) : "memory");
    const u64 e = ((u64)hi << 32) | lo;
    tag = (e ^ 0x9E3779B97F4A7C15ull) * 0xBF58476D1CE4E5B9ull + 0x632BE59BD9B4E019ull;
    if (tag == e) tag += 1;
  };
  const unsigned pwb = WS_PW + ct * (68 * 5120);
  const unsigned cell0 = w * 20480;
  const int grot = 3 * ((idx >> 3) & 1);
  auto gstep = [&](int s) { const int v = s + grot; return v >= 6 ? v - 6 : v; };
  auto cell_dma = [&](int v, int cell) {
    const unsigned so = pwb + (20 + 6 * v + gstep(2 + cell)) * 5120;
#pragma unroll
    for (int j = 0; j < 5; ++j) dma16c(v * 20480, cell * 5120 + j * 1024, lane * 16, sws, so + j * 1024);
  };
  auto ldw = [&](u32x4 (&d)[5], unsigned so) {
#pragma unroll
    for (int n = 0; n < 5; ++n) d[n] = __builtin_amdgcn_raw_buffer_load_b128(rws, lane * 16, so + n * 1024, AUX_SC1);
  };
  const unsigned pscr = PACK_OFF + (w == 7 ? 2 : (w & 3)) * 4096;
  auto pack_issue = [&]() {
    int u = 3 * b + (w & 3);
    asm volatile("" : "+s"(u));
    const int ks = u / 10, cg = u - 10 * ks;
    const unsigned ln = opaque_lane(), r8 = ln >> 3;
    const u32x4 srd = ks < 20 ? mk_srd(wo, 640 * 1280) : mk_srd(wd, 1536 * 1280);
    const unsigned base = ks < 16 ? 40 * ks : ks < 20 ? 160 * (ks - 16) + 16 : 32 * (ks - 20);
    const unsigned vr = (ks >= 16 && ks < 20) ? 20 * (r8 >> 2) + (r8 & 3) : r8;
    const unsigned vo = vr * 1280 + (ln & 7) * 16;
#pragma unroll
    for (int p = 0; p < 4; ++p) {
      const unsigned rp = ks < 16 ? 20 * (p >> 1) + 8 * (p & 1) : ks < 20 ? 40 * p : 8 * p;
      dma16(w < 7 ? pscr : PKF_OFF - p * 1024, p * 1024, vo, srd, (u < 680 && w < 7) ? (base + rp) * 1280 + cg * 128 : OOB);
    }
  };
  unsigned pra, pho, pso;
  int pks;
  const unsigned smask = w == 7 ? 0x88fu : 0x8fu;
  auto pack_prep = [&]() {
    const int u = 3 * b + (w & 3);
    pks = u / 10;
    pso = (u < 680 && w < 7) ? 4 * (u - 10 * pks) : 4000;
    pho = pks < 16 ? 512 : 2048;
    pra = pscr + (h * (pks < 16 ? 8 : 4) + q4) * 128 + p4 * 8;
    asm volatile("" : "+v"(pra)); asm volatile("" : "+s"(pho), "+s"(pso), "+s"(pks));
  };
  auto side = [&](int t) {
    if (__builtin_expect(!((smask >> t) & 1), 1)) return;
    if (t < 4) {
      const u32x4 fr = cat(ldstr(pra + t * 32), ldstr(pra + t * 32 + pho));
      const unsigned nt = pso + t, ctt = (nt * 205) >> 10;
      __builtin_amdgcn_raw_buffer_store_b128(fr, rws, lane * 16, nt < 40 ? WS_PW + (ctt * 63 + nt + pks * 5) * 1024 + ctt * (68 * 5120 - 68 * 1024) : OOB, AUX_SC1);
      STPAD();
    } else if (t == 7) {
      u32x2 tv; tv.x = (unsigned)tag; tv.y = (unsigned)(tag >> 32);
      __builtin_amdgcn_raw_buffer_store_b64(tv, rws, (w == 4 && opaque_lane() == 0) ? WS_PKF + b * 8 : OOB, 0, AUX_SC1);
    } else {
      const unsigned vo = opaque_lane() * 16;
      dma16c(PKF_OFF, 0, vo, sws, WS_PKF);
      dma16c(PKF_OFF, 1024, vo, sws, WS_PKF + 1024);
    }
  };
  auto pk_check = [&]() {
    const u32x4 p0 = lds128(PKF_OFF + lane * 16), p1 = lds128(PKF_OFF + 1024 + lane * 16);
    const unsigned lo = (unsigned)tag, hi = (unsigned)(tag >> 32);
    if (!__all(p0.x == lo && p0.z == lo && p0.y == hi && p0.w == hi && p1.x == lo && p1.z == lo && p1.y == hi && p1.w == hi)) {
      GAS u64* fp = (GAS u64*)((char*)ws + WS_PKF + opaque_lane() * 8);
#pragma unroll 1
      for (unsigned spin = 0; spin < (1u << 18); ++spin) {
        bool ok = true;
#pragma unroll
        for (int j = 0; j < 4; ++j) ok &= __hip_atomic_load(fp + 64 * j, RLX) == tag;
        if (__all(ok)) break;
        __builtin_amdgcn_s_sleep(2);
      }
    }
    asm volatile("" ::: "memory");
  };
  const int rot = ROT == 4 ? (0xEA40 >> (4 * ((a + (idx >> 3)) & 3))) & 15 : ROT == 2 ? 10 * ((a + (idx >> 3)) & 1) : 0;

  auto role = [&](auto RBT) {
    constexpr bool RB = decltype(RBT)::value;
    u32x4 wo_[3][5], wg_[2][5];
    auto tail = [&](int i) {
      if (i == 0) pk_check();
      if (RB) {
        if (i < 2) ldw(wo_[i], pwb + (2 * w + i) * 5120);
        else ldw(wg_[i - 2], pwb + (20 + 6 * w + gstep(i - 2)) * 5120);
      } else if (i >= 2) {
        ldw(wo_[i - 2], pwb + (2 * w + i - 2) * 5120);
      }
    };
    if (idx < 16) {
      const int g = gc, hd = w & 3;
      constexpr int par = RB ? 1 : 0;
      using K = P1<true, RB>;
      K k;
      k.srdx = mk_srd(x, 2048 * 1280);
      k.srdw = mk_srd(wqkv, 640 * 3840);
      unsigned gv0, gv1;
      asm volatile("buffer_load_ushort %0, %2, %3, 0 offen\n\tbuffer_load_ushort %1, %2, %3, 0 offen offset:1024"
                   : "=&v"(gv0), "=&v"(gv1) : "v"(tid * 2), "s"(mk_srd(gw, 1280)) : "memory");
      const int q0 = RB ? 12 + 2 * (w - 4) : 3 * w;
#pragma unroll
      for (int i = 0; i < K::NXP; ++i) {
        const int rho = 8 * (q0 + i) + (lane >> 3);
        const int pp = rho >= 80 ? 1 : 0, u = rho - 80 * pp;
        const int tok = 128 * a - 32 + 2 * u + pp;
        const unsigned v = tok * 1280 + (((lane & 7) ^ ((rho >> 1) & 7)) << 4);
        k.xv[i] = (u >= 9 && tok >= 0) ? v : OOB;
      }
      k.xd = K::XB + q0 * 1024;
#pragma unroll
      for (int j = 0; j < 2; ++j) {
        const unsigned I = (2 * w + j) * 64 + lane;
        const unsigned kb = (I >= 248) + (I >= 488) + (I >= 736);
        const unsigned i = I - 240 * kb - 8 * ((kb + 1) >> 1);
        const unsigned r = (i * 2185u) >> 16, rem = i - r * 30;
        const unsigned seg = (rem * 6554u) >> 16, ch = rem - seg * 10;
        const unsigned v = (8 * kb + r) * 3840 + seg * 1280 + g * 160 + ch * 16;
        k.wv[j] = i < 240 ? v : OOB;
      }
      k.wd0 = w * 2048;
      if (RB) pack_issue();
      k.prologue(rot);
      if (RB) pack_prep();
      k.xa0 = K::XB + par * 5 * 2048 + c * 128 + ((h ^ (c >> 1)) << 4);
      const unsigned kbo = h * 3840 + ((h + 1) >> 1) * 128;
      k.wa0 = kbo + q4 * 480 + (20 * hd + 4 * p4) * 2;
      const bool isgate = p4 == 3;
      k.gmask = isgate ? ~0u : 0u;
      asm volatile("" : "+v"(k.gmask));
      k.wa1 = isgate ? GATE_OFF + (8 * h + q4) * 8 : kbo + q4 * 480 + p4 * 160 + (20 * hd + 16) * 2;
      k.wa2 = k.wa1 + (isgate ? 32 : 1920);
      k.template run<RB ? 5 : 0, RB ? 5 : 0, 5>([&]() {
        asm volatile("s_waitcnt vmcnt(%2)" : "+v"(gv0), "+v"(gv1) : "i"(K::VPRO) : "memory");
        u32x2 v; v.x = gv0; v.y = 0;
        *(LAS u32x2*)(GATE_OFF + tid * 8) = v;
        if (!RB && tid < 128) { v.x = gv1; *(LAS u32x2*)(GATE_OFF + 4096 + tid * 8) = v; }
        make_tag(std::integral_constant<int, K::VPRO>{});
        WAITL();
        BAR();
        STAMP(1);
      }, side, tail);
      STAMP(2);
      u32x2 ql[5], kl[5], t2[5], vl[5], tt[5];
      const unsigned scr = cell0 + 15360;
#pragma unroll
      for (int i = 0; i < 5; ++i) {
        if (i) ql[i] = pk4(k.acc[i][0]);
        kl[i] = pk4(k.acc[i][1]); t2[i] = pk4(k.acc[i][2]); vl[i] = pk4(k.acc[i][3]);
        *(LAS u32x2*)(scr + i * 512 + c * 32 + h * 8) = t2[i];
      }
      float sgate[4];
#pragma unroll
      for (int i = 0; i < 4; ++i) sgate[i] = k.acc[i + 1][2].x;
      WAITL();
#pragma unroll
      for (int i = 0; i < 5; ++i) tt[i] = ldstr(scr + i * 512 + (4 * h + q4) * 32 + p4 * 8);
      f32x4 ba, bb;
#pragma unroll
      for (int r = 0; r < 4; ++r) {
        const int kap = 4 * h + r;
        ba[r] = kap >= c + 9 ? 0.f : -1e30f;
        bb[r] = (kap <= c && kap >= c - 7) ? 0.f : -1e30f;
      }
      const unsigned m0mask = h == 0 ? 0xffffffffu : 0u;
      u32x4 kf[5];
#pragma unroll
      for (int i = 0; i < 5; ++i) {
        auto s0 = __builtin_amdgcn_permlane16_swap(t2[i].x, t2[i].x, false, false);
        auto s1 = __builtin_amdgcn_permlane16_swap(t2[i].y, t2[i].y, false, false);
        kf[i].x = kl[i].x; kf[i].y = kl[i].y; kf[i].z = s0[1]; kf[i].w = s1[1];
      }
      constexpr float SC = 0.22360679774997896f * LOG2E;
      const unsigned ovo = WS_O + (h & 1) * (2 * 20480) + (2 * g + (hd >> 1)) * 1024 + (2 * (hd & 1) + (h >> 1)) * 256 + c * 16;
      const unsigned ovt = h == 2 ? WS_O + (32 + g) * 512 + hd * 128 + c * 8 : OOB;
      const unsigned ovg = (h == 3 && hd == 0) ? WS_G + g * 8192 + c * 4 : OOB;
      u32x2 oprev;
#pragma unroll
      for (int i = 1; i < 5; ++i) {
        u32x4 qf; qf.x = ql[i].x; qf.y = ql[i].y; qf.z = t2[i].x & m0mask; qf.w = t2[i].y & m0mask;
        f32x4 bam = ba;
        if (i == 1 && a == 0) bam = f32x4{-1e30f, -1e30f, -1e30f, -1e30f};
        const f32x4 sb = mfma(kf[i], qf, bb);
        const f32x4 sa = mfma(kf[i - 1], qf, bam);
        float m = fmaxf(fmaxf(fmaxf(sb.x, sb.y), fmaxf(sb.z, sb.w)), fmaxf(fmaxf(sa.x, sa.y), fmaxf(sa.z, sa.w)));
        m = xmax(m);
        const float mc = m * SC;
        f32x4 pa, pb;
#pragma unroll
        for (int r = 0; r < 4; ++r) {
          pb[r] = __builtin_amdgcn_exp2f(sb[r] * SC - mc);
          pa[r] = __builtin_amdgcn_exp2f(sa[r] * SC - mc);
        }
        float l = ((pb.x + pb.y) + (pb.z + pb.w)) + ((pa.x + pa.y) + (pa.z + pa.w));
        l = xsum(l);
        const float rl = __builtin_amdgcn_rcpf(l);
        const u32x4 pf = cat(pk4(pa * rl), pk4(pb * rl));
        const f32x4 z = {0.f, 0.f, 0.f, 0.f};
        const u32x2 oa = pk4(mfma(cat(vl[i - 1], vl[i]), pf, z));
        const f32x4 ob = mfma(cat(tt[i - 1], tt[i]), pf, z);
        const unsigned mt = 8 * a + 2 * (i - 1) + par;
        __builtin_amdgcn_raw_buffer_store_b64(pk4(ob), rws, ovt, mt * 20480, AUX_SC1);
        __builtin_amdgcn_raw_buffer_store_b32(f2u(sgate[i - 1]), rws, ovg, mt * 64, AUX_SC1);
        if (i & 1) {
          oprev = oa;
        } else {
          auto sx = __builtin_amdgcn_permlane16_swap(oprev.x, oa.x, false, false);
          auto sy = __builtin_amdgcn_permlane16_swap(oprev.y, oa.y, false, false);
          u32x4 o; o.x = sx[0]; o.y = sy[0]; o.z = sx[1]; o.w = sy[1];
          __builtin_amdgcn_raw_buffer_store_b128(o, rws, ovo, (mt - 2) * 20480, AUX_SC1);
          STPAD();
        }
      }
    } else {
      const int cj = gc;
      using K = P1<false, RB>;
      K k;
      const int mh = RB ? (w < 6 ? 1 : 0) : (w >> 1);
      const int up = (w >> 1) & 1, kp = w & 1;
      k.srdx = mk_srd(x, 2048 * 1280);
      k.srdw = mk_srd(up ? wu : wg, 640 * 3072);
#pragma unroll
      for (int i = 0; i < K::NXP; ++i) {
        const int rho = 8 * (4 * (w & 3) + i) + (lane >> 3);
        const int j = rho >> 4;
        const int tok = 128 * a + 32 * (j >> 1) + 2 * (rho & 15) + (j & 1);
        k.xv[i] = tok * 1280 + (((lane & 7) ^ ((rho >> 1) & 7)) << 4);
      }
      k.xd = K::XB + 4 * (w & 3) * 1024;
      {
        const int r = lane >> 3, p8 = lane & 7;
        const int s = ((r >> 1) & 1) + 2 * kp;
        k.wv[0] = (8 * kp + r) * 3072 + cj * 384 + (((((p8 >> 1) ^ s) << 1) | (p8 & 1)) << 4) + (RB ? 0 : 16 * 3072 + 128);
        k.wv[1] = k.wv[0] + 128; k.wv[2] = k.wv[0] + 256; k.wv[3] = k.wv[0] + 16 * 3072;
      }
      k.wd0 = RB ? (kp * 6 + 3 * up) * 1024 : ((kp + 2) * 6 + 3 * up + 1) * 1024;
      k.wd1 = ((kp + 2) * 6 + 3 * up) * 1024;
      if (RB) pack_issue();
      k.prologue(rot);
      if (RB) pack_prep();
      k.xa0 = K::XB + mh * 4 * 2048 + c * 128 + ((h ^ (c >> 1)) << 4);
      {
        const int s = ((q4 >> 1) & 1) + 2 * (h & 1);
        k.wa0 = (h * 6 + (RB ? 2 : (w & 1))) * 1024 + q4 * 128 + (((RB ? 2 * (w & 1) : 0) ^ s) << 5) + p4 * 8;
      }
      k.template run<RB ? 5 : 0, RB ? 5 : 0, 5>([&]() {
        make_tag(std::integral_constant<int, K::VPRO>{});
        BAR();
        STAMP(1);
      }, side, tail);
      STAMP(2);
      constexpr int NK = RB ? 1 : 2;
      const unsigned gso = WS_GU + ((8 * a + 4 * mh) * 48 + 6 * cj + (RB ? 4 + (w & 1) : 2 * (w & 1))) * 1024;
#pragma unroll
      for (int m = 0; m < 4; ++m) {
#pragma unroll
        for (int kk = 0; kk < NK; ++kk) {
          float v[8];
#pragma unroll
          for (int j = 0; j < 8; ++j) {
            const float gt = k.acc[m][2 * kk + (j >> 2)][j & 3], uu = k.acc[m][2 * NK + 2 * kk + (j >> 2)][j & 3];
            const float e = __builtin_amdgcn_exp2f(gt * -LOG2E);
            v[j] = gt * uu * __builtin_amdgcn_rcpf(1.f + e);
          }
          u32x4 o; o.x = pk2(v[0], v[1]); o.y = pk2(v[2], v[3]); o.z = pk2(v[4], v[5]); o.w = pk2(v[6], v[7]);
          __builtin_amdgcn_raw_buffer_store_b128(o, rws, lane * 16, gso + m * (48 * 1024) + kk * 1024, AUX_SC1);
          STPAD();
        }
      }
    }
    SB();
    asm volatile("" : "+v"(wo_[0][0]), "+v"(wo_[0][1]), "+v"(wo_[0][2]), "+v"(wo_[0][3]), "+v"(wo_[0][4]),
                      "+v"(wo_[1][0]), "+v"(wo_[1][1]), "+v"(wo_[1][2]), "+v"(wo_[1][3]), "+v"(wo_[1][4]));
    if (RB) asm volatile("" : "+v"(wg_[0][0]), "+v"(wg_[0][1]), "+v"(wg_[0][2]), "+v"(wg_[0][3]), "+v"(wg_[0][4]),
                              "+v"(wg_[1][0]), "+v"(wg_[1][1]), "+v"(wg_[1][2]), "+v"(wg_[1][3]), "+v"(wg_[1][4]));
    SB();
    if (RB) { cell_dma(w, 0); cell_dma(w - 4, 0); WAITV(10); }
    else WAITV(0);
    STAMP(3);
    WAITL();
    BAR();
    if (!RB && w == 0 && lane < 2) {
      GAS u64* fp = (GAS u64*)((char*)ws + (lane ? WS_DONE + b * 8 : WS_FLAG + (a * 16 + (idx < 16 ? 8 : 0) + gc) * 8));
      __hip_atomic_store(fp, tag, RLX);
    }
    STAMP(4);

    SB();
    const unsigned fo = WS_FLAG + ((rb >> 1) * 16 + c) * 8;
    auto wait_flags = [&](u32x2 v, bool need) {
#pragma unroll 1
      for (unsigned spin = 0; spin < (1u << 18); ++spin) {
        if (__all(!need || (v.x == (unsigned)tag && v.y == (unsigned)(tag >> 32)))) break;
        __builtin_amdgcn_s_sleep(1);
        ld64(v, fo, sws, 0);
        asm volatile("s_waitcnt vmcnt(0)" : "+v"(v) :: "memory");
      }
      asm volatile("" ::: "memory");
    };
    u32x2 fv;
    ld64(fv, fo, sws, 0);
    if (RB) { cell_dma(w, 1); cell_dma(w - 4, 1); }
    else {
#pragma unroll
      for (int n = 0; n < 5; ++n) ld128(wo_[2][n], lane * 16, sws, pwb + (16 + w) * 5120 + n * 1024);
    }
    asm volatile("s_waitcnt vmcnt(%1)" : "+v"(fv) : "i"(RB ? 10 : 5) : "memory");
    wait_flags(fv, c == 8 + w || (!RB && (c >> 1) == 4 + w));
    STAMP(5);
    f32x4 acc[4][5];
    float sg[4];
    {
      u32x4 af[2][4];
      u32x2 tl[4], th[4];
      const unsigned ob = WS_O + 4 * rb * 20480;
#pragma unroll
      for (int s = 0; s < 2; ++s)
#pragma unroll
        for (int m = 0; m < 4; ++m) ld128(af[s][m], lane * 16, sws, ob + m * 20480 + (2 * w + s) * 1024);
      if (!RB) {
#pragma unroll
        for (int m = 0; m < 4; ++m) {
          ld64(tl[m], lane * 8, sws, ob + m * 20480 + (16 + w) * 1024);
          ld64(th[m], lane * 8, sws, ob + m * 20480 + (16 + w) * 1024 + 512);
        }
      }
#pragma unroll
      for (int m = 0; m < 4; ++m) ld32(sg[m], c * 4, sws, WS_G + w * 8192 + (4 * rb + m) * 64);
      if (RB) { cell_dma(w, 2); cell_dma(w - 4, 2); }
      else {
#pragma unroll
        for (int j = 0; j < 10; ++j) ld128(wg_[j / 5][j % 5], lane * 16, sws, pwb + (20 + 6 * w + gstep(j / 5)) * 5120 + (j % 5) * 1024);
      }
      ld64(fv, fo, sws, 0);
      constexpr int NB = (RB ? 0 : 8) + 4 + 10 + 1;
      asm volatile("s_waitcnt vmcnt(%4)" : "+v"(af[0][0]), "+v"(af[0][1]), "+v"(af[0][2]), "+v"(af[0][3]) : "i"(NB + 4) : "memory");
#pragma unroll
      for (int m = 0; m < 4; ++m)
#pragma unroll
        for (int n = 0; n < 5; ++n) mul0(acc[m][n], wo_[0][n], af[0][m]);
      asm volatile("s_waitcnt vmcnt(%4)" : "+v"(af[1][0]), "+v"(af[1][1]), "+v"(af[1][2]), "+v"(af[1][3]) : "i"(NB) : "memory");
#pragma unroll
      for (int m = 0; m < 4; ++m)
#pragma unroll
        for (int n = 0; n < 5; ++n) mac(acc[m][n], wo_[1][n], af[1][m]);
      if (RB) { cell_dma(w, 3); cell_dma(w - 4, 3); }
      if (!RB) {
        asm volatile("s_waitcnt vmcnt(15)" : "+v"(tl[0]), "+v"(tl[1]), "+v"(tl[2]), "+v"(tl[3]), "+v"(th[0]), "+v"(th[1]), "+v"(th[2]), "+v"(th[3]),
                     "+v"(wo_[2][0]), "+v"(wo_[2][1]), "+v"(wo_[2][2]), "+v"(wo_[2][3]), "+v"(wo_[2][4]) :: "memory");
        u32x4 at[4];
#pragma unroll
        for (int m = 0; m < 4; ++m) at[m] = cat(tl[m], th[m]);
        asm volatile("s_nop 1" : "+v"(at[0]), "+v"(at[1]), "+v"(at[2]), "+v"(at[3]));
#pragma unroll
        for (int m = 0; m < 4; ++m)
#pragma unroll
          for (int n = 0; n < 5; ++n) mac(acc[m][n], wo_[2][n], at[m]);
      }
      asm volatile("s_waitcnt vmcnt(%4)" : "+v"(sg[0]), "+v"(sg[1]), "+v"(sg[2]), "+v"(sg[3]) : "i"(RB ? 21 : 11) : "memory");
    }
    MFMA_DONE();
#pragma unroll
    for (int m = 0; m < 4; ++m) {
      sg[m] = __builtin_amdgcn_exp2f(__builtin_amdgcn_fmed3f(sg[m], -60.f, 60.f) * LOG2E);
#pragma unroll
      for (int n = 0; n < 5; ++n) acc[m][n] *= sg[m];
      asm volatile("" : "+v"(acc[m][0]), "+v"(acc[m][1]), "+v"(acc[m][2]), "+v"(acc[m][3]), "+v"(acc[m][4]));
    }
    if (RB) asm volatile("s_waitcnt vmcnt(0)" : "+v"(fv) :: "memory");
    else    asm volatile("s_waitcnt vmcnt(0)" : "+v"(fv), "+v"(wg_[0][0]), "+v"(wg_[0][1]), "+v"(wg_[0][2]), "+v"(wg_[0][3]), "+v"(wg_[0][4]),
                         "+v"(wg_[1][0]), "+v"(wg_[1][1]), "+v"(wg_[1][2]), "+v"(wg_[1][3]), "+v"(wg_[1][4]) :: "memory");
    BAR();
    STAMP(6);
    wait_flags(fv, c == w);
    STAMP(7);
    {
      u32x4 af[6][4];
      const unsigned gb = WS_GU + (4 * rb * 48 + 6 * w) * 1024;
#pragma unroll
      for (int s = 0; s < 6; ++s) {
        const unsigned so = gb + gstep(s) * 1024;
#pragma unroll
        for (int m = 0; m < 4; ++m) ld128(af[s][m], lane * 16, sws, so + m * (48 * 1024));
      }
      u32x4 wc[5];
#pragma unroll
      for (int s = 0; s < 6; ++s) {
        if (s >= 1 && s < 5) {
#pragma unroll
          for (int n = 0; n < 5; ++n) wc[n] = lds128(cell0 + (s - 1) * 5120 + n * 1024 + lane * 16);
        }
        SB();
#pragma unroll
        for (int m = 0; m < 4; ++m) {
          asm volatile("s_waitcnt vmcnt(%1)" : "+v"(af[s][m]) : "i"(23 - 4 * s - m) : "memory");
#pragma unroll
          for (int n = 0; n < 5; ++n) mac(acc[m][n], wg_[s & 1][n], af[s][m]);
        }
        SB();
        if (s >= 1 && s < 5) {
#pragma unroll
          for (int n = 0; n < 5; ++n) wg_[(s + 1) & 1][n] = wc[n];
        }
      }
    }
    MFMA_DONE();
    STAMP(8);
#pragma unroll
    for (int m = 0; m < 4; ++m) {
      const float f = __builtin_amdgcn_rcpf(1.f + sg[m]);
#pragma unroll
      for (int n = 0; n < 5; ++n) *(LAS f32x4*)(cell0 + (m * 5 + n) * 1024 + lane * 16) = acc[m][n] * f;
    }
  };
  if (w < 4) role(std::false_type{}); else role(std::true_type{});

  if (b == 0 && w == 4) {
    GAS u64* dp = (GAS u64*)((char*)ws + WS_DONE + lane * 8);
#pragma unroll 1
    for (unsigned spin = 0; spin < (1u << 18); ++spin) {
      bool ok = true;
#pragma unroll
      for (int j = 0; j < 4; ++j) ok &= __hip_atomic_load(dp + j * 64, RLX) == tag;
      if (__all(ok)) break;
      __builtin_amdgcn_s_sleep(2);
    }
    if (lane == 0) __hip_atomic_store((GAS u64*)((char*)ws + WS_EPOCH), tag, RLX);
  }
  WAITL();
  BAR();
  STAMP(9);
  const int t0 = w < 4 ? 3 * w : 12 + 2 * (w - 4);
#pragma unroll
  for (int j = 0; j < 3; ++j) {
    if (j < 2 || w < 4) {
      const int tl = t0 + j;
      f32x4 s = *(const LAS f32x4*)(tl * 1024 + lane * 16);
#pragma unroll
      for (int v = 1; v < 8; ++v) s += *(const LAS f32x4*)(v * 20480 + tl * 1024 + lane * 16);
      const int m = tl / 5, n = tl - 5 * m;
      const int tok = 64 * rb + 32 * (m >> 1) + 2 * c + (m & 1);
      *(GAS u32x2*)((char*)out + tok * 1280 + ct * 160 + n * 32 + h * 8) = pk4(s);
    }
  }
  STAMP(10);
  if (AMP0 >= 0 && (AMPSEL == 0 || (AMPSEL == 1) == (idx < 16))) {
    const u64 d = amp_t1 - amp_t0, t = __builtin_amdgcn_s_memrealtime();
    while (__builtin_amdgcn_s_memrealtime() - t < d) __builtin_amdgcn_s_sleep(1);
  }
}
}

extern "C" void kernel_launch(void* const* d_in, const int* in_sizes, int n_in, void* d_out, int out_size, void* d_ws,
                              size_t ws_size, hipStream_t stream) {
  fused<<<256, 512, 0, stream>>>(d_in[0], d_in[1], d_in[2], d_in[4], d_in[5], d_in[6], d_in[7], d_out, d_ws);
}
```

```cpp
#include <hip/hip_runtime.h>
#include <hip/hip_bf16.h>
#include <stdint.h>
#include <type_traits>

#define STAMPS 0
#define BEARLY 0
#define CODEPF 1
#define TOUCH_NT ""
#define TOUCH 1
#define EXP 0
#define GLA 4
#define PART16 1
#define PL 2
#define PRE 1
#define ROT 4

#define LAS __attribute__((address_space(3)))
#define GAS __attribute__((address_space(1)))
typedef __bf16 bf16x8 __attribute__((ext_vector_type(8)));
typedef __bf16 bf16x2 __attribute__((ext_vector_type(2)));
typedef float f32x4 __attribute__((ext_vector_type(4)));
typedef float f32x2 __attribute__((ext_vector_type(2)));
typedef unsigned u32x4 __attribute__((ext_vector_type(4)));
typedef unsigned u32x2 __attribute__((ext_vector_type(2)));
typedef short s16x4 __attribute__((ext_vector_type(4)));
typedef unsigned long long u64;

namespace {
constexpr int LDS_BYTES = 163840;
constexpr unsigned GATE_OFF = 110592, PACK_OFF = 122880, PKF_OFF = 162816, CELL0 = 122880;
constexpr unsigned OOB = 0x40000000u;
constexpr unsigned CODE_BYTES = 43000;
constexpr unsigned WS_FLAG = 0;
constexpr unsigned WS_DONE = 4096;
constexpr unsigned WS_PKF = 8192;
constexpr unsigned WS_EPOCH = 12288;
constexpr unsigned WS_G = 16384;
constexpr unsigned WS_O = 131072;
constexpr unsigned WS_GU = WS_O + 128 * 40 * 512;
constexpr unsigned WS_PW = WS_GU + 128 * 48 * 1024;
constexpr unsigned WS_END = WS_PW + 8 * 68 * 5120;
constexpr unsigned WS_STAMP = 16u << 20;
constexpr int AUX_SC1 = 16;
constexpr float LOG2E = 1.4426950408889634f;

__device__ __forceinline__ u32x4 mk_srd(const void* p, unsigned bytes) {
  const u64 a = (u64)p;
  u32x4 r; r.x = (unsigned)a; r.y = (unsigned)(a >> 32) & 0xffffu; r.z = bytes; r.w = 0x00020000u; return r;
}
__device__ __forceinline__ void dma16(unsigned lds, unsigned imm, unsigned voff, u32x4 srd, unsigned soff) {
  asm volatile("s_add_u32 m0, %0, %4\n\ts_nop 0\n\tbuffer_load_dwordx4 %1, %2, %3 offen lds"
               :: "s"(lds), "v"(voff), "s"(srd), "s"(soff), "i"(imm) : "memory", "scc");
}
__device__ __forceinline__ void dma16c(unsigned lds, unsigned imm, unsigned voff, u32x4 srd, unsigned soff) {
  asm volatile("s_add_u32 m0, %0, %4\n\ts_nop 0\n\tbuffer_load_dwordx4 %1, %2, %3 offen sc1 lds"
               :: "s"(lds), "v"(voff), "s"(srd), "s"(soff), "i"(imm) : "memory", "scc");
}
__device__ __forceinline__ void ld128(u32x4& d, unsigned voff, u32x4 srd, unsigned soff) {
  asm volatile("buffer_load_dwordx4 %0, %1, %2, %3 offen sc1" : "=v"(d) : "v"(voff), "s"(srd), "s"(soff) : "memory");
}
__device__ __forceinline__ void ld64(u32x2& d, unsigned voff, u32x4 srd, unsigned soff) {
  asm volatile("buffer_load_dwordx2 %0, %1, %2, %3 offen sc1" : "=v"(d) : "v"(voff), "s"(srd), "s"(soff) : "memory");
}
__device__ __forceinline__ void ld32(float& d, unsigned voff, u32x4 srd, unsigned soff) {
  asm volatile("buffer_load_dword %0, %1, %2, %3 offen sc1" : "=v"(d) : "v"(voff), "s"(srd), "s"(soff) : "memory");
}
__device__ __forceinline__ u32x4 lds128(unsigned a) { return *(const LAS u32x4*)a; }
__device__ __forceinline__ u32x2 ldstr(unsigned a) {
  return __builtin_bit_cast(u32x2, __builtin_amdgcn_ds_read_tr16_b64_v4i16((LAS s16x4*)a));
}
__device__ __forceinline__ u32x4 cat(u32x2 a, u32x2 b) { u32x4 r; r.x = a.x; r.y = a.y; r.z = b.x; r.w = b.y; return r; }
__device__ __forceinline__ f32x4 mfma(u32x4 a, u32x4 b, f32x4 c) {
  return __builtin_amdgcn_mfma_f32_16x16x32_bf16(__builtin_bit_cast(bf16x8, a), __builtin_bit_cast(bf16x8, b), c, 0, 0, 0);
}
__device__ __forceinline__ void mac(f32x4& c, u32x4 a, u32x4 b) {
  asm volatile("v_mfma_f32_16x16x32_bf16 %0, %1, %2, %0" : "+v"(c) : "v"(a), "v"(b));
}
__device__ __forceinline__ void mul0(f32x4& c, u32x4 a, u32x4 b) {
  asm volatile("v_mfma_f32_16x16x32_bf16 %0, %1, %2, 0" : "=&v"(c) : "v"(a), "v"(b));
}
__device__ __forceinline__ unsigned pk2(float a, float b) {
  f32x2 t = {a, b};
  return __builtin_bit_cast(unsigned, __builtin_convertvector(t, bf16x2));
}
__device__ __forceinline__ u32x2 pk4(f32x4 v) { u32x2 r; r.x = pk2(v.x, v.y); r.y = pk2(v.z, v.w); return r; }
__device__ __forceinline__ float u2f(unsigned u) { return __builtin_bit_cast(float, u); }
__device__ __forceinline__ unsigned f2u(float f) { return __builtin_bit_cast(unsigned, f); }
#define SB() __builtin_amdgcn_sched_barrier(0)
#define WAITV(n) asm volatile("s_waitcnt vmcnt(%0)" :: "i"(n) : "memory")
#define WAITVL(n) asm volatile("s_waitcnt vmcnt(%0) lgkmcnt(0)" :: "i"(n) : "memory")
#define WAITL() asm volatile("s_waitcnt lgkmcnt(0)" ::: "memory")
#define BAR() asm volatile("s_barrier" ::: "memory")
#define STPAD() do { SB(); asm volatile("s_nop 1" ::: "memory"); SB(); } while (0)
#define MFMA_DONE() do { SB(); asm volatile("s_nop 7\n\ts_nop 1" ::: "memory"); SB(); } while (0)
#define RLX __ATOMIC_RELAXED, __HIP_MEMORY_SCOPE_AGENT
__device__ __forceinline__ unsigned rfl(unsigned v) { return __builtin_amdgcn_readfirstlane(v); }
__device__ __forceinline__ unsigned opaque_lane() { unsigned l = threadIdx.x & 63; asm volatile("" : "+v"(l)); return l; }

#define AMP0 -1
#define AMP1 -1
#define AMPSEL 0
#define STAMP(i) do {} while (0)

template <int I, int N, class F>
__device__ __forceinline__ void sfor(F&& f) {
  if constexpr (I < N) { f(std::integral_constant<int, I>{}); sfor<I + 1, N>(f); }
}
template <bool RB> struct Sched {
  static constexpr int NO = RB ? 2 : 3, OB = RB ? 8 : 16;
  static constexpr int upto(int st) { return st < 2 ? 4 * (st + 1) : st < NO ? 16 : st < NO + 6 ? OB + 4 * (st - NO + 1) : OB + 27; }
  static constexpr int issued(int st, int i) {
    const int base = upto(st + PL - 1), goal = upto(st + PL), every = goal - base > 4 ? 2 : 5;
    int n = base;
    for (int k = 0; k < i; ++k) if (k % every == 1 && n < goal) ++n;
    return n;
  }
};
__device__ __forceinline__ float xmax(float v) {
  auto r = __builtin_amdgcn_permlane16_swap(f2u(v), f2u(v), false, false);
  v = fmaxf(u2f(r[0]), u2f(r[1]));
  auto s = __builtin_amdgcn_permlane32_swap(f2u(v), f2u(v), false, false);
  return fmaxf(u2f(s[0]), u2f(s[1]));
}
__device__ __forceinline__ float xsum(float v) {
  auto r = __builtin_amdgcn_permlane16_swap(f2u(v), f2u(v), false, false);
  v = u2f(r[0]) + u2f(r[1]);
  auto s = __builtin_amdgcn_permlane32_swap(f2u(v), f2u(v), false, false);
  return u2f(s[0]) + u2f(s[1]);
}

template <bool QKV, bool RB>
struct P1 {
  static constexpr int NM = QKV ? 5 : 4;
  static constexpr int NN = QKV ? 4 : (RB ? 4 : 8);
  static constexpr int NWS = 3;
  static constexpr unsigned WSL = QKV ? 16384 : 24576, XB = NWS * WSL, XSL = QKV ? 20480 : 16384;
  static constexpr unsigned WROW = QKV ? 3840 : 3072;
  static constexpr int NWP = QKV ? 2 : (RB ? 4 : 2);
  static constexpr int NXE = QKV ? (RB ? 1 : 2) : (RB ? 2 : 0), NXO = QKV ? 1 : NXE;
  static constexpr int NXP = NXO + NXE;
  static constexpr int OO = NWP + NXO, OE = NWP + NXE;
  static constexpr bool HAND = !QKV;
  static constexpr int ht = 4;
  f32x4 acc[NM][NN];
  u32x4 srdx, srdw;
  unsigned xv[4], wv[4];
  unsigned xd;
  unsigned wd0, wd1;
  unsigned xa0, wa0, wa1, wa2;
  unsigned gmask;
  __device__ __forceinline__ void tick(int) {}

  unsigned sow, sox, gof;
  unsigned kg, kw, kx, kr;
  template <bool ODD, bool X = true>
  __device__ __forceinline__ void loads(unsigned wwr, unsigned xwr) {
    if (X && NXP) {
#pragma unroll
      for (int i = ODD ? NXE : 0; i < (ODD ? NXP : NXE); ++i) dma16(xwr + xd, i * 1024, xv[i], srdx, sox);
      if (ODD) { kx += 1; sox = (kx ^ (kg >> 1)) * 128; }
    }
    dma16(wwr + wd0, 0, wv[0], srdw, sow);
    dma16(wwr + wd0, 1024, wv[1], srdw, sow);
    if (!QKV && RB) {
      dma16(wwr + wd0, 2048, wv[2], srdw, sow);
      dma16(wwr + wd1, 0, wv[3], srdw, sow);
    }
  }
  __device__ __forceinline__ void other(unsigned wwr) {
    dma16(wwr + wd1, 1024, wv[3], srdw, sow + 128);
    dma16(wwr + wd1, 2048, wv[3], srdw, sow + 256);
  }
  __device__ __forceinline__ void next() { kw += 1; sow = (kw ^ kg) * (32 * WROW); asm volatile("" : "+s"(sow), "+s"(kw)); if (NXP) asm volatile("" : "+s"(sox), "+s"(kx)); }
  __device__ __forceinline__ void stage0(int rot) {
    kg = rot; kw = 0; kx = 0; kr = 0; sow = kg * (32 * WROW); sox = (kg >> 1) * 128; gof = kg * 256;
    if (HAND && !RB) { kw = 3; sow = (3 ^ kg) * (32 * WROW); return; }
#pragma unroll
    for (int i = 0; i < NXE; ++i) dma16(xd, i * 1024, xv[i], srdx, sox);
    loads<true>(0, 0); if (HAND) other(0); next();
  }
  __device__ __forceinline__ void stage12() {
    if (HAND && !RB) return;
    loads<false>(WSL, XSL); if (HAND) other(WSL); next();
    loads<true>(2 * WSL, XSL); if (HAND) other(2 * WSL); next();
  }
  static constexpr int VPRO = OE + OO + (HAND ? 4 : 0);
  struct Ad { unsigned x, w[4]; };
  __device__ __forceinline__ void addr(Ad& a, unsigned xb, unsigned wrd) {
    a.x = xb; a.w[0] = wa0 + wrd;
    if (QKV) {
      const unsigned s2 = (gmask & (gof - wrd)) + wrd;
      kr += 1; gof = (kr ^ kg) * 256;
      a.w[1] = wa1 + s2; a.w[2] = wa2 + s2;
      asm volatile("" : "+v"(a.x), "+v"(a.w[0]), "+v"(a.w[1]), "+v"(a.w[2]));
    } else if (RB) {
      a.w[1] = a.w[0] ^ 32;
      asm volatile("" : "+v"(a.x), "+v"(a.w[0]), "+v"(a.w[1]));
    } else {
      a.w[1] = a.w[0] ^ 32; a.w[2] = a.w[0] ^ 64; a.w[3] = a.w[0] ^ 96;
      asm volatile("" : "+v"(a.x), "+v"(a.w[0]), "+v"(a.w[1]), "+v"(a.w[2]), "+v"(a.w[3]));
    }
  }
  __device__ __forceinline__ unsigned wad(const Ad& a, int n, int hi) {
    if (QKV) return n == 2 ? a.w[1 + hi] : a.w[0] + (n == 0 ? 0 : n == 1 ? 160 : 320) + hi * 1920;
    if (RB) return a.w[n & 1] + (n >> 1) * 3072 + hi * 512;
    return a.w[n & 3] + (n >> 2) * 3072 + hi * 512;
  }
  __device__ __forceinline__ void rd_all(u32x4 (&xf)[NM], u32x4 (&wf)[NN], const Ad& a) {
#pragma unroll
    for (int n = 0; n < NN; ++n) wf[n] = cat(ldstr(wad(a, n, 0)), ldstr(wad(a, n, 1)));
#pragma unroll
    for (int m = 0; m < NM; ++m) xf[m] = lds128(a.x + m * 2048);
  }
  template <bool RD = true, class L>
  __device__ __forceinline__ void mm_rd(const u32x4 (&xc)[NM], const u32x4 (&wc)[NN], u32x4 (&xn)[NM], u32x4 (&wn)[NN], const Ad& a, L&& late) {
    u32x2 lo[NN];
    constexpr bool NMAJ = !QKV && !RB;
    constexpr bool EARLY = !QKV && RB && BEARLY;
    if (EARLY && RD) { rd_all(xn, wn, a); SB(); }
    int r = 0;
#pragma unroll
    for (int i = 0; i < NM * NN; ++i) {
      const int m = NMAJ ? i % NM : i / NN, n = NMAJ ? i / NM : i % NN;
      if (QKV && m == 0 && n == 0) continue;
      if (QKV && n == 3) mac(acc[m][3], xc[m], wc[3]);
      else               mac(acc[m][n], wc[n], xc[m]);
      SB();
      if (!RD || EARLY) continue;
      if (NMAJ) {
        if (i < 4) xn[i] = lds128(a.x + i * 2048);
        else if ((i & 3) < 2) {
          const int k = (i >> 2) - 1;
          if (i & 1) wn[k] = cat(lo[k], ldstr(wad(a, k, 1))); else lo[k] = ldstr(wad(a, k, 0));
        }
      } else {
        if (r < 2 * NN) {
          const int k = r >> 1;
          if (r & 1) wn[k] = cat(lo[k], ldstr(wad(a, k, 1))); else lo[k] = ldstr(wad(a, k, 0));
        } else if (r < 2 * NN + NM) {
          xn[r - 2 * NN] = lds128(a.x + (r - 2 * NN) * 2048);
        }
        else late(r - 2 * NN - NM);
        ++r;
      }
      SB();
    }
    if (NMAJ && RD) { wn[NN - 1] = cat(ldstr(wad(a, NN - 1, 0)), ldstr(wad(a, NN - 1, 1))); SB(); }
  }
  __device__ __forceinline__ void pin(u32x4 (&xf)[NM], u32x4 (&wf)[NN]) {
    asm volatile("" : "+v"(xf[0]), "+v"(xf[1]), "+v"(xf[2]), "+v"(xf[3]), "+v"(wf[0]), "+v"(wf[1]), "+v"(wf[2]), "+v"(wf[3]));
    if (NM == 5) asm volatile("" : "+v"(xf[NM - 1]));
    if (NN == 8) asm volatile("" : "+v"(wf[NN - 4]), "+v"(wf[NN - 3]), "+v"(wf[NN - 2]), "+v"(wf[NN - 1]));
  }
  template <int TL0, int TL1, class F, class G, class H>
  __device__ __forceinline__ void run(F&& pre, G&& extra, H&& tail) {
#pragma unroll
    for (int m = 0; m < NM; ++m)
#pragma unroll
      for (int n = 0; n < NN; ++n) { acc[m][n] = f32x4{0.f, 0.f, 0.f, 0.f}; asm volatile("" : "+v"(acc[m][n])); }
    pre();
    u32x4 xA[NM], wA[NN], xB[NM], wB[NN];
    Ad a;
    addr(a, xa0, 0);
    rd_all(xA, wA, a);
    if (!RB) __builtin_amdgcn_s_setprio(1);
    unsigned w0 = 0, w1 = WSL, w2 = 2 * WSL;
    unsigned xp = 2 * XSL, x0 = 0, x1 = XSL;
    int t = 0;
    unsigned nw1, nw2, nx1;
    auto adv0 = [&]() { nw1 = w2 + WSL; if (nw1 == NWS * WSL) nw1 = 0; asm volatile("" : "+s"(nw1)); };
    auto adv1 = [&]() { nw2 = nw1 + WSL; if (nw2 == NWS * WSL) nw2 = 0; asm volatile("" : "+s"(nw2)); };
    auto adv2 = [&]() { nx1 = x1 + XSL; if (nx1 == 3 * XSL) nx1 = 0; asm volatile("" : "+s"(nx1)); };
    auto adv3 = [&]() {
      w0 = w2; w1 = nw1; w2 = nw2; xp = x0; x0 = x1; x1 = nx1; t += 2;
      asm volatile("" : "+s"(w0), "+s"(w1), "+s"(w2), "+s"(xp), "+s"(x0), "+s"(x1), "+s"(t));
    };
    auto adv = [&]() { adv0(); adv1(); adv2(); adv3(); };
    asm volatile("" : "+s"(w0), "+s"(w1), "+s"(w2), "+s"(xp), "+s"(x0), "+s"(x1), "+s"(t));
    if (QKV) asm volatile("" : "+s"(gof), "+s"(kr));
    auto none = [](int) {};
    if (RB) {
      auto help = [&](int tt, unsigned wwr) { if (HAND && __builtin_expect(tt < ht, 0)) other(wwr); extra(tt); };
      pin(xA, wA);
      SB(); WAITVL(OO + (HAND ? 2 : 0)); tick(0); BAR(); tick(1); SB();
      loads<false>(w0, xp); help(0, w0); next(); addr(a, (xa0 ^ 64) + x0, w1); SB();
      auto half = [&]() {
        mm_rd(xA, wA, xB, wB, a, [&](int i) {
          if (QKV && i == 0) adv0();
          if (QKV && i == 2) adv1();
          if (QKV && i == 4) adv2();
        });
        pin(xB, wB);
        SB(); WAITVL(OE); tick(2 * t + 2); BAR(); tick(2 * t + 3); SB();
        loads<true>(w1, xp); help(t + 1, w1); next();
        addr(a, xa0 + x1, w2);
        if (QKV) adv3(); else adv();
        SB();
        mm_rd(xB, wB, xA, wA, a, none);
      };
#pragma unroll 1
      do {
        half();
        pin(xA, wA);
        SB(); WAITVL(OO); tick(2 * t); BAR(); tick(2 * t + 1); SB();
        loads<false>(w0, xp); help(t, w0); next(); addr(a, (xa0 ^ 64) + x0, w1); SB();
        if (QKV) asm volatile("" : "+s"(gof), "+s"(kr));
      } while (t < 14);
      half();
    } else {
      addr(a, (xa0 ^ 64) + x0, w1);
      auto trip = [&](auto EARLY) {
        constexpr bool E = EARLY.value;
        pin(xA, wA);
        SB(); if (E) WAITL(); else WAITVL(OO);
        tick(2 * t); BAR(); tick(2 * t + 1); SB();
        mm_rd(xA, wA, xB, wB, a, none);
        if (E && t < ht) extra(t); else loads<false>(w0, xp);
        next(); addr(a, xa0 + x1, w2); adv0(); adv1(); adv2(); SB();
        pin(xB, wB);
        SB(); if (E) WAITL(); else WAITVL(OE);
        tick(2 * t + 2); BAR(); tick(2 * t + 3); SB();
        mm_rd(xB, wB, xA, wA, a, none);
        if (E && t < ht) extra(t + 1); else loads<true>(w1, xp);
        next(); adv3(); addr(a, (xa0 ^ 64) + x0, w1); SB();
        if (QKV) asm volatile("" : "+s"(gof), "+s"(kr));
      };
      if (HAND) {
#pragma unroll 1
        do trip(std::true_type{}); while (t < ht + 2);
      }
#pragma unroll 1
      do trip(std::false_type{}); while (t < 16);
    }
    constexpr unsigned S16 = (16 % NWS) * WSL, S17 = (17 % NWS) * WSL, S18 = (18 % NWS) * WSL, S19 = (19 % NWS) * WSL, X8 = 2 * XSL, X9 = 0;
    pin(xA, wA);
    SB(); WAITVL(OO);
    tick(32); BAR(); tick(33); SB();
    if (RB) { loads<false, false>(S16, 0); tail(0); addr(a, (xa0 ^ 64) + X8, S17); SB(); mm_rd(xA, wA, xB, wB, a, none); }
    else    { mm_rd(xA, wA, xB, wB, a, none); loads<false, false>(S16, 0); tail(0); addr(a, xa0 + X9, S18); SB(); }
    pin(xB, wB);
    SB(); WAITVL(NWP + TL0); tick(34); BAR(); tick(35); SB();
    if (RB) { tail(1); addr(a, xa0 + X9, S18); SB(); mm_rd(xB, wB, xA, wA, a, none); }
    else    { mm_rd(xB, wB, xA, wA, a, none); tail(1); addr(a, (xa0 ^ 64) + X9, S19); SB(); }
    pin(xA, wA);
    SB(); WAITVL(TL0 + TL1); tick(36); BAR(); tick(37); SB();
    if (RB) { tail(2); addr(a, (xa0 ^ 64) + X9, S19); SB(); mm_rd(xA, wA, xB, wB, a, none); }
    else    { mm_rd(xA, wA, xB, wB, a, none); tail(2); SB(); }
    pin(xB, wB);
    SB(); WAITL(); tick(38); BAR(); tick(39); SB();
    if (RB) { tail(3); SB(); mm_rd<false>(xB, wB, xA, wA, a, none); }
    else    { mm_rd<false>(xB, wB, xA, wA, a, none); tail(3); SB(); }
    if (!RB) __builtin_amdgcn_s_setprio(0);
    MFMA_DONE();
    tick(40);
  }
};

__global__ __launch_bounds__(512, 2) void fused(const void* __restrict__ x, const void* __restrict__ wqkv,
                                                const void* __restrict__ wo, const void* __restrict__ wg,
                                                const void* __restrict__ wu, const void* __restrict__ wd,
                                                const void* __restrict__ gw, void* out, void* ws) {
  __shared__ __attribute__((aligned(16))) char smem[LDS_BYTES];
  asm volatile("" :: "s"((unsigned)(size_t)(LAS char*)smem));
  asm volatile("" :: "s"(x), "s"(wqkv), "s"(wo), "s"(wg), "s"(wu), "s"(wd), "s"(gw), "s"(out), "s"(ws));
  const int tid = threadIdx.x, lane = tid & 63;
  const int w = rfl(tid >> 6);
  const int h = lane >> 4, c = lane & 15, q4 = (lane & 15) >> 2, p4 = lane & 3;
  const int b = blockIdx.x, xcd = b & 7, idx = b >> 3;
  const int rh = xcd & 1, cq = xcd >> 1;
  const int a = 8 * rh + (idx & 7);
  const int gc = 2 * cq + ((idx >> 3) & 1);
  const int rb = 4 * xcd + (idx >> 3), ct = idx & 7;
  const __amdgpu_buffer_rsrc_t rws = __builtin_amdgcn_make_buffer_rsrc(ws, 0, WS_END, 0x00020000);
  const u32x4 sws = mk_srd(ws, WS_END);
  u64 amp_t0 = 0, amp_t1 = 0;
  STAMP(0);

  if (CODEPF && w == 7) {
    u64 pc;
    asm volatile("s_getpc_b64 %0" : "=s"(pc));
    asm volatile("s_mov_b32 m0, %0\n\ts_nop 0\n\tbuffer_load_dword %1, %2, 0 offen lds"
                 :: "s"(PKF_OFF), "v"(lane < 48 ? ((idx & 7) * 48 + lane) * 128 : OOB), "s"(mk_srd((const void*)(pc & ~127ull), CODE_BYTES)) : "memory");
  }
  u32x2 ev;
  u64 tag = 0;
  auto make_tag = [&](auto NV) {
    unsigned lo, hi;
    asm volatile("s_waitcnt vmcnt(%4)\n\tv_readfirstlane_b32 %0, %2\n\tv_readfirstlane_b32 %1, %3"
                 : "=s"(lo), "=s"(hi) : "v"(ev.x), "v"(ev.y), "i"(NV.value) : "memory");
    const u64 e = ((u64)hi << 32) | lo;
    tag = (e ^ 0x9E3779B97F4A7C15ull) * 0xBF58476D1CE4E5B9ull + 0x632BE59BD9B4E019ull;
    if (tag == e) tag += 1;
  };
  const unsigned pwb = WS_PW + ct * (68 * 5120);
  const int grot = 3 * ((idx >> 3) & 1);
  auto gstep = [&](int s) { const int v = s + grot; return v >= 6 ? v - 6 : v; };
  auto cell_dma = [&](int v, int cell) {
    const unsigned so = pwb + (20 + 6 * v + gstep(2 + cell)) * 5120;
#pragma unroll
    for (int j = 0; j < 5; ++j) dma16c(cell ? v * 15360 : CELL0 + v * 5120, (cell ? cell - 1 : 0) * 5120 + j * 1024, lane * 16, sws, so + j * 1024);
  };
  auto ldw = [&](u32x4 (&d)[5], unsigned so) {
#pragma unroll
    for (int n = 0; n < 5; ++n) d[n] = __builtin_amdgcn_raw_buffer_load_b128(rws, lane * 16, so + n * 1024, AUX_SC1);
  };
  const unsigned pscr = PACK_OFF + (w & 3) * 8192;
  const int pu0 = (b - 128) * 4 + w;
  unsigned pra[2], prb[2], pn[2], pb[2];
  auto pack_issue = [&]() {
    const u64 po = (u64)wo, pd = (u64)wd;
#pragma unroll
    for (int r = 0; r < 2; ++r) {
      const int u = pu0 + 512 * r;
      const int ks = u / 10, cg = u - 10 * ks;
      const unsigned r8 = lane >> 3;
      const u32x4 srd = mk_srd((const void*)(ks < 20 ? po : pd), ks < 20 ? 640 * 1280 : 1536 * 1280);
      const unsigned base = ks < 16 ? 40 * ks : ks < 20 ? 160 * (ks - 16) + 16 : 32 * (ks - 20);
      const unsigned vr = (ks >= 16 && ks < 20) ? 20 * (r8 >> 2) + (r8 & 3) : r8;
      const unsigned vo = vr * 1280 + (lane & 7) * 16;
#pragma unroll
      for (int p = 0; p < 4; ++p) {
        const unsigned rp = ks < 16 ? 20 * (p >> 1) + 8 * (p & 1) : ks < 20 ? 40 * p : 8 * p;
        dma16(pscr, r * 4096 + p * 1024, vo, srd, u < 680 ? (base + rp) * 1280 + cg * 128 : OOB);
      }
      pra[r] = pscr + r * 4096 + (h * (ks < 16 ? 8 : 4) + q4) * 128 + p4 * 8;
      prb[r] = pra[r] + (ks < 16 ? 512 : 2048);
      pn[r] = 4 * cg; pb[r] = u < 680 ? WS_PW + ks * 5120 : OOB;
    }
    asm volatile("" : "+v"(pra[0]), "+v"(prb[0]), "+v"(pra[1]), "+v"(prb[1]));
    asm volatile("" : "+s"(pn[0]), "+s"(pb[0]), "+s"(pn[1]), "+s"(pb[1]));
  };
  const unsigned smask = w != 4 ? 0u : idx >= 16 ? 0x1040u : 0x1000u;
  auto side_b = [&](int t) {
    if (__builtin_expect(!((smask >> t) & 1), 1)) return;
    if (t == 6) {
      u32x2 tv; tv.x = (unsigned)tag; tv.y = (unsigned)(tag >> 32);
      __builtin_amdgcn_raw_buffer_store_b64(tv, rws, opaque_lane() == 0 ? WS_PKF + (b - 128) * 8 : OOB, 0, AUX_SC1);
    } else {
      dma16c(PKF_OFF, 0, opaque_lane() * 16, sws, WS_PKF);
    }
  };
  auto side_f = [&](int t) {
    {
      if (t == 0) WAITV(0);
      const u32x4 f0 = cat(ldstr(pra[0] + t * 32), ldstr(prb[0] + t * 32)), f1 = cat(ldstr(pra[1] + t * 32), ldstr(prb[1] + t * 32));
      const unsigned n0 = pn[0] + t, c0 = (n0 * 205) >> 10, n1 = pn[1] + t, c1 = (n1 * 205) >> 10;
      __builtin_amdgcn_raw_buffer_store_b128(f0, rws, lane * 16, pb[0] + n0 * 1024 + c0 * (68 * 5120 - 5120), AUX_SC1);
      STPAD();
      __builtin_amdgcn_raw_buffer_store_b128(f1, rws, lane * 16, pb[1] + n1 * 1024 + c1 * (68 * 5120 - 5120), AUX_SC1);
      STPAD();
    }
  };
  auto pk_check = [&]() {
    const u32x4 p0 = lds128(PKF_OFF + lane * 16);
    const unsigned lo = (unsigned)tag, hi = (unsigned)(tag >> 32);
    if (!__all(p0.x == lo && p0.z == lo && p0.y == hi && p0.w == hi)) {
      GAS u64* fp = (GAS u64*)((char*)ws + WS_PKF + opaque_lane() * 8);
#pragma unroll 1
      for (unsigned spin = 0; spin < (1u << 18); ++spin) {
        bool ok = true;
#pragma unroll
        for (int j = 0; j < 2; ++j) ok &= __hip_atomic_load(fp + 64 * j, RLX) == tag;
        if (__all(ok)) break;
        __builtin_amdgcn_s_sleep(2);
      }
    }
    asm volatile("" ::: "memory");
  };
  const int rot = (a + (idx >> 3)) & (ROT - 1);

  const int t0 = w < 4 ? 3 * w : 12 + 2 * (w - 4);
  float sg0, sg1, sg2;
  auto role = [&](auto RBT) {
    constexpr bool RB = decltype(RBT)::value;
    u32x4 wo_[3][5], wg_[2][5];
    asm volatile("global_load_dwordx2 %0, %1, %2 sc1" : "=v"(ev) : "v"(WS_EPOCH), "s"(ws) : "memory");
    auto tail = [&](int i) {
      if (i == 0) pk_check();
      if (RB) {
        if (i < 2) ldw(wo_[i], pwb + (2 * w + i) * 5120);
        else ldw(wg_[i - 2], pwb + (20 + 6 * w + gstep(i - 2)) * 5120);
        if (i == 1) cell_dma(w - 4, 0);
        if (i == 2) cell_dma(w, 0);
      } else if (i >= 2) {
        ldw(wo_[i - 2], pwb + (2 * w + i - 2) * 5120);
      }
    };
    if (idx < 16) {
      const int g = gc, hd = w & 3;
      constexpr int par = RB ? 1 : 0;
      using K = P1<true, RB>;
      K k;
      k.srdx = mk_srd(x, 2048 * 1280);
      k.srdw = mk_srd(wqkv, 640 * 3840);
      unsigned gv0, gv1;
      asm volatile("buffer_load_ushort %0, %2, %3, 0 offen\n\tbuffer_load_ushort %1, %2, %3, 0 offen offset:1024"
                   : "=&v"(gv0), "=&v"(gv1) : "v"(tid * 2), "s"(mk_srd(gw, 1280)) : "memory");
      const int q0 = RB ? 12 + 2 * (w - 4) : 3 * w;
      k.xd = K::XB + q0 * 1024; k.wd0 = w * 2048;
      asm volatile("" : "+s"(k.xd), "+s"(k.wd0));
#pragma unroll
      for (int i = 0; i < K::NXP; ++i) {
        const int rho = 8 * (q0 + i) + (lane >> 3);
        const int pp = rho >= 80 ? 1 : 0, u = rho - 80 * pp;
        const int tok = 128 * a - 32 + 2 * u + pp;
        const unsigned v = tok * 1280 + (((lane & 7) ^ ((rho >> 1) & 7)) << 4);
        k.xv[i] = (u >= 9 && tok >= 0) ? v : OOB;
      }
#pragma unroll
      for (int j = 0; j < 2; ++j) {
        const unsigned I = (2 * w + j) * 64 + lane;
        const unsigned kb = (I >= 248) + (I >= 488) + (I >= 736);
        const unsigned i = I - 240 * kb - 8 * ((kb + 1) >> 1);
        const unsigned r = (i * 2185u) >> 16, rem = i - r * 30;
        const unsigned seg = (rem * 6554u) >> 16, ch = rem - seg * 10;
        const unsigned v = (8 * kb + r) * 3840 + seg * 1280 + g * 160 + ch * 16;
        k.wv[j] = i < 240 ? v : OOB;
      }
      STAMP(11);
      k.stage0(rot);
      BAR();
      k.stage12();
      STAMP(12);
      k.xa0 = (K::XB + par * 5 * 2048 + c * 128 + ((h ^ (c >> 1)) << 4)) ^ ((rot & 1) << 6);
      const unsigned kbo = h * 3840 + ((h + 1) >> 1) * 128;
      k.wa0 = kbo + q4 * 480 + (20 * hd + 4 * p4) * 2;
      const bool isgate = p4 == 3;
      k.gmask = isgate ? ~0u : 0u;
      asm volatile("" : "+v"(k.gmask));
      k.wa1 = isgate ? GATE_OFF + (8 * h + q4) * 8 : kbo + q4 * 480 + p4 * 160 + (20 * hd + 16) * 2;
      k.wa2 = k.wa1 + (isgate ? 32 : 1920);
      k.template run<RB ? 5 : 0, RB ? 10 : 0>([&]() {
        asm volatile("s_waitcnt vmcnt(%2)" : "+v"(gv0), "+v"(gv1) : "i"(K::VPRO) : "memory");
        u32x2 v; v.x = gv0; v.y = 0;
        *(LAS u32x2*)(GATE_OFF + tid * 8) = v;
        if (!RB && tid < 128) { v.x = gv1; *(LAS u32x2*)(GATE_OFF + 4096 + tid * 8) = v; }
        make_tag(std::integral_constant<int, K::VPRO>{});
        WAITL();
        BAR();
        STAMP(1);
      }, [&](int t) { if (RB) side_b(t); }, tail);
      STAMP(2);
      u32x2 ql[5], kl[5], t2[5], vl[5], tt[5];
      const unsigned scr = w * 15360 + 10240;
#pragma unroll
      for (int i = 0; i < 5; ++i) {
        if (i) ql[i] = pk4(k.acc[i][0]);
        kl[i] = pk4(k.acc[i][1]); t2[i] = pk4(k.acc[i][2]); vl[i] = pk4(k.acc[i][3]);
        *(LAS u32x2*)(scr + i * 512 + c * 32 + h * 8) = t2[i];
      }
      float sgate[4];
#pragma unroll
      for (int i = 0; i < 4; ++i) {
        const unsigned v = f2u(k.acc[i + 1][2].x);
        const unsigned v2 = __builtin_amdgcn_permlane32_swap(v, v, false, false)[1];
        const unsigned v3 = __builtin_amdgcn_permlane16_swap(v2, v2, false, false)[1];
        sgate[i] = __builtin_amdgcn_exp2f(__builtin_amdgcn_fmed3f(u2f(v3), -60.f, 60.f) * LOG2E);
      }
      WAITL();
#pragma unroll
      for (int i = 0; i < 5; ++i) tt[i] = ldstr(scr + i * 512 + (4 * h + q4) * 32 + p4 * 8);
      f32x4 ba, bb;
#pragma unroll
      for (int r = 0; r < 4; ++r) {
        const int kap = 4 * h + r;
        ba[r] = kap >= c + 9 ? 0.f : -1e30f;
        bb[r] = (kap <= c && kap >= c - 7) ? 0.f : -1e30f;
      }
      const unsigned m0mask = h == 0 ? 0xffffffffu : 0u;
      u32x4 kf[5];
#pragma unroll
      for (int i = 0; i < 5; ++i) {
        auto s0 = __builtin_amdgcn_permlane16_swap(t2[i].x, t2[i].x, false, false);
        auto s1 = __builtin_amdgcn_permlane16_swap(t2[i].y, t2[i].y, false, false);
        kf[i].x = kl[i].x; kf[i].y = kl[i].y; kf[i].z = s0[1]; kf[i].w = s1[1];
      }
      constexpr float SC = 0.22360679774997896f * LOG2E;
      const unsigned ovo = WS_O + (h & 1) * (2 * 20480) + (2 * g + (hd >> 1)) * 1024 + (2 * (hd & 1) + (h >> 1)) * 256 + c * 16;
      const unsigned ovt = h == 2 ? WS_O + (32 + g) * 512 + hd * 128 + c * 8 : OOB;
      const unsigned ovg = (h == 3 && hd == 0 && g == 0) ? WS_G + c * 4 : OOB;
      u32x2 oprev;
#pragma unroll
      for (int i = 1; i < 5; ++i) {
        u32x4 qf; qf.x = ql[i].x; qf.y = ql[i].y; qf.z = t2[i].x & m0mask; qf.w = t2[i].y & m0mask;
        f32x4 bam = ba;
        if (i == 1 && a == 0) bam = f32x4{-1e30f, -1e30f, -1e30f, -1e30f};
        const f32x4 sb = mfma(kf[i], qf, bb);
        const f32x4 sa = mfma(kf[i - 1], qf, bam);
        float m = fmaxf(fmaxf(fmaxf(sb.x, sb.y), fmaxf(sb.z, sb.w)), fmaxf(fmaxf(sa.x, sa.y), fmaxf(sa.z, sa.w)));
        m = xmax(m);
        const float mc = m * SC;
        f32x4 pa, pb;
#pragma unroll
        for (int r = 0; r < 4; ++r) {
          pb[r] = __builtin_amdgcn_exp2f(sb[r] * SC - mc);
          pa[r] = __builtin_amdgcn_exp2f(sa[r] * SC - mc);
        }
        float l = ((pb.x + pb.y) + (pb.z + pb.w)) + ((pa.x + pa.y) + (pa.z + pa.w));
        l = xsum(l);
        const float rl = __builtin_amdgcn_rcpf(l);
        const u32x4 pf = cat(pk4(pa * rl), pk4(pb * rl));
        const f32x4 z = {0.f, 0.f, 0.f, 0.f};
        const u32x2 oa = pk4(mfma(cat(vl[i - 1], vl[i]), pf, z) * sgate[i - 1]);
        const f32x4 ob = mfma(cat(tt[i - 1], tt[i]), pf, z) * sgate[i - 1];
        const unsigned mt = 8 * a + 2 * (i - 1) + par;
        __builtin_amdgcn_raw_buffer_store_b64(pk4(ob), rws, ovt, mt * 20480, AUX_SC1);
        __builtin_amdgcn_raw_buffer_store_b32(f2u(sgate[i - 1]), rws, ovg, mt * 64, AUX_SC1);
        if (i & 1) {
          oprev = oa;
        } else {
          auto sx = __builtin_amdgcn_permlane16_swap(oprev.x, oa.x, false, false);
          auto sy = __builtin_amdgcn_permlane16_swap(oprev.y, oa.y, false, false);
          u32x4 o; o.x = sx[0]; o.y = sy[0]; o.z = sx[1]; o.w = sy[1];
          __builtin_amdgcn_raw_buffer_store_b128(o, rws, ovo, (mt - 2) * 20480, AUX_SC1);
          STPAD();
        }
      }
    } else {
      const int cj = gc;
      using K = P1<false, RB>;
      K k;
      const int mh = RB ? (w < 6 ? 1 : 0) : (w >> 1);
      const int up = (w >> 1) & 1, kp = w & 1;
      k.srdx = mk_srd(x, 2048 * 1280);
      k.srdw = mk_srd(up ? wu : wg, 640 * 3072);
#pragma unroll
      for (int i = 0; i < K::NXP; ++i) {
        const int rho = 8 * (4 * (w & 3) + i) + (lane >> 3);
        const int j = rho >> 4;
        const int tok = 128 * a + 32 * (j >> 1) + 2 * (rho & 15) + (j & 1);
        k.xv[i] = tok * 1280 + (((lane & 7) ^ ((rho >> 1) & 7)) << 4);
      }
      k.xd = K::XB + 4 * (w & 3) * 1024;
      {
        const int r = lane >> 3, p8 = lane & 7;
        const int s = ((r >> 1) & 1) + 2 * kp;
        k.wv[0] = (8 * kp + r) * 3072 + cj * 384 + (((((p8 >> 1) ^ s) << 1) | (p8 & 1)) << 4) + (RB ? 0 : 16 * 3072 + 128);
        k.wv[1] = k.wv[0] + 128; k.wv[2] = k.wv[0] + 256; k.wv[3] = k.wv[0] + 16 * 3072;
      }
      k.wd0 = RB ? (kp * 6 + 3 * up) * 1024 : ((kp + 2) * 6 + 3 * up + 1) * 1024;
      k.wd1 = ((kp + 2) * 6 + 3 * up) * 1024;
      STAMP(11);
      k.stage0(rot);
      BAR();
      if (!RB) pack_issue();
      k.stage12();
      STAMP(12);
      k.xa0 = (K::XB + mh * 4 * 2048 + c * 128 + ((h ^ (c >> 1)) << 4)) ^ ((rot & 1) << 6);
      {
        const int s = ((q4 >> 1) & 1) + 2 * (h & 1);
        k.wa0 = (h * 6 + (RB ? 2 : (w & 1))) * 1024 + q4 * 128 + (((RB ? 2 * (w & 1) : 0) ^ s) << 5) + p4 * 8;
      }
      k.template run<RB ? 5 : 0, RB ? 10 : 0>([&]() {
        make_tag(std::integral_constant<int, RB ? K::VPRO : 8>{});
        BAR();
        STAMP(1);
      }, [&](int t) { if (RB) side_b(t); else side_f(t); }, tail);
      STAMP(2);
      constexpr int NK = RB ? 1 : 2;
      const unsigned gso = WS_GU + ((8 * a + 4 * mh) * 48 + 6 * cj + (RB ? 4 + (w & 1) : 2 * (w & 1))) * 1024;
#pragma unroll
      for (int m = 0; m < 4; ++m) {
#pragma unroll
        for (int kk = 0; kk < NK; ++kk) {
          float v[8];
#pragma unroll
          for (int j = 0; j < 8; j += 2) {
            const float g0 = k.acc[m][2 * kk + (j >> 2)][j & 3], u0 = k.acc[m][2 * NK + 2 * kk + (j >> 2)][j & 3];
            const float g1 = k.acc[m][2 * kk + (j >> 2)][(j & 3) + 1], u1 = k.acc[m][2 * NK + 2 * kk + (j >> 2)][(j & 3) + 1];
            const float d0 = 1.f + __builtin_amdgcn_exp2f(fminf(g0 * -LOG2E, 60.f)), d1 = 1.f + __builtin_amdgcn_exp2f(fminf(g1 * -LOG2E, 60.f));
            const float r = __builtin_amdgcn_rcpf(d0 * d1);
            v[j] = g0 * u0 * (r * d1); v[j + 1] = g1 * u1 * (r * d0);
          }
          u32x4 o; o.x = pk2(v[0], v[1]); o.y = pk2(v[2], v[3]); o.z = pk2(v[4], v[5]); o.w = pk2(v[6], v[7]);
          __builtin_amdgcn_raw_buffer_store_b128(o, rws, lane * 16, gso + m * (48 * 1024) + kk * 1024, AUX_SC1);
          STPAD();
        }
      }
    }
    SB();
    asm volatile("" : "+v"(wo_[0][0]), "+v"(wo_[0][1]), "+v"(wo_[0][2]), "+v"(wo_[0][3]), "+v"(wo_[0][4]),
                      "+v"(wo_[1][0]), "+v"(wo_[1][1]), "+v"(wo_[1][2]), "+v"(wo_[1][3]), "+v"(wo_[1][4]));
    if (RB) asm volatile("" : "+v"(wg_[0][0]), "+v"(wg_[0][1]), "+v"(wg_[0][2]), "+v"(wg_[0][3]), "+v"(wg_[0][4]),
                              "+v"(wg_[1][0]), "+v"(wg_[1][1]), "+v"(wg_[1][2]), "+v"(wg_[1][3]), "+v"(wg_[1][4]));
    SB();
    if (RB && idx >= 16) { if (PRE > 0) cell_dma(w - 4, 1); if (PRE > 1) cell_dma(w, 1); if (PRE > 2) cell_dma(w - 4, 2); WAITV(5 * PRE); }
    else WAITV(0);
    STAMP(3);
    WAITL();
    BAR();
    if (!RB && w == 0 && lane < 2) {
      GAS u64* fp = (GAS u64*)((char*)ws + (lane ? WS_DONE + b * 8 : WS_FLAG + (a * 16 + (idx < 16 ? 8 : 0) + gc) * 8));
      __hip_atomic_store(fp, tag, RLX);
    }
    STAMP(4);

    SB();
    const unsigned fo = WS_FLAG + ((rb >> 1) * 16 + c) * 8;
    auto wait_flags = [&](u32x2& v, bool need) {
#pragma unroll 1
      for (unsigned spin = 0; spin < (1u << 18); ++spin) {
        if (__all(!need || (v.x == (unsigned)tag && v.y == (unsigned)(tag >> 32)))) break;
        __builtin_amdgcn_s_sleep(1);
        ld64(v, fo, sws, 0);
        asm volatile("s_waitcnt vmcnt(0)" : "+v"(v) :: "memory");
      }
      asm volatile("" ::: "memory");
    };
    u32x2 fv;
    if (RB) {
      if (idx < 16) { if (PRE > 0) cell_dma(w - 4, 1); if (PRE > 1) cell_dma(w, 1); if (PRE > 2) cell_dma(w - 4, 2); }
      if (PRE < 1) cell_dma(w - 4, 1); if (PRE < 2) cell_dma(w, 1); if (PRE < 3) cell_dma(w - 4, 2);
      cell_dma(w, 2); cell_dma(w - 4, 3); cell_dma(w, 3);
    } else {
#pragma unroll
      for (int n = 0; n < 5; ++n) ld128(wo_[2][n], lane * 16, sws, pwb + (16 + w) * 5120 + n * 1024);
#pragma unroll
      for (int j = 0; j < 10; ++j) ld128(wg_[j / 5][j % 5], lane * 16, sws, pwb + (20 + 6 * w + gstep(j / 5)) * 5120 + (j % 5) * 1024);
      if (w == 0) {
        ld64(fv, fo, sws, 0);
        asm volatile("s_waitcnt vmcnt(0)" : "+v"(fv) :: "memory");
        wait_flags(fv, c >= 8);
      }
    }
    if (RB) __builtin_amdgcn_s_setprio(1);
    BAR();
    STAMP(5);
    f32x4 acc[4][5];
    u32x4 ag[6][4];
    auto tk = [](int) {};
    tk(0);
    const unsigned gb = (EXP & 1) ? pwb : WS_GU + (4 * rb * 48 + 6 * w) * 1024;
    auto ldg = [&](int s, int m) { ld128(ag[s][m], lane * 16, sws, gb + gstep(s) * 1024 + m * (48 * 1024)); };
    unsigned ca0 = CELL0 + w * 5120 + lane * 16, ca1 = w * 15360 + lane * 16;
    asm volatile("" : "+v"(ca0), "+v"(ca1));
    {
      using S = Sched<RB>;
      constexpr int NO = S::NO, OB = S::OB;
      u32x4 af[2][4], at[4];
      u32x2 tl[4], th[4];
      const unsigned ob = WS_O + 4 * rb * 20480;
      auto req = [&](auto IT) {
        constexpr int i = IT.value;
        if constexpr (i < 8) ld128(af[i >> 2][i & 3], lane * 16, sws, ob + (i & 3) * 20480 + (2 * w + (i >> 2)) * 1024);
        else if constexpr (i < OB) ld64((i & 1) ? th[(i - 8) >> 1] : tl[(i - 8) >> 1], lane * 8, sws, ob + ((i - 8) >> 1) * 20480 + (16 + w) * 1024 + (i & 1) * 512);
        else if constexpr (i < OB + 24) ldg((i - OB) >> 2, (i - OB) & 3);
        else ld32(i == OB + 24 ? sg0 : i == OB + 25 ? sg1 : sg2, c * 4, sws, WS_G + (4 * rb + min(t0 + i - OB - 24, 19) / 5) * 64);
      };
      sfor<0, S::upto(PL - 1)>(req);
      if (RB) { __builtin_amdgcn_s_setprio(0); WAITV(S::upto(PL - 1)); }
      else if (w == 0) wait_flags(fv, c < 8);
      STAMP(6);
      BAR();
      STAMP(7);
      tk(1);
      u32x4 wc[5];
      sfor<0, NO + 6>([&](auto ST) {
        constexpr int st = ST.value, s = st - NO;
        if constexpr (st == 2 && !RB) {
          asm volatile("s_waitcnt vmcnt(%8)" : "+v"(tl[0]), "+v"(tl[1]), "+v"(tl[2]), "+v"(tl[3]), "+v"(th[0]), "+v"(th[1]), "+v"(th[2]), "+v"(th[3])
                       : "i"(S::issued(st, 0) - 16) : "memory");
#pragma unroll
          for (int m = 0; m < 4; ++m) at[m] = cat(tl[m], th[m]);
          asm volatile("s_nop 1" : "+v"(at[0]), "+v"(at[1]), "+v"(at[2]), "+v"(at[3]));
        }
        sfor<0, 20>([&](auto IT) {
          constexpr int i = IT.value, m = i / 5, n = i % 5, is = S::issued(st, i);
          if constexpr (n == 0) {
            if constexpr (s >= 0)      asm volatile("s_waitcnt vmcnt(%1)" : "+v"(ag[s][m]) : "i"(is - (OB + 4 * s + m) - 1) : "memory");
            else if constexpr (st < 2) asm volatile("s_waitcnt vmcnt(%1)" : "+v"(af[st][m]) : "i"(is - (4 * st + m) - 1) : "memory");
          }
          if constexpr (st == 0 && i == 0 && !RB)
            asm volatile("" : "+v"(wo_[2][0]), "+v"(wo_[2][1]), "+v"(wo_[2][2]), "+v"(wo_[2][3]), "+v"(wo_[2][4]),
                              "+v"(wg_[0][0]), "+v"(wg_[0][1]), "+v"(wg_[0][2]), "+v"(wg_[0][3]), "+v"(wg_[0][4]),
                              "+v"(wg_[1][0]), "+v"(wg_[1][1]), "+v"(wg_[1][2]), "+v"(wg_[1][3]), "+v"(wg_[1][4]));
          if constexpr (st == 0)     mul0(acc[m][n], wo_[0][n], af[0][m]);
          else if constexpr (s >= 0) mac(acc[m][n], wg_[s & 1][n], ag[s][m]);
          else if constexpr (st < 2) mac(acc[m][n], wo_[1][n], af[1][m]);
          else                       mac(acc[m][n], wo_[2][n], at[m]);
          SB();
          if constexpr (m == 0 && s >= 1 && s < 5) wc[n] = lds128((s > 1 ? ca1 + (s - 2) * 5120 : ca0) + n * 1024);
          if constexpr (S::issued(st, i + 1) > is) req(std::integral_constant<int, is>{});
          SB();
        });
        tk(2 + st);
        if constexpr (s >= 1 && s < 5) {
          asm volatile("" : "+v"(wc[0]), "+v"(wc[1]), "+v"(wc[2]), "+v"(wc[3]), "+v"(wc[4]));
#pragma unroll
          for (int n = 0; n < 5; ++n) wg_[(s + 1) & 1][n] = wc[n];
        }
      });
      asm volatile("s_waitcnt vmcnt(0)" : "+v"(sg0), "+v"(sg1), "+v"(sg2) :: "memory");
    }
    MFMA_DONE();
    STAMP(8);
#pragma unroll
    for (int j = 0; j < 10; ++j) *(LAS u32x4*)(ca1 + j * 1024) = cat(pk4(acc[(2 * j) / 5][(2 * j) % 5]), pk4(acc[(2 * j + 1) / 5][(2 * j + 1) % 5]));
    tk(12);
  };
  if (w < 4) role(std::false_type{}); else role(std::true_type{});

  if (b == 0 && w == 4) {
    GAS u64* dp = (GAS u64*)((char*)ws + WS_DONE + lane * 8);
#pragma unroll 1
    for (unsigned spin = 0; spin < (1u << 18); ++spin) {
      bool ok = true;
#pragma unroll
      for (int j = 0; j < 4; ++j) ok &= __hip_atomic_load(dp + j * 64, RLX) == tag;
      if (__all(ok)) break;
      __builtin_amdgcn_s_sleep(2);
    }
    if (lane == 0) __hip_atomic_store((GAS u64*)((char*)ws + WS_EPOCH), tag, RLX);
  }
  WAITL();
  BAR();
  STAMP(9);
  {
    const float f0 = __builtin_amdgcn_rcpf(1.f + sg0), f1 = __builtin_amdgcn_rcpf(1.f + sg1), f2 = __builtin_amdgcn_rcpf(1.f + sg2);
    auto sum3 = [=](auto NT) {
      constexpr int N = NT.value;
      u32x2 p[N][8];
#pragma unroll
      for (int j = 0; j < N; ++j) {
        unsigned ra = ((t0 + j) >> 1) * 1024 + ((t0 + j) & 1) * 8 + lane * 16, rb_ = ra + 4 * 15360;
        asm volatile("" : "+v"(ra), "+v"(rb_));
#pragma unroll
        for (int v = 0; v < 8; ++v) p[j][v] = *(const LAS u32x2*)((v < 4 ? ra : rb_) + (v & 3) * 15360);
      }
      SB();
#pragma unroll
      for (int j = 0; j < N; ++j) {
        f32x4 s = {0.f, 0.f, 0.f, 0.f};
#pragma unroll
        for (int v = 0; v < 8; ++v) {
          const unsigned px = p[j][v].x, py = p[j][v].y;
          s += f32x4{u2f(px << 16), u2f(px & 0xffff0000u), u2f(py << 16), u2f(py & 0xffff0000u)};
        }
        const int tl = t0 + j, m = tl / 5, n = tl - 5 * m;
        const int tok = 64 * rb + 32 * (m >> 1) + 2 * c + (m & 1);
        *(GAS u32x2*)((char*)out + tok * 1280 + ct * 160 + n * 32 + h * 8) = pk4(s * (j == 0 ? f0 : j == 1 ? f1 : f2));
      }
    };
    if (w < 4) sum3(std::integral_constant<int, 3>{}); else sum3(std::integral_constant<int, 2>{});
  }
  STAMP(10);
  if (AMP0 >= 0 && (AMPSEL == 0 || (AMPSEL == 1) == (idx < 16))) {
    const u64 d = amp_t1 - amp_t0, t = __builtin_amdgcn_s_memrealtime();
    while (__builtin_amdgcn_s_memrealtime() - t < d) __builtin_amdgcn_s_sleep(1);
  }
}
}

extern "C" void kernel_launch(void* const* d_in, const int* in_sizes, int n_in, void* d_out, int out_size, void* d_ws,
                              size_t ws_size, hipStream_t stream) {
  fused<<<256, 512, 0, stream>>>(d_in[0], d_in[1], d_in[2], d_in[4], d_in[5], d_in[6], d_in[7], d_out, d_ws);
}
```

```cpp
#include <hip/hip_runtime.h>
#include <hip/hip_bf16.h>
#include <stdint.h>
#include <type_traits>

#define STAMPS 0
#define BEARLY 0
#define CODEPF 1
#define TOUCH_NT ""
#define TOUCH 1
#define EXP 0
#define GLA 4
#define PART16 1
#define PL 2
#define PRE 1
#define ROT 4

#define LAS __attribute__((address_space(3)))
#define GAS __attribute__((address_space(1)))
typedef __bf16 bf16x8 __attribute__((ext_vector_type(8)));
typedef __bf16 bf16x2 __attribute__((ext_vector_type(2)));
typedef float f32x4 __attribute__((ext_vector_type(4)));
typedef float f32x2 __attribute__((ext_vector_type(2)));
typedef unsigned u32x4 __attribute__((ext_vector_type(4)));
typedef unsigned u32x2 __attribute__((ext_vector_type(2)));
typedef short s16x4 __attribute__((ext_vector_type(4)));
typedef unsigned long long u64;

namespace {
constexpr int LDS_BYTES = 163840;
constexpr unsigned GATE_OFF = 110592, PACK_OFF = 122880, PKF_OFF = 162816, CELL0 = 122880;
constexpr unsigned OOB = 0x40000000u;
constexpr unsigned CODE_BYTES = 43000;
constexpr unsigned WS_FLAG = 0;
constexpr unsigned WS_DONE = 4096;
constexpr unsigned WS_PKF = 8192;
constexpr unsigned WS_EPOCH = 12288;
constexpr unsigned WS_G = 16384;
constexpr unsigned WS_O = 131072;
constexpr unsigned WS_GU = WS_O + 128 * 40 * 512;
constexpr unsigned WS_PW = WS_GU + 128 * 48 * 1024;
constexpr unsigned WS_END = WS_PW + 8 * 68 * 5120;
constexpr unsigned WS_STAMP = 16u << 20;
constexpr int AUX_SC1 = 16;
constexpr float LOG2E = 1.4426950408889634f;

__device__ __forceinline__ u32x4 mk_srd(const void* p, unsigned bytes) {
  const u64 a = (u64)p;
  u32x4 r; r.x = (unsigned)a; r.y = (unsigned)(a >> 32) & 0xffffu; r.z = bytes; r.w = 0x00020000u; return r;
}
__device__ __forceinline__ void dma16(unsigned lds, unsigned imm, unsigned voff, u32x4 srd, unsigned soff) {
  asm volatile("s_add_u32 m0, %0, %4\n\ts_nop 0\n\tbuffer_load_dwordx4 %1, %2, %3 offen lds"
               :: "s"(lds), "v"(voff), "s"(srd), "s"(soff), "i"(imm) : "memory", "scc");
}
__device__ __forceinline__ void dma16c(unsigned lds, unsigned imm, unsigned voff, u32x4 srd, unsigned soff) {
  asm volatile("s_add_u32 m0, %0, %4\n\ts_nop 0\n\tbuffer_load_dwordx4 %1, %2, %3 offen sc1 lds"
               :: "s"(lds), "v"(voff), "s"(srd), "s"(soff), "i"(imm) : "memory", "scc");
}
__device__ __forceinline__ void ld128(u32x4& d, unsigned voff, u32x4 srd, unsigned soff) {
  asm volatile("buffer_load_dwordx4 %0, %1, %2, %3 offen sc1" : "=v"(d) : "v"(voff), "s"(srd), "s"(soff) : "memory");
}
__device__ __forceinline__ void ld64(u32x2& d, unsigned voff, u32x4 srd, unsigned soff) {
  asm volatile("buffer_load_dwordx2 %0, %1, %2, %3 offen sc1" : "=v"(d) : "v"(voff), "s"(srd), "s"(soff) : "memory");
}
__device__ __forceinline__ void ld32(float& d, unsigned voff, u32x4 srd, unsigned soff) {
  asm volatile("buffer_load_dword %0, %1, %2, %3 offen sc1" : "=v"(d) : "v"(voff), "s"(srd), "s"(soff) : "memory");
}
__device__ __forceinline__ u32x4 lds128(unsigned a) { return *(const LAS u32x4*)a; }
__device__ __forceinline__ u32x2 ldstr(unsigned a) {
  return __builtin_bit_cast(u32x2, __builtin_amdgcn_ds_read_tr16_b64_v4i16((LAS s16x4*)a));
}
__device__ __forceinline__ u32x4 cat(u32x2 a, u32x2 b) { u32x4 r; r.x = a.x; r.y = a.y; r.z = b.x; r.w = b.y; return r; }
__device__ __forceinline__ f32x4 mfma(u32x4 a, u32x4 b, f32x4 c) {
  return __builtin_amdgcn_mfma_f32_16x16x32_bf16(__builtin_bit_cast(bf16x8, a), __builtin_bit_cast(bf16x8, b), c, 0, 0, 0);
}
__device__ __forceinline__ void mac(f32x4& c, u32x4 a, u32x4 b) {
  asm volatile("v_mfma_f32_16x16x32_bf16 %0, %1, %2, %0" : "+v"(c) : "v"(a), "v"(b));
}
__device__ __forceinline__ void mul0(f32x4& c, u32x4 a, u32x4 b) {
  asm volatile("v_mfma_f32_16x16x32_bf16 %0, %1, %2, 0" : "=&v"(c) : "v"(a), "v"(b));
}
__device__ __forceinline__ unsigned pk2(float a, float b) {
  f32x2 t = {a, b};
  return __builtin_bit_cast(unsigned, __builtin_convertvector(t, bf16x2));
}
__device__ __forceinline__ u32x2 pk4(f32x4 v) { u32x2 r; r.x = pk2(v.x, v.y); r.y = pk2(v.z, v.w); return r; }
__device__ __forceinline__ float u2f(unsigned u) { return __builtin_bit_cast(float, u); }
__device__ __forceinline__ unsigned f2u(float f) { return __builtin_bit_cast(unsigned, f); }
#define SB() __builtin_amdgcn_sched_barrier(0)
#define WAITV(n) asm volatile("s_waitcnt vmcnt(%0)" :: "i"(n) : "memory")
#define WAITVL(n) asm volatile("s_waitcnt vmcnt(%0) lgkmcnt(0)" :: "i"(n) : "memory")
#define WAITL() asm volatile("s_waitcnt lgkmcnt(0)" ::: "memory")
#define BAR() asm volatile("s_barrier" ::: "memory")
#define STPAD() do { SB(); asm volatile("s_nop 1" ::: "memory"); SB(); } while (0)
#define MFMA_DONE() do { SB(); asm volatile("s_nop 7\n\ts_nop 1" ::: "memory"); SB(); } while (0)
#define RLX __ATOMIC_RELAXED, __HIP_MEMORY_SCOPE_AGENT
__device__ __forceinline__ unsigned rfl(unsigned v) { return __builtin_amdgcn_readfirstlane(v); }
__device__ __forceinline__ unsigned opaque_lane() { unsigned l = threadIdx.x & 63; asm volatile("" : "+v"(l)); return l; }

#define AMP0 -1
#define AMP1 -1
#define AMPSEL 0
#define STAMP(i) do {} while (0)

template <int I, int N, class F>
__device__ __forceinline__ void sfor(F&& f) {
  if constexpr (I < N) { f(std::integral_constant<int, I>{}); sfor<I + 1, N>(f); }
}
template <bool RB> struct Sched {
  static constexpr int NO = RB ? 2 : 3, OB = RB ? 8 : 16;
  static constexpr int upto(int st) { return st < 2 ? 4 * (st + 1) : st < NO ? 16 : st < NO + 6 ? OB + 4 * (st - NO + 1) : OB + 27; }
  static constexpr int issued(int st, int i) {
    const int base = upto(st + PL - 1), goal = upto(st + PL), every = goal - base > 4 ? 2 : 5;
    int n = base;
    for (int k = 0; k < i; ++k) if (k % every == 1 && n < goal) ++n;
    return n;
  }
};
__device__ __forceinline__ float xmax(float v) {
  auto r = __builtin_amdgcn_permlane16_swap(f2u(v), f2u(v), false, false);
  v = fmaxf(u2f(r[0]), u2f(r[1]));
  auto s = __builtin_amdgcn_permlane32_swap(f2u(v), f2u(v), false, false);
  return fmaxf(u2f(s[0]), u2f(s[1]));
}
__device__ __forceinline__ float xsum(float v) {
  auto r = __builtin_amdgcn_permlane16_swap(f2u(v), f2u(v), false, false);
  v = u2f(r[0]) + u2f(r[1]);
  auto s = __builtin_amdgcn_permlane32_swap(f2u(v), f2u(v), false, false);
  return u2f(s[0]) + u2f(s[1]);
}

template <bool QKV, bool RB>
struct P1 {
  static constexpr int NM = QKV ? 5 : 4;
  static constexpr int NN = QKV ? 4 : (RB ? 4 : 8);
  static constexpr int NWS = 3;
  static constexpr unsigned WSL = QKV ? 16384 : 24576, XB = NWS * WSL, XSL = QKV ? 20480 : 16384;
  static constexpr unsigned WROW = QKV ? 3840 : 3072;
  static constexpr int NWP = QKV ? 2 : (RB ? 4 : 2);
  static constexpr int NXE = QKV ? (RB ? 1 : 2) : (RB ? 2 : 0), NXO = QKV ? 1 : NXE;
  static constexpr int NXP = NXO + NXE;
  static constexpr int OO = NWP + NXO, OE = NWP + NXE;
  static constexpr bool HAND = !QKV;
  static constexpr int ht = 4;
  f32x4 acc[NM][NN];
  u32x4 srdx, srdw;
  unsigned xv[4], wv[4];
  unsigned xd;
  unsigned wd0, wd1;
  unsigned xa0, wa0, wa1, wa2;
  unsigned gmask;
  __device__ __forceinline__ void tick(int) {}

  unsigned sow, sox, gof;
  unsigned kg, kw, kx, kr;
  template <bool ODD, bool X = true>
  __device__ __forceinline__ void loads(unsigned wwr, unsigned xwr) {
    if (X && NXP) {
#pragma unroll
      for (int i = ODD ? NXE : 0; i < (ODD ? NXP : NXE); ++i) dma16(xwr + xd, i * 1024, xv[i], srdx, sox);
      if (ODD) { kx += 1; sox = (kx ^ (kg >> 1)) * 128; }
    }
    dma16(wwr + wd0, 0, wv[0], srdw, sow);
    dma16(wwr + wd0, 1024, wv[1], srdw, sow);
    if (!QKV && RB) {
      dma16(wwr + wd0, 2048, wv[2], srdw, sow);
      dma16(wwr + wd1, 0, wv[3], srdw, sow);
    }
  }
  __device__ __forceinline__ void other(unsigned wwr) {
    dma16(wwr + wd1, 1024, wv[3], srdw, sow + 128);
    dma16(wwr + wd1, 2048, wv[3], srdw, sow + 256);
  }
  __device__ __forceinline__ void next() { kw += 1; sow = (kw ^ kg) * (32 * WROW); asm volatile("" : "+s"(sow), "+s"(kw)); if (NXP) asm volatile("" : "+s"(sox), "+s"(kx)); }
  __device__ __forceinline__ void stage0(int rot) {
    kg = rot; kw = 0; kx = 0; kr = 0; sow = kg * (32 * WROW); sox = (kg >> 1) * 128; gof = kg * 256;
    if (HAND && !RB) { kw = 3; sow = (3 ^ kg) * (32 * WROW); return; }
#pragma unroll
    for (int i = 0; i < NXE; ++i) dma16(xd, i * 1024, xv[i], srdx, sox);
    loads<true>(0, 0); if (HAND) other(0); next();
  }
  __device__ __forceinline__ void stage12() {
    if (HAND && !RB) return;
    loads<false>(WSL, XSL); if (HAND) other(WSL); next();
    loads<true>(2 * WSL, XSL); if (HAND) other(2 * WSL); next();
  }
  static constexpr int VPRO = OE + OO + (HAND ? 4 : 0);
  struct Ad { unsigned x, w[4]; };
  __device__ __forceinline__ void addr(Ad& a, unsigned xb, unsigned wrd) {
    a.x = xb; a.w[0] = wa0 + wrd;
    if (QKV) {
      const unsigned s2 = (gmask & (gof - wrd)) + wrd;
      kr += 1; gof = (kr ^ kg) * 256;
      a.w[1] = wa1 + s2; a.w[2] = wa2 + s2;
      asm volatile("" : "+v"(a.x), "+v"(a.w[0]), "+v"(a.w[1]), "+v"(a.w[2]));
    } else if (RB) {
      a.w[1] = a.w[0] ^ 32;
      asm volatile("" : "+v"(a.x), "+v"(a.w[0]), "+v"(a.w[1]));
    } else {
      a.w[1] = a.w[0] ^ 32; a.w[2] = a.w[0] ^ 64; a.w[3] = a.w[0] ^ 96;
      asm volatile("" : "+v"(a.x), "+v"(a.w[0]), "+v"(a.w[1]), "+v"(a.w[2]), "+v"(a.w[3]));
    }
  }
  __device__ __forceinline__ unsigned wad(const Ad& a, int n, int hi) {
    if (QKV) return n == 2 ? a.w[1 + hi] : a.w[0] + (n == 0 ? 0 : n == 1 ? 160 : 320) + hi * 1920;
    if (RB) return a.w[n & 1] + (n >> 1) * 3072 + hi * 512;
    return a.w[n & 3] + (n >> 2) * 3072 + hi * 512;
  }
  __device__ __forceinline__ void rd_all(u32x4 (&xf)[NM], u32x4 (&wf)[NN], const Ad& a) {
#pragma unroll
    for (int n = 0; n < NN; ++n) wf[n] = cat(ldstr(wad(a, n, 0)), ldstr(wad(a, n, 1)));
#pragma unroll
    for (int m = 0; m < NM; ++m) xf[m] = lds128(a.x + m * 2048);
  }
  template <bool RD = true, class L>
  __device__ __forceinline__ void mm_rd(const u32x4 (&xc)[NM], const u32x4 (&wc)[NN], u32x4 (&xn)[NM], u32x4 (&wn)[NN], const Ad& a, L&& late) {
    u32x2 lo[NN];
    constexpr bool NMAJ = !QKV && !RB;
    constexpr bool EARLY = !QKV && RB && BEARLY;
    if (EARLY && RD) { rd_all(xn, wn, a); SB(); }
    int r = 0;
#pragma unroll
    for (int i = 0; i < NM * NN; ++i) {
      const int m = NMAJ ? i % NM : i / NN, n = NMAJ ? i / NM : i % NN;
      if (QKV && m == 0 && n == 0) continue;
      if (QKV && n == 3) mac(acc[m][3], xc[m], wc[3]);
      else               mac(acc[m][n], wc[n], xc[m]);
      SB();
      if (!RD || EARLY) continue;
      if (NMAJ) {
        if (i < 4) xn[i] = lds128(a.x + i * 2048);
        else if ((i & 3) < 2) {
          const int k = (i >> 2) - 1;
          if (i & 1) wn[k] = cat(lo[k], ldstr(wad(a, k, 1))); else lo[k] = ldstr(wad(a, k, 0));
        }
      } else {
        if (r < 2 * NN) {
          const int k = r >> 1;
          if (r & 1) wn[k] = cat(lo[k], ldstr(wad(a, k, 1))); else lo[k] = ldstr(wad(a, k, 0));
        } else if (r < 2 * NN + NM) {
          xn[r - 2 * NN] = lds128(a.x + (r - 2 * NN) * 2048);
        }
        else late(r - 2 * NN - NM);
        ++r;
      }
      SB();
    }
    if (NMAJ && RD) { wn[NN - 1] = cat(ldstr(wad(a, NN - 1, 0)), ldstr(wad(a, NN - 1, 1))); SB(); }
  }
  __device__ __forceinline__ void pin(u32x4 (&xf)[NM], u32x4 (&wf)[NN]) {
    asm volatile("" : "+v"(xf[0]), "+v"(xf[1]), "+v"(xf[2]), "+v"(xf[3]), "+v"(wf[0]), "+v"(wf[1]), "+v"(wf[2]), "+v"(wf[3]));
    if (NM == 5) asm volatile("" : "+v"(xf[NM - 1]));
    if (NN == 8) asm volatile("" : "+v"(wf[NN - 4]), "+v"(wf[NN - 3]), "+v"(wf[NN - 2]), "+v"(wf[NN - 1]));
  }
  template <int TL0, int TL1, class F, class G, class H>
  __device__ __forceinline__ void run(F&& pre, G&& extra, H&& tail) {
#pragma unroll
    for (int m = 0; m < NM; ++m)
#pragma unroll
      for (int n = 0; n < NN; ++n) { acc[m][n] = f32x4{0.f, 0.f, 0.f, 0.f}; asm volatile("" : "+v"(acc[m][n])); }
    pre();
    u32x4 xA[NM], wA[NN], xB[NM], wB[NN];
    Ad a;
    addr(a, xa0, 0);
    rd_all(xA, wA, a);
    if (!RB) __builtin_amdgcn_s_setprio(1);
    unsigned w0 = 0, w1 = WSL, w2 = 2 * WSL;
    unsigned xp = 2 * XSL, x0 = 0, x1 = XSL;
    int t = 0;
    unsigned nw1, nw2, nx1;
    auto adv0 = [&]() { nw1 = w2 + WSL; if (nw1 == NWS * WSL) nw1 = 0; asm volatile("" : "+s"(nw1)); };
    auto adv1 = [&]() { nw2 = nw1 + WSL; if (nw2 == NWS * WSL) nw2 = 0; asm volatile("" : "+s"(nw2)); };
    auto adv2 = [&]() { nx1 = x1 + XSL; if (nx1 == 3 * XSL) nx1 = 0; asm volatile("" : "+s"(nx1)); };
    auto adv3 = [&]() {
      w0 = w2; w1 = nw1; w2 = nw2; xp = x0; x0 = x1; x1 = nx1; t += 2;
      asm volatile("" : "+s"(w0), "+s"(w1), "+s"(w2), "+s"(xp), "+s"(x0), "+s"(x1), "+s"(t));
    };
    auto adv = [&]() { adv0(); adv1(); adv2(); adv3(); };
    asm volatile("" : "+s"(w0), "+s"(w1), "+s"(w2), "+s"(xp), "+s"(x0), "+s"(x1), "+s"(t));
    if (QKV) asm volatile("" : "+s"(gof), "+s"(kr));
    auto none = [](int) {};
    if (RB) {
      auto help = [&](int tt, unsigned wwr) { if (HAND && __builtin_expect(tt < ht, 0)) other(wwr); extra(tt); };
      pin(xA, wA);
      SB(); WAITVL(OO + (HAND ? 2 : 0)); tick(0); BAR(); tick(1); SB();
      loads<false>(w0, xp); help(0, w0); next(); addr(a, (xa0 ^ 64) + x0, w1); SB();
      auto half = [&]() {
        mm_rd(xA, wA, xB, wB, a, [&](int i) {
          if (QKV && i == 0) adv0();
          if (QKV && i == 2) adv1();
          if (QKV && i == 4) adv2();
        });
        pin(xB, wB);
        SB(); WAITVL(OE); tick(2 * t + 2); BAR(); tick(2 * t + 3); SB();
        loads<true>(w1, xp); help(t + 1, w1); next();
        addr(a, xa0 + x1, w2);
        if (QKV) adv3(); else adv();
        SB();
        mm_rd(xB, wB, xA, wA, a, none);
      };
#pragma unroll 1
      do {
        half();
        pin(xA, wA);
        SB(); WAITVL(OO); tick(2 * t); BAR(); tick(2 * t + 1); SB();
        loads<false>(w0, xp); help(t, w0); next(); addr(a, (xa0 ^ 64) + x0, w1); SB();
        if (QKV) asm volatile("" : "+s"(gof), "+s"(kr));
      } while (t < 14);
      half();
    } else {
      addr(a, (xa0 ^ 64) + x0, w1);
      auto trip = [&](auto EARLY) {
        constexpr bool E = EARLY.value;
        pin(xA, wA);
        SB(); if (E) WAITL(); else WAITVL(OO);
        tick(2 * t); BAR(); tick(2 * t + 1); SB();
        mm_rd(xA, wA, xB, wB, a, none);
        if (E && t < ht) extra(t); else loads<false>(w0, xp);
        next(); addr(a, xa0 + x1, w2); adv0(); adv1(); adv2(); SB();
        pin(xB, wB);
        SB(); if (E) WAITL(); else WAITVL(OE);
        tick(2 * t + 2); BAR(); tick(2 * t + 3); SB();
        mm_rd(xB, wB, xA, wA, a, none);
        if (E && t < ht) extra(t + 1); else loads<true>(w1, xp);
        next(); adv3(); addr(a, (xa0 ^ 64) + x0, w1); SB();
        if (QKV) asm volatile("" : "+s"(gof), "+s"(kr));
      };
      if (HAND) {
#pragma unroll 1
        do trip(std::true_type{}); while (t < ht + 2);
      }
#pragma unroll 1
      do trip(std::false_type{}); while (t < 16);
    }
    constexpr unsigned S16 = (16 % NWS) * WSL, S17 = (17 % NWS) * WSL, S18 = (18 % NWS) * WSL, S19 = (19 % NWS) * WSL, X8 = 2 * XSL, X9 = 0;
    pin(xA, wA);
    SB(); WAITVL(OO);
    tick(32); BAR(); tick(33); SB();
    if (RB) { loads<false, false>(S16, 0); tail(0); addr(a, (xa0 ^ 64) + X8, S17); SB(); mm_rd(xA, wA, xB, wB, a, none); }
    else    { mm_rd(xA, wA, xB, wB, a, none); loads<false, false>(S16, 0); tail(0); addr(a, xa0 + X9, S18); SB(); }
    pin(xB, wB);
    SB(); WAITVL(NWP + TL0); tick(34); BAR(); tick(35); SB();
    if (RB) { tail(1); addr(a, xa0 + X9, S18); SB(); mm_rd(xB, wB, xA, wA, a, none); }
    else    { mm_rd(xB, wB, xA, wA, a, none); tail(1); addr(a, (xa0 ^ 64) + X9, S19); SB(); }
    pin(xA, wA);
    SB(); WAITVL(TL0 + TL1); tick(36); BAR(); tick(37); SB();
    if (RB) { tail(2); addr(a, (xa0 ^ 64) + X9, S19); SB(); mm_rd(xA, wA, xB, wB, a, none); }
    else    { mm_rd(xA, wA, xB, wB, a, none); tail(2); SB(); }
    pin(xB, wB);
    SB(); WAITL(); tick(38); BAR(); tick(39); SB();
    if (RB) { tail(3); SB(); mm_rd<false>(xB, wB, xA, wA, a, none); }
    else    { mm_rd<false>(xB, wB, xA, wA, a, none); tail(3); SB(); }
    if (!RB) __builtin_amdgcn_s_setprio(0);
    MFMA_DONE();
    tick(40);
  }
};

__global__ __launch_bounds__(512, 2) void fused(const void* __restrict__ x, const void* __restrict__ wqkv,
                                                const void* __restrict__ wo, const void* __restrict__ wg,
                                                const void* __restrict__ wu, const void* __restrict__ wd,
                                                const void* __restrict__ gw, void* out, void* ws) {
  __shared__ __attribute__((aligned(16))) char smem[LDS_BYTES];
  asm volatile("" :: "s"((unsigned)(size_t)(LAS char*)smem));
  asm volatile("" :: "s"(x), "s"(wqkv), "s"(wo), "s"(wg), "s"(wu), "s"(wd), "s"(gw), "s"(out), "s"(ws));
  const int tid = threadIdx.x, lane = tid & 63;
  const int w = rfl(tid >> 6);
  const int h = lane >> 4, c = lane & 15, q4 = (lane & 15) >> 2, p4 = lane & 3;
  const int b = blockIdx.x, xcd = b & 7, idx = b >> 3;
  const int rh = xcd & 1, cq = xcd >> 1;
  const int a = 8 * rh + (idx & 7);
  const int gc = 2 * cq + ((idx >> 3) & 1);
  const int rb = 4 * xcd + (idx >> 3), ct = idx & 7;
  const __amdgpu_buffer_rsrc_t rws = __builtin_amdgcn_make_buffer_rsrc(ws, 0, WS_END, 0x00020000);
  const u32x4 sws = mk_srd(ws, WS_END);
  u64 amp_t0 = 0, amp_t1 = 0;
  STAMP(0);

  if (CODEPF && w == 7) {
    u64 pc;
    asm volatile("s_getpc_b64 %0" : "=s"(pc));
    asm volatile("s_mov_b32 m0, %0\n\ts_nop 0\n\tbuffer_load_dword %1, %2, 0 offen lds"
                 :: "s"(PKF_OFF), "v"(lane < 48 ? ((idx & 7) * 48 + lane) * 128 : OOB), "s"(mk_srd((const void*)(pc & ~127ull), CODE_BYTES)) : "memory");
  }
  u32x2 ev;
  u64 tag = 0;
  auto make_tag = [&](auto NV) {
    unsigned lo, hi;
    asm volatile("s_waitcnt vmcnt(%4)\n\tv_readfirstlane_b32 %0, %2\n\tv_readfirstlane_b32 %1, %3"
                 : "=s"(lo), "=s"(hi) : "v"(ev.x), "v"(ev.y), "i"(NV.value) : "memory");
    const u64 e = ((u64)hi << 32) | lo;
    tag = (e ^ 0x9E3779B97F4A7C15ull) * 0xBF58476D1CE4E5B9ull + 0x632BE59BD9B4E019ull;
    if (tag == e) tag += 1;
  };
  const unsigned pwb = WS_PW + ct * (68 * 5120);
  const int grot = 3 * ((idx >> 3) & 1);
  auto gstep = [&](int s) { const int v = s + grot; return v >= 6 ? v - 6 : v; };
  auto cell_dma = [&](int v, int cell) {
    const unsigned so = pwb + (20 + 6 * v + gstep(2 + cell)) * 5120;
#pragma unroll
    for (int j = 0; j < 5; ++j) dma16c(cell ? v * 15360 : CELL0 + v * 5120, (cell ? cell - 1 : 0) * 5120 + j * 1024, lane * 16, sws, so + j * 1024);
  };
  auto ldw = [&](u32x4 (&d)[5], unsigned so) {
#pragma unroll
    for (int n = 0; n < 5; ++n) d[n] = __builtin_amdgcn_raw_buffer_load_b128(rws, lane * 16, so + n * 1024, AUX_SC1);
  };
  const unsigned pscr = PACK_OFF + (w & 3) * 8192;
  const int pu0 = (b - 128) * 4 + w;
  unsigned pra[2], prb[2], pn[2], pb[2];
  auto pack_issue = [&]() {
    const u64 po = (u64)wo, pd = (u64)wd;
#pragma unroll
    for (int r = 0; r < 2; ++r) {
      const int u = pu0 + 512 * r;
      const int ks = u / 10, cg = u - 10 * ks;
      const unsigned r8 = lane >> 3;
      const u32x4 srd = mk_srd((const void*)(ks < 20 ? po : pd), ks < 20 ? 640 * 1280 : 1536 * 1280);
      const unsigned base = ks < 16 ? 40 * ks : ks < 20 ? 160 * (ks - 16) + 16 : 32 * (ks - 20);
      const unsigned vr = (ks >= 16 && ks < 20) ? 20 * (r8 >> 2) + (r8 & 3) : r8;
      const unsigned vo = vr * 1280 + (lane & 7) * 16;
#pragma unroll
      for (int p = 0; p < 4; ++p) {
        const unsigned rp = ks < 16 ? 20 * (p >> 1) + 8 * (p & 1) : ks < 20 ? 40 * p : 8 * p;
        dma16(pscr, r * 4096 + p * 1024, vo, srd, u < 680 ? (base + rp) * 1280 + cg * 128 : OOB);
      }
      pra[r] = pscr + r * 4096 + (h * (ks < 16 ? 8 : 4) + q4) * 128 + p4 * 8;
      prb[r] = pra[r] + (ks < 16 ? 512 : 2048);
      pn[r] = 4 * cg; pb[r] = u < 680 ? WS_PW + ks * 5120 : OOB;
    }
    asm volatile("" : "+v"(pra[0]), "+v"(prb[0]), "+v"(pra[1]), "+v"(prb[1]));
    asm volatile("" : "+s"(pn[0]), "+s"(pb[0]), "+s"(pn[1]), "+s"(pb[1]));
  };
  const unsigned smask = w != 4 ? 0u : idx >= 16 ? 0x1040u : 0x1000u;
  auto side_b = [&](int t) {
    if (__builtin_expect(!((smask >> t) & 1), 1)) return;
    if (t == 6) {
      u32x2 tv; tv.x = (unsigned)tag; tv.y = (unsigned)(tag >> 32);
      __builtin_amdgcn_raw_buffer_store_b64(tv, rws, opaque_lane() == 0 ? WS_PKF + (b - 128) * 8 : OOB, 0, AUX_SC1);
    } else {
      dma16c(PKF_OFF, 0, opaque_lane() * 16, sws, WS_PKF);
    }
  };
  auto side_f = [&](int t) {
    {
      if (t == 0) WAITV(0);
      const u32x4 f0 = cat(ldstr(pra[0] + t * 32), ldstr(prb[0] + t * 32)), f1 = cat(ldstr(pra[1] + t * 32), ldstr(prb[1] + t * 32));
      const unsigned n0 = pn[0] + t, c0 = (n0 * 205) >> 10, n1 = pn[1] + t, c1 = (n1 * 205) >> 10;
      __builtin_amdgcn_raw_buffer_store_b128(f0, rws, lane * 16, pb[0] + n0 * 1024 + c0 * (68 * 5120 - 5120), AUX_SC1);
      STPAD();
      __builtin_amdgcn_raw_buffer_store_b128(f1, rws, lane * 16, pb[1] + n1 * 1024 + c1 * (68 * 5120 - 5120), AUX_SC1);
      STPAD();
    }
  };
  auto pk_check = [&]() {
    const u32x4 p0 = lds128(PKF_OFF + lane * 16);
    const unsigned lo = (unsigned)tag, hi = (unsigned)(tag >> 32);
    if (!__all(p0.x == lo && p0.z == lo && p0.y == hi && p0.w == hi)) {
      GAS u64* fp = (GAS u64*)((char*)ws + WS_PKF + opaque_lane() * 8);
#pragma unroll 1
      for (unsigned spin = 0; spin < (1u << 18); ++spin) {
        bool ok = true;
#pragma unroll
        for (int j = 0; j < 2; ++j) ok &= __hip_atomic_load(fp + 64 * j, RLX) == tag;
        if (__all(ok)) break;
        __builtin_amdgcn_s_sleep(2);
      }
    }
    asm volatile("" ::: "memory");
  };
  const int rot = (a + (idx >> 3)) & (ROT - 1);

  const int t0 = w < 4 ? 3 * w : 12 + 2 * (w - 4);
  float sg0, sg1, sg2;
  auto role = [&](auto RBT) {
    constexpr bool RB = decltype(RBT)::value;
    u32x4 wo_[3][5], wg_[2][5];
    asm volatile("global_load_dwordx2 %0, %1, %2 sc1" : "=v"(ev) : "v"(WS_EPOCH), "s"(ws) : "memory");
    auto tail = [&](int i) {
      if (i == 0) pk_check();
      if (RB) {
        if (i < 2) ldw(wo_[i], pwb + (2 * w + i) * 5120);
        else ldw(wg_[i - 2], pwb + (20 + 6 * w + gstep(i - 2)) * 5120);
        if (i == 1) cell_dma(w - 4, 0);
        if (i == 2) cell_dma(w, 0);
      } else if (i >= 2) {
        ldw(wo_[i - 2], pwb + (2 * w + i - 2) * 5120);
      }
    };
    if (idx < 16) {
      const int g = gc, hd = w & 3;
      constexpr int par = RB ? 1 : 0;
      using K = P1<true, RB>;
      K k;
      k.srdx = mk_srd(x, 2048 * 1280);
      k.srdw = mk_srd(wqkv, 640 * 3840);
      unsigned gv0, gv1;
      asm volatile("buffer_load_ushort %0, %2, %3, 0 offen\n\tbuffer_load_ushort %1, %2, %3, 0 offen offset:1024"
                   : "=&v"(gv0), "=&v"(gv1) : "v"(tid * 2), "s"(mk_srd(gw, 1280)) : "memory");
      const int q0 = RB ? 12 + 2 * (w - 4) : 3 * w;
      k.xd = K::XB + q0 * 1024; k.wd0 = w * 2048;
      asm volatile("" : "+s"(k.xd), "+s"(k.wd0));
#pragma unroll
      for (int i = 0; i < K::NXP; ++i) {
        const int rho = 8 * (q0 + i) + (lane >> 3);
        const int pp = rho >= 80 ? 1 : 0, u = rho - 80 * pp;
        const int tok = 128 * a - 32 + 2 * u + pp;
        const unsigned v = tok * 1280 + (((lane & 7) ^ ((rho >> 1) & 7)) << 4);
        k.xv[i] = (u >= 9 && tok >= 0) ? v : OOB;
      }
#pragma unroll
      for (int j = 0; j < 2; ++j) {
        const unsigned I = (2 * w + j) * 64 + lane;
        const unsigned kb = (I >= 248) + (I >= 488) + (I >= 736);
        const unsigned i = I - 240 * kb - 8 * ((kb + 1) >> 1);
        const unsigned r = (i * 2185u) >> 16, rem = i - r * 30;
        const unsigned seg = (rem * 6554u) >> 16, ch = rem - seg * 10;
        const unsigned v = (8 * kb + r) * 3840 + seg * 1280 + g * 160 + ch * 16;
        k.wv[j] = i < 240 ? v : OOB;
      }
      STAMP(11);
      k.stage0(rot);
      BAR();
      k.stage12();
      STAMP(12);
      k.xa0 = (K::XB + par * 5 * 2048 + c * 128 + ((h ^ (c >> 1)) << 4)) ^ ((rot & 1) << 6);
      const unsigned kbo = h * 3840 + ((h + 1) >> 1) * 128;
      k.wa0 = kbo + q4 * 480 + (20 * hd + 4 * p4) * 2;
      const bool isgate = p4 == 3;
      k.gmask = isgate ? ~0u : 0u;
      asm volatile("" : "+v"(k.gmask));
      k.wa1 = isgate ? GATE_OFF + (8 * h + q4) * 8 : kbo + q4 * 480 + p4 * 160 + (20 * hd + 16) * 2;
      k.wa2 = k.wa1 + (isgate ? 32 : 1920);
      k.template run<RB ? 5 : 0, RB ? 10 : 0>([&]() {
        asm volatile("s_waitcnt vmcnt(%2)" : "+v"(gv0), "+v"(gv1) : "i"(K::VPRO) : "memory");
        u32x2 v; v.x = gv0; v.y = 0;
        *(LAS u32x2*)(GATE_OFF + tid * 8) = v;
        if (!RB && tid < 128) { v.x = gv1; *(LAS u32x2*)(GATE_OFF + 4096 + tid * 8) = v; }
        make_tag(std::integral_constant<int, K::VPRO>{});
        WAITL();
        BAR();
        STAMP(1);
      }, [&](int t) { if (RB) side_b(t); }, tail);
      STAMP(2);
      u32x2 ql[5], kl[5], t2[5], vl[5], tt[5];
      const unsigned scr = w * 15360 + 10240;
#pragma unroll
      for (int i = 0; i < 5; ++i) {
        if (i) ql[i] = pk4(k.acc[i][0]);
        kl[i] = pk4(k.acc[i][1]); t2[i] = pk4(k.acc[i][2]); vl[i] = pk4(k.acc[i][3]);
        *(LAS u32x2*)(scr + i * 512 + c * 32 + h * 8) = t2[i];
      }
      float sgate[4];
#pragma unroll
      for (int i = 0; i < 4; ++i) {
        const unsigned v = f2u(k.acc[i + 1][2].x);
        const unsigned v2 = __builtin_amdgcn_permlane32_swap(v, v, false, false)[1];
        const unsigned v3 = __builtin_amdgcn_permlane16_swap(v2, v2, false, false)[1];
        sgate[i] = __builtin_amdgcn_exp2f(__builtin_amdgcn_fmed3f(u2f(v3), -60.f, 60.f) * LOG2E);
      }
      WAITL();
#pragma unroll
      for (int i = 0; i < 5; ++i) tt[i] = ldstr(scr + i * 512 + (4 * h + q4) * 32 + p4 * 8);
      f32x4 ba, bb;
#pragma unroll
      for (int r = 0; r < 4; ++r) {
        const int kap = 4 * h + r;
        ba[r] = kap >= c + 9 ? 0.f : -1e30f;
        bb[r] = (kap <= c && kap >= c - 7) ? 0.f : -1e30f;
      }
      const unsigned m0mask = h == 0 ? 0xffffffffu : 0u;
      u32x4 kf[5];
#pragma unroll
      for (int i = 0; i < 5; ++i) {
        auto s0 = __builtin_amdgcn_permlane16_swap(t2[i].x, t2[i].x, false, false);
        auto s1 = __builtin_amdgcn_permlane16_swap(t2[i].y, t2[i].y, false, false);
        kf[i].x = kl[i].x; kf[i].y = kl[i].y; kf[i].z = s0[1]; kf[i].w = s1[1];
      }
      constexpr float SC = 0.22360679774997896f * LOG2E;
      const unsigned ovo = WS_O + (h & 1) * (2 * 20480) + (2 * g + (hd >> 1)) * 1024 + (2 * (hd & 1) + (h >> 1)) * 256 + c * 16;
      const unsigned ovt = h == 2 ? WS_O + (32 + g) * 512 + hd * 128 + c * 8 : OOB;
      const unsigned ovg = (h == 3 && hd == 0 && g == 0) ? WS_G + c * 4 : OOB;
      u32x2 oprev;
#pragma unroll
      for (int i = 1; i < 5; ++i) {
        u32x4 qf; qf.x = ql[i].x; qf.y = ql[i].y; qf.z = t2[i].x & m0mask; qf.w = t2[i].y & m0mask;
        f32x4 bam = ba;
        if (i == 1 && a == 0) bam = f32x4{-1e30f, -1e30f, -1e30f, -1e30f};
        const f32x4 sb = mfma(kf[i], qf, bb);
        const f32x4 sa = mfma(kf[i - 1], qf, bam);
        float m = fmaxf(fmaxf(fmaxf(sb.x, sb.y), fmaxf(sb.z, sb.w)), fmaxf(fmaxf(sa.x, sa.y), fmaxf(sa.z, sa.w)));
        m = xmax(m);
        const float mc = m * SC;
        f32x4 pa, pb;
#pragma unroll
        for (int r = 0; r < 4; ++r) {
          pb[r] = __builtin_amdgcn_exp2f(sb[r] * SC - mc);
          pa[r] = __builtin_amdgcn_exp2f(sa[r] * SC - mc);
        }
        float l = ((pb.x + pb.y) + (pb.z + pb.w)) + ((pa.x + pa.y) + (pa.z + pa.w));
        l = xsum(l);
        const float rl = __builtin_amdgcn_rcpf(l);
        const u32x4 pf = cat(pk4(pa * rl), pk4(pb * rl));
        const f32x4 z = {0.f, 0.f, 0.f, 0.f};
        const u32x2 oa = pk4(mfma(cat(vl[i - 1], vl[i]), pf, z) * sgate[i - 1]);
        const f32x4 ob = mfma(cat(tt[i - 1], tt[i]), pf, z) * sgate[i - 1];
        const unsigned mt = 8 * a + 2 * (i - 1) + par;
        __builtin_amdgcn_raw_buffer_store_b64(pk4(ob), rws, ovt, mt * 20480, AUX_SC1);
        __builtin_amdgcn_raw_buffer_store_b32(f2u(sgate[i - 1]), rws, ovg, mt * 64, AUX_SC1);
        if (i & 1) {
          oprev = oa;
        } else {
          auto sx = __builtin_amdgcn_permlane16_swap(oprev.x, oa.x, false, false);
          auto sy = __builtin_amdgcn_permlane16_swap(oprev.y, oa.y, false, false);
          u32x4 o; o.x = sx[0]; o.y = sy[0]; o.z = sx[1]; o.w = sy[1];
          __builtin_amdgcn_raw_buffer_store_b128(o, rws, ovo, (mt - 2) * 20480, AUX_SC1);
          STPAD();
        }
      }
    } else {
      const int cj = gc;
      using K = P1<false, RB>;
      K k;
      const int mh = RB ? (w < 6 ? 1 : 0) : (w >> 1);
      const int up = (w >> 1) & 1, kp = w & 1;
      k.srdx = mk_srd(x, 2048 * 1280);
      k.srdw = mk_srd(up ? wu : wg, 640 * 3072);
#pragma unroll
      for (int i = 0; i < K::NXP; ++i) {
        const int rho = 8 * (4 * (w & 3) + i) + (lane >> 3);
        const int j = rho >> 4;
        const int tok = 128 * a + 32 * (j >> 1) + 2 * (rho & 15) + (j & 1);
        k.xv[i] = tok * 1280 + (((lane & 7) ^ ((rho >> 1) & 7)) << 4);
      }
      k.xd = K::XB + 4 * (w & 3) * 1024;
      {
        const int r = lane >> 3, p8 = lane & 7;
        const int s = ((r >> 1) & 1) + 2 * kp;
        k.wv[0] = (8 * kp + r) * 3072 + cj * 384 + (((((p8 >> 1) ^ s) << 1) | (p8 & 1)) << 4) + (RB ? 0 : 16 * 3072 + 128);
        k.wv[1] = k.wv[0] + 128; k.wv[2] = k.wv[0] + 256; k.wv[3] = k.wv[0] + 16 * 3072;
      }
      k.wd0 = RB ? (kp * 6 + 3 * up) * 1024 : ((kp + 2) * 6 + 3 * up + 1) * 1024;
      k.wd1 = ((kp + 2) * 6 + 3 * up) * 1024;
      STAMP(11);
      k.stage0(rot);
      BAR();
      if (!RB) pack_issue();
      k.stage12();
      STAMP(12);
      k.xa0 = (K::XB + mh * 4 * 2048 + c * 128 + ((h ^ (c >> 1)) << 4)) ^ ((rot & 1) << 6);
      {
        const int s = ((q4 >> 1) & 1) + 2 * (h & 1);
        k.wa0 = (h * 6 + (RB ? 2 : (w & 1))) * 1024 + q4 * 128 + (((RB ? 2 * (w & 1) : 0) ^ s) << 5) + p4 * 8;
      }
      k.template run<RB ? 5 : 0, RB ? 10 : 0>([&]() {
        make_tag(std::integral_constant<int, RB ? K::VPRO : 8>{});
        BAR();
        STAMP(1);
      }, [&](int t) { if (RB) side_b(t); else side_f(t); }, tail);
      STAMP(2);
      constexpr int NK = RB ? 1 : 2;
      const unsigned gso = WS_GU + ((8 * a + 4 * mh) * 48 + 6 * cj + (RB ? 4 + (w & 1) : 2 * (w & 1))) * 1024;
#pragma unroll
      for (int m = 0; m < 4; ++m) {
#pragma unroll
        for (int kk = 0; kk < NK; ++kk) {
          float v[8];
#pragma unroll
          for (int j = 0; j < 8; j += 2) {
            const float g0 = k.acc[m][2 * kk + (j >> 2)][j & 3], u0 = k.acc[m][2 * NK + 2 * kk + (j >> 2)][j & 3];
            const float g1 = k.acc[m][2 * kk + (j >> 2)][(j & 3) + 1], u1 = k.acc[m][2 * NK + 2 * kk + (j >> 2)][(j & 3) + 1];
            const float d0 = 1.f + __builtin_amdgcn_exp2f(fminf(g0 * -LOG2E, 60.f)), d1 = 1.f + __builtin_amdgcn_exp2f(fminf(g1 * -LOG2E, 60.f));
            const float r = __builtin_amdgcn_rcpf(d0 * d1);
            v[j] = g0 * u0 * (r * d1); v[j + 1] = g1 * u1 * (r * d0);
          }
          u32x4 o; o.x = pk2(v[0], v[1]); o.y = pk2(v[2], v[3]); o.z = pk2(v[4], v[5]); o.w = pk2(v[6], v[7]);
          __builtin_amdgcn_raw_buffer_store_b128(o, rws, lane * 16, gso + m * (48 * 1024) + kk * 1024, AUX_SC1);
          STPAD();
        }
      }
    }
    SB();
    asm volatile("" : "+v"(wo_[0][0]), "+v"(wo_[0][1]), "+v"(wo_[0][2]), "+v"(wo_[0][3]), "+v"(wo_[0][4]),
                      "+v"(wo_[1][0]), "+v"(wo_[1][1]), "+v"(wo_[1][2]), "+v"(wo_[1][3]), "+v"(wo_[1][4]));
    if (RB) asm volatile("" : "+v"(wg_[0][0]), "+v"(wg_[0][1]), "+v"(wg_[0][2]), "+v"(wg_[0][3]), "+v"(wg_[0][4]),
                              "+v"(wg_[1][0]), "+v"(wg_[1][1]), "+v"(wg_[1][2]), "+v"(wg_[1][3]), "+v"(wg_[1][4]));
    SB();
    if (RB && idx >= 16) { if (PRE > 0) cell_dma(w - 4, 1); if (PRE > 1) cell_dma(w, 1); if (PRE > 2) cell_dma(w - 4, 2); WAITV(5 * PRE); }
    else WAITV(0);
    STAMP(3);
    WAITL();
    BAR();
    if (!RB && w == 0 && lane < 2) {
      GAS u64* fp = (GAS u64*)((char*)ws + (lane ? WS_DONE + b * 8 : WS_FLAG + (a * 16 + (idx < 16 ? 8 : 0) + gc) * 8));
      __hip_atomic_store(fp, tag, RLX);
    }
    STAMP(4);

    SB();
    const unsigned fo = WS_FLAG + ((rb >> 1) * 16 + c) * 8;
    auto wait_flags = [&](u32x2& v, bool need) {
#pragma unroll 1
      for (unsigned spin = 0; spin < (1u << 18); ++spin) {
        if (__all(!need || (v.x == (unsigned)tag && v.y == (unsigned)(tag >> 32)))) break;
        __builtin_amdgcn_s_sleep(1);
        ld64(v, fo, sws, 0);
        asm volatile("s_waitcnt vmcnt(0)" : "+v"(v) :: "memory");
      }
      asm volatile("" ::: "memory");
    };
    u32x2 fv;
    if (RB) {
      if (idx < 16) { if (PRE > 0) cell_dma(w - 4, 1); if (PRE > 1) cell_dma(w, 1); if (PRE > 2) cell_dma(w - 4, 2); }
      if (PRE < 1) cell_dma(w - 4, 1); if (PRE < 2) cell_dma(w, 1); if (PRE < 3) cell_dma(w - 4, 2);
      cell_dma(w, 2); cell_dma(w - 4, 3); cell_dma(w, 3);
    } else {
#pragma unroll
      for (int n = 0; n < 5; ++n) ld128(wo_[2][n], lane * 16, sws, pwb + (16 + w) * 5120 + n * 1024);
#pragma unroll
      for (int j = 0; j < 10; ++j) ld128(wg_[j / 5][j % 5], lane * 16, sws, pwb + (20 + 6 * w + gstep(j / 5)) * 5120 + (j % 5) * 1024);
      if (w == 0) {
        ld64(fv, fo, sws, 0);
        asm volatile("s_waitcnt vmcnt(0)" : "+v"(fv) :: "memory");
        wait_flags(fv, c >= 8);
      }
    }
    if (RB) __builtin_amdgcn_s_setprio(1);
    BAR();
    STAMP(5);
    f32x4 acc[4][5];
    u32x4 ag[6][4];
    auto tk = [](int) {};
    tk(0);
    const unsigned gb = (EXP & 1) ? pwb : WS_GU + (4 * rb * 48 + 6 * w) * 1024;
    auto ldg = [&](int s, int m) { ld128(ag[s][m], lane * 16, sws, gb + gstep(s) * 1024 + m * (48 * 1024)); };
    unsigned ca0 = CELL0 + w * 5120 + lane * 16, ca1 = w * 15360 + lane * 16;
    asm volatile("" : "+v"(ca0), "+v"(ca1));
    {
      using S = Sched<RB>;
      constexpr int NO = S::NO, OB = S::OB;
      u32x4 af[2][4], at[4];
      u32x2 tl[4], th[4];
      const unsigned ob = WS_O + 4 * rb * 20480;
      auto req = [&](auto IT) {
        constexpr int i = IT.value;
        if constexpr (i < 8) ld128(af[i >> 2][i & 3], lane * 16, sws, ob + (i & 3) * 20480 + (2 * w + (i >> 2)) * 1024);
        else if constexpr (i < OB) ld64((i & 1) ? th[(i - 8) >> 1] : tl[(i - 8) >> 1], lane * 8, sws, ob + ((i - 8) >> 1) * 20480 + (16 + w) * 1024 + (i & 1) * 512);
        else if constexpr (i < OB + 24) ldg((i - OB) >> 2, (i - OB) & 3);
        else ld32(i == OB + 24 ? sg0 : i == OB + 25 ? sg1 : sg2, c * 4, sws, WS_G + (4 * rb + min(t0 + i - OB - 24, 19) / 5) * 64);
      };
      sfor<0, S::upto(PL - 1)>(req);
      if (RB) { __builtin_amdgcn_s_setprio(0); WAITV(S::upto(PL - 1)); }
      else if (w == 0) wait_flags(fv, c < 8);
      STAMP(6);
      BAR();
      STAMP(7);
      tk(1);
      u32x4 wc[5];
      sfor<0, NO + 6>([&](auto ST) {
        constexpr int st = ST.value, s = st - NO;
        if constexpr (st == 2 && !RB) {
          asm volatile("s_waitcnt vmcnt(%8)" : "+v"(tl[0]), "+v"(tl[1]), "+v"(tl[2]), "+v"(tl[3]), "+v"(th[0]), "+v"(th[1]), "+v"(th[2]), "+v"(th[3])
                       : "i"(S::issued(st, 0) - 16) : "memory");
#pragma unroll
          for (int m = 0; m < 4; ++m) at[m] = cat(tl[m], th[m]);
          asm volatile("s_nop 1" : "+v"(at[0]), "+v"(at[1]), "+v"(at[2]), "+v"(at[3]));
        }
        sfor<0, 20>([&](auto IT) {
          constexpr int i = IT.value, m = i / 5, n = i % 5, is = S::issued(st, i);
          if constexpr (n == 0) {
            if constexpr (s >= 0)      asm volatile("s_waitcnt vmcnt(%1)" : "+v"(ag[s][m]) : "i"(is - (OB + 4 * s + m) - 1) : "memory");
            else if constexpr (st < 2) asm volatile("s_waitcnt vmcnt(%1)" : "+v"(af[st][m]) : "i"(is - (4 * st + m) - 1) : "memory");
          }
          if constexpr (st == 0 && i == 0 && !RB)
            asm volatile("" : "+v"(wo_[2][0]), "+v"(wo_[2][1]), "+v"(wo_[2][2]), "+v"(wo_[2][3]), "+v"(wo_[2][4]),
                              "+v"(wg_[0][0]), "+v"(wg_[0][1]), "+v"(wg_[0][2]), "+v"(wg_[0][3]), "+v"(wg_[0][4]),
                              "+v"(wg_[1][0]), "+v"(wg_[1][1]), "+v"(wg_[1][2]), "+v"(wg_[1][3]), "+v"(wg_[1][4]));
          if constexpr (st == 0)     mul0(acc[m][n], wo_[0][n], af[0][m]);
          else if constexpr (s >= 0) mac(acc[m][n], wg_[s & 1][n], ag[s][m]);
          else if constexpr (st < 2) mac(acc[m][n], wo_[1][n], af[1][m]);
          else                       mac(acc[m][n], wo_[2][n], at[m]);
          SB();
          if constexpr (m == 0 && s >= 1 && s < 5) wc[n] = lds128((s > 1 ? ca1 + (s - 2) * 5120 : ca0) + n * 1024);
          if constexpr (S::issued(st, i + 1) > is) req(std::integral_constant<int, is>{});
          SB();
        });
        tk(2 + st);
        if constexpr (s >= 1 && s < 5) {
          asm volatile("" : "+v"(wc[0]), "+v"(wc[1]), "+v"(wc[2]), "+v"(wc[3]), "+v"(wc[4]));
#pragma unroll
          for (int n = 0; n < 5; ++n) wg_[(s + 1) & 1][n] = wc[n];
        }
      });
      asm volatile("s_waitcnt vmcnt(0)" : "+v"(sg0), "+v"(sg1), "+v"(sg2) :: "memory");
    }
    MFMA_DONE();
    STAMP(8);
#pragma unroll
    for (int j = 0; j < 10; ++j) *(LAS u32x4*)(ca1 + j * 1024) = cat(pk4(acc[(2 * j) / 5][(2 * j) % 5]), pk4(acc[(2 * j + 1) / 5][(2 * j + 1) % 5]));
    tk(12);
  };
  if (w < 4) role(std::false_type{}); else role(std::true_type{});

  if (b == 0 && w == 4) {
    GAS u64* dp = (GAS u64*)((char*)ws + WS_DONE + lane * 8);
#pragma unroll 1
    for (unsigned spin = 0; spin < (1u << 18); ++spin) {
      bool ok = true;
#pragma unroll
      for (int j = 0; j < 4; ++j) ok &= __hip_atomic_load(dp + j * 64, RLX) == tag;
      if (__all(ok)) break;
      __builtin_amdgcn_s_sleep(2);
    }
    if (lane == 0) __hip_atomic_store((GAS u64*)((char*)ws + WS_EPOCH), tag, RLX);
  }
  WAITL();
  BAR();
  STAMP(9);
  {
    const float f0 = __builtin_amdgcn_rcpf(1.f + sg0), f1 = __builtin_amdgcn_rcpf(1.f + sg1), f2 = __builtin_amdgcn_rcpf(1.f + sg2);
    auto sum3 = [=](auto NT) {
      constexpr int N = NT.value;
      u32x2 p[N][8];
#pragma unroll
      for (int j = 0; j < N; ++j) {
        unsigned ra = ((t0 + j) >> 1) * 1024 + ((t0 + j) & 1) * 8 + lane * 16, rb_ = ra + 4 * 15360;
        asm volatile("" : "+v"(ra), "+v"(rb_));
#pragma unroll
        for (int v = 0; v < 8; ++v) p[j][v] = *(const LAS u32x2*)((v < 4 ? ra : rb_) + (v & 3) * 15360);
      }
      SB();
#pragma unroll
      for (int j = 0; j < N; ++j) {
        f32x4 s = {0.f, 0.f, 0.f, 0.f};
#pragma unroll
        for (int v = 0; v < 8; ++v) {
          const unsigned px = p[j][v].x, py = p[j][v].y;
          s += f32x4{u2f(px << 16), u2f(px & 0xffff0000u), u2f(py << 16), u2f(py & 0xffff0000u)};
        }
        const int tl = t0 + j, m = tl / 5, n = tl - 5 * m;
        const int tok = 64 * rb + 32 * (m >> 1) + 2 * c + (m & 1);
        *(GAS u32x2*)((char*)out + tok * 1280 + ct * 160 + n * 32 + h * 8) = pk4(s * (j == 0 ? f0 : j == 1 ? f1 : f2));
      }
    };
    if (w < 4) sum3(std::integral_constant<int, 3>{}); else sum3(std::integral_constant<int, 2>{});
  }
  STAMP(10);
  if (threadIdx.x == 0) __hip_atomic_store((GAS u64*)((char*)ws + WS_STAMP) + blockIdx.x, (u64)__builtin_amdgcn_s_memrealtime(), RLX);
  if (AMP0 >= 0 && (AMPSEL == 0 || (AMPSEL == 1) == (idx < 16))) {
    const u64 d = amp_t1 - amp_t0, t = __builtin_amdgcn_s_memrealtime();
    while (__builtin_amdgcn_s_memrealtime() - t < d) __builtin_amdgcn_s_sleep(1);
  }
}
__global__ void k_tail(void* ws, int amp) {
  u64 t0;
  asm volatile("s_memrealtime %0\n\ts_waitcnt lgkmcnt(0)" : "=s"(t0) :: "memory");
  u64 m = 0;
  for (int j = 0; j < 4; ++j) { const u64 v = __hip_atomic_load((GAS u64*)((char*)ws + WS_STAMP) + threadIdx.x + 64 * j, RLX); m = v > m ? v : m; }
  for (int o = 32; o; o >>= 1) { const u64 v = __shfl_xor(m, o); m = v > m ? v : m; }
  u64 d = (t0 - m) * amp; if (d > 5000) d = 5000;
  const u64 t = __builtin_amdgcn_s_memrealtime();
  while (__builtin_amdgcn_s_memrealtime() - t < d) __builtin_amdgcn_s_sleep(1);
}
}

extern "C" void kernel_launch(void* const* d_in, const int* in_sizes, int n_in, void* d_out, int out_size, void* d_ws,
                              size_t ws_size, hipStream_t stream) {
  fused<<<256, 512, 0, stream>>>(d_in[0], d_in[1], d_in[2], d_in[4], d_in[5], d_in[6], d_in[7], d_out, d_ws);
  k_tail<<<1, 64, 0, stream>>>(d_ws, 0);
}
```

```cpp
#include <hip/hip_runtime.h>
#include <hip/hip_bf16.h>
#include <stdint.h>
#include <type_traits>

#define STAMPS 0
#define BEARLY 0
#define CODEPF 1
#define TOUCH_NT ""
#define TOUCH 1
#define EXP 0
#define GLA 4
#define PART16 1
#define PL 2
#define PRE 1
#define OUTFL ""
#define ROT 4

#define LAS __attribute__((address_space(3)))
#define GAS __attribute__((address_space(1)))
typedef __bf16 bf16x8 __attribute__((ext_vector_type(8)));
typedef __bf16 bf16x2 __attribute__((ext_vector_type(2)));
typedef float f32x4 __attribute__((ext_vector_type(4)));
typedef float f32x2 __attribute__((ext_vector_type(2)));
typedef unsigned u32x4 __attribute__((ext_vector_type(4)));
typedef unsigned u32x2 __attribute__((ext_vector_type(2)));
typedef short s16x4 __attribute__((ext_vector_type(4)));
typedef unsigned long long u64;

namespace {
constexpr int LDS_BYTES = 163840;
constexpr unsigned GATE_OFF = 110592, PACK_OFF = 122880, PKF_OFF = 162816, CELL0 = 122880;
constexpr unsigned OOB = 0x40000000u;
constexpr unsigned CODE_BYTES = 43000;
constexpr unsigned WS_FLAG = 0;
constexpr unsigned WS_DONE = 4096;
constexpr unsigned WS_PKF = 8192;
constexpr unsigned WS_EPOCH = 12288;
constexpr unsigned WS_G = 16384;
constexpr unsigned WS_O = 131072;
constexpr unsigned WS_GU = WS_O + 128 * 40 * 512;
constexpr unsigned WS_PW = WS_GU + 128 * 48 * 1024;
constexpr unsigned WS_END = WS_PW + 8 * 68 * 5120;
constexpr unsigned WS_STAMP = 16u << 20;
constexpr int AUX_SC1 = 16;
constexpr float LOG2E = 1.4426950408889634f;

__device__ __forceinline__ u32x4 mk_srd(const void* p, unsigned bytes) {
  const u64 a = (u64)p;
  u32x4 r; r.x = (unsigned)a; r.y = (unsigned)(a >> 32) & 0xffffu; r.z = bytes; r.w = 0x00020000u; return r;
}
__device__ __forceinline__ void dma16(unsigned lds, unsigned imm, unsigned voff, u32x4 srd, unsigned soff) {
  asm volatile("s_add_u32 m0, %0, %4\n\ts_nop 0\n\tbuffer_load_dwordx4 %1, %2, %3 offen lds"
               :: "s"(lds), "v"(voff), "s"(srd), "s"(soff), "i"(imm) : "memory", "scc");
}
__device__ __forceinline__ void dma16c(unsigned lds, unsigned imm, unsigned voff, u32x4 srd, unsigned soff) {
  asm volatile("s_add_u32 m0, %0, %4\n\ts_nop 0\n\tbuffer_load_dwordx4 %1, %2, %3 offen sc1 lds"
               :: "s"(lds), "v"(voff), "s"(srd), "s"(soff), "i"(imm) : "memory", "scc");
}
__device__ __forceinline__ void ld128(u32x4& d, unsigned voff, u32x4 srd, unsigned soff) {
  asm volatile("buffer_load_dwordx4 %0, %1, %2, %3 offen sc1" : "=v"(d) : "v"(voff), "s"(srd), "s"(soff) : "memory");
}
__device__ __forceinline__ void ld64(u32x2& d, unsigned voff, u32x4 srd, unsigned soff) {
  asm volatile("buffer_load_dwordx2 %0, %1, %2, %3 offen sc1" : "=v"(d) : "v"(voff), "s"(srd), "s"(soff) : "memory");
}
__device__ __forceinline__ void ld32(float& d, unsigned voff, u32x4 srd, unsigned soff) {
  asm volatile("buffer_load_dword %0, %1, %2, %3 offen sc1" : "=v"(d) : "v"(voff), "s"(srd), "s"(soff) : "memory");
}
__device__ __forceinline__ u32x4 lds128(unsigned a) { return *(const LAS u32x4*)a; }
__device__ __forceinline__ u32x2 ldstr(unsigned a) {
  return __builtin_bit_cast(u32x2, __builtin_amdgcn_ds_read_tr16_b64_v4i16((LAS s16x4*)a));
}
__device__ __forceinline__ u32x4 cat(u32x2 a, u32x2 b) { u32x4 r; r.x = a.x; r.y = a.y; r.z = b.x; r.w = b.y; return r; }
__device__ __forceinline__ f32x4 mfma(u32x4 a, u32x4 b, f32x4 c) {
  return __builtin_amdgcn_mfma_f32_16x16x32_bf16(__builtin_bit_cast(bf16x8, a), __builtin_bit_cast(bf16x8, b), c, 0, 0, 0);
}
__device__ __forceinline__ void mac(f32x4& c, u32x4 a, u32x4 b) {
  asm volatile("v_mfma_f32_16x16x32_bf16 %0, %1, %2, %0" : "+v"(c) : "v"(a), "v"(b));
}
__device__ __forceinline__ void mul0(f32x4& c, u32x4 a, u32x4 b) {
  asm volatile("v_mfma_f32_16x16x32_bf16 %0, %1, %2, 0" : "=&v"(c) : "v"(a), "v"(b));
}
__device__ __forceinline__ unsigned pk2(float a, float b) {
  f32x2 t = {a, b};
  return __builtin_bit_cast(unsigned, __builtin_convertvector(t, bf16x2));
}
__device__ __forceinline__ u32x2 pk4(f32x4 v) { u32x2 r; r.x = pk2(v.x, v.y); r.y = pk2(v.z, v.w); return r; }
__device__ __forceinline__ float u2f(unsigned u) { return __builtin_bit_cast(float, u); }
__device__ __forceinline__ unsigned f2u(float f) { return __builtin_bit_cast(unsigned, f); }
#define SB() __builtin_amdgcn_sched_barrier(0)
#define WAITV(n) asm volatile("s_waitcnt vmcnt(%0)" :: "i"(n) : "memory")
#define WAITVL(n) asm volatile("s_waitcnt vmcnt(%0) lgkmcnt(0)" :: "i"(n) : "memory")
#define WAITL() asm volatile("s_waitcnt lgkmcnt(0)" ::: "memory")
#define BAR() asm volatile("s_barrier" ::: "memory")
#define STPAD() do { SB(); asm volatile("s_nop 1" ::: "memory"); SB(); } while (0)
#define MFMA_DONE() do { SB(); asm volatile("s_nop 7\n\ts_nop 1" ::: "memory"); SB(); } while (0)
#define RLX __ATOMIC_RELAXED, __HIP_MEMORY_SCOPE_AGENT
__device__ __forceinline__ unsigned rfl(unsigned v) { return __builtin_amdgcn_readfirstlane(v); }
__device__ __forceinline__ unsigned opaque_lane() { unsigned l = threadIdx.x & 63; asm volatile("" : "+v"(l)); return l; }

#define AMP0 -1
#define AMP1 -1
#define AMPSEL 0
#define STAMP(i) do {} while (0)

template <int I, int N, class F>
__device__ __forceinline__ void sfor(F&& f) {
  if constexpr (I < N) { f(std::integral_constant<int, I>{}); sfor<I + 1, N>(f); }
}
template <bool RB> struct Sched {
  static constexpr int NO = RB ? 2 : 3, OB = RB ? 8 : 16;
  static constexpr int upto(int st) { return st < 2 ? 4 * (st + 1) : st < NO ? 16 : st < NO + 6 ? OB + 4 * (st - NO + 1) : OB + 27; }
  static constexpr int issued(int st, int i) {
    const int base = upto(st + PL - 1), goal = upto(st + PL), every = goal - base > 4 ? 2 : 5;
    int n = base;
    for (int k = 0; k < i; ++k) if (k % every == 1 && n < goal) ++n;
    return n;
  }
};
__device__ __forceinline__ float xmax(float v) {
  auto r = __builtin_amdgcn_permlane16_swap(f2u(v), f2u(v), false, false);
  v = fmaxf(u2f(r[0]), u2f(r[1]));
  auto s = __builtin_amdgcn_permlane32_swap(f2u(v), f2u(v), false, false);
  return fmaxf(u2f(s[0]), u2f(s[1]));
}
__device__ __forceinline__ float xsum(float v) {
  auto r = __builtin_amdgcn_permlane16_swap(f2u(v), f2u(v), false, false);
  v = u2f(r[0]) + u2f(r[1]);
  auto s = __builtin_amdgcn_permlane32_swap(f2u(v), f2u(v), false, false);
  return u2f(s[0]) + u2f(s[1]);
}

template <bool QKV, bool RB>
struct P1 {
  static constexpr int NM = QKV ? 5 : 4;
  static constexpr int NN = QKV ? 4 : (RB ? 4 : 8);
  static constexpr int NWS = 3;
  static constexpr unsigned WSL = QKV ? 16384 : 24576, XB = NWS * WSL, XSL = QKV ? 20480 : 16384;
  static constexpr unsigned WROW = QKV ? 3840 : 3072;
  static constexpr int NWP = QKV ? 2 : (RB ? 4 : 2);
  static constexpr int NXE = QKV ? (RB ? 1 : 2) : (RB ? 2 : 0), NXO = QKV ? 1 : NXE;
  static constexpr int NXP = NXO + NXE;
  static constexpr int OO = NWP + NXO, OE = NWP + NXE;
  static constexpr bool HAND = !QKV;
  static constexpr int ht = 4;
  f32x4 acc[NM][NN];
  u32x4 srdx, srdw;
  unsigned xv[4], wv[4];
  unsigned xd;
  unsigned wd0, wd1;
  unsigned xa0, wa0, wa1, wa2;
  unsigned gmask;
  __device__ __forceinline__ void tick(int) {}

  unsigned sow, sox, gof;
  unsigned kg, kw, kx, kr;
  template <bool ODD, bool X = true>
  __device__ __forceinline__ void loads(unsigned wwr, unsigned xwr) {
    if (X && NXP) {
#pragma unroll
      for (int i = ODD ? NXE : 0; i < (ODD ? NXP : NXE); ++i) dma16(xwr + xd, i * 1024, xv[i], srdx, sox);
      if (ODD) { kx += 1; sox = (kx ^ (kg >> 1)) * 128; }
    }
    dma16(wwr + wd0, 0, wv[0], srdw, sow);
    dma16(wwr + wd0, 1024, wv[1], srdw, sow);
    if (!QKV && RB) {
      dma16(wwr + wd0, 2048, wv[2], srdw, sow);
      dma16(wwr + wd1, 0, wv[3], srdw, sow);
    }
  }
  __device__ __forceinline__ void other(unsigned wwr) {
    dma16(wwr + wd1, 1024, wv[3], srdw, sow + 128);
    dma16(wwr + wd1, 2048, wv[3], srdw, sow + 256);
  }
  __device__ __forceinline__ void next() { kw += 1; sow = (kw ^ kg) * (32 * WROW); asm volatile("" : "+s"(sow), "+s"(kw)); if (NXP) asm volatile("" : "+s"(sox), "+s"(kx)); }
  __device__ __forceinline__ void stage0(int rot) {
    kg = rot; kw = 0; kx = 0; kr = 0; sow = kg * (32 * WROW); sox = (kg >> 1) * 128; gof = kg * 256;
    if (HAND && !RB) { kw = 3; sow = (3 ^ kg) * (32 * WROW); return; }
#pragma unroll
    for (int i = 0; i < NXE; ++i) dma16(xd, i * 1024, xv[i], srdx, sox);
    loads<true>(0, 0); if (HAND) other(0); next();
  }
  __device__ __forceinline__ void stage12() {
    if (HAND && !RB) return;
    loads<false>(WSL, XSL); if (HAND) other(WSL); next();
    loads<true>(2 * WSL, XSL); if (HAND) other(2 * WSL); next();
  }
  static constexpr int VPRO = OE + OO + (HAND ? 4 : 0);
  struct Ad { unsigned x, w[4]; };
  __device__ __forceinline__ void addr(Ad& a, unsigned xb, unsigned wrd) {
    a.x = xb; a.w[0] = wa0 + wrd;
    if (QKV) {
      const unsigned s2 = (gmask & (gof - wrd)) + wrd;
      kr += 1; gof = (kr ^ kg) * 256;
      a.w[1] = wa1 + s2; a.w[2] = wa2 + s2;
      asm volatile("" : "+v"(a.x), "+v"(a.w[0]), "+v"(a.w[1]), "+v"(a.w[2]));
    } else if (RB) {
      a.w[1] = a.w[0] ^ 32;
      asm volatile("" : "+v"(a.x), "+v"(a.w[0]), "+v"(a.w[1]));
    } else {
      a.w[1] = a.w[0] ^ 32; a.w[2] = a.w[0] ^ 64; a.w[3] = a.w[0] ^ 96;
      asm volatile("" : "+v"(a.x), "+v"(a.w[0]), "+v"(a.w[1]), "+v"(a.w[2]), "+v"(a.w[3]));
    }
  }
  __device__ __forceinline__ unsigned wad(const Ad& a, int n, int hi) {
    if (QKV) return n == 2 ? a.w[1 + hi] : a.w[0] + (n == 0 ? 0 : n == 1 ? 160 : 320) + hi * 1920;
    if (RB) return a.w[n & 1] + (n >> 1) * 3072 + hi * 512;
    return a.w[n & 3] + (n >> 2) * 3072 + hi * 512;
  }
  __device__ __forceinline__ void rd_all(u32x4 (&xf)[NM], u32x4 (&wf)[NN], const Ad& a) {
#pragma unroll
    for (int n = 0; n < NN; ++n) wf[n] = cat(ldstr(wad(a, n, 0)), ldstr(wad(a, n, 1)));
#pragma unroll
    for (int m = 0; m < NM; ++m) xf[m] = lds128(a.x + m * 2048);
  }
  template <bool RD = true, class L>
  __device__ __forceinline__ void mm_rd(const u32x4 (&xc)[NM], const u32x4 (&wc)[NN], u32x4 (&xn)[NM], u32x4 (&wn)[NN], const Ad& a, L&& late) {
    u32x2 lo[NN];
    constexpr bool NMAJ = !QKV && !RB;
    constexpr bool EARLY = !QKV && RB && BEARLY;
    if (EARLY && RD) { rd_all(xn, wn, a); SB(); }
    int r = 0;
#pragma unroll
    for (int i = 0; i < NM * NN; ++i) {
      const int m = NMAJ ? i % NM : i / NN, n = NMAJ ? i / NM : i % NN;
      if (QKV && m == 0 && n == 0) continue;
      if (QKV && n == 3) mac(acc[m][3], xc[m], wc[3]);
      else               mac(acc[m][n], wc[n], xc[m]);
      SB();
      if (!RD || EARLY) continue;
      if (NMAJ) {
        if (i < 4) xn[i] = lds128(a.x + i * 2048);
        else if ((i & 3) < 2) {
          const int k = (i >> 2) - 1;
          if (i & 1) wn[k] = cat(lo[k], ldstr(wad(a, k, 1))); else lo[k] = ldstr(wad(a, k, 0));
        }
      } else {
        if (r < 2 * NN) {
          const int k = r >> 1;
          if (r & 1) wn[k] = cat(lo[k], ldstr(wad(a, k, 1))); else lo[k] = ldstr(wad(a, k, 0));
        } else if (r < 2 * NN + NM) {
          xn[r - 2 * NN] = lds128(a.x + (r - 2 * NN) * 2048);
        }
        else late(r - 2 * NN - NM);
        ++r;
      }
      SB();
    }
    if (NMAJ && RD) { wn[NN - 1] = cat(ldstr(wad(a, NN - 1, 0)), ldstr(wad(a, NN - 1, 1))); SB(); }
  }
  __device__ __forceinline__ void pin(u32x4 (&xf)[NM], u32x4 (&wf)[NN]) {
    asm volatile("" : "+v"(xf[0]), "+v"(xf[1]), "+v"(xf[2]), "+v"(xf[3]), "+v"(wf[0]), "+v"(wf[1]), "+v"(wf[2]), "+v"(wf[3]));
    if (NM == 5) asm volatile("" : "+v"(xf[NM - 1]));
    if (NN == 8) asm volatile("" : "+v"(wf[NN - 4]), "+v"(wf[NN - 3]), "+v"(wf[NN - 2]), "+v"(wf[NN - 1]));
  }
  template <int TL0, int TL1, class F, class G, class H>
  __device__ __forceinline__ void run(F&& pre, G&& extra, H&& tail) {
#pragma unroll
    for (int m = 0; m < NM; ++m)
#pragma unroll
      for (int n = 0; n < NN; ++n) { acc[m][n] = f32x4{0.f, 0.f, 0.f, 0.f}; asm volatile("" : "+v"(acc[m][n])); }
    pre();
    u32x4 xA[NM], wA[NN], xB[NM], wB[NN];
    Ad a;
    addr(a, xa0, 0);
    rd_all(xA, wA, a);
    if (!RB) __builtin_amdgcn_s_setprio(1);
    unsigned w0 = 0, w1 = WSL, w2 = 2 * WSL;
    unsigned xp = 2 * XSL, x0 = 0, x1 = XSL;
    int t = 0;
    unsigned nw1, nw2, nx1;
    auto adv0 = [&]() { nw1 = w2 + WSL; if (nw1 == NWS * WSL) nw1 = 0; asm volatile("" : "+s"(nw1)); };
    auto adv1 = [&]() { nw2 = nw1 + WSL; if (nw2 == NWS * WSL) nw2 = 0; asm volatile("" : "+s"(nw2)); };
    auto adv2 = [&]() { nx1 = x1 + XSL; if (nx1 == 3 * XSL) nx1 = 0; asm volatile("" : "+s"(nx1)); };
    auto adv3 = [&]() {
      w0 = w2; w1 = nw1; w2 = nw2; xp = x0; x0 = x1; x1 = nx1; t += 2;
      asm volatile("" : "+s"(w0), "+s"(w1), "+s"(w2), "+s"(xp), "+s"(x0), "+s"(x1), "+s"(t));
    };
    auto adv = [&]() { adv0(); adv1(); adv2(); adv3(); };
    asm volatile("" : "+s"(w0), "+s"(w1), "+s"(w2), "+s"(xp), "+s"(x0), "+s"(x1), "+s"(t));
    if (QKV) asm volatile("" : "+s"(gof), "+s"(kr));
    auto none = [](int) {};
    if (RB) {
      auto help = [&](int tt, unsigned wwr) { if (HAND && __builtin_expect(tt < ht, 0)) other(wwr); extra(tt); };
      pin(xA, wA);
      SB(); WAITVL(OO + (HAND ? 2 : 0)); tick(0); BAR(); tick(1); SB();
      loads<false>(w0, xp); help(0, w0); next(); addr(a, (xa0 ^ 64) + x0, w1); SB();
      auto half = [&]() {
        mm_rd(xA, wA, xB, wB, a, [&](int i) {
          if (QKV && i == 0) adv0();
          if (QKV && i == 2) adv1();
          if (QKV && i == 4) adv2();
        });
        pin(xB, wB);
        SB(); WAITVL(OE); tick(2 * t + 2); BAR(); tick(2 * t + 3); SB();
        loads<true>(w1, xp); help(t + 1, w1); next();
        addr(a, xa0 + x1, w2);
        if (QKV) adv3(); else adv();
        SB();
        mm_rd(xB, wB, xA, wA, a, none);
      };
#pragma unroll 1
      do {
        half();
        pin(xA, wA);
        SB(); WAITVL(OO); tick(2 * t); BAR(); tick(2 * t + 1); SB();
        loads<false>(w0, xp); help(t, w0); next(); addr(a, (xa0 ^ 64) + x0, w1); SB();
        if (QKV) asm volatile("" : "+s"(gof), "+s"(kr));
      } while (t < 14);
      half();
    } else {
      addr(a, (xa0 ^ 64) + x0, w1);
      auto trip = [&](auto EARLY) {
        constexpr bool E = EARLY.value;
        pin(xA, wA);
        SB(); if (E) WAITL(); else WAITVL(OO);
        tick(2 * t); BAR(); tick(2 * t + 1); SB();
        mm_rd(xA, wA, xB, wB, a, none);
        if (E && t < ht) extra(t); else loads<false>(w0, xp);
        next(); addr(a, xa0 + x1, w2); adv0(); adv1(); adv2(); SB();
        pin(xB, wB);
        SB(); if (E) WAITL(); else WAITVL(OE);
        tick(2 * t + 2); BAR(); tick(2 * t + 3); SB();
        mm_rd(xB, wB, xA, wA, a, none);
        if (E && t < ht) extra(t + 1); else loads<true>(w1, xp);
        next(); adv3(); addr(a, (xa0 ^ 64) + x0, w1); SB();
        if (QKV) asm volatile("" : "+s"(gof), "+s"(kr));
      };
      if (HAND) {
#pragma unroll 1
        do trip(std::true_type{}); while (t < ht + 2);
      }
#pragma unroll 1
      do trip(std::false_type{}); while (t < 16);
    }
    constexpr unsigned S16 = (16 % NWS) * WSL, S17 = (17 % NWS) * WSL, S18 = (18 % NWS) * WSL, S19 = (19 % NWS) * WSL, X8 = 2 * XSL, X9 = 0;
    pin(xA, wA);
    SB(); WAITVL(OO);
    tick(32); BAR(); tick(33); SB();
    if (RB) { loads<false, false>(S16, 0); tail(0); addr(a, (xa0 ^ 64) + X8, S17); SB(); mm_rd(xA, wA, xB, wB, a, none); }
    else    { mm_rd(xA, wA, xB, wB, a, none); loads<false, false>(S16, 0); tail(0); addr(a, xa0 + X9, S18); SB(); }
    pin(xB, wB);
    SB(); WAITVL(NWP + TL0); tick(34); BAR(); tick(35); SB();
    if (RB) { tail(1); addr(a, xa0 + X9, S18); SB(); mm_rd(xB, wB, xA, wA, a, none); }
    else    { mm_rd(xB, wB, xA, wA, a, none); tail(1); addr(a, (xa0 ^ 64) + X9, S19); SB(); }
    pin(xA, wA);
    SB(); WAITVL(TL0 + TL1); tick(36); BAR(); tick(37); SB();
    if (RB) { tail(2); addr(a, (xa0 ^ 64) + X9, S19); SB(); mm_rd(xA, wA, xB, wB, a, none); }
    else    { mm_rd(xA, wA, xB, wB, a, none); tail(2); SB(); }
    pin(xB, wB);
    SB(); WAITL(); tick(38); BAR(); tick(39); SB();
    if (RB) { tail(3); SB(); mm_rd<false>(xB, wB, xA, wA, a, none); }
    else    { mm_rd<false>(xB, wB, xA, wA, a, none); tail(3); SB(); }
    if (!RB) __builtin_amdgcn_s_setprio(0);
    MFMA_DONE();
    tick(40);
  }
};

__global__ __launch_bounds__(512, 2) void fused(const void* __restrict__ x, const void* __restrict__ wqkv,
                                                const void* __restrict__ wo, const void* __restrict__ wg,
                                                const void* __restrict__ wu, const void* __restrict__ wd,
                                                const void* __restrict__ gw, void* out, void* ws) {
  __shared__ __attribute__((aligned(16))) char smem[LDS_BYTES];
  asm volatile("" :: "s"((unsigned)(size_t)(LAS char*)smem));
  asm volatile("" :: "s"(x), "s"(wqkv), "s"(wo), "s"(wg), "s"(wu), "s"(wd), "s"(gw), "s"(out), "s"(ws));
  const int tid = threadIdx.x, lane = tid & 63;
  const int w = rfl(tid >> 6);
  const int h = lane >> 4, c = lane & 15, q4 = (lane & 15) >> 2, p4 = lane & 3;
  const int b = blockIdx.x, xcd = b & 7, idx = b >> 3;
  const int rh = xcd & 1, cq = xcd >> 1;
  const int a = 8 * rh + (idx & 7);
  const int gc = 2 * cq + ((idx >> 3) & 1);
  const int rb = 4 * xcd + (idx >> 3), ct = idx & 7;
  const __amdgpu_buffer_rsrc_t rws = __builtin_amdgcn_make_buffer_rsrc(ws, 0, WS_END, 0x00020000);
  const u32x4 sws = mk_srd(ws, WS_END);
  u64 amp_t0 = 0, amp_t1 = 0;
  STAMP(0);

  if (CODEPF && w == 7) {
    u64 pc;
    asm volatile("s_getpc_b64 %0" : "=s"(pc));
    asm volatile("s_mov_b32 m0, %0\n\ts_nop 0\n\tbuffer_load_dword %1, %2, 0 offen lds"
                 :: "s"(PKF_OFF), "v"(lane < 48 ? ((idx & 7) * 48 + lane) * 128 : OOB), "s"(mk_srd((const void*)(pc & ~127ull), CODE_BYTES)) : "memory");
  }
  u32x2 ev;
  u64 tag = 0;
  auto make_tag = [&](auto NV) {
    unsigned lo, hi;
    asm volatile("s_waitcnt vmcnt(%4)\n\tv_readfirstlane_b32 %0, %2\n\tv_readfirstlane_b32 %1, %3"
                 : "=s"(lo), "=s"(hi) : "v"(ev.x), "v"(ev.y), "i"(NV.value) : "memory");
    const u64 e = ((u64)hi << 32) | lo;
    tag = (e ^ 0x9E3779B97F4A7C15ull) * 0xBF58476D1CE4E5B9ull + 0x632BE59BD9B4E019ull;
    if (tag == e) tag += 1;
  };
  const unsigned pwb = WS_PW + ct * (68 * 5120);
  const int grot = 3 * ((idx >> 3) & 1);
  auto gstep = [&](int s) { const int v = s + grot; return v >= 6 ? v - 6 : v; };
  auto cell_dma = [&](int v, int cell) {
    const unsigned so = pwb + (20 + 6 * v + gstep(2 + cell)) * 5120;
#pragma unroll
    for (int j = 0; j < 5; ++j) dma16c(cell ? v * 15360 : CELL0 + v * 5120, (cell ? cell - 1 : 0) * 5120 + j * 1024, lane * 16, sws, so + j * 1024);
  };
  auto ldw = [&](u32x4 (&d)[5], unsigned so) {
#pragma unroll
    for (int n = 0; n < 5; ++n) d[n] = __builtin_amdgcn_raw_buffer_load_b128(rws, lane * 16, so + n * 1024, AUX_SC1);
  };
  const unsigned pscr = PACK_OFF + (w & 3) * 8192;
  const int pu0 = (b - 128) * 4 + w;
  unsigned pra[2], prb[2], pn[2], pb[2];
  auto pack_issue = [&]() {
    const u64 po = (u64)wo, pd = (u64)wd;
#pragma unroll
    for (int r = 0; r < 2; ++r) {
      const int u = pu0 + 512 * r;
      const int ks = u / 10, cg = u - 10 * ks;
      const unsigned r8 = lane >> 3;
      const u32x4 srd = mk_srd((const void*)(ks < 20 ? po : pd), ks < 20 ? 640 * 1280 : 1536 * 1280);
      const unsigned base = ks < 16 ? 40 * ks : ks < 20 ? 160 * (ks - 16) + 16 : 32 * (ks - 20);
      const unsigned vr = (ks >= 16 && ks < 20) ? 20 * (r8 >> 2) + (r8 & 3) : r8;
      const unsigned vo = vr * 1280 + (lane & 7) * 16;
#pragma unroll
      for (int p = 0; p < 4; ++p) {
        const unsigned rp = ks < 16 ? 20 * (p >> 1) + 8 * (p & 1) : ks < 20 ? 40 * p : 8 * p;
        dma16(pscr, r * 4096 + p * 1024, vo, srd, u < 680 ? (base + rp) * 1280 + cg * 128 : OOB);
      }
      pra[r] = pscr + r * 4096 + (h * (ks < 16 ? 8 : 4) + q4) * 128 + p4 * 8;
      prb[r] = pra[r] + (ks < 16 ? 512 : 2048);
      pn[r] = 4 * cg; pb[r] = u < 680 ? WS_PW + ks * 5120 : OOB;
    }
    asm volatile("" : "+v"(pra[0]), "+v"(prb[0]), "+v"(pra[1]), "+v"(prb[1]));
    asm volatile("" : "+s"(pn[0]), "+s"(pb[0]), "+s"(pn[1]), "+s"(pb[1]));
  };
  const unsigned smask = w != 4 ? 0u : idx >= 16 ? 0x1040u : 0x1000u;
  auto side_b = [&](int t) {
    if (__builtin_expect(!((smask >> t) & 1), 1)) return;
    if (t == 6) {
      u32x2 tv; tv.x = (unsigned)tag; tv.y = (unsigned)(tag >> 32);
      __builtin_amdgcn_raw_buffer_store_b64(tv, rws, opaque_lane() == 0 ? WS_PKF + (b - 128) * 8 : OOB, 0, AUX_SC1);
    } else {
      dma16c(PKF_OFF, 0, opaque_lane() * 16, sws, WS_PKF);
    }
  };
  auto side_f = [&](int t) {
    {
      if (t == 0) WAITV(0);
      const u32x4 f0 = cat(ldstr(pra[0] + t * 32), ldstr(prb[0] + t * 32)), f1 = cat(ldstr(pra[1] + t * 32), ldstr(prb[1] + t * 32));
      const unsigned n0 = pn[0] + t, c0 = (n0 * 205) >> 10, n1 = pn[1] + t, c1 = (n1 * 205) >> 10;
      __builtin_amdgcn_raw_buffer_store_b128(f0, rws, lane * 16, pb[0] + n0 * 1024 + c0 * (68 * 5120 - 5120), AUX_SC1);
      STPAD();
      __builtin_amdgcn_raw_buffer_store_b128(f1, rws, lane * 16, pb[1] + n1 * 1024 + c1 * (68 * 5120 - 5120), AUX_SC1);
      STPAD();
    }
  };
  auto pk_check = [&]() {
    const u32x4 p0 = lds128(PKF_OFF + lane * 16);
    const unsigned lo = (unsigned)tag, hi = (unsigned)(tag >> 32);
    if (!__all(p0.x == lo && p0.z == lo && p0.y == hi && p0.w == hi)) {
      GAS u64* fp = (GAS u64*)((char*)ws + WS_PKF + opaque_lane() * 8);
#pragma unroll 1
      for (unsigned spin = 0; spin < (1u << 18); ++spin) {
        bool ok = true;
#pragma unroll
        for (int j = 0; j < 2; ++j) ok &= __hip_atomic_load(fp + 64 * j, RLX) == tag;
        if (__all(ok)) break;
        __builtin_amdgcn_s_sleep(2);
      }
    }
    asm volatile("" ::: "memory");
  };
  const int rot = (a + (idx >> 3)) & (ROT - 1);

  const int t0 = w < 4 ? 3 * w : 12 + 2 * (w - 4);
  float sg0, sg1, sg2;
  auto role = [&](auto RBT) {
    constexpr bool RB = decltype(RBT)::value;
    u32x4 wo_[3][5], wg_[2][5];
    asm volatile("global_load_dwordx2 %0, %1, %2 sc1" : "=v"(ev) : "v"(WS_EPOCH), "s"(ws) : "memory");
    auto tail = [&](int i) {
      if (i == 0) pk_check();
      if (RB) {
        if (i < 2) ldw(wo_[i], pwb + (2 * w + i) * 5120);
        else ldw(wg_[i - 2], pwb + (20 + 6 * w + gstep(i - 2)) * 5120);
        if (i == 1) cell_dma(w - 4, 0);
        if (i == 2) cell_dma(w, 0);
      } else if (i >= 2) {
        ldw(wo_[i - 2], pwb + (2 * w + i - 2) * 5120);
      }
    };
    if (idx < 16) {
      const int g = gc, hd = w & 3;
      constexpr int par = RB ? 1 : 0;
      using K = P1<true, RB>;
      K k;
      k.srdx = mk_srd(x, 2048 * 1280);
      k.srdw = mk_srd(wqkv, 640 * 3840);
      unsigned gv0, gv1;
      asm volatile("buffer_load_ushort %0, %2, %3, 0 offen\n\tbuffer_load_ushort %1, %2, %3, 0 offen offset:1024"
                   : "=&v"(gv0), "=&v"(gv1) : "v"(tid * 2), "s"(mk_srd(gw, 1280)) : "memory");
      const int q0 = RB ? 12 + 2 * (w - 4) : 3 * w;
      k.xd = K::XB + q0 * 1024; k.wd0 = w * 2048;
      asm volatile("" : "+s"(k.xd), "+s"(k.wd0));
#pragma unroll
      for (int i = 0; i < K::NXP; ++i) {
        const int rho = 8 * (q0 + i) + (lane >> 3);
        const int pp = rho >= 80 ? 1 : 0, u = rho - 80 * pp;
        const int tok = 128 * a - 32 + 2 * u + pp;
        const unsigned v = tok * 1280 + (((lane & 7) ^ ((rho >> 1) & 7)) << 4);
        k.xv[i] = (u >= 9 && tok >= 0) ? v : OOB;
      }
#pragma unroll
      for (int j = 0; j < 2; ++j) {
        const unsigned I = (2 * w + j) * 64 + lane;
        const unsigned kb = (I >= 248) + (I >= 488) + (I >= 736);
        const unsigned i = I - 240 * kb - 8 * ((kb + 1) >> 1);
        const unsigned r = (i * 2185u) >> 16, rem = i - r * 30;
        const unsigned seg = (rem * 6554u) >> 16, ch = rem - seg * 10;
        const unsigned v = (8 * kb + r) * 3840 + seg * 1280 + g * 160 + ch * 16;
        k.wv[j] = i < 240 ? v : OOB;
      }
      STAMP(11);
      k.stage0(rot);
      BAR();
      k.stage12();
      STAMP(12);
      k.xa0 = (K::XB + par * 5 * 2048 + c * 128 + ((h ^ (c >> 1)) << 4)) ^ ((rot & 1) << 6);
      const unsigned kbo = h * 3840 + ((h + 1) >> 1) * 128;
      k.wa0 = kbo + q4 * 480 + (20 * hd + 4 * p4) * 2;
      const bool isgate = p4 == 3;
      k.gmask = isgate ? ~0u : 0u;
      asm volatile("" : "+v"(k.gmask));
      k.wa1 = isgate ? GATE_OFF + (8 * h + q4) * 8 : kbo + q4 * 480 + p4 * 160 + (20 * hd + 16) * 2;
      k.wa2 = k.wa1 + (isgate ? 32 : 1920);
      k.template run<RB ? 5 : 0, RB ? 10 : 0>([&]() {
        asm volatile("s_waitcnt vmcnt(%2)" : "+v"(gv0), "+v"(gv1) : "i"(K::VPRO) : "memory");
        u32x2 v; v.x = gv0; v.y = 0;
        *(LAS u32x2*)(GATE_OFF + tid * 8) = v;
        if (!RB && tid < 128) { v.x = gv1; *(LAS u32x2*)(GATE_OFF + 4096 + tid * 8) = v; }
        make_tag(std::integral_constant<int, K::VPRO>{});
        WAITL();
        BAR();
        STAMP(1);
      }, [&](int t) { if (RB) side_b(t); }, tail);
      STAMP(2);
      u32x2 ql[5], kl[5], t2[5], vl[5], tt[5];
      const unsigned scr = w * 15360 + 10240;
#pragma unroll
      for (int i = 0; i < 5; ++i) {
        if (i) ql[i] = pk4(k.acc[i][0]);
        kl[i] = pk4(k.acc[i][1]); t2[i] = pk4(k.acc[i][2]); vl[i] = pk4(k.acc[i][3]);
        *(LAS u32x2*)(scr + i * 512 + c * 32 + h * 8) = t2[i];
      }
      float sgate[4];
#pragma unroll
      for (int i = 0; i < 4; ++i) {
        const unsigned v = f2u(k.acc[i + 1][2].x);
        const unsigned v2 = __builtin_amdgcn_permlane32_swap(v, v, false, false)[1];
        const unsigned v3 = __builtin_amdgcn_permlane16_swap(v2, v2, false, false)[1];
        sgate[i] = __builtin_amdgcn_exp2f(__builtin_amdgcn_fmed3f(u2f(v3), -60.f, 60.f) * LOG2E);
      }
      WAITL();
#pragma unroll
      for (int i = 0; i < 5; ++i) tt[i] = ldstr(scr + i * 512 + (4 * h + q4) * 32 + p4 * 8);
      f32x4 ba, bb;
#pragma unroll
      for (int r = 0; r < 4; ++r) {
        const int kap = 4 * h + r;
        ba[r] = kap >= c + 9 ? 0.f : -1e30f;
        bb[r] = (kap <= c && kap >= c - 7) ? 0.f : -1e30f;
      }
      const unsigned m0mask = h == 0 ? 0xffffffffu : 0u;
      u32x4 kf[5];
#pragma unroll
      for (int i = 0; i < 5; ++i) {
        auto s0 = __builtin_amdgcn_permlane16_swap(t2[i].x, t2[i].x, false, false);
        auto s1 = __builtin_amdgcn_permlane16_swap(t2[i].y, t2[i].y, false, false);
        kf[i].x = kl[i].x; kf[i].y = kl[i].y; kf[i].z = s0[1]; kf[i].w = s1[1];
      }
      constexpr float SC = 0.22360679774997896f * LOG2E;
      const unsigned ovo = WS_O + (h & 1) * (2 * 20480) + (2 * g + (hd >> 1)) * 1024 + (2 * (hd & 1) + (h >> 1)) * 256 + c * 16;
      const unsigned ovt = h == 2 ? WS_O + (32 + g) * 512 + hd * 128 + c * 8 : OOB;
      const unsigned ovg = (h == 3 && hd == 0 && g == 0) ? WS_G + c * 4 : OOB;
      u32x2 oprev;
#pragma unroll
      for (int i = 1; i < 5; ++i) {
        u32x4 qf; qf.x = ql[i].x; qf.y = ql[i].y; qf.z = t2[i].x & m0mask; qf.w = t2[i].y & m0mask;
        f32x4 bam = ba;
        if (i == 1 && a == 0) bam = f32x4{-1e30f, -1e30f, -1e30f, -1e30f};
        const f32x4 sb = mfma(kf[i], qf, bb);
        const f32x4 sa = mfma(kf[i - 1], qf, bam);
        float m = fmaxf(fmaxf(fmaxf(sb.x, sb.y), fmaxf(sb.z, sb.w)), fmaxf(fmaxf(sa.x, sa.y), fmaxf(sa.z, sa.w)));
        m = xmax(m);
        const float mc = m * SC;
        f32x4 pa, pb;
#pragma unroll
        for (int r = 0; r < 4; ++r) {
          pb[r] = __builtin_amdgcn_exp2f(sb[r] * SC - mc);
          pa[r] = __builtin_amdgcn_exp2f(sa[r] * SC - mc);
        }
        float l = ((pb.x + pb.y) + (pb.z + pb.w)) + ((pa.x + pa.y) + (pa.z + pa.w));
        l = xsum(l);
        const float rl = __builtin_amdgcn_rcpf(l);
        const u32x4 pf = cat(pk4(pa * rl), pk4(pb * rl));
        const f32x4 z = {0.f, 0.f, 0.f, 0.f};
        const u32x2 oa = pk4(mfma(cat(vl[i - 1], vl[i]), pf, z) * sgate[i - 1]);
        const f32x4 ob = mfma(cat(tt[i - 1], tt[i]), pf, z) * sgate[i - 1];
        const unsigned mt = 8 * a + 2 * (i - 1) + par;
        __builtin_amdgcn_raw_buffer_store_b64(pk4(ob), rws, ovt, mt * 20480, AUX_SC1);
        __builtin_amdgcn_raw_buffer_store_b32(f2u(sgate[i - 1]), rws, ovg, mt * 64, AUX_SC1);
        if (i & 1) {
          oprev = oa;
        } else {
          auto sx = __builtin_amdgcn_permlane16_swap(oprev.x, oa.x, false, false);
          auto sy = __builtin_amdgcn_permlane16_swap(oprev.y, oa.y, false, false);
          u32x4 o; o.x = sx[0]; o.y = sy[0]; o.z = sx[1]; o.w = sy[1];
          __builtin_amdgcn_raw_buffer_store_b128(o, rws, ovo, (mt - 2) * 20480, AUX_SC1);
          STPAD();
        }
      }
    } else {
      const int cj = gc;
      using K = P1<false, RB>;
      K k;
      const int mh = RB ? (w < 6 ? 1 : 0) : (w >> 1);
      const int up = (w >> 1) & 1, kp = w & 1;
      k.srdx = mk_srd(x, 2048 * 1280);
      k.srdw = mk_srd(up ? wu : wg, 640 * 3072);
#pragma unroll
      for (int i = 0; i < K::NXP; ++i) {
        const int rho = 8 * (4 * (w & 3) + i) + (lane >> 3);
        const int j = rho >> 4;
        const int tok = 128 * a + 32 * (j >> 1) + 2 * (rho & 15) + (j & 1);
        k.xv[i] = tok * 1280 + (((lane & 7) ^ ((rho >> 1) & 7)) << 4);
      }
      k.xd = K::XB + 4 * (w & 3) * 1024;
      {
        const int r = lane >> 3, p8 = lane & 7;
        const int s = ((r >> 1) & 1) + 2 * kp;
        k.wv[0] = (8 * kp + r) * 3072 + cj * 384 + (((((p8 >> 1) ^ s) << 1) | (p8 & 1)) << 4) + (RB ? 0 : 16 * 3072 + 128);
        k.wv[1] = k.wv[0] + 128; k.wv[2] = k.wv[0] + 256; k.wv[3] = k.wv[0] + 16 * 3072;
      }
      k.wd0 = RB ? (kp * 6 + 3 * up) * 1024 : ((kp + 2) * 6 + 3 * up + 1) * 1024;
      k.wd1 = ((kp + 2) * 6 + 3 * up) * 1024;
      STAMP(11);
      k.stage0(rot);
      BAR();
      if (!RB) pack_issue();
      k.stage12();
      STAMP(12);
      k.xa0 = (K::XB + mh * 4 * 2048 + c * 128 + ((h ^ (c >> 1)) << 4)) ^ ((rot & 1) << 6);
      {
        const int s = ((q4 >> 1) & 1) + 2 * (h & 1);
        k.wa0 = (h * 6 + (RB ? 2 : (w & 1))) * 1024 + q4 * 128 + (((RB ? 2 * (w & 1) : 0) ^ s) << 5) + p4 * 8;
      }
      k.template run<RB ? 5 : 0, RB ? 10 : 0>([&]() {
        make_tag(std::integral_constant<int, RB ? K::VPRO : 8>{});
        BAR();
        STAMP(1);
      }, [&](int t) { if (RB) side_b(t); else side_f(t); }, tail);
      STAMP(2);
      constexpr int NK = RB ? 1 : 2;
      const unsigned gso = WS_GU + ((8 * a + 4 * mh) * 48 + 6 * cj + (RB ? 4 + (w & 1) : 2 * (w & 1))) * 1024;
#pragma unroll
      for (int m = 0; m < 4; ++m) {
#pragma unroll
        for (int kk = 0; kk < NK; ++kk) {
          float v[8];
#pragma unroll
          for (int j = 0; j < 8; j += 2) {
            const float g0 = k.acc[m][2 * kk + (j >> 2)][j & 3], u0 = k.acc[m][2 * NK + 2 * kk + (j >> 2)][j & 3];
            const float g1 = k.acc[m][2 * kk + (j >> 2)][(j & 3) + 1], u1 = k.acc[m][2 * NK + 2 * kk + (j >> 2)][(j & 3) + 1];
            const float d0 = 1.f + __builtin_amdgcn_exp2f(fminf(g0 * -LOG2E, 60.f)), d1 = 1.f + __builtin_amdgcn_exp2f(fminf(g1 * -LOG2E, 60.f));
            const float r = __builtin_amdgcn_rcpf(d0 * d1);
            v[j] = g0 * u0 * (r * d1); v[j + 1] = g1 * u1 * (r * d0);
          }
          u32x4 o; o.x = pk2(v[0], v[1]); o.y = pk2(v[2], v[3]); o.z = pk2(v[4], v[5]); o.w = pk2(v[6], v[7]);
          __builtin_amdgcn_raw_buffer_store_b128(o, rws, lane * 16, gso + m * (48 * 1024) + kk * 1024, AUX_SC1);
          STPAD();
        }
      }
    }
    SB();
    asm volatile("" : "+v"(wo_[0][0]), "+v"(wo_[0][1]), "+v"(wo_[0][2]), "+v"(wo_[0][3]), "+v"(wo_[0][4]),
                      "+v"(wo_[1][0]), "+v"(wo_[1][1]), "+v"(wo_[1][2]), "+v"(wo_[1][3]), "+v"(wo_[1][4]));
    if (RB) asm volatile("" : "+v"(wg_[0][0]), "+v"(wg_[0][1]), "+v"(wg_[0][2]), "+v"(wg_[0][3]), "+v"(wg_[0][4]),
                              "+v"(wg_[1][0]), "+v"(wg_[1][1]), "+v"(wg_[1][2]), "+v"(wg_[1][3]), "+v"(wg_[1][4]));
    SB();
    if (RB && idx >= 16) { if (PRE > 0) cell_dma(w - 4, 1); if (PRE > 1) cell_dma(w, 1); if (PRE > 2) cell_dma(w - 4, 2); WAITV(5 * PRE); }
    else WAITV(0);
    STAMP(3);
    WAITL();
    BAR();
    if (!RB && w == 0 && lane < 2) {
      GAS u64* fp = (GAS u64*)((char*)ws + (lane ? WS_DONE + b * 8 : WS_FLAG + (a * 16 + (idx < 16 ? 8 : 0) + gc) * 8));
      __hip_atomic_store(fp, tag, RLX);
    }
    STAMP(4);

    SB();
    const unsigned fo = WS_FLAG + ((rb >> 1) * 16 + c) * 8;
    auto wait_flags = [&](u32x2& v, bool need) {
#pragma unroll 1
      for (unsigned spin = 0; spin < (1u << 18); ++spin) {
        if (__all(!need || (v.x == (unsigned)tag && v.y == (unsigned)(tag >> 32)))) break;
        __builtin_amdgcn_s_sleep(1);
        ld64(v, fo, sws, 0);
        asm volatile("s_waitcnt vmcnt(0)" : "+v"(v) :: "memory");
      }
      asm volatile("" ::: "memory");
    };
    u32x2 fv;
    if (RB) {
      if (idx < 16) { if (PRE > 0) cell_dma(w - 4, 1); if (PRE > 1) cell_dma(w, 1); if (PRE > 2) cell_dma(w - 4, 2); }
      if (PRE < 1) cell_dma(w - 4, 1); if (PRE < 2) cell_dma(w, 1); if (PRE < 3) cell_dma(w - 4, 2);
      cell_dma(w, 2); cell_dma(w - 4, 3); cell_dma(w, 3);
    } else {
#pragma unroll
      for (int n = 0; n < 5; ++n) ld128(wo_[2][n], lane * 16, sws, pwb + (16 + w) * 5120 + n * 1024);
#pragma unroll
      for (int j = 0; j < 10; ++j) ld128(wg_[j / 5][j % 5], lane * 16, sws, pwb + (20 + 6 * w + gstep(j / 5)) * 5120 + (j % 5) * 1024);
      if (w == 0) {
        ld64(fv, fo, sws, 0);
        asm volatile("s_waitcnt vmcnt(0)" : "+v"(fv) :: "memory");
        wait_flags(fv, c >= 8);
      }
    }
    if (RB) __builtin_amdgcn_s_setprio(1);
    BAR();
    STAMP(5);
    f32x4 acc[4][5];
    u32x4 ag[6][4];
    auto tk = [](int) {};
    tk(0);
    const unsigned gb = (EXP & 1) ? pwb : WS_GU + (4 * rb * 48 + 6 * w) * 1024;
    auto ldg = [&](int s, int m) { ld128(ag[s][m], lane * 16, sws, gb + gstep(s) * 1024 + m * (48 * 1024)); };
    unsigned ca0 = CELL0 + w * 5120 + lane * 16, ca1 = w * 15360 + lane * 16;
    asm volatile("" : "+v"(ca0), "+v"(ca1));
    {
      using S = Sched<RB>;
      constexpr int NO = S::NO, OB = S::OB;
      u32x4 af[2][4], at[4];
      u32x2 tl[4], th[4];
      const unsigned ob = WS_O + 4 * rb * 20480;
      auto req = [&](auto IT) {
        constexpr int i = IT.value;
        if constexpr (i < 8) ld128(af[i >> 2][i & 3], lane * 16, sws, ob + (i & 3) * 20480 + (2 * w + (i >> 2)) * 1024);
        else if constexpr (i < OB) ld64((i & 1) ? th[(i - 8) >> 1] : tl[(i - 8) >> 1], lane * 8, sws, ob + ((i - 8) >> 1) * 20480 + (16 + w) * 1024 + (i & 1) * 512);
        else if constexpr (i < OB + 24) ldg((i - OB) >> 2, (i - OB) & 3);
        else ld32(i == OB + 24 ? sg0 : i == OB + 25 ? sg1 : sg2, c * 4, sws, WS_G + (4 * rb + min(t0 + i - OB - 24, 19) / 5) * 64);
      };
      sfor<0, S::upto(PL - 1)>(req);
      if (RB) { __builtin_amdgcn_s_setprio(0); WAITV(S::upto(PL - 1)); }
      else if (w == 0) wait_flags(fv, c < 8);
      STAMP(6);
      BAR();
      STAMP(7);
      tk(1);
      u32x4 wc[5];
      sfor<0, NO + 6>([&](auto ST) {
        constexpr int st = ST.value, s = st - NO;
        if constexpr (st == 2 && !RB) {
          asm volatile("s_waitcnt vmcnt(%8)" : "+v"(tl[0]), "+v"(tl[1]), "+v"(tl[2]), "+v"(tl[3]), "+v"(th[0]), "+v"(th[1]), "+v"(th[2]), "+v"(th[3])
                       : "i"(S::issued(st, 0) - 16) : "memory");
#pragma unroll
          for (int m = 0; m < 4; ++m) at[m] = cat(tl[m], th[m]);
          asm volatile("s_nop 1" : "+v"(at[0]), "+v"(at[1]), "+v"(at[2]), "+v"(at[3]));
        }
        sfor<0, 20>([&](auto IT) {
          constexpr int i = IT.value, m = i / 5, n = i % 5, is = S::issued(st, i);
          if constexpr (n == 0) {
            if constexpr (s >= 0)      asm volatile("s_waitcnt vmcnt(%1)" : "+v"(ag[s][m]) : "i"(is - (OB + 4 * s + m) - 1) : "memory");
            else if constexpr (st < 2) asm volatile("s_waitcnt vmcnt(%1)" : "+v"(af[st][m]) : "i"(is - (4 * st + m) - 1) : "memory");
          }
          if constexpr (st == 0 && i == 0 && !RB)
            asm volatile("" : "+v"(wo_[2][0]), "+v"(wo_[2][1]), "+v"(wo_[2][2]), "+v"(wo_[2][3]), "+v"(wo_[2][4]),
                              "+v"(wg_[0][0]), "+v"(wg_[0][1]), "+v"(wg_[0][2]), "+v"(wg_[0][3]), "+v"(wg_[0][4]),
                              "+v"(wg_[1][0]), "+v"(wg_[1][1]), "+v"(wg_[1][2]), "+v"(wg_[1][3]), "+v"(wg_[1][4]));
          if constexpr (st == 0)     mul0(acc[m][n], wo_[0][n], af[0][m]);
          else if constexpr (s >= 0) mac(acc[m][n], wg_[s & 1][n], ag[s][m]);
          else if constexpr (st < 2) mac(acc[m][n], wo_[1][n], af[1][m]);
          else                       mac(acc[m][n], wo_[2][n], at[m]);
          SB();
          if constexpr (m == 0 && s >= 1 && s < 5) wc[n] = lds128((s > 1 ? ca1 + (s - 2) * 5120 : ca0) + n * 1024);
          if constexpr (S::issued(st, i + 1) > is) req(std::integral_constant<int, is>{});
          SB();
        });
        tk(2 + st);
        if constexpr (s >= 1 && s < 5) {
          asm volatile("" : "+v"(wc[0]), "+v"(wc[1]), "+v"(wc[2]), "+v"(wc[3]), "+v"(wc[4]));
#pragma unroll
          for (int n = 0; n < 5; ++n) wg_[(s + 1) & 1][n] = wc[n];
        }
      });
      asm volatile("s_waitcnt vmcnt(0)" : "+v"(sg0), "+v"(sg1), "+v"(sg2) :: "memory");
    }
    MFMA_DONE();
    STAMP(8);
#pragma unroll
    for (int j = 0; j < 10; ++j) *(LAS u32x4*)(ca1 + j * 1024) = cat(pk4(acc[(2 * j) / 5][(2 * j) % 5]), pk4(acc[(2 * j + 1) / 5][(2 * j + 1) % 5]));
    tk(12);
  };
  if (w < 4) role(std::false_type{}); else role(std::true_type{});

  if (b == 0 && w == 4) {
    GAS u64* dp = (GAS u64*)((char*)ws + WS_DONE + lane * 8);
#pragma unroll 1
    for (unsigned spin = 0; spin < (1u << 18); ++spin) {
      bool ok = true;
#pragma unroll
      for (int j = 0; j < 4; ++j) ok &= __hip_atomic_load(dp + j * 64, RLX) == tag;
      if (__all(ok)) break;
      __builtin_amdgcn_s_sleep(2);
    }
    if (lane == 0) __hip_atomic_store((GAS u64*)((char*)ws + WS_EPOCH), tag, RLX);
  }
  WAITL();
  BAR();
  STAMP(9);
  {
    u32x2 p[3][8];
#pragma unroll
    for (int j = 0; j < 3; ++j) {
      unsigned ra = ((t0 + j) >> 1) * 1024 + ((t0 + j) & 1) * 8 + lane * 16, rb_ = ra + 4 * 15360;
      asm volatile("" : "+v"(ra), "+v"(rb_));
#pragma unroll
      for (int v = 0; v < 8; ++v) p[j][v] = *(const LAS u32x2*)((v < 4 ? ra : rb_) + (v & 3) * 15360);
    }
    SB();
#pragma unroll
    for (int j = 0; j < 3; ++j) {
      if (j < 2 || w < 4) {
        f32x4 s = {0.f, 0.f, 0.f, 0.f};
#pragma unroll
        for (int v = 0; v < 8; ++v) {
          const unsigned px = p[j][v].x, py = p[j][v].y;
          const f32x4 q = {u2f(px << 16), u2f(px & 0xffff0000u), u2f(py << 16), u2f(py & 0xffff0000u)};
          s = v ? s + q : q;
        }
        const int tl = t0 + j, m = tl / 5, n = tl - 5 * m;
        const int tok = 64 * rb + 32 * (m >> 1) + 2 * c + (m & 1);
        const u32x2 ov = pk4(s * __builtin_amdgcn_rcpf(1.f + (j == 0 ? sg0 : j == 1 ? sg1 : sg2)));
        asm volatile("global_store_dwordx2 %0, %1, %2" OUTFL :: "v"(tok * 1280 + ct * 160 + n * 32 + h * 8), "v"(ov), "s"(out) : "memory");
      }
    }
  }
  STAMP(10);
  if (AMP0 >= 0 && (AMPSEL == 0 || (AMPSEL == 1) == (idx < 16))) {
    const u64 d = amp_t1 - amp_t0, t = __builtin_amdgcn_s_memrealtime();
    while (__builtin_amdgcn_s_memrealtime() - t < d) __builtin_amdgcn_s_sleep(1);
  }
}
}

extern "C" void kernel_launch(void* const* d_in, const int* in_sizes, int n_in, void* d_out, int out_size, void* d_ws,
                              size_t ws_size, hipStream_t stream) {
  fused<<<256, 512, 0, stream>>>(d_in[0], d_in[1], d_in[2], d_in[4], d_in[5], d_in[6], d_in[7], d_out, d_ws);
}
```

```cpp
#include <hip/hip_runtime.h>
#include <hip/hip_bf16.h>
#include <stdint.h>
#include <type_traits>

#define STAMPS 0
#define BEARLY 0
#define CODEPF 1
#define TOUCH_NT ""
#define TOUCH 1
#define EXP 0
#define GLA 4
#define PART16 1
#define PL 2
#define PRE 2
#define OUTFL ""
#define PREA 0
#define ROT 4

#define LAS __attribute__((address_space(3)))
#define GAS __attribute__((address_space(1)))
typedef __bf16 bf16x8 __attribute__((ext_vector_type(8)));
typedef __bf16 bf16x2 __attribute__((ext_vector_type(2)));
typedef float f32x4 __attribute__((ext_vector_type(4)));
typedef float f32x2 __attribute__((ext_vector_type(2)));
typedef unsigned u32x4 __attribute__((ext_vector_type(4)));
typedef unsigned u32x2 __attribute__((ext_vector_type(2)));
typedef short s16x4 __attribute__((ext_vector_type(4)));
typedef unsigned long long u64;

namespace {
constexpr int LDS_BYTES = 163840;
constexpr unsigned GATE_OFF = 110592, PACK_OFF = 122880, PKF_OFF = 162816, CELL0 = 122880;
constexpr unsigned OOB = 0x40000000u;
constexpr unsigned CODE_BYTES = 40000;
constexpr unsigned WS_FLAG = 0;
constexpr unsigned WS_DONE = 4096;
constexpr unsigned WS_PKF = 8192;
constexpr unsigned WS_EPOCH = 12288;
constexpr unsigned WS_G = 16384;
constexpr unsigned WS_O = 131072;
constexpr unsigned WS_GU = WS_O + 128 * 40 * 512;
constexpr unsigned WS_PW = WS_GU + 128 * 48 * 1024;
constexpr unsigned WS_END = WS_PW + 8 * 68 * 5120;
constexpr unsigned WS_STAMP = 16u << 20;
constexpr int AUX_SC1 = 16;
constexpr float LOG2E = 1.4426950408889634f;

__device__ __forceinline__ u32x4 mk_srd(u64 a, unsigned bytes) {
  u32x4 r; r.x = (unsigned)a; r.y = (unsigned)(a >> 32) & 0xffffu; r.z = bytes; r.w = 0x00020000u; return r;
}
__device__ __forceinline__ void dma16(unsigned lds, unsigned imm, unsigned voff, u32x4 srd, unsigned soff) {
  asm volatile("s_add_u32 m0, %0, %4\n\ts_nop 0\n\tbuffer_load_dwordx4 %1, %2, %3 offen lds"
               :: "s"(lds), "v"(voff), "s"(srd), "s"(soff), "i"(imm) : "memory", "scc");
}
__device__ __forceinline__ void dma16c(unsigned lds, unsigned imm, unsigned voff, u32x4 srd, unsigned soff) {
  asm volatile("s_add_u32 m0, %0, %4\n\ts_nop 0\n\tbuffer_load_dwordx4 %1, %2, %3 offen sc1 lds"
               :: "s"(lds), "v"(voff), "s"(srd), "s"(soff), "i"(imm) : "memory", "scc");
}
__device__ __forceinline__ void ld128(u32x4& d, unsigned voff, u32x4 srd, unsigned soff) {
  asm volatile("buffer_load_dwordx4 %0, %1, %2, %3 offen sc1" : "=v"(d) : "v"(voff), "s"(srd), "s"(soff) : "memory");
}
__device__ __forceinline__ void ld64(u32x2& d, unsigned voff, u32x4 srd, unsigned soff) {
  asm volatile("buffer_load_dwordx2 %0, %1, %2, %3 offen sc1" : "=v"(d) : "v"(voff), "s"(srd), "s"(soff) : "memory");
}
__device__ __forceinline__ void ld32(float& d, unsigned voff, u32x4 srd, unsigned soff) {
  asm volatile("buffer_load_dword %0, %1, %2, %3 offen sc1" : "=v"(d) : "v"(voff), "s"(srd), "s"(soff) : "memory");
}
__device__ __forceinline__ u32x4 lds128(unsigned a) { return *(const LAS u32x4*)a; }
__device__ __forceinline__ u32x2 ldstr(unsigned a) {
  return __builtin_bit_cast(u32x2, __builtin_amdgcn_ds_read_tr16_b64_v4i16((LAS s16x4*)a));
}
__device__ __forceinline__ u32x4 cat(u32x2 a, u32x2 b) { u32x4 r; r.x = a.x; r.y = a.y; r.z = b.x; r.w = b.y; return r; }
__device__ __forceinline__ f32x4 mfma(u32x4 a, u32x4 b, f32x4 c) {
  return __builtin_amdgcn_mfma_f32_16x16x32_bf16(__builtin_bit_cast(bf16x8, a), __builtin_bit_cast(bf16x8, b), c, 0, 0, 0);
}
__device__ __forceinline__ void mac(f32x4& c, u32x4 a, u32x4 b) {
  asm volatile("v_mfma_f32_16x16x32_bf16 %0, %1, %2, %0" : "+v"(c) : "v"(a), "v"(b));
}
__device__ __forceinline__ void mul0(f32x4& c, u32x4 a, u32x4 b) {
  asm volatile("v_mfma_f32_16x16x32_bf16 %0, %1, %2, 0" : "=&v"(c) : "v"(a), "v"(b));
}
__device__ __forceinline__ unsigned pk2(float a, float b) {
  f32x2 t = {a, b};
  return __builtin_bit_cast(unsigned, __builtin_convertvector(t, bf16x2));
}
__device__ __forceinline__ u32x2 pk4(f32x4 v) { u32x2 r; r.x = pk2(v.x, v.y); r.y = pk2(v.z, v.w); return r; }
__device__ __forceinline__ float u2f(unsigned u) { return __builtin_bit_cast(float, u); }
__device__ __forceinline__ unsigned f2u(float f) { return __builtin_bit_cast(unsigned, f); }
#define SB() __builtin_amdgcn_sched_barrier(0)
#define WAITV(n) asm volatile("s_waitcnt vmcnt(%0)" :: "i"(n) : "memory")
#define WAITVL(n) asm volatile("s_waitcnt vmcnt(%0) lgkmcnt(0)" :: "i"(n) : "memory")
#define WAITL() asm volatile("s_waitcnt lgkmcnt(0)" ::: "memory")
#define BAR() asm volatile("s_barrier" ::: "memory")
#define STPAD() do { SB(); asm volatile("s_nop 1" ::: "memory"); SB(); } while (0)
#define MFMA_DONE() do { SB(); asm volatile("s_nop 7\n\ts_nop 1" ::: "memory"); SB(); } while (0)
#define RLX __ATOMIC_RELAXED, __HIP_MEMORY_SCOPE_AGENT
__device__ __forceinline__ unsigned rfl(unsigned v) { return __builtin_amdgcn_readfirstlane(v); }
__device__ __forceinline__ unsigned opaque_lane() { unsigned l = threadIdx.x & 63; asm volatile("" : "+v"(l)); return l; }

#define AMP0 -1
#define AMP1 -1
#define AMPSEL 0
#define STAMP(i) do {} while (0)

template <int I, int N, class F>
__device__ __forceinline__ void sfor(F&& f) {
  if constexpr (I < N) { f(std::integral_constant<int, I>{}); sfor<I + 1, N>(f); }
}
template <bool RB> struct Sched {
  static constexpr int NO = RB ? 2 : 3, OB = RB ? 8 : 16;
  static constexpr int gpos(int s, int m) { return OB + 5 * s + m; }
  static constexpr int upto(int st) { return st < 2 ? 4 * (st + 1) : st < NO ? 16 : st < NO + 5 ? OB + 5 * (st - NO + 1) : st == NO + 5 ? OB + 29 : OB + 32; }
  static constexpr int issued(int st, int i) {
    const int base = upto(st + PL - 1), goal = upto(st + PL), every = goal - base > 5 ? 2 : goal - base > 4 ? 4 : 5;
    int n = base;
    for (int k = 0; k < i; ++k) if (k % every == 1 && n < goal) ++n;
    return n;
  }
};
__device__ __forceinline__ float max3(float a, float b, float c) { float r; asm("v_max3_f32 %0, %1, %2, %3" : "=v"(r) : "v"(a), "v"(b), "v"(c)); return r; }
__device__ __forceinline__ float max2(float a, float b) { float r; asm("v_max_f32 %0, %1, %2" : "=v"(r) : "v"(a), "v"(b)); return r; }

template <bool QKV, bool RB>
struct P1 {
  static constexpr int NM = QKV ? 5 : 4;
  static constexpr int NN = QKV ? 4 : (RB ? 4 : 8);
  static constexpr int NWS = 3;
  static constexpr unsigned WSL = QKV ? 16384 : 24576, XB = NWS * WSL, XSL = QKV ? 20480 : 16384;
  static constexpr unsigned WROW = QKV ? 3840 : 3072;
  static constexpr int NWP = QKV ? 2 : (RB ? 4 : 2);
  static constexpr int NXE = QKV ? (RB ? 1 : 2) : (RB ? 2 : 0), NXO = QKV ? 1 : NXE;
  static constexpr int NXP = NXO + NXE;
  static constexpr int OO = NWP + NXO, OE = NWP + NXE;
  static constexpr bool HAND = !QKV;
  static constexpr int ht = 4;
  f32x4 acc[NM][NN];
  u32x4 srdx, srdw;
  unsigned xv[4], wv[4];
  unsigned xd;
  unsigned wd0, wd1;
  unsigned xa0, wa0, wa1, wa2;
  unsigned gmask;
  __device__ __forceinline__ void tick(int) {}

  unsigned sow, sox, gof;
  unsigned kg, kw, kx, kr;
  template <bool ODD, bool X = true>
  __device__ __forceinline__ void loads(unsigned wwr, unsigned xwr) {
    if (X && NXP) {
#pragma unroll
      for (int i = ODD ? NXE : 0; i < (ODD ? NXP : NXE); ++i) dma16(xwr + xd, i * 1024, xv[i], srdx, sox);
      if (ODD) { kx += 1; sox = (kx ^ (kg >> 1)) * 128; }
    }
    dma16(wwr + wd0, 0, wv[0], srdw, sow);
    dma16(wwr + wd0, 1024, wv[1], srdw, sow);
    if (!QKV && RB) {
      dma16(wwr + wd0, 2048, wv[2], srdw, sow);
      dma16(wwr + wd1, 0, wv[3], srdw, sow);
    }
  }
  __device__ __forceinline__ void other(unsigned wwr) {
    dma16(wwr + wd1, 1024, wv[3], srdw, sow + 128);
    dma16(wwr + wd1, 2048, wv[3], srdw, sow + 256);
  }
  __device__ __forceinline__ void next() { kw += 1; sow = (kw ^ kg) * (32 * WROW); asm volatile("" : "+s"(sow), "+s"(kw)); if (NXP) asm volatile("" : "+s"(sox), "+s"(kx)); }
  __device__ __forceinline__ void stage0(int rot) {
    kg = rot; kw = 0; kx = 0; kr = 0; sow = kg * (32 * WROW); sox = (kg >> 1) * 128; gof = kg * 256;
    if (HAND && !RB) { kw = 3; sow = (3 ^ kg) * (32 * WROW); return; }
#pragma unroll
    for (int i = 0; i < NXE; ++i) dma16(xd, i * 1024, xv[i], srdx, sox);
    loads<true>(0, 0); if (HAND) other(0); next();
  }
  __device__ __forceinline__ void stage12() {
    if (HAND && !RB) return;
    loads<false>(WSL, XSL); if (HAND) other(WSL); next();
    loads<true>(2 * WSL, XSL); if (HAND) other(2 * WSL); next();
  }
  static constexpr int VPRO = OE + OO + (HAND ? 4 : 0);
  struct Ad { unsigned x, w[4]; };
  __device__ __forceinline__ void addr(Ad& a, unsigned xb, unsigned wrd) {
    a.x = xb; a.w[0] = wa0 + wrd;
    if (QKV) {
      const unsigned s2 = (gmask & (gof - wrd)) + wrd;
      kr += 1; gof = (kr ^ kg) * 256;
      a.w[1] = wa1 + s2; a.w[2] = wa2 + s2;
      asm volatile("" : "+v"(a.x), "+v"(a.w[0]), "+v"(a.w[1]), "+v"(a.w[2]));
    } else if (RB) {
      a.w[1] = a.w[0] ^ 32;
      asm volatile("" : "+v"(a.x), "+v"(a.w[0]), "+v"(a.w[1]));
    } else {
      a.w[1] = a.w[0] ^ 32; a.w[2] = a.w[0] ^ 64; a.w[3] = a.w[0] ^ 96;
      asm volatile("" : "+v"(a.x), "+v"(a.w[0]), "+v"(a.w[1]), "+v"(a.w[2]), "+v"(a.w[3]));
    }
  }
  __device__ __forceinline__ unsigned wad(const Ad& a, int n, int hi) {
    if (QKV) return n == 2 ? a.w[1 + hi] : a.w[0] + (n == 0 ? 0 : n == 1 ? 160 : 320) + hi * 1920;
    if (RB) return a.w[n & 1] + (n >> 1) * 3072 + hi * 512;
    return a.w[n & 3] + (n >> 2) * 3072 + hi * 512;
  }
  __device__ __forceinline__ void rd_all(u32x4 (&xf)[NM], u32x4 (&wf)[NN], const Ad& a) {
#pragma unroll
    for (int n = 0; n < NN; ++n) wf[n] = cat(ldstr(wad(a, n, 0)), ldstr(wad(a, n, 1)));
#pragma unroll
    for (int m = 0; m < NM; ++m) xf[m] = lds128(a.x + m * 2048);
  }
  template <bool RD = true, class L>
  __device__ __forceinline__ void mm_rd(const u32x4 (&xc)[NM], const u32x4 (&wc)[NN], u32x4 (&xn)[NM], u32x4 (&wn)[NN], const Ad& a, L&& late) {
    u32x2 lo[NN];
    constexpr bool NMAJ = !QKV && !RB;
    constexpr bool EARLY = !QKV && RB && BEARLY;
    if (EARLY && RD) { rd_all(xn, wn, a); SB(); }
    int r = 0;
#pragma unroll
    for (int i = 0; i < NM * NN; ++i) {
      const int m = NMAJ ? i % NM : i / NN, n = NMAJ ? i / NM : i % NN;
      if (QKV && m == 0 && n == 0) continue;
      if (QKV && n == 3) mac(acc[m][3], xc[m], wc[3]);
      else               mac(acc[m][n], wc[n], xc[m]);
      SB();
      if (!RD || EARLY) continue;
      if (NMAJ) {
        if (i < 4) xn[i] = lds128(a.x + i * 2048);
        else if ((i & 3) < 2) {
          const int k = (i >> 2) - 1;
          if (i & 1) wn[k] = cat(lo[k], ldstr(wad(a, k, 1))); else lo[k] = ldstr(wad(a, k, 0));
        }
      } else {
        if (r < 2 * NN) {
          const int k = r >> 1;
          if (r & 1) wn[k] = cat(lo[k], ldstr(wad(a, k, 1))); else lo[k] = ldstr(wad(a, k, 0));
        } else if (r < 2 * NN + NM) {
          xn[r - 2 * NN] = lds128(a.x + (r - 2 * NN) * 2048);
        }
        else late(r - 2 * NN - NM);
        ++r;
      }
      SB();
    }
    if (NMAJ && RD) { wn[NN - 1] = cat(ldstr(wad(a, NN - 1, 0)), ldstr(wad(a, NN - 1, 1))); SB(); }
  }
  __device__ __forceinline__ void pin(u32x4 (&xf)[NM], u32x4 (&wf)[NN]) {
    asm volatile("" : "+v"(xf[0]), "+v"(xf[1]), "+v"(xf[2]), "+v"(xf[3]), "+v"(wf[0]), "+v"(wf[1]), "+v"(wf[2]), "+v"(wf[3]));
    if (NM == 5) asm volatile("" : "+v"(xf[NM - 1]));
    if (NN == 8) asm volatile("" : "+v"(wf[NN - 4]), "+v"(wf[NN - 3]), "+v"(wf[NN - 2]), "+v"(wf[NN - 1]));
  }
  __device__ __forceinline__ void zero() {
#pragma unroll
    for (int m = 0; m < NM; ++m)
#pragma unroll
      for (int n = 0; n < NN; ++n) { acc[m][n] = f32x4{0.f, 0.f, 0.f, 0.f}; asm volatile("" : "+v"(acc[m][n])); }
  }
  template <int TL0, int TL1, class F, class G, class H>
  __device__ __forceinline__ void run(F&& pre, G&& extra, H&& tail) {
    pre();
    u32x4 xA[NM], wA[NN], xB[NM], wB[NN];
    Ad a;
    addr(a, xa0, 0);
    rd_all(xA, wA, a);
    if (!RB) __builtin_amdgcn_s_setprio(1);
    unsigned w0 = 0, w1 = WSL, w2 = 2 * WSL;
    unsigned xp = 2 * XSL, x0 = 0, x1 = XSL;
    int t = 0;
    unsigned nw1, nw2, nx1;
    auto adv0 = [&]() { nw1 = w2 + WSL; if (nw1 == NWS * WSL) nw1 = 0; asm volatile("" : "+s"(nw1)); };
    auto adv1 = [&]() { nw2 = nw1 + WSL; if (nw2 == NWS * WSL) nw2 = 0; asm volatile("" : "+s"(nw2)); };
    auto adv2 = [&]() { nx1 = x1 + XSL; if (nx1 == 3 * XSL) nx1 = 0; asm volatile("" : "+s"(nx1)); };
    auto adv3 = [&]() {
      w0 = w2; w1 = nw1; w2 = nw2; xp = x0; x0 = x1; x1 = nx1; t += 2;
      asm volatile("" : "+s"(w0), "+s"(w1), "+s"(w2), "+s"(xp), "+s"(x0), "+s"(x1), "+s"(t));
    };
    auto adv = [&]() { adv0(); adv1(); adv2(); adv3(); };
    asm volatile("" : "+s"(w0), "+s"(w1), "+s"(w2), "+s"(xp), "+s"(x0), "+s"(x1), "+s"(t));
    if (QKV) asm volatile("" : "+s"(gof), "+s"(kr));
    auto none = [](int) {};
    if (RB) {
      auto help = [&](int tt, unsigned wwr) { if (HAND && __builtin_expect(tt < ht, 0)) other(wwr); extra(tt); };
      pin(xA, wA);
      SB(); WAITVL(OO + (HAND ? 2 : 0)); tick(0); BAR(); tick(1); SB();
      loads<false>(w0, xp); help(0, w0); next(); addr(a, (xa0 ^ 64) + x0, w1); SB();
      auto half = [&]() {
        mm_rd(xA, wA, xB, wB, a, [&](int i) {
          if (QKV && i == 0) adv0();
          if (QKV && i == 2) adv1();
          if (QKV && i == 4) adv2();
        });
        pin(xB, wB);
        SB(); WAITVL(OE); tick(2 * t + 2); BAR(); tick(2 * t + 3); SB();
        loads<true>(w1, xp); help(t + 1, w1); next();
        addr(a, xa0 + x1, w2);
        if (QKV) adv3(); else adv();
        SB();
        mm_rd(xB, wB, xA, wA, a, none);
      };
#pragma unroll 1
      do {
        half();
        pin(xA, wA);
        SB(); WAITVL(OO); tick(2 * t); BAR(); tick(2 * t + 1); SB();
        loads<false>(w0, xp); help(t, w0); next(); addr(a, (xa0 ^ 64) + x0, w1); SB();
        if (QKV) asm volatile("" : "+s"(gof), "+s"(kr));
      } while (t < 14);
      half();
    } else {
      addr(a, (xa0 ^ 64) + x0, w1);
      auto trip = [&](auto EARLY) {
        constexpr bool E = EARLY.value;
        pin(xA, wA);
        SB(); if (E) WAITL(); else WAITVL(OO);
        tick(2 * t); BAR(); tick(2 * t + 1); SB();
        mm_rd(xA, wA, xB, wB, a, none);
        if (E && t < ht) extra(t); else loads<false>(w0, xp);
        next(); addr(a, xa0 + x1, w2); adv0(); adv1(); adv2(); SB();
        pin(xB, wB);
        SB(); if (E) WAITL(); else WAITVL(OE);
        tick(2 * t + 2); BAR(); tick(2 * t + 3); SB();
        mm_rd(xB, wB, xA, wA, a, none);
        if (E && t < ht) extra(t + 1); else loads<true>(w1, xp);
        next(); adv3(); addr(a, (xa0 ^ 64) + x0, w1); SB();
        if (QKV) asm volatile("" : "+s"(gof), "+s"(kr));
      };
      if (HAND) {
#pragma unroll 1
        do trip(std::true_type{}); while (t < ht + 2);
      }
#pragma unroll 1
      do trip(std::false_type{}); while (t < 16);
    }
    constexpr unsigned S16 = (16 % NWS) * WSL, S17 = (17 % NWS) * WSL, S18 = (18 % NWS) * WSL, S19 = (19 % NWS) * WSL, X8 = 2 * XSL, X9 = 0;
    pin(xA, wA);
    SB(); WAITVL(OO);
    tick(32); BAR(); tick(33); SB();
    if (RB) { loads<false, false>(S16, 0); tail(0); addr(a, (xa0 ^ 64) + X8, S17); SB(); mm_rd(xA, wA, xB, wB, a, none); }
    else    { mm_rd(xA, wA, xB, wB, a, none); loads<false, false>(S16, 0); tail(0); addr(a, xa0 + X9, S18); SB(); }
    pin(xB, wB);
    SB(); WAITVL(NWP + TL0); tick(34); BAR(); tick(35); SB();
    if (RB) { tail(1); addr(a, xa0 + X9, S18); SB(); mm_rd(xB, wB, xA, wA, a, none); }
    else    { mm_rd(xB, wB, xA, wA, a, none); tail(1); addr(a, (xa0 ^ 64) + X9, S19); SB(); }
    pin(xA, wA);
    SB(); WAITVL(TL0 + TL1); tick(36); BAR(); tick(37); SB();
    if (RB) { tail(2); addr(a, (xa0 ^ 64) + X9, S19); SB(); mm_rd(xA, wA, xB, wB, a, none); }
    else    { mm_rd(xA, wA, xB, wB, a, none); tail(2); SB(); }
    pin(xB, wB);
    SB(); WAITL(); tick(38); BAR(); tick(39); SB();
    if (RB) { tail(3); SB(); mm_rd<false>(xB, wB, xA, wA, a, none); }
    else    { mm_rd<false>(xB, wB, xA, wA, a, none); tail(3); SB(); }
    if (!RB) __builtin_amdgcn_s_setprio(0);
    MFMA_DONE();
    tick(40);
  }
};

__global__ __launch_bounds__(512, 2) void fused(const void* x_, const void* wqkv_, const void* wo_p, const void* wg_p,
                                                const void* wu_p, const void* wd_p, const void* gw_, void* out_, void* ws_) {
  __shared__ __attribute__((aligned(16))) char smem[LDS_BYTES];
  asm volatile("" :: "s"((unsigned)(size_t)(LAS char*)smem));
  const int tid = threadIdx.x, lane = tid & 63;
  const int w = rfl(tid >> 6);
  const int h = lane >> 4, c = lane & 15, q4 = (lane & 15) >> 2, p4 = lane & 3;
  const int b = blockIdx.x, xcd = b & 7, idx = b >> 3;
  const int rh = xcd & 1, cq = xcd >> 1;
  const int a = 8 * rh + (idx & 7);
  const int gc = 2 * cq + ((idx >> 3) & 1);
  const int rb = 4 * xcd + (idx >> 3), ct = idx & 7;
  u64 x, wqkv, wo, wg, wu, wd, gw;
  char* ws;
  __amdgpu_buffer_rsrc_t rws;
  u32x4 sws;
  auto bind = [&]() {
    x = (u64)x_; wqkv = (u64)wqkv_; wo = (u64)wo_p; wg = (u64)wg_p; wu = (u64)wu_p; wd = (u64)wd_p; gw = (u64)gw_;
    u64 pw = (u64)ws_;
    asm volatile("" : "+s"(x), "+s"(wqkv), "+s"(wo), "+s"(wg), "+s"(wu), "+s"(wd), "+s"(gw), "+s"(pw));
    ws = (char*)pw;
    rws = __builtin_amdgcn_make_buffer_rsrc(ws, 0, WS_END, 0x00020000);
    sws = mk_srd(pw, WS_END);
  };
  u64 amp_t0 = 0, amp_t1 = 0;

  if (CODEPF) {
    u64 pc;
    asm volatile("s_getpc_b64 %0" : "=s"(pc));
    asm volatile("s_mov_b32 m0, %0\n\ts_nop 0\n\tbuffer_load_dword %1, %2, 0 offen lds"
                 :: "s"(PKF_OFF), "v"(lane < 6 ? ((idx & 7) * 48 + w * 6 + lane) * 128 : OOB), "s"(mk_srd(pc & ~127ull, CODE_BYTES)) : "memory");
  }
  u32x2 ev;
  u64 tag = 0;
  auto make_tag = [&](auto NV) {
    unsigned lo, hi;
    asm volatile("s_waitcnt vmcnt(%4)\n\tv_readfirstlane_b32 %0, %2\n\tv_readfirstlane_b32 %1, %3"
                 : "=s"(lo), "=s"(hi) : "v"(ev.x), "v"(ev.y), "i"(NV.value) : "memory");
    const u64 e = ((u64)hi << 32) | lo;
    tag = (e ^ 0x9E3779B97F4A7C15ull) * 0xBF58476D1CE4E5B9ull + 0x632BE59BD9B4E019ull;
    if (tag == e) tag += 1;
  };
  const unsigned pwb = WS_PW + ct * (68 * 5120);
  const int grot = 3 * ((idx >> 3) & 1);
  auto gstep = [&](int s) { const int v = s + grot; return v >= 6 ? v - 6 : v; };
  auto cell_dma = [&](int v, int cell) {
    const unsigned so = pwb + (20 + 6 * v + gstep(2 + cell)) * 5120;
#pragma unroll
    for (int j = 0; j < 5; ++j) dma16c(cell ? v * 15360 : CELL0 + v * 5120, (cell ? cell - 1 : 0) * 5120 + j * 1024, lane * 16, sws, so + j * 1024);
  };
  auto ldw = [&](u32x4 (&d)[5], unsigned so) {
#pragma unroll
    for (int n = 0; n < 5; ++n) d[n] = __builtin_amdgcn_raw_buffer_load_b128(rws, lane * 16, so + n * 1024, AUX_SC1);
  };
  const unsigned pscr = PACK_OFF + (w & 3) * 8192;
  const int pu0 = (b - 128) * 4 + w;
  unsigned pra[2], prb[2], pn[2], pb[2];
  auto pack_issue = [&]() {
    const u64 po = wo, pd = wd;
#pragma unroll
    for (int r = 0; r < 2; ++r) {
      const int u = pu0 + 512 * r;
      const int ks = u / 10, cg = u - 10 * ks;
      const unsigned r8 = lane >> 3;
      const u32x4 srd = mk_srd(ks < 20 ? po : pd, ks < 20 ? 640 * 1280 : 1536 * 1280);
      const unsigned base = ks < 16 ? 40 * ks : ks < 20 ? 160 * (ks - 16) + 16 : 32 * (ks - 20);
      const unsigned vr = (ks >= 16 && ks < 20) ? 20 * (r8 >> 2) + (r8 & 3) : r8;
      const unsigned vo = vr * 1280 + (lane & 7) * 16;
#pragma unroll
      for (int p = 0; p < 4; ++p) {
        const unsigned rp = ks < 16 ? 20 * (p >> 1) + 8 * (p & 1) : ks < 20 ? 40 * p : 8 * p;
        dma16(pscr, r * 4096 + p * 1024, vo, srd, u < 680 ? (base + rp) * 1280 + cg * 128 : OOB);
      }
      pra[r] = pscr + r * 4096 + (h * (ks < 16 ? 8 : 4) + q4) * 128 + p4 * 8;
      prb[r] = pra[r] + (ks < 16 ? 512 : 2048);
      pn[r] = 4 * cg; pb[r] = u < 680 ? WS_PW + ks * 5120 : OOB;
    }
    asm volatile("" : "+v"(pra[0]), "+v"(prb[0]), "+v"(pra[1]), "+v"(prb[1]));
    asm volatile("" : "+s"(pn[0]), "+s"(pb[0]), "+s"(pn[1]), "+s"(pb[1]));
  };
  const unsigned smask = w != 4 ? 0u : idx >= 16 ? 0x1040u : 0x1000u;
  auto side_b = [&](int t) {
    if (__builtin_expect(!((smask >> t) & 1), 1)) return;
    if (t == 6) {
      u32x2 tv; tv.x = (unsigned)tag; tv.y = (unsigned)(tag >> 32);
      __builtin_amdgcn_raw_buffer_store_b64(tv, rws, opaque_lane() == 0 ? WS_PKF + (b - 128) * 8 : OOB, 0, AUX_SC1);
    } else {
      dma16c(PKF_OFF, 0, opaque_lane() * 16, sws, WS_PKF);
    }
  };
  auto side_f = [&](int t) {
    {
      if (t == 0) WAITV(0);
      const u32x4 f0 = cat(ldstr(pra[0] + t * 32), ldstr(prb[0] + t * 32)), f1 = cat(ldstr(pra[1] + t * 32), ldstr(prb[1] + t * 32));
      const unsigned n0 = pn[0] + t, c0 = (n0 * 205) >> 10, n1 = pn[1] + t, c1 = (n1 * 205) >> 10;
      __builtin_amdgcn_raw_buffer_store_b128(f0, rws, lane * 16, pb[0] + n0 * 1024 + c0 * (68 * 5120 - 5120), AUX_SC1);
      STPAD();
      __builtin_amdgcn_raw_buffer_store_b128(f1, rws, lane * 16, pb[1] + n1 * 1024 + c1 * (68 * 5120 - 5120), AUX_SC1);
      STPAD();
    }
  };
  auto pk_check = [&]() {
    const u32x4 p0 = lds128(PKF_OFF + lane * 16);
    const unsigned lo = (unsigned)tag, hi = (unsigned)(tag >> 32);
    if (!__all(p0.x == lo && p0.z == lo && p0.y == hi && p0.w == hi)) {
      GAS u64* fp = (GAS u64*)((char*)ws + WS_PKF + opaque_lane() * 8);
#pragma unroll 1
      for (unsigned spin = 0; spin < (1u << 18); ++spin) {
        bool ok = true;
#pragma unroll
        for (int j = 0; j < 2; ++j) ok &= __hip_atomic_load(fp + 64 * j, RLX) == tag;
        if (__all(ok)) break;
        __builtin_amdgcn_s_sleep(2);
      }
    }
    asm volatile("" ::: "memory");
  };
  const int rot = (a + 2 * ((idx >> 3) & 1) + (idx >> 4)) & (ROT - 1);

  const int t0 = 2 * w + min(w, 4);
  float sg0, sg1, sg2;
  auto role = [&](auto RBT) {
    constexpr bool RB = decltype(RBT)::value;
    u32x4 wo_[3][5], wg_[2][5];
    auto tail = [&](int i) {
      if (i == 0) pk_check();
      if (RB) {
        if (i < 2) ldw(wo_[i], pwb + (2 * w + i) * 5120);
        else ldw(wg_[i - 2], pwb + (20 + 6 * w + gstep(i - 2)) * 5120);
        if (i == 1) cell_dma(w - 4, 0);
        if (i == 2) cell_dma(w, 0);
      } else if (i >= 2) {
        ldw(wo_[i - 2], pwb + (2 * w + i - 2) * 5120);
      }
    };
    auto late_w = [&]() {
#pragma unroll
      for (int n = 0; n < 5; ++n) ld128(wo_[2][n], lane * 16, sws, pwb + (16 + w) * 5120 + n * 1024);
#pragma unroll
      for (int j = 0; j < 10; ++j) ld128(wg_[j / 5][j % 5], lane * 16, sws, pwb + (20 + 6 * w + gstep(j / 5)) * 5120 + (j % 5) * 1024);
    };
    if (idx < 16) {
      const int g = gc, hd = w & 3;
      constexpr int par = RB ? 1 : 0;
      using K = P1<true, RB>;
      K k;
      const int q0 = RB ? 12 + 2 * (w - 4) : 3 * w;
      k.xd = K::XB + q0 * 1024; k.wd0 = w * 2048;
      asm volatile("" : "+s"(k.xd), "+s"(k.wd0));
#pragma unroll
      for (int i = 0; i < K::NXP; ++i) {
        const int rho = 8 * (q0 + i) + (lane >> 3);
        const int pp = rho >= 80 ? 1 : 0, u = rho - 80 * pp;
        const int tok = 128 * a - 32 + 2 * u + pp;
        const unsigned v = tok * 1280 + (((lane & 7) ^ ((rho >> 1) & 7)) << 4);
        k.xv[i] = (u >= 9 && tok >= 0) ? v : OOB;
      }
#pragma unroll
      for (int j = 0; j < 2; ++j) {
        const unsigned I = (2 * w + j) * 64 + lane;
        const unsigned kb = (I >= 248) + (I >= 488) + (I >= 736);
        const unsigned i = I - 240 * kb - 8 * ((kb + 1) >> 1);
        const unsigned r = (i * 2185u) >> 16, rem = i - r * 30;
        const unsigned seg = (rem * 6554u) >> 16, ch = rem - seg * 10;
        const unsigned v = (8 * kb + r) * 3840 + seg * 1280 + g * 160 + ch * 16;
        k.wv[j] = i < 240 ? v : OOB;
      }
      k.xa0 = (K::XB + par * 5 * 2048 + c * 128 + ((h ^ (c >> 1)) << 4)) ^ ((rot & 1) << 6);
      const unsigned kbo = h * 3840 + ((h + 1) >> 1) * 128;
      k.wa0 = kbo + q4 * 480 + (20 * hd + 4 * p4) * 2;
      const bool isgate = p4 == 3;
      k.gmask = isgate ? ~0u : 0u;
      asm volatile("" : "+v"(k.gmask));
      k.wa1 = isgate ? GATE_OFF + (8 * h + q4) * 8 : kbo + q4 * 480 + p4 * 160 + (20 * hd + 16) * 2;
      k.wa2 = k.wa1 + (isgate ? 32 : 1920);
      f32x4 ba, bb, ba1;
#pragma unroll
      for (int r = 0; r < 4; ++r) {
        const int kap = 4 * h + r;
        ba[r] = kap >= c + 9 ? 0.f : -1e30f;
        bb[r] = (kap <= c && kap >= c - 7) ? 0.f : -1e30f;
        ba1[r] = a == 0 ? -1e30f : ba[r];
      }
      unsigned m0mask = h == 0 ? 0xffffffffu : 0u;
      unsigned scw = w * 15360 + 10240 + c * 32 + h * 8, sck = w * 15360 + 10240 + c * 32 + 8,
               sct = w * 15360 + 10240 + (4 * h + q4) * 32 + p4 * 8;
      unsigned ovo = WS_O + (h & 1) * (2 * 20480) + (2 * g + (hd >> 1)) * 1024 + (2 * (hd & 1) + (h >> 1)) * 256 + c * 16;
      unsigned ovt = h == 2 ? WS_O + (32 + g) * 512 + hd * 128 + c * 8 : OOB;
      asm volatile("" : "+v"(ba), "+v"(bb), "+v"(ba1), "+v"(m0mask), "+v"(scw), "+v"(sck), "+v"(sct), "+v"(ovo), "+v"(ovt));
      asm volatile("" : "+v"(k.xv[0]), "+v"(k.xv[1]), "+v"(k.xv[K::NXP - 1]), "+v"(k.wv[0]), "+v"(k.wv[1]), "+v"(k.xa0), "+v"(k.wa0), "+v"(k.wa1), "+v"(k.wa2));
      k.zero();
      bind();
      STAMP(0);
      k.srdx = mk_srd(x, 2048 * 1280);
      k.srdw = mk_srd(wqkv, 640 * 3840);
      unsigned gv0, gv1;
      asm volatile("global_load_dwordx2 %0, %1, %2 sc1" : "=v"(ev) : "v"(WS_EPOCH), "s"(ws) : "memory");
      asm volatile("buffer_load_ushort %0, %2, %3, 0 offen\n\tbuffer_load_ushort %1, %2, %3, 0 offen offset:1024"
                   : "=&v"(gv0), "=&v"(gv1) : "v"(tid * 2), "s"(mk_srd(gw, 1280)) : "memory");
      STAMP(11);
      k.stage0(rot);
      BAR();
      k.stage12();
      STAMP(12);
      k.template run<RB ? 5 : 0, RB ? 10 : 0>([&]() {
        asm volatile("s_waitcnt vmcnt(%2)" : "+v"(gv0), "+v"(gv1) : "i"(K::VPRO) : "memory");
        u32x2 v; v.x = gv0; v.y = 0;
        *(LAS u32x2*)(GATE_OFF + tid * 8) = v;
        if (!RB && tid < 128) { v.x = gv1; *(LAS u32x2*)(GATE_OFF + 4096 + tid * 8) = v; }
        make_tag(std::integral_constant<int, K::VPRO>{});
        WAITL();
        BAR();
        STAMP(1);
      }, [&](int t) { if (RB) side_b(t); }, tail);
      STAMP(2);
      u32x2 ql[5], kl[5], t2[5], vl[5], tt[5], k4[5];
      unsigned sb16[4];
#pragma unroll
      for (int i = 0; i < 5; ++i) { t2[i] = pk4(k.acc[i][2]); *(LAS u32x2*)(scw + i * 512) = t2[i]; }
#pragma unroll
      for (int i = 0; i < 5; ++i) { if (i) ql[i] = pk4(k.acc[i][0]); kl[i] = pk4(k.acc[i][1]); }
      WAITL();
#pragma unroll
      for (int i = 0; i < 5; ++i) k4[i] = *(const LAS u32x2*)(sck + i * 512);
#pragma unroll
      for (int i = 0; i < 4; ++i) sb16[i] = *(const LAS unsigned*)(sck + (i + 1) * 512 + 16);
#pragma unroll
      for (int i = 0; i < 5; ++i) tt[i] = ldstr(sct + i * 512);
#pragma unroll
      for (int i = 0; i < 5; ++i) vl[i] = pk4(k.acc[i][3]);
      constexpr float SC = 0.22360679774997896f * LOG2E;
      f32x4 sa[4], sb[4];
#pragma unroll
      for (int i = 1; i < 5; ++i) {
        u32x4 qf; qf.x = ql[i].x; qf.y = ql[i].y; qf.z = t2[i].x & m0mask; qf.w = t2[i].y & m0mask;
        sb[i - 1] = mfma(cat(kl[i], k4[i]), qf, bb);
        sa[i - 1] = mfma(cat(kl[i - 1], k4[i - 1]), qf, i == 1 ? ba1 : ba);
      }
      asm volatile("s_nop 7\n\ts_nop 0" : "+v"(sa[0]), "+v"(sa[1]), "+v"(sa[2]), "+v"(sa[3]), "+v"(sb[0]), "+v"(sb[1]), "+v"(sb[2]), "+v"(sb[3]));
      float mx[4], es[4];
#pragma unroll
      for (int i = 0; i < 4; ++i)
        mx[i] = max3(max3(sb[i].x, sb[i].y, sb[i].z), max3(sb[i].w, sa[i].x, sa[i].y), max2(sa[i].z, sa[i].w));
#pragma unroll
      for (int i = 0; i < 4; ++i) {
        auto r = __builtin_amdgcn_permlane16_swap(f2u(mx[i]), f2u(mx[i]), false, false);
        mx[i] = max2(u2f(r[0]), u2f(r[1]));
      }
#pragma unroll
      for (int i = 0; i < 4; ++i) {
        auto r = __builtin_amdgcn_permlane32_swap(f2u(mx[i]), f2u(mx[i]), false, false);
        mx[i] = max2(u2f(r[0]), u2f(r[1])) * SC;
        es[i] = __builtin_amdgcn_exp2f(__builtin_amdgcn_fmed3f(u2f(sb16[i] << 16), -60.f, 60.f) * LOG2E);
      }
      u32x4 pf[4];
#pragma unroll
      for (int i = 0; i < 4; ++i) {
        f32x4 pa, pb;
#pragma unroll
        for (int r = 0; r < 4; ++r) {
          pa[r] = __builtin_amdgcn_exp2f(sa[i][r] * SC - mx[i]);
          pb[r] = __builtin_amdgcn_exp2f(sb[i][r] * SC - mx[i]);
        }
        pf[i] = cat(pk4(pa), pk4(pb));
      }
      const f32x4 z = {0.f, 0.f, 0.f, 0.f};
      const u32x4 ones = {0x3f803f80u, 0x3f803f80u, 0x3f803f80u, 0x3f803f80u};
      f32x4 ls[4], oa[4], ob[4];
#pragma unroll
      for (int i = 0; i < 4; ++i) {
        ls[i] = mfma(ones, pf[i], z);
        oa[i] = mfma(cat(vl[i], vl[i + 1]), pf[i], z);
        ob[i] = mfma(cat(tt[i], tt[i + 1]), pf[i], z);
      }
      const unsigned mt0 = 8 * a + par;
      u32x2 op[4];
#pragma unroll
      for (int i = 0; i < 4; ++i) {
        const float f = es[i] * __builtin_amdgcn_rcpf(ls[i].x);
        op[i] = pk4(oa[i] * f);
        __builtin_amdgcn_raw_buffer_store_b64(pk4(ob[i] * f), rws, ovt, (mt0 + 2 * i) * 20480, AUX_SC1);
        if (i & 1) {
          auto sx = __builtin_amdgcn_permlane16_swap(op[i - 1].x, op[i].x, false, false);
          auto sy = __builtin_amdgcn_permlane16_swap(op[i - 1].y, op[i].y, false, false);
          u32x4 o; o.x = sx[0]; o.y = sy[0]; o.z = sx[1]; o.w = sy[1];
          __builtin_amdgcn_raw_buffer_store_b128(o, rws, ovo, (mt0 + 2 * i - 2) * 20480, AUX_SC1);
          STPAD();
        }
      }
      if (g == 0 && hd == 0) {
        const float e = h == 0 ? es[0] : h == 1 ? es[1] : h == 2 ? es[2] : es[3];
        __builtin_amdgcn_raw_buffer_store_b32(f2u(e), rws, WS_G + h * 128 + c * 4, mt0 * 64, AUX_SC1);
      }
    } else {
      const int cj = gc;
      using K = P1<false, RB>;
      K k;
      const int mh = RB ? (w < 6 ? 1 : 0) : (w >> 1);
      const int up = (w >> 1) & 1, kp = w & 1;
#pragma unroll
      for (int i = 0; i < K::NXP; ++i) {
        const int rho = 8 * (4 * (w & 3) + i) + (lane >> 3);
        const int j = rho >> 4;
        const int tok = 128 * a + 32 * (j >> 1) + 2 * (rho & 15) + (j & 1);
        k.xv[i] = tok * 1280 + (((lane & 7) ^ ((rho >> 1) & 7)) << 4);
      }
      k.xd = K::XB + 4 * (w & 3) * 1024;
      {
        const int r = lane >> 3, p8 = lane & 7;
        const int s = ((r >> 1) & 1) + 2 * kp;
        k.wv[0] = (8 * kp + r) * 3072 + cj * 384 + (((((p8 >> 1) ^ s) << 1) | (p8 & 1)) << 4) + (RB ? 0 : 16 * 3072 + 128);
        k.wv[1] = k.wv[0] + 128; k.wv[2] = k.wv[0] + 256; k.wv[3] = k.wv[0] + 16 * 3072;
      }
      k.wd0 = RB ? (kp * 6 + 3 * up) * 1024 : ((kp + 2) * 6 + 3 * up + 1) * 1024;
      k.wd1 = ((kp + 2) * 6 + 3 * up) * 1024;
      k.xa0 = (K::XB + mh * 4 * 2048 + c * 128 + ((h ^ (c >> 1)) << 4)) ^ ((rot & 1) << 6);
      {
        const int s = ((q4 >> 1) & 1) + 2 * (h & 1);
        k.wa0 = (h * 6 + (RB ? 2 : (w & 1))) * 1024 + q4 * 128 + (((RB ? 2 * (w & 1) : 0) ^ s) << 5) + p4 * 8;
      }
      if (RB) asm volatile("" : "+v"(k.xv[0]), "+v"(k.xv[1]), "+v"(k.xv[2]), "+v"(k.xv[3]), "+v"(k.wv[2]), "+v"(k.wv[3]));
      asm volatile("" : "+v"(k.wv[0]), "+v"(k.wv[1]), "+v"(k.xa0), "+v"(k.wa0));
      k.zero();
      bind();
      STAMP(0);
      const u64 pg = wg, pu = wu;
      k.srdx = mk_srd(x, 2048 * 1280);
      k.srdw = mk_srd(up ? pu : pg, 640 * 3072);
      asm volatile("global_load_dwordx2 %0, %1, %2 sc1" : "=v"(ev) : "v"(WS_EPOCH), "s"(ws) : "memory");
      STAMP(11);
      k.stage0(rot);
      BAR();
      if (!RB) pack_issue();
      k.stage12();
      STAMP(12);
      k.template run<RB ? 5 : 0, RB ? 10 : 0>([&]() {
        make_tag(std::integral_constant<int, RB ? K::VPRO : 8>{});
        BAR();
        STAMP(1);
      }, [&](int t) { if (RB) side_b(t); else side_f(t); }, tail);
      STAMP(2);
      constexpr int NK = RB ? 1 : 2;
      float nl2e = -LOG2E;
      asm volatile("" : "+s"(nl2e));
      const unsigned gso = WS_GU + ((8 * a + 4 * mh) * 48 + 6 * cj + (RB ? 4 + (w & 1) : 2 * (w & 1))) * 1024;
#pragma unroll
      for (int m = 0; m < 4; ++m) {
#pragma unroll
        for (int kk = 0; kk < NK; ++kk) {
          float v[8];
#pragma unroll
          for (int j = 0; j < 8; j += 2) {
            const f32x2 g2 = {k.acc[m][2 * kk + (j >> 2)][j & 3], k.acc[m][2 * kk + (j >> 2)][(j & 3) + 1]};
            const f32x2 u2 = {k.acc[m][2 * NK + 2 * kk + (j >> 2)][j & 3], k.acc[m][2 * NK + 2 * kk + (j >> 2)][(j & 3) + 1]};
            const f32x2 a2 = g2 * nl2e;
            const f32x2 d2 = f32x2{__builtin_amdgcn_exp2f(a2.x), __builtin_amdgcn_exp2f(a2.y)} + 1.f;
            const f32x2 r2 = g2 * u2 * f32x2{__builtin_amdgcn_rcpf(d2.x), __builtin_amdgcn_rcpf(d2.y)};
            v[j] = r2.x; v[j + 1] = r2.y;
          }
          u32x4 o; o.x = pk2(v[0], v[1]); o.y = pk2(v[2], v[3]); o.z = pk2(v[4], v[5]); o.w = pk2(v[6], v[7]);
          __builtin_amdgcn_raw_buffer_store_b128(o, rws, lane * 16, gso + m * (48 * 1024) + kk * 1024, AUX_SC1);
          STPAD();
        }
      }
    }
    SB();
    STAMP(13);
    asm volatile("" : "+v"(wo_[0][0]), "+v"(wo_[0][1]), "+v"(wo_[0][2]), "+v"(wo_[0][3]), "+v"(wo_[0][4]),
                      "+v"(wo_[1][0]), "+v"(wo_[1][1]), "+v"(wo_[1][2]), "+v"(wo_[1][3]), "+v"(wo_[1][4]));
    if (RB) asm volatile("" : "+v"(wg_[0][0]), "+v"(wg_[0][1]), "+v"(wg_[0][2]), "+v"(wg_[0][3]), "+v"(wg_[0][4]),
                              "+v"(wg_[1][0]), "+v"(wg_[1][1]), "+v"(wg_[1][2]), "+v"(wg_[1][3]), "+v"(wg_[1][4]));
    SB();
    STAMP(14);
    if (RB && idx >= 16) { if (PRE > 0) cell_dma(w - 4, 1); if (PRE > 1) cell_dma(w, 1); if (PRE > 2) cell_dma(w - 4, 2); WAITV(5 * PRE); }
    else if (RB || !PREA) WAITV(0);
    else { late_w(); WAITV(15); }
    STAMP(3);
    WAITL();
    BAR();
    if (!RB && w == 0 && lane < 2) {
      GAS u64* fp = (GAS u64*)((char*)ws + (lane ? WS_DONE + b * 8 : WS_FLAG + (a * 16 + (idx < 16 ? 8 : 0) + gc) * 8));
      __hip_atomic_store(fp, tag, RLX);
    }
    STAMP(4);

    SB();
    const unsigned fo = WS_FLAG + ((rb >> 1) * 16 + c) * 8;
    auto wait_flags = [&](u32x2& v, bool need) {
#pragma unroll 1
      for (unsigned spin = 0; spin < (1u << 18); ++spin) {
        if (__all(!need || (v.x == (unsigned)tag && v.y == (unsigned)(tag >> 32)))) break;
        ld64(v, fo, sws, 0);
        asm volatile("s_waitcnt vmcnt(0)" : "+v"(v) :: "memory");
      }
      asm volatile("" ::: "memory");
    };
    u32x2 fv;
    if (RB) {
      if (idx < 16) { if (PRE > 0) cell_dma(w - 4, 1); if (PRE > 1) cell_dma(w, 1); if (PRE > 2) cell_dma(w - 4, 2); }
      if (PRE < 1) cell_dma(w - 4, 1); if (PRE < 2) cell_dma(w, 1); if (PRE < 3) cell_dma(w - 4, 2);
      cell_dma(w, 2);
    } else {
      if (!PREA) late_w();
      if (w == 0) {
        ld64(fv, fo, sws, 0);
        asm volatile("s_waitcnt vmcnt(0)" : "+v"(fv) :: "memory");
        wait_flags(fv, c >= 8);
      }
    }
    if (RB) __builtin_amdgcn_s_setprio(1);
    BAR();
    STAMP(5);
    f32x4 acc[4][5];
    u32x4 ag[6][4];
    auto tk = [](int) {};
    tk(0);
    const unsigned gb = (EXP & 1) ? pwb : WS_GU + (4 * rb * 48 + 6 * w) * 1024;
    auto ldg = [&](int s, int m) { ld128(ag[s][m], lane * 16, sws, gb + gstep(s) * 1024 + m * (48 * 1024)); };
    unsigned ca0 = CELL0 + w * 5120 + lane * 16, ca1 = w * 15360 + lane * 16;
    asm volatile("" : "+v"(ca0), "+v"(ca1));
    {
      using S = Sched<RB>;
      constexpr int NO = S::NO, OB = S::OB;
      u32x4 af[2][4], at[4];
      u32x2 tl[4], th[4];
      const unsigned ob = WS_O + 4 * rb * 20480;
      auto req = [&](auto IT) {
        constexpr int i = IT.value;
        if constexpr (i < 8) ld128(af[i >> 2][i & 3], lane * 16, sws, ob + (i & 3) * 20480 + (2 * w + (i >> 2)) * 1024);
        else if constexpr (i < OB) ld64((i & 1) ? th[(i - 8) >> 1] : tl[(i - 8) >> 1], lane * 8, sws, ob + ((i - 8) >> 1) * 20480 + (16 + w) * 1024 + (i & 1) * 512);
        else if constexpr (i < OB + 29 && (i - OB) % 5 < 4) ldg((i - OB) / 5, (i - OB) % 5);
        else if constexpr (i < OB + 29) dma16c(w * 15360, 10240 + ((i - OB) / 5) * 1024, lane * 16, sws, pwb + (20 + 6 * w + gstep(5)) * 5120 + ((i - OB) / 5) * 1024);
        else ld32(i == OB + 29 ? sg0 : i == OB + 30 ? sg1 : sg2, c * 4, sws, WS_G + (4 * rb + min(t0 + i - OB - 29, 19) / 5) * 64);
      };
      sfor<0, S::upto(PL - 1)>(req);
      if (RB) { __builtin_amdgcn_s_setprio(0); WAITV(S::upto(PL - 1)); }
      else if (w == 0) wait_flags(fv, c < 8);
      STAMP(6);
      BAR();
      STAMP(7);
      tk(1);
      u32x4 wc[5];
      sfor<0, NO + 6>([&](auto ST) {
        constexpr int st = ST.value, s = st - NO;
        if constexpr (st == 2 && !RB) {
          asm volatile("s_waitcnt vmcnt(%8)" : "+v"(tl[0]), "+v"(tl[1]), "+v"(tl[2]), "+v"(tl[3]), "+v"(th[0]), "+v"(th[1]), "+v"(th[2]), "+v"(th[3])
                       : "i"(S::issued(st, 0) - 16) : "memory");
#pragma unroll
          for (int m = 0; m < 4; ++m) at[m] = cat(tl[m], th[m]);
          asm volatile("s_nop 1" : "+v"(at[0]), "+v"(at[1]), "+v"(at[2]), "+v"(at[3]));
        }
        sfor<0, 20>([&](auto IT) {
          constexpr int i = IT.value, m = i / 5, n = i % 5, is = S::issued(st, i);
          if constexpr (n == 0) {
            if constexpr (s >= 0)      asm volatile("s_waitcnt vmcnt(%1)" : "+v"(ag[s][m]) : "i"(is - S::gpos(s, m) - 1) : "memory");
            else if constexpr (st < 2) asm volatile("s_waitcnt vmcnt(%1)" : "+v"(af[st][m]) : "i"(is - (4 * st + m) - 1) : "memory");
          }
          if constexpr (st == 0 && i == 0 && !RB)
            asm volatile("" : "+v"(wo_[2][0]), "+v"(wo_[2][1]), "+v"(wo_[2][2]), "+v"(wo_[2][3]), "+v"(wo_[2][4]),
                              "+v"(wg_[0][0]), "+v"(wg_[0][1]), "+v"(wg_[0][2]), "+v"(wg_[0][3]), "+v"(wg_[0][4]),
                              "+v"(wg_[1][0]), "+v"(wg_[1][1]), "+v"(wg_[1][2]), "+v"(wg_[1][3]), "+v"(wg_[1][4]));
          if constexpr (st == 0)     mul0(acc[m][n], wo_[0][n], af[0][m]);
          else if constexpr (s >= 0) mac(acc[m][n], wg_[s & 1][n], ag[s][m]);
          else if constexpr (st < 2) mac(acc[m][n], wo_[1][n], af[1][m]);
          else                       mac(acc[m][n], wo_[2][n], at[m]);
          SB();
          if constexpr (s == 4 && i == 9) WAITV(is - S::gpos(4, 4) - 1);
          if constexpr (m == (s == 4 ? 2 : 0) && s >= 1 && s < 5) wc[n] = lds128((s > 1 ? ca1 + (s - 2) * 5120 : ca0) + n * 1024);
          if constexpr (S::issued(st, i + 1) > is) req(std::integral_constant<int, is>{});
          SB();
        });
        tk(2 + st);
        if constexpr (s >= 1 && s < 5) {
          asm volatile("" : "+v"(wc[0]), "+v"(wc[1]), "+v"(wc[2]), "+v"(wc[3]), "+v"(wc[4]));
#pragma unroll
          for (int n = 0; n < 5; ++n) wg_[(s + 1) & 1][n] = wc[n];
        }
      });
      asm volatile("s_waitcnt vmcnt(0)" : "+v"(sg0), "+v"(sg1), "+v"(sg2) :: "memory");
    }
    MFMA_DONE();
    STAMP(8);
#pragma unroll
    for (int j = 0; j < 10; ++j) {
      const int ta = j < 4 ? 3 * j : j < 6 ? 6 * j - 22 : 2 * j, tb = j < 4 ? ta + 1 : j < 6 ? ta + 3 : ta + 1;
      *(LAS u32x4*)(ca1 + j * 1024) = cat(pk4(acc[ta / 5][ta % 5]), pk4(acc[tb / 5][tb % 5]));
    }
    tk(12);
  };
  if (w < 4) role(std::false_type{}); else role(std::true_type{});

  if (b == 0 && w == 4) {
    GAS u64* dp = (GAS u64*)((char*)ws_ + WS_DONE + lane * 8);
#pragma unroll 1
    for (unsigned spin = 0; spin < (1u << 18); ++spin) {
      bool ok = true;
#pragma unroll
      for (int j = 0; j < 4; ++j) ok &= __hip_atomic_load(dp + j * 64, RLX) == tag;
      if (__all(ok)) break;
      __builtin_amdgcn_s_sleep(2);
    }
    if (lane == 0) __hip_atomic_store((GAS u64*)((char*)ws_ + WS_EPOCH), tag, RLX);
  }
  sg0 = __builtin_amdgcn_rcpf(1.f + sg0); sg1 = __builtin_amdgcn_rcpf(1.f + sg1); sg2 = __builtin_amdgcn_rcpf(1.f + sg2);
  auto ooff = [&](int tl) {
    const int m = tl / 5, n = tl - 5 * m;
    return (unsigned)((64 * rb + 32 * (m >> 1) + 2 * c + (m & 1)) * 1280 + ct * 160 + n * 32 + h * 8);
  };
  unsigned oo0 = ooff(t0), oo1 = ooff(t0 + 1), oo2 = ooff(t0 + 2);
  asm volatile("" : "+v"(sg0), "+v"(sg1), "+v"(sg2), "+v"(oo0), "+v"(oo1), "+v"(oo2));
  WAITL();
  BAR();
  STAMP(9);
  {
    u32x4 p[8];
    u32x2 p2[8];
    unsigned ra = (w < 4 ? w : w + 2) * 1024 + lane * 16, rb_ = ra + 4 * 15360;
    unsigned rc = (4 + (w >> 1)) * 1024 + (w & 1) * 8 + lane * 16, rd = rc + 4 * 15360;
    asm volatile("" : "+v"(ra), "+v"(rb_), "+v"(rc), "+v"(rd));
#pragma unroll
    for (int v = 0; v < 8; ++v) p[v] = lds128((v < 4 ? ra : rb_) + (v & 3) * 15360);
    if (w < 4) {
#pragma unroll
      for (int v = 0; v < 8; ++v) { p2[v] = *(const LAS u32x2*)((v < 4 ? rc : rd) + (v & 3) * 15360); SB(); }
    }
    SB();
#pragma unroll
    for (int j = 0; j < 3; ++j) {
      if (j < 2 || w < 4) {
        f32x4 s = {0.f, 0.f, 0.f, 0.f};
#pragma unroll
        for (int v = 0; v < 8; ++v) {
          const unsigned px = j == 0 ? p[v].x : j == 1 ? p[v].z : p2[v].x, py = j == 0 ? p[v].y : j == 1 ? p[v].w : p2[v].y;
          const f32x4 q = {u2f(px << 16), u2f(px & 0xffff0000u), u2f(py << 16), u2f(py & 0xffff0000u)};
          s = v ? s + q : q;
        }
        const u32x2 ov = pk4(s * (j == 0 ? sg0 : j == 1 ? sg1 : sg2));
        asm volatile("global_store_dwordx2 %0, %1, %2" OUTFL :: "v"(j == 0 ? oo0 : j == 1 ? oo1 : oo2), "v"(ov), "s"(out_) : "memory");
      }
    }
  }
  STAMP(10);
  if (AMP0 >= 0 && (AMPSEL == 0 || (AMPSEL == 1) == (idx < 16))) {
    const u64 d = amp_t1 - amp_t0, t = __builtin_amdgcn_s_memrealtime();
    while (__builtin_amdgcn_s_memrealtime() - t < d) __builtin_amdgcn_s_sleep(1);
  }
}
}

extern "C" void kernel_launch(void* const* d_in, const int* in_sizes, int n_in, void* d_out, int out_size, void* d_ws,
                              size_t ws_size, hipStream_t stream) {
  fused<<<256, 512, 0, stream>>>(d_in[0], d_in[1], d_in[2], d_in[4], d_in[5], d_in[6], d_in[7], d_out, d_ws);
}
```

```cpp
#include <hip/hip_runtime.h>
#include <hip/hip_bf16.h>
#include <stdint.h>
#include <type_traits>

#define STAMPS 0
#define BEARLY 0
#define CODEPF 1
#define TOUCH_NT ""
#define TOUCH 1
#define EXP 0
#define GLA 4
#define PART16 1
#define PL 2
#define PRE 2
#define OUTFL ""
#define PREA 0
#define ROT 4

#define LAS __attribute__((address_space(3)))
#define GAS __attribute__((address_space(1)))
typedef __bf16 bf16x8 __attribute__((ext_vector_type(8)));
typedef __bf16 bf16x2 __attribute__((ext_vector_type(2)));
typedef float f32x4 __attribute__((ext_vector_type(4)));
typedef float f32x2 __attribute__((ext_vector_type(2)));
typedef unsigned u32x4 __attribute__((ext_vector_type(4)));
typedef unsigned u32x2 __attribute__((ext_vector_type(2)));
typedef short s16x4 __attribute__((ext_vector_type(4)));
typedef unsigned long long u64;

namespace {
constexpr int LDS_BYTES = 163840;
constexpr unsigned GATE_OFF = 110592, PACK_OFF = 122880, PKF_OFF = 162816, CELL0 = 122880;
constexpr unsigned OOB = 0x40000000u;
constexpr unsigned CODE_BYTES = 40000;
constexpr unsigned WS_FLAG = 0;
constexpr unsigned WS_DONE = 4096;
constexpr unsigned WS_PKF = 8192;
constexpr unsigned WS_EPOCH = 12288;
constexpr unsigned WS_G = 16384;
constexpr unsigned WS_O = 131072;
constexpr unsigned WS_GU = WS_O + 128 * 40 * 512;
constexpr unsigned WS_PW = WS_GU + 128 * 48 * 1024;
constexpr unsigned WS_END = WS_PW + 8 * 68 * 5120;
constexpr unsigned WS_STAMP = 16u << 20;
constexpr int AUX_SC1 = 16;
constexpr float LOG2E = 1.4426950408889634f;

__device__ __forceinline__ u32x4 mk_srd(u64 a, unsigned bytes) {
  u32x4 r; r.x = (unsigned)a; r.y = (unsigned)(a >> 32) & 0xffffu; r.z = bytes; r.w = 0x00020000u; return r;
}
__device__ __forceinline__ void dma16(unsigned lds, unsigned imm, unsigned voff, u32x4 srd, unsigned soff) {
  asm volatile("s_add_u32 m0, %0, %4\n\ts_nop 0\n\tbuffer_load_dwordx4 %1, %2, %3 offen lds"
               :: "s"(lds), "v"(voff), "s"(srd), "s"(soff), "i"(imm) : "memory", "scc");
}
__device__ __forceinline__ void dma16c(unsigned lds, unsigned imm, unsigned voff, u32x4 srd, unsigned soff) {
  asm volatile("s_add_u32 m0, %0, %4\n\ts_nop 0\n\tbuffer_load_dwordx4 %1, %2, %3 offen sc1 lds"
               :: "s"(lds), "v"(voff), "s"(srd), "s"(soff), "i"(imm) : "memory", "scc");
}
__device__ __forceinline__ void ld128(u32x4& d, unsigned voff, u32x4 srd, unsigned soff) {
  asm volatile("buffer_load_dwordx4 %0, %1, %2, %3 offen sc1" : "=v"(d) : "v"(voff), "s"(srd), "s"(soff) : "memory");
}
__device__ __forceinline__ void ld64(u32x2& d, unsigned voff, u32x4 srd, unsigned soff) {
  asm volatile("buffer_load_dwordx2 %0, %1, %2, %3 offen sc1" : "=v"(d) : "v"(voff), "s"(srd), "s"(soff) : "memory");
}
__device__ __forceinline__ void ld32(float& d, unsigned voff, u32x4 srd, unsigned soff) {
  asm volatile("buffer_load_dword %0, %1, %2, %3 offen sc1" : "=v"(d) : "v"(voff), "s"(srd), "s"(soff) : "memory");
}
__device__ __forceinline__ u32x4 lds128(unsigned a) { return *(const LAS u32x4*)a; }
__device__ __forceinline__ u32x2 ldstr(unsigned a) {
  return __builtin_bit_cast(u32x2, __builtin_amdgcn_ds_read_tr16_b64_v4i16((LAS s16x4*)a));
}
__device__ __forceinline__ u32x4 cat(u32x2 a, u32x2 b) { u32x4 r; r.x = a.x; r.y = a.y; r.z = b.x; r.w = b.y; return r; }
__device__ __forceinline__ f32x4 mfma(u32x4 a, u32x4 b, f32x4 c) {
  return __builtin_amdgcn_mfma_f32_16x16x32_bf16(__builtin_bit_cast(bf16x8, a), __builtin_bit_cast(bf16x8, b), c, 0, 0, 0);
}
__device__ __forceinline__ void mac(f32x4& c, u32x4 a, u32x4 b) {
  asm volatile("v_mfma_f32_16x16x32_bf16 %0, %1, %2, %0" : "+v"(c) : "v"(a), "v"(b));
}
__device__ __forceinline__ void mul0(f32x4& c, u32x4 a, u32x4 b) {
  asm volatile("v_mfma_f32_16x16x32_bf16 %0, %1, %2, 0" : "=&v"(c) : "v"(a), "v"(b));
}
__device__ __forceinline__ unsigned pk2(float a, float b) {
  f32x2 t = {a, b};
  return __builtin_bit_cast(unsigned, __builtin_convertvector(t, bf16x2));
}
__device__ __forceinline__ u32x2 pk4(f32x4 v) { u32x2 r; r.x = pk2(v.x, v.y); r.y = pk2(v.z, v.w); return r; }
__device__ __forceinline__ float u2f(unsigned u) { return __builtin_bit_cast(float, u); }
__device__ __forceinline__ unsigned f2u(float f) { return __builtin_bit_cast(unsigned, f); }
#define SB() __builtin_amdgcn_sched_barrier(0)
#define WAITV(n) asm volatile("s_waitcnt vmcnt(%0)" :: "i"(n) : "memory")
#define WAITVL(n) asm volatile("s_waitcnt vmcnt(%0) lgkmcnt(0)" :: "i"(n) : "memory")
#define WAITL() asm volatile("s_waitcnt lgkmcnt(0)" ::: "memory")
#define BAR() asm volatile("s_barrier" ::: "memory")
#define STPAD() do { SB(); asm volatile("s_nop 1" ::: "memory"); SB(); } while (0)
#define MFMA_DONE() do { SB(); asm volatile("s_nop 7\n\ts_nop 1" ::: "memory"); SB(); } while (0)
#define RLX __ATOMIC_RELAXED, __HIP_MEMORY_SCOPE_AGENT
__device__ __forceinline__ unsigned rfl(unsigned v) { return __builtin_amdgcn_readfirstlane(v); }
__device__ __forceinline__ unsigned opaque_lane() { unsigned l = threadIdx.x & 63; asm volatile("" : "+v"(l)); return l; }

#define AMP0 -1
#define AMP1 -1
#define AMPSEL 0
#define STAMP(i) do {} while (0)

template <int I, int N, class F>
__device__ __forceinline__ void sfor(F&& f) {
  if constexpr (I < N) { f(std::integral_constant<int, I>{}); sfor<I + 1, N>(f); }
}
template <bool RB> struct Sched {
  static constexpr int NO = RB ? 2 : 3, OB = RB ? 8 : 16;
  static constexpr int gpos(int s, int m) { return OB + 5 * s + m; }
  static constexpr int upto(int st) { return st < 2 ? 4 * (st + 1) : st < NO ? 16 : st < NO + 5 ? OB + 5 * (st - NO + 1) : st == NO + 5 ? OB + 29 : OB + 32; }
  static constexpr int issued(int st, int i) {
    const int base = upto(st + PL - 1), goal = upto(st + PL), every = goal - base > 5 ? 2 : goal - base > 4 ? 4 : 5;
    int n = base;
    for (int k = 0; k < i; ++k) if (k % every == 1 && n < goal) ++n;
    return n;
  }
};
__device__ __forceinline__ float max3(float a, float b, float c) { float r; asm("v_max3_f32 %0, %1, %2, %3" : "=v"(r) : "v"(a), "v"(b), "v"(c)); return r; }
__device__ __forceinline__ float max2(float a, float b) { float r; asm("v_max_f32 %0, %1, %2" : "=v"(r) : "v"(a), "v"(b)); return r; }

template <bool QKV, bool RB>
struct P1 {
  static constexpr int NM = QKV ? 5 : 4;
  static constexpr int NN = QKV ? 4 : (RB ? 4 : 8);
  static constexpr int NWS = 3;
  static constexpr unsigned WSL = QKV ? 16384 : 24576, XB = NWS * WSL, XSL = QKV ? 20480 : 16384;
  static constexpr unsigned WROW = QKV ? 3840 : 3072;
  static constexpr int NWP = QKV ? 2 : (RB ? 4 : 2);
  static constexpr int NXE = QKV ? (RB ? 1 : 2) : (RB ? 2 : 0), NXO = QKV ? 1 : NXE;
  static constexpr int NXP = NXO + NXE;
  static constexpr int OO = NWP + NXO, OE = NWP + NXE;
  static constexpr bool HAND = !QKV;
  static constexpr int ht = 4;
  f32x4 acc[NM][NN];
  u32x4 srdx, srdw;
  unsigned xv[4], wv[4];
  unsigned xd;
  unsigned wd0, wd1;
  unsigned xa0, wa0, wa1, wa2;
  unsigned gmask;
  __device__ __forceinline__ void tick(int) {}

  unsigned sow, sox, gof;
  unsigned kg, kw, kx, kr;
  template <bool ODD, bool X = true>
  __device__ __forceinline__ void loads(unsigned wwr, unsigned xwr) {
    if (X && NXP) {
#pragma unroll
      for (int i = ODD ? NXE : 0; i < (ODD ? NXP : NXE); ++i) dma16(xwr + xd, i * 1024, xv[i], srdx, sox);
      if (ODD) { kx += 1; sox = (kx ^ (kg >> 1)) * 128; }
    }
    dma16(wwr + wd0, 0, wv[0], srdw, sow);
    dma16(wwr + wd0, 1024, wv[1], srdw, sow);
    if (!QKV && RB) {
      dma16(wwr + wd0, 2048, wv[2], srdw, sow);
      dma16(wwr + wd1, 0, wv[3], srdw, sow);
    }
  }
  __device__ __forceinline__ void other(unsigned wwr) {
    dma16(wwr + wd1, 1024, wv[3], srdw, sow + 128);
    dma16(wwr + wd1, 2048, wv[3], srdw, sow + 256);
  }
  __device__ __forceinline__ void next() { kw += 1; sow = (kw ^ kg) * (32 * WROW); asm volatile("" : "+s"(sow), "+s"(kw)); if (NXP) asm volatile("" : "+s"(sox), "+s"(kx)); }
  __device__ __forceinline__ void stage0(int rot) {
    kg = rot; kw = 0; kx = 0; kr = 0; sow = kg * (32 * WROW); sox = (kg >> 1) * 128; gof = kg * 256;
    if (HAND && !RB) { kw = 3; sow = (3 ^ kg) * (32 * WROW); return; }
#pragma unroll
    for (int i = 0; i < NXE; ++i) dma16(xd, i * 1024, xv[i], srdx, sox);
    loads<true>(0, 0); if (HAND) other(0); next();
  }
  __device__ __forceinline__ void stage1() { if (HAND && !RB) return; loads<false>(WSL, XSL); if (HAND) other(WSL); next(); }
  __device__ __forceinline__ void stage2() { if (HAND && !RB) return; loads<true>(2 * WSL, XSL); if (HAND) other(2 * WSL); next(); }
  static constexpr int VPRO = OE + OO + (HAND ? 4 : 0);
  struct Ad { unsigned x, w[4]; };
  __device__ __forceinline__ void addr(Ad& a, unsigned xb, unsigned wrd) {
    a.x = xb; a.w[0] = wa0 + wrd;
    if (QKV) {
      const unsigned s2 = (gmask & (gof - wrd)) + wrd;
      kr += 1; gof = (kr ^ kg) * 256;
      a.w[1] = wa1 + s2; a.w[2] = wa2 + s2;
      asm volatile("" : "+v"(a.x), "+v"(a.w[0]), "+v"(a.w[1]), "+v"(a.w[2]));
    } else if (RB) {
      a.w[1] = a.w[0] ^ 32;
      asm volatile("" : "+v"(a.x), "+v"(a.w[0]), "+v"(a.w[1]));
    } else {
      a.w[1] = a.w[0] ^ 32; a.w[2] = a.w[0] ^ 64; a.w[3] = a.w[0] ^ 96;
      asm volatile("" : "+v"(a.x), "+v"(a.w[0]), "+v"(a.w[1]), "+v"(a.w[2]), "+v"(a.w[3]));
    }
  }
  __device__ __forceinline__ unsigned wad(const Ad& a, int n, int hi) {
    if (QKV) return n == 2 ? a.w[1 + hi] : a.w[0] + (n == 0 ? 0 : n == 1 ? 160 : 320) + hi * 1920;
    if (RB) return a.w[n & 1] + (n >> 1) * 3072 + hi * 512;
    return a.w[n & 3] + (n >> 2) * 3072 + hi * 512;
  }
  __device__ __forceinline__ void rd_all(u32x4 (&xf)[NM], u32x4 (&wf)[NN], const Ad& a) {
#pragma unroll
    for (int n = 0; n < NN; ++n) wf[n] = cat(ldstr(wad(a, n, 0)), ldstr(wad(a, n, 1)));
#pragma unroll
    for (int m = 0; m < NM; ++m) xf[m] = lds128(a.x + m * 2048);
  }
  template <bool RD = true, class L>
  __device__ __forceinline__ void mm_rd(const u32x4 (&xc)[NM], const u32x4 (&wc)[NN], u32x4 (&xn)[NM], u32x4 (&wn)[NN], const Ad& a, L&& late) {
    u32x2 lo[NN];
    constexpr bool NMAJ = !QKV && !RB;
    constexpr bool EARLY = !QKV && RB && BEARLY;
    if (EARLY && RD) { rd_all(xn, wn, a); SB(); }
    int r = 0;
#pragma unroll
    for (int i = 0; i < NM * NN; ++i) {
      const int m = NMAJ ? i % NM : i / NN, n = NMAJ ? i / NM : i % NN;
      if (QKV && m == 0 && n == 0) continue;
      if (QKV && n == 3) mac(acc[m][3], xc[m], wc[3]);
      else               mac(acc[m][n], wc[n], xc[m]);
      SB();
      if (!RD || EARLY) continue;
      if (NMAJ) {
        if (i < 4) xn[i] = lds128(a.x + i * 2048);
        else if ((i & 3) < 2) {
          const int k = (i >> 2) - 1;
          if (i & 1) wn[k] = cat(lo[k], ldstr(wad(a, k, 1))); else lo[k] = ldstr(wad(a, k, 0));
        }
      } else {
        if (r < 2 * NN) {
          const int k = r >> 1;
          if (r & 1) wn[k] = cat(lo[k], ldstr(wad(a, k, 1))); else lo[k] = ldstr(wad(a, k, 0));
        } else if (r < 2 * NN + NM) {
          xn[r - 2 * NN] = lds128(a.x + (r - 2 * NN) * 2048);
        }
        else late(r - 2 * NN - NM);
        ++r;
      }
      SB();
    }
    if (NMAJ && RD) { wn[NN - 1] = cat(ldstr(wad(a, NN - 1, 0)), ldstr(wad(a, NN - 1, 1))); SB(); }
  }
  __device__ __forceinline__ void pin(u32x4 (&xf)[NM], u32x4 (&wf)[NN]) {
    asm volatile("" : "+v"(xf[0]), "+v"(xf[1]), "+v"(xf[2]), "+v"(xf[3]), "+v"(wf[0]), "+v"(wf[1]), "+v"(wf[2]), "+v"(wf[3]));
    if (NM == 5) asm volatile("" : "+v"(xf[NM - 1]));
    if (NN == 8) asm volatile("" : "+v"(wf[NN - 4]), "+v"(wf[NN - 3]), "+v"(wf[NN - 2]), "+v"(wf[NN - 1]));
  }
  __device__ __forceinline__ void zero() {
#pragma unroll
    for (int m = 0; m < NM; ++m)
#pragma unroll
      for (int n = 0; n < NN; ++n) { acc[m][n] = f32x4{0.f, 0.f, 0.f, 0.f}; asm volatile("" : "+v"(acc[m][n])); }
  }
  template <int TL0, int TL1, class F, class G, class H>
  __device__ __forceinline__ void run(F&& pre, G&& extra, H&& tail) {
    pre();
    u32x4 xA[NM], wA[NN], xB[NM], wB[NN];
    Ad a;
    addr(a, xa0, 0);
    rd_all(xA, wA, a);
    if (!RB) __builtin_amdgcn_s_setprio(1);
    unsigned w0 = 0, w1 = WSL, w2 = 2 * WSL;
    unsigned xp = 2 * XSL, x0 = 0, x1 = XSL;
    int t = 0;
    unsigned nw1, nw2, nx1;
    auto adv0 = [&]() { nw1 = w2 + WSL; if (nw1 == NWS * WSL) nw1 = 0; asm volatile("" : "+s"(nw1)); };
    auto adv1 = [&]() { nw2 = nw1 + WSL; if (nw2 == NWS * WSL) nw2 = 0; asm volatile("" : "+s"(nw2)); };
    auto adv2 = [&]() { nx1 = x1 + XSL; if (nx1 == 3 * XSL) nx1 = 0; asm volatile("" : "+s"(nx1)); };
    auto adv3 = [&]() {
      w0 = w2; w1 = nw1; w2 = nw2; xp = x0; x0 = x1; x1 = nx1; t += 2;
      asm volatile("" : "+s"(w0), "+s"(w1), "+s"(w2), "+s"(xp), "+s"(x0), "+s"(x1), "+s"(t));
    };
    auto adv = [&]() { adv0(); adv1(); adv2(); adv3(); };
    asm volatile("" : "+s"(w0), "+s"(w1), "+s"(w2), "+s"(xp), "+s"(x0), "+s"(x1), "+s"(t));
    if (QKV) asm volatile("" : "+s"(gof), "+s"(kr));
    auto none = [](int) {};
    if (RB) {
      auto help = [&](int tt, unsigned wwr) { if (HAND && __builtin_expect(tt < ht, 0)) other(wwr); extra(tt); };
      pin(xA, wA);
      SB(); WAITVL(OO + (HAND ? 2 : 0)); tick(0); BAR(); tick(1); SB();
      loads<false>(w0, xp); help(0, w0); next(); addr(a, (xa0 ^ 64) + x0, w1); SB();
      auto half = [&]() {
        mm_rd(xA, wA, xB, wB, a, [&](int i) {
          if (QKV && i == 0) adv0();
          if (QKV && i == 2) adv1();
          if (QKV && i == 4) adv2();
        });
        pin(xB, wB);
        SB(); WAITVL(OE); tick(2 * t + 2); BAR(); tick(2 * t + 3); SB();
        loads<true>(w1, xp); help(t + 1, w1); next();
        addr(a, xa0 + x1, w2);
        if (QKV) adv3(); else adv();
        SB();
        mm_rd(xB, wB, xA, wA, a, none);
      };
#pragma unroll 1
      do {
        half();
        pin(xA, wA);
        SB(); WAITVL(OO); tick(2 * t); BAR(); tick(2 * t + 1); SB();
        loads<false>(w0, xp); help(t, w0); next(); addr(a, (xa0 ^ 64) + x0, w1); SB();
        if (QKV) asm volatile("" : "+s"(gof), "+s"(kr));
      } while (t < 14);
      half();
    } else {
      addr(a, (xa0 ^ 64) + x0, w1);
      auto trip = [&](auto EARLY) {
        constexpr bool E = EARLY.value;
        pin(xA, wA);
        SB(); if (E) WAITL(); else WAITVL(OO);
        tick(2 * t); BAR(); tick(2 * t + 1); SB();
        mm_rd(xA, wA, xB, wB, a, none);
        if (E && t < ht) extra(t); else loads<false>(w0, xp);
        next(); addr(a, xa0 + x1, w2); adv0(); adv1(); adv2(); SB();
        pin(xB, wB);
        SB(); if (E) WAITL(); else WAITVL(OE);
        tick(2 * t + 2); BAR(); tick(2 * t + 3); SB();
        mm_rd(xB, wB, xA, wA, a, none);
        if (E && t < ht) extra(t + 1); else loads<true>(w1, xp);
        next(); adv3(); addr(a, (xa0 ^ 64) + x0, w1); SB();
        if (QKV) asm volatile("" : "+s"(gof), "+s"(kr));
      };
      if (HAND) {
#pragma unroll 1
        do trip(std::true_type{}); while (t < ht + 2);
      }
#pragma unroll 1
      do trip(std::false_type{}); while (t < 16);
    }
    constexpr unsigned S16 = (16 % NWS) * WSL, S17 = (17 % NWS) * WSL, S18 = (18 % NWS) * WSL, S19 = (19 % NWS) * WSL, X8 = 2 * XSL, X9 = 0;
    pin(xA, wA);
    SB(); WAITVL(OO);
    tick(32); BAR(); tick(33); SB();
    if (RB) { loads<false, false>(S16, 0); tail(0); addr(a, (xa0 ^ 64) + X8, S17); SB(); mm_rd(xA, wA, xB, wB, a, none); }
    else    { mm_rd(xA, wA, xB, wB, a, none); loads<false, false>(S16, 0); tail(0); addr(a, xa0 + X9, S18); SB(); }
    pin(xB, wB);
    SB(); WAITVL(NWP + TL0); tick(34); BAR(); tick(35); SB();
    if (RB) { tail(1); addr(a, xa0 + X9, S18); SB(); mm_rd(xB, wB, xA, wA, a, none); }
    else    { mm_rd(xB, wB, xA, wA, a, none); tail(1); addr(a, (xa0 ^ 64) + X9, S19); SB(); }
    pin(xA, wA);
    SB(); WAITVL(TL0 + TL1); tick(36); BAR(); tick(37); SB();
    if (RB) { tail(2); addr(a, (xa0 ^ 64) + X9, S19); SB(); mm_rd(xA, wA, xB, wB, a, none); }
    else    { mm_rd(xA, wA, xB, wB, a, none); tail(2); SB(); }
    pin(xB, wB);
    SB(); WAITL(); tick(38); BAR(); tick(39); SB();
    if (RB) { tail(3); SB(); mm_rd<false>(xB, wB, xA, wA, a, none); }
    else    { mm_rd<false>(xB, wB, xA, wA, a, none); tail(3); SB(); }
    if (!RB) __builtin_amdgcn_s_setprio(0);
    MFMA_DONE();
    tick(40);
  }
};

__global__ __launch_bounds__(512, 2) void fused(const void* x_, const void* wqkv_, const void* wo_p, const void* wg_p,
                                                const void* wu_p, const void* wd_p, const void* gw_, void* out_, void* ws_) {
  __shared__ __attribute__((aligned(16))) char smem[LDS_BYTES];
  asm volatile("" :: "s"((unsigned)(size_t)(LAS char*)smem));
  const int tid = threadIdx.x, lane = tid & 63;
  const int w = rfl(tid >> 6);
  const int h = lane >> 4, c = lane & 15, q4 = (lane & 15) >> 2, p4 = lane & 3;
  const int b = blockIdx.x, xcd = b & 7, idx = b >> 3;
  const int rh = xcd & 1, cq = xcd >> 1;
  const int a = 8 * rh + (idx & 7);
  const int gc = 2 * cq + ((idx >> 3) & 1);
  const int rb = 4 * xcd + (idx >> 3), ct = idx & 7;
  u64 x, wqkv, wo, wg, wu, wd, gw;
  char* ws;
  __amdgpu_buffer_rsrc_t rws;
  u32x4 sws;
  auto bind = [&]() {
    x = (u64)x_; wqkv = (u64)wqkv_; wo = (u64)wo_p; wg = (u64)wg_p; wu = (u64)wu_p; wd = (u64)wd_p; gw = (u64)gw_;
    u64 pw = (u64)ws_;
    asm volatile("" : "+s"(x), "+s"(wqkv), "+s"(wo), "+s"(wg), "+s"(wu), "+s"(wd), "+s"(gw), "+s"(pw));
    ws = (char*)pw;
    rws = __builtin_amdgcn_make_buffer_rsrc(ws, 0, WS_END, 0x00020000);
    sws = mk_srd(pw, WS_END);
  };
  u64 amp_t0 = 0, amp_t1 = 0;

  if (CODEPF) {
    u64 pc;
    asm volatile("s_getpc_b64 %0" : "=s"(pc));
    asm volatile("s_mov_b32 m0, %0\n\ts_nop 0\n\tbuffer_load_dword %1, %2, 0 offen lds"
                 :: "s"(PKF_OFF), "v"(lane < 6 ? ((idx & 7) * 48 + w * 6 + lane) * 128 : OOB), "s"(mk_srd(pc & ~127ull, CODE_BYTES)) : "memory");
  }
  u32x2 ev;
  u64 tag = 0;
  auto make_tag = [&](auto NV) {
    unsigned lo, hi;
    asm volatile("s_waitcnt vmcnt(%4)\n\tv_readfirstlane_b32 %0, %2\n\tv_readfirstlane_b32 %1, %3"
                 : "=s"(lo), "=s"(hi) : "v"(ev.x), "v"(ev.y), "i"(NV.value) : "memory");
    const u64 e = ((u64)hi << 32) | lo;
    tag = (e ^ 0x9E3779B97F4A7C15ull) * 0xBF58476D1CE4E5B9ull + 0x632BE59BD9B4E019ull;
    if (tag == e) tag += 1;
  };
  const unsigned pwb = WS_PW + ct * (68 * 5120);
  const int grot = 3 * ((idx >> 3) & 1);
  auto gstep = [&](int s) { const int v = s + grot; return v >= 6 ? v - 6 : v; };
  auto cell_dma = [&](int v, int cell) {
    const unsigned so = pwb + (20 + 6 * v + gstep(2 + cell)) * 5120;
#pragma unroll
    for (int j = 0; j < 5; ++j) dma16c(cell ? v * 15360 : CELL0 + v * 5120, (cell ? cell - 1 : 0) * 5120 + j * 1024, lane * 16, sws, so + j * 1024);
  };
  auto ldw = [&](u32x4 (&d)[5], unsigned so) {
#pragma unroll
    for (int n = 0; n < 5; ++n) d[n] = __builtin_amdgcn_raw_buffer_load_b128(rws, lane * 16, so + n * 1024, AUX_SC1);
  };
  const unsigned pscr = PACK_OFF + (w & 3) * 8192;
  const int pu0 = (b - 128) * 4 + w;
  unsigned pra[2], prb[2], pn[2], pb[2];
  auto pack_issue = [&]() {
    const u64 po = wo, pd = wd;
#pragma unroll
    for (int r = 0; r < 2; ++r) {
      const int u = pu0 + 512 * r;
      const int ks = u / 10, cg = u - 10 * ks;
      const unsigned r8 = lane >> 3;
      const u32x4 srd = mk_srd(ks < 20 ? po : pd, ks < 20 ? 640 * 1280 : 1536 * 1280);
      const unsigned base = ks < 16 ? 40 * ks : ks < 20 ? 160 * (ks - 16) + 16 : 32 * (ks - 20);
      const unsigned vr = (ks >= 16 && ks < 20) ? 20 * (r8 >> 2) + (r8 & 3) : r8;
      const unsigned vo = vr * 1280 + (lane & 7) * 16;
#pragma unroll
      for (int p = 0; p < 4; ++p) {
        const unsigned rp = ks < 16 ? 20 * (p >> 1) + 8 * (p & 1) : ks < 20 ? 40 * p : 8 * p;
        dma16(pscr, r * 4096 + p * 1024, vo, srd, u < 680 ? (base + rp) * 1280 + cg * 128 : OOB);
      }
      pra[r] = pscr + r * 4096 + (h * (ks < 16 ? 8 : 4) + q4) * 128 + p4 * 8;
      prb[r] = pra[r] + (ks < 16 ? 512 : 2048);
      pn[r] = 4 * cg; pb[r] = u < 680 ? WS_PW + ks * 5120 : OOB;
    }
    asm volatile("" : "+v"(pra[0]), "+v"(prb[0]), "+v"(pra[1]), "+v"(prb[1]));
    asm volatile("" : "+s"(pn[0]), "+s"(pb[0]), "+s"(pn[1]), "+s"(pb[1]));
  };
  const unsigned smask = w != 4 ? 0u : idx >= 16 ? 0x1040u : 0x1000u;
  auto side_b = [&](int t) {
    if (__builtin_expect(!((smask >> t) & 1), 1)) return;
    if (t == 6) {
      u32x2 tv; tv.x = (unsigned)tag; tv.y = (unsigned)(tag >> 32);
      __builtin_amdgcn_raw_buffer_store_b64(tv, rws, opaque_lane() == 0 ? WS_PKF + (b - 128) * 8 : OOB, 0, AUX_SC1);
    } else {
      dma16c(PKF_OFF, 0, opaque_lane() * 16, sws, WS_PKF);
    }
  };
  auto side_f = [&](int t) {
    {
      if (t == 0) WAITV(0);
      const u32x4 f0 = cat(ldstr(pra[0] + t * 32), ldstr(prb[0] + t * 32)), f1 = cat(ldstr(pra[1] + t * 32), ldstr(prb[1] + t * 32));
      const unsigned n0 = pn[0] + t, c0 = (n0 * 205) >> 10, n1 = pn[1] + t, c1 = (n1 * 205) >> 10;
      __builtin_amdgcn_raw_buffer_store_b128(f0, rws, lane * 16, pb[0] + n0 * 1024 + c0 * (68 * 5120 - 5120), AUX_SC1);
      STPAD();
      __builtin_amdgcn_raw_buffer_store_b128(f1, rws, lane * 16, pb[1] + n1 * 1024 + c1 * (68 * 5120 - 5120), AUX_SC1);
      STPAD();
    }
  };
  auto pk_check = [&]() {
    const u32x4 p0 = lds128(PKF_OFF + lane * 16);
    const unsigned lo = (unsigned)tag, hi = (unsigned)(tag >> 32);
    if (!__all(p0.x == lo && p0.z == lo && p0.y == hi && p0.w == hi)) {
      GAS u64* fp = (GAS u64*)((char*)ws + WS_PKF + opaque_lane() * 8);
#pragma unroll 1
      for (unsigned spin = 0; spin < (1u << 18); ++spin) {
        bool ok = true;
#pragma unroll
        for (int j = 0; j < 2; ++j) ok &= __hip_atomic_load(fp + 64 * j, RLX) == tag;
        if (__all(ok)) break;
        __builtin_amdgcn_s_sleep(2);
      }
    }
    asm volatile("" ::: "memory");
  };
  const int rot = (a + 2 * ((idx >> 3) & 1) + (idx >> 4)) & (ROT - 1);

  const int t0 = 2 * w + min(w, 4);
  float sg0, sg1, sg2;
  auto role = [&](auto RBT) {
    constexpr bool RB = decltype(RBT)::value;
    u32x4 wo_[3][5], wg_[2][5];
    auto tail = [&](int i) {
      if (i == 0) pk_check();
      if (RB) {
        if (i < 2) ldw(wo_[i], pwb + (2 * w + i) * 5120);
        else ldw(wg_[i - 2], pwb + (20 + 6 * w + gstep(i - 2)) * 5120);
        if (i == 1) cell_dma(w - 4, 0);
        if (i == 2) cell_dma(w, 0);
      } else if (i >= 2) {
        ldw(wo_[i - 2], pwb + (2 * w + i - 2) * 5120);
      }
    };
    auto late_w = [&]() {
#pragma unroll
      for (int n = 0; n < 5; ++n) ld128(wo_[2][n], lane * 16, sws, pwb + (16 + w) * 5120 + n * 1024);
#pragma unroll
      for (int j = 0; j < 10; ++j) ld128(wg_[j / 5][j % 5], lane * 16, sws, pwb + (20 + 6 * w + gstep(j / 5)) * 5120 + (j % 5) * 1024);
    };
    if (idx < 16) {
      const int g = gc, hd = w & 3;
      constexpr int par = RB ? 1 : 0;
      using K = P1<true, RB>;
      K k;
      const int q0 = RB ? 12 + 2 * (w - 4) : 3 * w;
      k.xd = K::XB + q0 * 1024; k.wd0 = w * 2048;
      asm volatile("" : "+s"(k.xd), "+s"(k.wd0));
#pragma unroll
      for (int i = 0; i < K::NXP; ++i) {
        const int rho = 8 * (q0 + i) + (lane >> 3);
        const int pp = rho >= 80 ? 1 : 0, u = rho - 80 * pp;
        const int tok = 128 * a - 32 + 2 * u + pp;
        const unsigned v = tok * 1280 + (((lane & 7) ^ ((rho >> 1) & 7)) << 4);
        k.xv[i] = (u >= 9 && tok >= 0) ? v : OOB;
      }
#pragma unroll
      for (int j = 0; j < 2; ++j) {
        const unsigned I = (2 * w + j) * 64 + lane;
        const unsigned kb = (I >= 248) + (I >= 488) + (I >= 736);
        const unsigned i = I - 240 * kb - 8 * ((kb + 1) >> 1);
        const unsigned r = (i * 2185u) >> 16, rem = i - r * 30;
        const unsigned seg = (rem * 6554u) >> 16, ch = rem - seg * 10;
        const unsigned v = (8 * kb + r) * 3840 + seg * 1280 + g * 160 + ch * 16;
        k.wv[j] = i < 240 ? v : OOB;
      }
      asm volatile("" : "+v"(k.xv[0]), "+v"(k.xv[1]), "+v"(k.xv[K::NXP - 1]), "+v"(k.wv[0]), "+v"(k.wv[1]));
      bind();
      STAMP(0);
      k.srdx = mk_srd(x, 2048 * 1280);
      k.srdw = mk_srd(wqkv, 640 * 3840);
      unsigned gv0, gv1;
      asm volatile("global_load_dwordx2 %0, %1, %2 sc1" : "=v"(ev) : "v"(WS_EPOCH), "s"(ws) : "memory");
      asm volatile("buffer_load_ushort %0, %2, %3, 0 offen\n\tbuffer_load_ushort %1, %2, %3, 0 offen offset:1024"
                   : "=&v"(gv0), "=&v"(gv1) : "v"(tid * 2), "s"(mk_srd(gw, 1280)) : "memory");
      STAMP(11);
      k.stage0(rot);
      BAR();
      k.stage1();
      k.xa0 = (K::XB + par * 5 * 2048 + c * 128 + ((h ^ (c >> 1)) << 4)) ^ ((rot & 1) << 6);
      const unsigned kbo = h * 3840 + ((h + 1) >> 1) * 128;
      k.wa0 = kbo + q4 * 480 + (20 * hd + 4 * p4) * 2;
      const bool isgate = p4 == 3;
      k.gmask = isgate ? ~0u : 0u;
      asm volatile("" : "+v"(k.gmask));
      k.wa1 = isgate ? GATE_OFF + (8 * h + q4) * 8 : kbo + q4 * 480 + p4 * 160 + (20 * hd + 16) * 2;
      k.wa2 = k.wa1 + (isgate ? 32 : 1920);
      f32x4 ba, bb, ba1;
#pragma unroll
      for (int r = 0; r < 4; ++r) {
        const int kap = 4 * h + r;
        ba[r] = kap >= c + 9 ? 0.f : -1e30f;
        bb[r] = (kap <= c && kap >= c - 7) ? 0.f : -1e30f;
        ba1[r] = a == 0 ? -1e30f : ba[r];
      }
      unsigned m0mask = h == 0 ? 0xffffffffu : 0u;
      unsigned scw = w * 15360 + 10240 + c * 32 + h * 8, sck = w * 15360 + 10240 + c * 32 + 8,
               sct = w * 15360 + 10240 + (4 * h + q4) * 32 + p4 * 8;
      unsigned ovo = WS_O + (h & 1) * (2 * 20480) + (2 * g + (hd >> 1)) * 1024 + (2 * (hd & 1) + (h >> 1)) * 256 + c * 16;
      unsigned ovt = h == 2 ? WS_O + (32 + g) * 512 + hd * 128 + c * 8 : OOB;
      asm volatile("" : "+v"(ba), "+v"(bb), "+v"(ba1), "+v"(m0mask), "+v"(scw), "+v"(sck), "+v"(sct), "+v"(ovo), "+v"(ovt));
      asm volatile("" : "+v"(k.xa0), "+v"(k.wa0));
      k.zero();
      k.stage2();
      STAMP(12);
      k.template run<RB ? 5 : 0, RB ? 10 : 0>([&]() {
        asm volatile("s_waitcnt vmcnt(%2)" : "+v"(gv0), "+v"(gv1) : "i"(K::VPRO) : "memory");
        u32x2 v; v.x = gv0; v.y = 0;
        *(LAS u32x2*)(GATE_OFF + tid * 8) = v;
        if (!RB && tid < 128) { v.x = gv1; *(LAS u32x2*)(GATE_OFF + 4096 + tid * 8) = v; }
        make_tag(std::integral_constant<int, K::VPRO>{});
        WAITL();
        BAR();
        STAMP(1);
      }, [&](int t) { if (RB) side_b(t); }, tail);
      STAMP(2);
      u32x2 ql[5], kl[5], t2[5], vl[5], tt[5], k4[5];
      unsigned sb16[4];
#pragma unroll
      for (int i = 0; i < 5; ++i) { t2[i] = pk4(k.acc[i][2]); *(LAS u32x2*)(scw + i * 512) = t2[i]; }
#pragma unroll
      for (int i = 0; i < 5; ++i) { if (i) ql[i] = pk4(k.acc[i][0]); kl[i] = pk4(k.acc[i][1]); }
      WAITL();
#pragma unroll
      for (int i = 0; i < 5; ++i) k4[i] = *(const LAS u32x2*)(sck + i * 512);
#pragma unroll
      for (int i = 0; i < 4; ++i) sb16[i] = *(const LAS unsigned*)(sck + (i + 1) * 512 + 16);
#pragma unroll
      for (int i = 0; i < 5; ++i) tt[i] = ldstr(sct + i * 512);
#pragma unroll
      for (int i = 0; i < 5; ++i) vl[i] = pk4(k.acc[i][3]);
      constexpr float SC = 0.22360679774997896f * LOG2E;
      f32x4 sa[4], sb[4];
#pragma unroll
      for (int i = 1; i < 5; ++i) {
        u32x4 qf; qf.x = ql[i].x; qf.y = ql[i].y; qf.z = t2[i].x & m0mask; qf.w = t2[i].y & m0mask;
        sb[i - 1] = mfma(cat(kl[i], k4[i]), qf, bb);
        sa[i - 1] = mfma(cat(kl[i - 1], k4[i - 1]), qf, i == 1 ? ba1 : ba);
      }
      asm volatile("s_nop 7\n\ts_nop 0" : "+v"(sa[0]), "+v"(sa[1]), "+v"(sa[2]), "+v"(sa[3]), "+v"(sb[0]), "+v"(sb[1]), "+v"(sb[2]), "+v"(sb[3]));
      float mx[4], es[4];
#pragma unroll
      for (int i = 0; i < 4; ++i)
        mx[i] = max3(max3(sb[i].x, sb[i].y, sb[i].z), max3(sb[i].w, sa[i].x, sa[i].y), max2(sa[i].z, sa[i].w));
#pragma unroll
      for (int i = 0; i < 4; ++i) {
        auto r = __builtin_amdgcn_permlane16_swap(f2u(mx[i]), f2u(mx[i]), false, false);
        mx[i] = max2(u2f(r[0]), u2f(r[1]));
      }
#pragma unroll
      for (int i = 0; i < 4; ++i) {
        auto r = __builtin_amdgcn_permlane32_swap(f2u(mx[i]), f2u(mx[i]), false, false);
        mx[i] = max2(u2f(r[0]), u2f(r[1])) * SC;
        es[i] = __builtin_amdgcn_exp2f(__builtin_amdgcn_fmed3f(u2f(sb16[i] << 16), -60.f, 60.f) * LOG2E);
      }
      u32x4 pf[4];
#pragma unroll
      for (int i = 0; i < 4; ++i) {
        f32x4 pa, pb;
#pragma unroll
        for (int r = 0; r < 4; ++r) {
          pa[r] = __builtin_amdgcn_exp2f(sa[i][r] * SC - mx[i]);
          pb[r] = __builtin_amdgcn_exp2f(sb[i][r] * SC - mx[i]);
        }
        pf[i] = cat(pk4(pa), pk4(pb));
      }
      const f32x4 z = {0.f, 0.f, 0.f, 0.f};
      const u32x4 ones = {0x3f803f80u, 0x3f803f80u, 0x3f803f80u, 0x3f803f80u};
      f32x4 ls[4], oa[4], ob[4];
#pragma unroll
      for (int i = 0; i < 4; ++i) {
        ls[i] = mfma(ones, pf[i], z);
        oa[i] = mfma(cat(vl[i], vl[i + 1]), pf[i], z);
        ob[i] = mfma(cat(tt[i], tt[i + 1]), pf[i], z);
      }
      const unsigned mt0 = 8 * a + par;
      u32x2 op[4];
#pragma unroll
      for (int i = 0; i < 4; ++i) {
        const float f = es[i] * __builtin_amdgcn_rcpf(ls[i].x);
        op[i] = pk4(oa[i] * f);
        __builtin_amdgcn_raw_buffer_store_b64(pk4(ob[i] * f), rws, ovt, (mt0 + 2 * i) * 20480, AUX_SC1);
        if (i & 1) {
          auto sx = __builtin_amdgcn_permlane16_swap(op[i - 1].x, op[i].x, false, false);
          auto sy = __builtin_amdgcn_permlane16_swap(op[i - 1].y, op[i].y, false, false);
          u32x4 o; o.x = sx[0]; o.y = sy[0]; o.z = sx[1]; o.w = sy[1];
          __builtin_amdgcn_raw_buffer_store_b128(o, rws, ovo, (mt0 + 2 * i - 2) * 20480, AUX_SC1);
          STPAD();
        }
      }
      if (g == 0 && hd == 0) {
        const float e = h == 0 ? es[0] : h == 1 ? es[1] : h == 2 ? es[2] : es[3];
        __builtin_amdgcn_raw_buffer_store_b32(f2u(e), rws, WS_G + h * 128 + c * 4, mt0 * 64, AUX_SC1);
      }
    } else {
      const int cj = gc;
      using K = P1<false, RB>;
      K k;
      const int mh = RB ? (w < 6 ? 1 : 0) : (w >> 1);
      const int up = (w >> 1) & 1, kp = w & 1;
#pragma unroll
      for (int i = 0; i < K::NXP; ++i) {
        const int rho = 8 * (4 * (w & 3) + i) + (lane >> 3);
        const int j = rho >> 4;
        const int tok = 128 * a + 32 * (j >> 1) + 2 * (rho & 15) + (j & 1);
        k.xv[i] = tok * 1280 + (((lane & 7) ^ ((rho >> 1) & 7)) << 4);
      }
      k.xd = K::XB + 4 * (w & 3) * 1024;
      {
        const int r = lane >> 3, p8 = lane & 7;
        const int s = ((r >> 1) & 1) + 2 * kp;
        k.wv[0] = (8 * kp + r) * 3072 + cj * 384 + (((((p8 >> 1) ^ s) << 1) | (p8 & 1)) << 4) + (RB ? 0 : 16 * 3072 + 128);
        k.wv[1] = k.wv[0] + 128; k.wv[2] = k.wv[0] + 256; k.wv[3] = k.wv[0] + 16 * 3072;
      }
      k.wd0 = RB ? (kp * 6 + 3 * up) * 1024 : ((kp + 2) * 6 + 3 * up + 1) * 1024;
      k.wd1 = ((kp + 2) * 6 + 3 * up) * 1024;
      if (RB) asm volatile("" : "+v"(k.xv[0]), "+v"(k.xv[1]), "+v"(k.xv[2]), "+v"(k.xv[3]), "+v"(k.wv[2]), "+v"(k.wv[3]));
      asm volatile("" : "+v"(k.wv[0]), "+v"(k.wv[1]));
      bind();
      STAMP(0);
      const u64 pg = wg, pu = wu;
      k.srdx = mk_srd(x, 2048 * 1280);
      k.srdw = mk_srd(up ? pu : pg, 640 * 3072);
      asm volatile("global_load_dwordx2 %0, %1, %2 sc1" : "=v"(ev) : "v"(WS_EPOCH), "s"(ws) : "memory");
      STAMP(11);
      k.stage0(rot);
      BAR();
      if (!RB) pack_issue();
      k.stage1();
      k.xa0 = (K::XB + mh * 4 * 2048 + c * 128 + ((h ^ (c >> 1)) << 4)) ^ ((rot & 1) << 6);
      {
        const int s = ((q4 >> 1) & 1) + 2 * (h & 1);
        k.wa0 = (h * 6 + (RB ? 2 : (w & 1))) * 1024 + q4 * 128 + (((RB ? 2 * (w & 1) : 0) ^ s) << 5) + p4 * 8;
      }
      asm volatile("" : "+v"(k.xa0), "+v"(k.wa0));
      k.zero();
      k.stage2();
      STAMP(12);
      k.template run<RB ? 5 : 0, RB ? 10 : 0>([&]() {
        make_tag(std::integral_constant<int, RB ? K::VPRO : 8>{});
        BAR();
        STAMP(1);
      }, [&](int t) { if (RB) side_b(t); else side_f(t); }, tail);
      STAMP(2);
      constexpr int NK = RB ? 1 : 2;
      float nl2e = -LOG2E;
      asm volatile("" : "+s"(nl2e));
      const unsigned gso = WS_GU + ((8 * a + 4 * mh) * 48 + 6 * cj + (RB ? 4 + (w & 1) : 2 * (w & 1))) * 1024;
#pragma unroll
      for (int m = 0; m < 4; ++m) {
#pragma unroll
        for (int kk = 0; kk < NK; ++kk) {
          float v[8];
#pragma unroll
          for (int j = 0; j < 8; j += 2) {
            const f32x2 g2 = {k.acc[m][2 * kk + (j >> 2)][j & 3], k.acc[m][2 * kk + (j >> 2)][(j & 3) + 1]};
            const f32x2 u2 = {k.acc[m][2 * NK + 2 * kk + (j >> 2)][j & 3], k.acc[m][2 * NK + 2 * kk + (j >> 2)][(j & 3) + 1]};
            const f32x2 a2 = g2 * nl2e;
            const f32x2 d2 = f32x2{__builtin_amdgcn_exp2f(a2.x), __builtin_amdgcn_exp2f(a2.y)} + 1.f;
            const f32x2 r2 = g2 * u2 * f32x2{__builtin_amdgcn_rcpf(d2.x), __builtin_amdgcn_rcpf(d2.y)};
            v[j] = r2.x; v[j + 1] = r2.y;
          }
          u32x4 o; o.x = pk2(v[0], v[1]); o.y = pk2(v[2], v[3]); o.z = pk2(v[4], v[5]); o.w = pk2(v[6], v[7]);
          __builtin_amdgcn_raw_buffer_store_b128(o, rws, lane * 16, gso + m * (48 * 1024) + kk * 1024, AUX_SC1);
          STPAD();
        }
      }
    }
    SB();
    STAMP(13);
    asm volatile("" : "+v"(wo_[0][0]), "+v"(wo_[0][1]), "+v"(wo_[0][2]), "+v"(wo_[0][3]), "+v"(wo_[0][4]),
                      "+v"(wo_[1][0]), "+v"(wo_[1][1]), "+v"(wo_[1][2]), "+v"(wo_[1][3]), "+v"(wo_[1][4]));
    if (RB) asm volatile("" : "+v"(wg_[0][0]), "+v"(wg_[0][1]), "+v"(wg_[0][2]), "+v"(wg_[0][3]), "+v"(wg_[0][4]),
                              "+v"(wg_[1][0]), "+v"(wg_[1][1]), "+v"(wg_[1][2]), "+v"(wg_[1][3]), "+v"(wg_[1][4]));
    SB();
    STAMP(14);
    if (RB && idx >= 16) { if (PRE > 0) cell_dma(w - 4, 1); if (PRE > 1) cell_dma(w, 1); if (PRE > 2) cell_dma(w - 4, 2); WAITV(5 * PRE); }
    else if (RB || !PREA) WAITV(0);
    else { late_w(); WAITV(15); }
    STAMP(3);
    WAITL();
    BAR();
    if (!RB && w == 0 && lane < 2) {
      GAS u64* fp = (GAS u64*)((char*)ws + (lane ? WS_DONE + b * 8 : WS_FLAG + (a * 16 + (idx < 16 ? 8 : 0) + gc) * 8));
      __hip_atomic_store(fp, tag, RLX);
    }
    STAMP(4);

    SB();
    const unsigned fo = WS_FLAG + ((rb >> 1) * 16 + c) * 8;
    auto wait_flags = [&](u32x2& v, bool need) {
#pragma unroll 1
      for (unsigned spin = 0; spin < (1u << 18); ++spin) {
        if (__all(!need || (v.x == (unsigned)tag && v.y == (unsigned)(tag >> 32)))) break;
        ld64(v, fo, sws, 0);
        asm volatile("s_waitcnt vmcnt(0)" : "+v"(v) :: "memory");
      }
      asm volatile("" ::: "memory");
    };
    u32x2 fv;
    if (RB) {
      if (idx < 16) { if (PRE > 0) cell_dma(w - 4, 1); if (PRE > 1) cell_dma(w, 1); if (PRE > 2) cell_dma(w - 4, 2); }
      if (PRE < 1) cell_dma(w - 4, 1); if (PRE < 2) cell_dma(w, 1); if (PRE < 3) cell_dma(w - 4, 2);
      cell_dma(w, 2);
    } else {
      if (!PREA) late_w();
      if (w == 0) {
        ld64(fv, fo, sws, 0);
        asm volatile("s_waitcnt vmcnt(0)" : "+v"(fv) :: "memory");
        wait_flags(fv, c >= 8);
      }
    }
    if (RB) __builtin_amdgcn_s_setprio(1);
    BAR();
    STAMP(5);
    f32x4 acc[4][5];
    u32x4 ag[6][4];
    auto tk = [](int) {};
    tk(0);
    const unsigned gb = (EXP & 1) ? pwb : WS_GU + (4 * rb * 48 + 6 * w) * 1024;
    auto ldg = [&](int s, int m) { ld128(ag[s][m], lane * 16, sws, gb + gstep(s) * 1024 + m * (48 * 1024)); };
    unsigned ca0 = CELL0 + w * 5120 + lane * 16, ca1 = w * 15360 + lane * 16;
    asm volatile("" : "+v"(ca0), "+v"(ca1));
    {
      using S = Sched<RB>;
      constexpr int NO = S::NO, OB = S::OB;
      u32x4 af[2][4], at[4];
      u32x2 tl[4], th[4];
      const unsigned ob = WS_O + 4 * rb * 20480;
      auto req = [&](auto IT) {
        constexpr int i = IT.value;
        if constexpr (i < 8) ld128(af[i >> 2][i & 3], lane * 16, sws, ob + (i & 3) * 20480 + (2 * w + (i >> 2)) * 1024);
        else if constexpr (i < OB) ld64((i & 1) ? th[(i - 8) >> 1] : tl[(i - 8) >> 1], lane * 8, sws, ob + ((i - 8) >> 1) * 20480 + (16 + w) * 1024 + (i & 1) * 512);
        else if constexpr (i < OB + 29 && (i - OB) % 5 < 4) ldg((i - OB) / 5, (i - OB) % 5);
        else if constexpr (i < OB + 29) dma16c(w * 15360, 10240 + ((i - OB) / 5) * 1024, lane * 16, sws, pwb + (20 + 6 * w + gstep(5)) * 5120 + ((i - OB) / 5) * 1024);
        else ld32(i == OB + 29 ? sg0 : i == OB + 30 ? sg1 : sg2, c * 4, sws, WS_G + (4 * rb + min(t0 + i - OB - 29, 19) / 5) * 64);
      };
      sfor<0, S::upto(PL - 1)>(req);
      if (RB) { __builtin_amdgcn_s_setprio(0); WAITV(S::upto(PL - 1)); }
      else if (w == 0) wait_flags(fv, c < 8);
      STAMP(6);
      BAR();
      STAMP(7);
      tk(1);
      u32x4 wc[5];
      sfor<0, NO + 6>([&](auto ST) {
        constexpr int st = ST.value, s = st - NO;
        if constexpr (st == 2 && !RB) {
          asm volatile("s_waitcnt vmcnt(%8)" : "+v"(tl[0]), "+v"(tl[1]), "+v"(tl[2]), "+v"(tl[3]), "+v"(th[0]), "+v"(th[1]), "+v"(th[2]), "+v"(th[3])
                       : "i"(S::issued(st, 0) - 16) : "memory");
#pragma unroll
          for (int m = 0; m < 4; ++m) at[m] = cat(tl[m], th[m]);
          asm volatile("s_nop 1" : "+v"(at[0]), "+v"(at[1]), "+v"(at[2]), "+v"(at[3]));
        }
        sfor<0, 20>([&](auto IT) {
          constexpr int i = IT.value, m = i / 5, n = i % 5, is = S::issued(st, i);
          if constexpr (n == 0) {
            if constexpr (s >= 0)      asm volatile("s_waitcnt vmcnt(%1)" : "+v"(ag[s][m]) : "i"(is - S::gpos(s, m) - 1) : "memory");
            else if constexpr (st < 2) asm volatile("s_waitcnt vmcnt(%1)" : "+v"(af[st][m]) : "i"(is - (4 * st + m) - 1) : "memory");
          }
          if constexpr (st == 0 && i == 0 && !RB)
            asm volatile("" : "+v"(wo_[2][0]), "+v"(wo_[2][1]), "+v"(wo_[2][2]), "+v"(wo_[2][3]), "+v"(wo_[2][4]),
                              "+v"(wg_[0][0]), "+v"(wg_[0][1]), "+v"(wg_[0][2]), "+v"(wg_[0][3]), "+v"(wg_[0][4]),
                              "+v"(wg_[1][0]), "+v"(wg_[1][1]), "+v"(wg_[1][2]), "+v"(wg_[1][3]), "+v"(wg_[1][4]));
          if constexpr (st == 0)     mul0(acc[m][n], wo_[0][n], af[0][m]);
          else if constexpr (s >= 0) mac(acc[m][n], wg_[s & 1][n], ag[s][m]);
          else if constexpr (st < 2) mac(acc[m][n], wo_[1][n], af[1][m]);
          else                       mac(acc[m][n], wo_[2][n], at[m]);
          SB();
          if constexpr (s == 4 && i == 9) WAITV(is - S::gpos(4, 4) - 1);
          if constexpr (m == (s == 4 ? 2 : 0) && s >= 1 && s < 5) wc[n] = lds128((s > 1 ? ca1 + (s - 2) * 5120 : ca0) + n * 1024);
          if constexpr (S::issued(st, i + 1) > is) req(std::integral_constant<int, is>{});
          SB();
        });
        tk(2 + st);
        if constexpr (s >= 1 && s < 5) {
          asm volatile("" : "+v"(wc[0]), "+v"(wc[1]), "+v"(wc[2]), "+v"(wc[3]), "+v"(wc[4]));
#pragma unroll
          for (int n = 0; n < 5; ++n) wg_[(s + 1) & 1][n] = wc[n];
        }
      });
      asm volatile("s_waitcnt vmcnt(0)" : "+v"(sg0), "+v"(sg1), "+v"(sg2) :: "memory");
    }
    MFMA_DONE();
    STAMP(8);
#pragma unroll
    for (int j = 0; j < 10; ++j) {
      const int ta = j < 4 ? 3 * j : j < 6 ? 6 * j - 22 : 2 * j, tb = j < 4 ? ta + 1 : j < 6 ? ta + 3 : ta + 1;
      *(LAS u32x4*)(ca1 + j * 1024) = cat(pk4(acc[ta / 5][ta % 5]), pk4(acc[tb / 5][tb % 5]));
    }
    tk(12);
  };
  if (w < 4) role(std::false_type{}); else role(std::true_type{});

  if (b == 0 && w == 4) {
    GAS u64* dp = (GAS u64*)((char*)ws_ + WS_DONE + lane * 8);
#pragma unroll 1
    for (unsigned spin = 0; spin < (1u << 18); ++spin) {
      bool ok = true;
#pragma unroll
      for (int j = 0; j < 4; ++j) ok &= __hip_atomic_load(dp + j * 64, RLX) == tag;
      if (__all(ok)) break;
      __builtin_amdgcn_s_sleep(2);
    }
    if (lane == 0) __hip_atomic_store((GAS u64*)((char*)ws_ + WS_EPOCH), tag, RLX);
  }
  sg0 = __builtin_amdgcn_rcpf(1.f + sg0); sg1 = __builtin_amdgcn_rcpf(1.f + sg1); sg2 = __builtin_amdgcn_rcpf(1.f + sg2);
  auto ooff = [&](int tl) {
    const int m = tl / 5, n = tl - 5 * m;
    return (unsigned)((64 * rb + 32 * (m >> 1) + 2 * c + (m & 1)) * 1280 + ct * 160 + n * 32 + h * 8);
  };
  unsigned oo0 = ooff(t0), oo1 = ooff(t0 + 1), oo2 = ooff(t0 + 2);
  asm volatile("" : "+v"(sg0), "+v"(sg1), "+v"(sg2), "+v"(oo0), "+v"(oo1), "+v"(oo2));
  WAITL();
  BAR();
  STAMP(9);
  {
    u32x4 p[8];
    u32x2 p2[8];
    unsigned ra = (w < 4 ? w : w + 2) * 1024 + lane * 16, rb_ = ra + 4 * 15360;
    unsigned rc = (4 + (w >> 1)) * 1024 + (w & 1) * 8 + lane * 16, rd = rc + 4 * 15360;
    asm volatile("" : "+v"(ra), "+v"(rb_), "+v"(rc), "+v"(rd));
#pragma unroll
    for (int v = 0; v < 8; ++v) p[v] = lds128((v < 4 ? ra : rb_) + (v & 3) * 15360);
    if (w < 4) {
#pragma unroll
      for (int v = 0; v < 8; ++v) { p2[v] = *(const LAS u32x2*)((v < 4 ? rc : rd) + (v & 3) * 15360); SB(); }
    }
    SB();
#pragma unroll
    for (int j = 0; j < 3; ++j) {
      if (j < 2 || w < 4) {
        f32x4 s = {0.f, 0.f, 0.f, 0.f};
#pragma unroll
        for (int v = 0; v < 8; ++v) {
          const unsigned px = j == 0 ? p[v].x : j == 1 ? p[v].z : p2[v].x, py = j == 0 ? p[v].y : j == 1 ? p[v].w : p2[v].y;
          const f32x4 q = {u2f(px << 16), u2f(px & 0xffff0000u), u2f(py << 16), u2f(py & 0xffff0000u)};
          s = v ? s + q : q;
        }
        const u32x2 ov = pk4(s * (j == 0 ? sg0 : j == 1 ? sg1 : sg2));
        asm volatile("global_store_dwordx2 %0, %1, %2" OUTFL :: "v"(j == 0 ? oo0 : j == 1 ? oo1 : oo2), "v"(ov), "s"(out_) : "memory");
      }
    }
  }
  STAMP(10);
  if (AMP0 >= 0 && (AMPSEL == 0 || (AMPSEL == 1) == (idx < 16))) {
    const u64 d = amp_t1 - amp_t0, t = __builtin_amdgcn_s_memrealtime();
    while (__builtin_amdgcn_s_memrealtime() - t < d) __builtin_amdgcn_s_sleep(1);
  }
}
}

extern "C" void kernel_launch(void* const* d_in, const int* in_sizes, int n_in, void* d_out, int out_size, void* d_ws,
                              size_t ws_size, hipStream_t stream) {
  fused<<<256, 512, 0, stream>>>(d_in[0], d_in[1], d_in[2], d_in[4], d_in[5], d_in[6], d_in[7], d_out, d_ws);
}
```

```cpp
#include <hip/hip_runtime.h>
#include <hip/hip_bf16.h>
#include <stdint.h>
#include <type_traits>

#define STAMPS 0
#define BEARLY 0
#define CODEPF 1
#define TOUCH_NT ""
#define TOUCH 1
#define EXP 0
#define GLA 4
#define PART16 1
#define PL 2
#define PRE 2
#define OUTFL ""
#define PREA 0
#define WGR 4
#define GPOLL 1
#define ROT 4

#define LAS __attribute__((address_space(3)))
#define GAS __attribute__((address_space(1)))
typedef __bf16 bf16x8 __attribute__((ext_vector_type(8)));
typedef __bf16 bf16x2 __attribute__((ext_vector_type(2)));
typedef float f32x4 __attribute__((ext_vector_type(4)));
typedef float f32x2 __attribute__((ext_vector_type(2)));
typedef unsigned u32x4 __attribute__((ext_vector_type(4)));
typedef unsigned u32x2 __attribute__((ext_vector_type(2)));
typedef short s16x4 __attribute__((ext_vector_type(4)));
typedef unsigned long long u64;

namespace {
constexpr int LDS_BYTES = 163840;
constexpr unsigned GATE_OFF = 110592, PACK_OFF = 122880, PKF_OFF = 162816, CELL0 = 122880;
constexpr unsigned OOB = 0x40000000u;
#define CODE_B 40000
constexpr unsigned CODE_BYTES = CODE_B;
constexpr unsigned WS_FLAG = 0;
constexpr unsigned WS_DONE = 4096;
constexpr unsigned WS_PKF = 8192;
constexpr unsigned WS_EPOCH = 12288;
constexpr unsigned WS_G = 16384;
constexpr unsigned WS_O = 131072;
constexpr unsigned WS_GU = WS_O + 128 * 40 * 512;
constexpr unsigned WS_PW = WS_GU + 128 * 48 * 1024;
constexpr unsigned WS_END = WS_PW + 8 * 68 * 5120;
constexpr unsigned WS_STAMP = 16u << 20;
constexpr int AUX_SC1 = 16;
constexpr float LOG2E = 1.4426950408889634f;

__device__ __forceinline__ u32x4 mk_srd(u64 a, unsigned bytes) {
  u32x4 r; r.x = (unsigned)a; r.y = (unsigned)(a >> 32) & 0xffffu; r.z = bytes; r.w = 0x00020000u; return r;
}
__device__ __forceinline__ void dma16(unsigned lds, unsigned imm, unsigned voff, u32x4 srd, unsigned soff) {
  asm volatile("s_add_u32 m0, %0, %4\n\ts_nop 0\n\tbuffer_load_dwordx4 %1, %2, %3 offen lds"
               :: "s"(lds), "v"(voff), "s"(srd), "s"(soff), "i"(imm) : "memory", "scc");
}
__device__ __forceinline__ void dma16c(unsigned lds, unsigned imm, unsigned voff, u32x4 srd, unsigned soff) {
  asm volatile("s_add_u32 m0, %0, %4\n\ts_nop 0\n\tbuffer_load_dwordx4 %1, %2, %3 offen sc1 lds"
               :: "s"(lds), "v"(voff), "s"(srd), "s"(soff), "i"(imm) : "memory", "scc");
}
__device__ __forceinline__ void ld128(u32x4& d, unsigned voff, u32x4 srd, unsigned soff) {
  asm volatile("buffer_load_dwordx4 %0, %1, %2, %3 offen sc1" : "=v"(d) : "v"(voff), "s"(srd), "s"(soff) : "memory");
}
__device__ __forceinline__ void ld64(u32x2& d, unsigned voff, u32x4 srd, unsigned soff) {
  asm volatile("buffer_load_dwordx2 %0, %1, %2, %3 offen sc1" : "=v"(d) : "v"(voff), "s"(srd), "s"(soff) : "memory");
}
__device__ __forceinline__ void ld32(float& d, unsigned voff, u32x4 srd, unsigned soff) {
  asm volatile("buffer_load_dword %0, %1, %2, %3 offen sc1" : "=v"(d) : "v"(voff), "s"(srd), "s"(soff) : "memory");
}
__device__ __forceinline__ u32x4 lds128(unsigned a) { return *(const LAS u32x4*)a; }
__device__ __forceinline__ u32x2 ldstr(unsigned a) {
  return __builtin_bit_cast(u32x2, __builtin_amdgcn_ds_read_tr16_b64_v4i16((LAS s16x4*)a));
}
__device__ __forceinline__ u32x4 cat(u32x2 a, u32x2 b) { u32x4 r; r.x = a.x; r.y = a.y; r.z = b.x; r.w = b.y; return r; }
__device__ __forceinline__ f32x4 mfma(u32x4 a, u32x4 b, f32x4 c) {
  return __builtin_amdgcn_mfma_f32_16x16x32_bf16(__builtin_bit_cast(bf16x8, a), __builtin_bit_cast(bf16x8, b), c, 0, 0, 0);
}
__device__ __forceinline__ void mac(f32x4& c, u32x4 a, u32x4 b) {
  asm volatile("v_mfma_f32_16x16x32_bf16 %0, %1, %2, %0" : "+v"(c) : "v"(a), "v"(b));
}
__device__ __forceinline__ void mul0(f32x4& c, u32x4 a, u32x4 b) {
  asm volatile("v_mfma_f32_16x16x32_bf16 %0, %1, %2, 0" : "=&v"(c) : "v"(a), "v"(b));
}
__device__ __forceinline__ unsigned pk2(float a, float b) {
  f32x2 t = {a, b};
  return __builtin_bit_cast(unsigned, __builtin_convertvector(t, bf16x2));
}
__device__ __forceinline__ u32x2 pk4(f32x4 v) { u32x2 r; r.x = pk2(v.x, v.y); r.y = pk2(v.z, v.w); return r; }
__device__ __forceinline__ float u2f(unsigned u) { return __builtin_bit_cast(float, u); }
__device__ __forceinline__ unsigned f2u(float f) { return __builtin_bit_cast(unsigned, f); }
#define SB() __builtin_amdgcn_sched_barrier(0)
#define WAITV(n) asm volatile("s_waitcnt vmcnt(%0)" :: "i"(n) : "memory")
#define WAITVL(n) asm volatile("s_waitcnt vmcnt(%0) lgkmcnt(0)" :: "i"(n) : "memory")
#define WAITL() asm volatile("s_waitcnt lgkmcnt(0)" ::: "memory")
#define BAR() asm volatile("s_barrier" ::: "memory")
#define STPAD() do { SB(); asm volatile("s_nop 1" ::: "memory"); SB(); } while (0)
#define MFMA_DONE() do { SB(); asm volatile("s_nop 7\n\ts_nop 1" ::: "memory"); SB(); } while (0)
#define RLX __ATOMIC_RELAXED, __HIP_MEMORY_SCOPE_AGENT
__device__ __forceinline__ unsigned rfl(unsigned v) { return __builtin_amdgcn_readfirstlane(v); }
__device__ __forceinline__ unsigned opaque_lane() { unsigned l = threadIdx.x & 63; asm volatile("" : "+v"(l)); return l; }

#define AMP0 -1
#define AMP1 -1
#define AMPSEL 0
#define STAMP(i) do {} while (0)

template <int I, int N, class F>
__device__ __forceinline__ void sfor(F&& f) {
  if constexpr (I < N) { f(std::integral_constant<int, I>{}); sfor<I + 1, N>(f); }
}
template <bool RB> struct Sched {
  static constexpr int NO = RB ? 2 : 3, OB = RB ? 8 : 16;
  static constexpr int gpos(int s, int m) { return OB + 5 * s + m; }
  static constexpr int upto(int st) { return st < 2 ? 4 * (st + 1) : st < NO ? 16 : st < NO + 5 ? OB + 5 * (st - NO + 1) : st == NO + 5 ? OB + 29 : OB + 32; }
  static constexpr int issued(int st, int i) {
    const int base = upto(st + PL - 1), goal = upto(st + PL), every = goal - base > 5 ? 2 : goal - base > 4 ? 4 : 5;
    int n = base;
    for (int k = 0; k < i; ++k) if (k % every == 1 && n < goal) ++n;
    return n;
  }
};
__device__ __forceinline__ float max3(float a, float b, float c) { float r; asm("v_max3_f32 %0, %1, %2, %3" : "=v"(r) : "v"(a), "v"(b), "v"(c)); return r; }
__device__ __forceinline__ float max2(float a, float b) { float r; asm("v_max_f32 %0, %1, %2" : "=v"(r) : "v"(a), "v"(b)); return r; }

template <bool QKV, bool RB>
struct P1 {
  static constexpr int NM = QKV ? 5 : 4;
  static constexpr int NN = QKV ? 4 : (RB ? 4 : 8);
  static constexpr int NWS = 3;
  static constexpr unsigned WSL = QKV ? 16384 : 24576, XB = NWS * WSL, XSL = QKV ? 20480 : 16384;
  static constexpr unsigned WROW = QKV ? 3840 : 3072;
  static constexpr int NWP = QKV ? 2 : (RB ? 4 : 2);
  static constexpr int NXE = QKV ? (RB ? 1 : 2) : (RB ? 2 : 0), NXO = QKV ? 1 : NXE;
  static constexpr int NXP = NXO + NXE;
  static constexpr int OO = NWP + NXO, OE = NWP + NXE;
  static constexpr bool HAND = !QKV;
  static constexpr int ht = 4;
  f32x4 acc[NM][NN];
  u32x4 srdx, srdw;
  unsigned xv[4], wv[4];
  unsigned xd;
  unsigned wd0, wd1;
  unsigned xa0, wa0, wa1, wa2;
  unsigned gmask;
  __device__ __forceinline__ void tick(int) {}

  unsigned sow, sox, gof;
  unsigned kg, kw, kx, kr;
  template <bool ODD, bool X = true>
  __device__ __forceinline__ void loads(unsigned wwr, unsigned xwr) {
    if (X && NXP) {
#pragma unroll
      for (int i = ODD ? NXE : 0; i < (ODD ? NXP : NXE); ++i) dma16(xwr + xd, i * 1024, xv[i], srdx, sox);
      if (ODD) { kx += 1; sox = (kx ^ (kg >> 1)) * 128; }
    }
    dma16(wwr + wd0, 0, wv[0], srdw, sow);
    dma16(wwr + wd0, 1024, wv[1], srdw, sow);
    if (!QKV && RB) {
      dma16(wwr + wd0, 2048, wv[2], srdw, sow);
      dma16(wwr + wd1, 0, wv[3], srdw, sow);
    }
  }
  __device__ __forceinline__ void other(unsigned wwr) {
    dma16(wwr + wd1, 1024, wv[3], srdw, sow + 128);
    dma16(wwr + wd1, 2048, wv[3], srdw, sow + 256);
  }
  __device__ __forceinline__ void next() { kw += 1; sow = (kw ^ kg) * (32 * WROW); asm volatile("" : "+s"(sow), "+s"(kw)); if (NXP) asm volatile("" : "+s"(sox), "+s"(kx)); }
  __device__ __forceinline__ void stage0(int rot) {
    kg = rot; kw = 0; kx = 0; kr = 0; sow = kg * (32 * WROW); sox = (kg >> 1) * 128; gof = kg * 256;
    if (HAND && !RB) { kw = 3; sow = (3 ^ kg) * (32 * WROW); return; }
#pragma unroll
    for (int i = 0; i < NXE; ++i) dma16(xd, i * 1024, xv[i], srdx, sox);
    loads<true>(0, 0); if (HAND) other(0); next();
  }
  __device__ __forceinline__ void stage1() { if (HAND && !RB) return; loads<false>(WSL, XSL); if (HAND) other(WSL); next(); }
  __device__ __forceinline__ void stage2() { if (HAND && !RB) return; loads<true>(2 * WSL, XSL); if (HAND) other(2 * WSL); next(); }
  static constexpr int VPRO = OE + OO + (HAND ? 4 : 0);
  struct Ad { unsigned x, w[4]; };
  __device__ __forceinline__ void addr(Ad& a, unsigned xb, unsigned wrd) {
    a.x = xb; a.w[0] = wa0 + wrd;
    if (QKV) {
      const unsigned s2 = (gmask & (gof - wrd)) + wrd;
      kr += 1; gof = (kr ^ kg) * 256;
      a.w[1] = wa1 + s2; a.w[2] = wa2 + s2;
      asm volatile("" : "+v"(a.x), "+v"(a.w[0]), "+v"(a.w[1]), "+v"(a.w[2]));
    } else if (RB) {
      a.w[1] = a.w[0] ^ 32;
      asm volatile("" : "+v"(a.x), "+v"(a.w[0]), "+v"(a.w[1]));
    } else {
      a.w[1] = a.w[0] ^ 32; a.w[2] = a.w[0] ^ 64; a.w[3] = a.w[0] ^ 96;
      asm volatile("" : "+v"(a.x), "+v"(a.w[0]), "+v"(a.w[1]), "+v"(a.w[2]), "+v"(a.w[3]));
    }
  }
  __device__ __forceinline__ unsigned wad(const Ad& a, int n, int hi) {
    if (QKV) return n == 2 ? a.w[1 + hi] : a.w[0] + (n == 0 ? 0 : n == 1 ? 160 : 320) + hi * 1920;
    if (RB) return a.w[n & 1] + (n >> 1) * 3072 + hi * 512;
    return a.w[n & 3] + (n >> 2) * 3072 + hi * 512;
  }
  __device__ __forceinline__ void rd_all(u32x4 (&xf)[NM], u32x4 (&wf)[NN], const Ad& a) {
#pragma unroll
    for (int n = 0; n < NN; ++n) wf[n] = cat(ldstr(wad(a, n, 0)), ldstr(wad(a, n, 1)));
#pragma unroll
    for (int m = 0; m < NM; ++m) xf[m] = lds128(a.x + m * 2048);
  }
  template <bool RD = true, class L>
  __device__ __forceinline__ void mm_rd(const u32x4 (&xc)[NM], const u32x4 (&wc)[NN], u32x4 (&xn)[NM], u32x4 (&wn)[NN], const Ad& a, L&& late) {
    u32x2 lo[NN];
    constexpr bool NMAJ = !QKV && !RB;
    constexpr bool EARLY = !QKV && RB && BEARLY;
    if (EARLY && RD) { rd_all(xn, wn, a); SB(); }
    int r = 0;
#pragma unroll
    for (int i = 0; i < NM * NN; ++i) {
      const int m = NMAJ ? i % NM : i / NN, n = NMAJ ? i / NM : i % NN;
      if (QKV && m == 0 && n == 0) continue;
      if (QKV && n == 3) mac(acc[m][3], xc[m], wc[3]);
      else               mac(acc[m][n], wc[n], xc[m]);
      SB();
      if (!RD || EARLY) continue;
      if (NMAJ) {
        if (i < 4) xn[i] = lds128(a.x + i * 2048);
        else if ((i & 3) < 2) {
          const int k = (i >> 2) - 1;
          if (i & 1) wn[k] = cat(lo[k], ldstr(wad(a, k, 1))); else lo[k] = ldstr(wad(a, k, 0));
        }
      } else {
        if (r < 2 * NN) {
          const int k = r >> 1;
          if (r & 1) wn[k] = cat(lo[k], ldstr(wad(a, k, 1))); else lo[k] = ldstr(wad(a, k, 0));
        } else if (r < 2 * NN + NM) {
          xn[r - 2 * NN] = lds128(a.x + (r - 2 * NN) * 2048);
        }
        else late(r - 2 * NN - NM);
        ++r;
      }
      SB();
    }
    if (NMAJ && RD) { wn[NN - 1] = cat(ldstr(wad(a, NN - 1, 0)), ldstr(wad(a, NN - 1, 1))); SB(); }
  }
  __device__ __forceinline__ void pin(u32x4 (&xf)[NM], u32x4 (&wf)[NN]) {
    asm volatile("" : "+v"(xf[0]), "+v"(xf[1]), "+v"(xf[2]), "+v"(xf[3]), "+v"(wf[0]), "+v"(wf[1]), "+v"(wf[2]), "+v"(wf[3]));
    if (NM == 5) asm volatile("" : "+v"(xf[NM - 1]));
    if (NN == 8) asm volatile("" : "+v"(wf[NN - 4]), "+v"(wf[NN - 3]), "+v"(wf[NN - 2]), "+v"(wf[NN - 1]));
  }
  __device__ __forceinline__ void zero() {
#pragma unroll
    for (int m = 0; m < NM; ++m)
#pragma unroll
      for (int n = 0; n < NN; ++n) { acc[m][n] = f32x4{0.f, 0.f, 0.f, 0.f}; asm volatile("" : "+v"(acc[m][n])); }
  }
  template <int TL0, int TL1, class F, class G, class H>
  __device__ __forceinline__ void run(F&& pre, G&& extra, H&& tail) {
    pre();
    u32x4 xA[NM], wA[NN], xB[NM], wB[NN];
    Ad a;
    addr(a, xa0, 0);
    rd_all(xA, wA, a);
    if (!RB) __builtin_amdgcn_s_setprio(1);
    unsigned w0 = 0, w1 = WSL, w2 = 2 * WSL;
    unsigned xp = 2 * XSL, x0 = 0, x1 = XSL;
    int t = 0;
    unsigned nw1, nw2, nx1;
    auto adv0 = [&]() { nw1 = w2 + WSL; if (nw1 == NWS * WSL) nw1 = 0; asm volatile("" : "+s"(nw1)); };
    auto adv1 = [&]() { nw2 = nw1 + WSL; if (nw2 == NWS * WSL) nw2 = 0; asm volatile("" : "+s"(nw2)); };
    auto adv2 = [&]() { nx1 = x1 + XSL; if (nx1 == 3 * XSL) nx1 = 0; asm volatile("" : "+s"(nx1)); };
    auto adv3 = [&]() {
      w0 = w2; w1 = nw1; w2 = nw2; xp = x0; x0 = x1; x1 = nx1; t += 2;
      asm volatile("" : "+s"(w0), "+s"(w1), "+s"(w2), "+s"(xp), "+s"(x0), "+s"(x1), "+s"(t));
    };
    auto adv = [&]() { adv0(); adv1(); adv2(); adv3(); };
    asm volatile("" : "+s"(w0), "+s"(w1), "+s"(w2), "+s"(xp), "+s"(x0), "+s"(x1), "+s"(t));
    if (QKV) asm volatile("" : "+s"(gof), "+s"(kr));
    auto none = [](int) {};
    if (RB) {
      auto help = [&](int tt, unsigned wwr) { if (HAND && __builtin_expect(tt < ht, 0)) other(wwr); extra(tt); };
      pin(xA, wA);
      SB(); WAITVL(OO + (HAND ? 2 : 0)); tick(0); BAR(); tick(1); SB();
      loads<false>(w0, xp); help(0, w0); next(); addr(a, (xa0 ^ 64) + x0, w1); SB();
      auto half = [&]() {
        mm_rd(xA, wA, xB, wB, a, [&](int i) {
          if (QKV && i == 0) adv0();
          if (QKV && i == 2) adv1();
          if (QKV && i == 4) adv2();
        });
        pin(xB, wB);
        SB(); WAITVL(OE); tick(2 * t + 2); BAR(); tick(2 * t + 3); SB();
        loads<true>(w1, xp); help(t + 1, w1); next();
        addr(a, xa0 + x1, w2);
        if (QKV) adv3(); else adv();
        SB();
        mm_rd(xB, wB, xA, wA, a, none);
      };
#pragma unroll 1
      do {
        half();
        pin(xA, wA);
        SB(); WAITVL(OO); tick(2 * t); BAR(); tick(2 * t + 1); SB();
        loads<false>(w0, xp); help(t, w0); next(); addr(a, (xa0 ^ 64) + x0, w1); SB();
        if (QKV) asm volatile("" : "+s"(gof), "+s"(kr));
      } while (t < 14);
      half();
    } else {
      addr(a, (xa0 ^ 64) + x0, w1);
      auto trip = [&](auto EARLY) {
        constexpr bool E = EARLY.value;
        pin(xA, wA);
        SB(); if (E) WAITL(); else WAITVL(OO);
        tick(2 * t); BAR(); tick(2 * t + 1); SB();
        mm_rd(xA, wA, xB, wB, a, none);
        if (E && t < ht) extra(t); else loads<false>(w0, xp);
        next(); addr(a, xa0 + x1, w2); adv0(); adv1(); adv2(); SB();
        pin(xB, wB);
        SB(); if (E) WAITL(); else WAITVL(OE);
        tick(2 * t + 2); BAR(); tick(2 * t + 3); SB();
        mm_rd(xB, wB, xA, wA, a, none);
        if (E && t < ht) extra(t + 1); else loads<true>(w1, xp);
        next(); adv3(); addr(a, (xa0 ^ 64) + x0, w1); SB();
        if (QKV) asm volatile("" : "+s"(gof), "+s"(kr));
      };
      if (HAND) {
#pragma unroll 1
        do trip(std::true_type{}); while (t < ht + 2);
      }
#pragma unroll 1
      do trip(std::false_type{}); while (t < 16);
    }
    constexpr unsigned S16 = (16 % NWS) * WSL, S17 = (17 % NWS) * WSL, S18 = (18 % NWS) * WSL, S19 = (19 % NWS) * WSL, X8 = 2 * XSL, X9 = 0;
    pin(xA, wA);
    SB(); WAITVL(OO);
    tick(32); BAR(); tick(33); SB();
    if (RB) { loads<false, false>(S16, 0); tail(0); addr(a, (xa0 ^ 64) + X8, S17); SB(); mm_rd(xA, wA, xB, wB, a, none); }
    else    { mm_rd(xA, wA, xB, wB, a, none); loads<false, false>(S16, 0); tail(0); addr(a, xa0 + X9, S18); SB(); }
    pin(xB, wB);
    SB(); WAITVL(NWP + TL0); tick(34); BAR(); tick(35); SB();
    if (RB) { tail(1); addr(a, xa0 + X9, S18); SB(); mm_rd(xB, wB, xA, wA, a, none); }
    else    { mm_rd(xB, wB, xA, wA, a, none); tail(1); addr(a, (xa0 ^ 64) + X9, S19); SB(); }
    pin(xA, wA);
    SB(); WAITVL(TL0 + TL1); tick(36); BAR(); tick(37); SB();
    if (RB) { tail(2); addr(a, (xa0 ^ 64) + X9, S19); SB(); mm_rd(xA, wA, xB, wB, a, none); }
    else    { mm_rd(xA, wA, xB, wB, a, none); tail(2); SB(); }
    pin(xB, wB);
    SB(); WAITL(); tick(38); BAR(); tick(39); SB();
    if (RB) { tail(3); SB(); mm_rd<false>(xB, wB, xA, wA, a, none); }
    else    { mm_rd<false>(xB, wB, xA, wA, a, none); tail(3); SB(); }
    if (!RB) __builtin_amdgcn_s_setprio(0);
    MFMA_DONE();
    tick(40);
  }
};

__global__ __launch_bounds__(512, 2) void fused(const void* x_, const void* wqkv_, const void* wo_p, const void* wg_p,
                                                const void* wu_p, const void* wd_p, const void* gw_, void* out_, void* ws_) {
  __shared__ __attribute__((aligned(16))) char smem[LDS_BYTES];
  asm volatile("" :: "s"((unsigned)(size_t)(LAS char*)smem));
  const int tid = threadIdx.x, lane = tid & 63;
  const int w = rfl(tid >> 6);
  const int h = lane >> 4, c = lane & 15, q4 = (lane & 15) >> 2, p4 = lane & 3;
  const int b = blockIdx.x, xcd = b & 7, idx = b >> 3;
  const int rh = xcd & 1, cq = xcd >> 1;
  const int a = 8 * rh + (idx & 7);
  const int gc = 2 * cq + ((idx >> 3) & 1);
  const int rb = 4 * xcd + (idx >> 3), ct = idx & 7;
  u64 x, wqkv, wo, wg, wu, wd, gw;
  char* ws;
  __amdgpu_buffer_rsrc_t rws;
  u32x4 sws;
  auto bind = [&]() {
    x = (u64)x_; wqkv = (u64)wqkv_; wo = (u64)wo_p; wg = (u64)wg_p; wu = (u64)wu_p; wd = (u64)wd_p; gw = (u64)gw_;
    u64 pw = (u64)ws_;
    asm volatile("" : "+s"(x), "+s"(wqkv), "+s"(wo), "+s"(wg), "+s"(wu), "+s"(wd), "+s"(gw), "+s"(pw));
    ws = (char*)pw;
    rws = __builtin_amdgcn_make_buffer_rsrc(ws, 0, WS_END, 0x00020000);
    sws = mk_srd(pw, WS_END);
  };
  u64 amp_t0 = 0, amp_t1 = 0;

  if (CODEPF) {
    u64 pc;
    asm volatile("s_getpc_b64 %0" : "=s"(pc));
    asm volatile("s_mov_b32 m0, %0\n\ts_nop 0\n\tbuffer_load_dword %1, %2, 0 offen lds"
                 :: "s"(PKF_OFF), "v"(lane < 6 ? ((idx & 7) * 48 + w * 6 + lane) * 128 : OOB), "s"(mk_srd(pc & ~127ull, CODE_BYTES)) : "memory");
  }
  u32x2 ev;
  u64 tag = 0;
  auto make_tag = [&](auto NV) {
    unsigned lo, hi;
    asm volatile("s_waitcnt vmcnt(%4)\n\tv_readfirstlane_b32 %0, %2\n\tv_readfirstlane_b32 %1, %3"
                 : "=s"(lo), "=s"(hi) : "v"(ev.x), "v"(ev.y), "i"(NV.value) : "memory");
    const u64 e = ((u64)hi << 32) | lo;
    tag = (e ^ 0x9E3779B97F4A7C15ull) * 0xBF58476D1CE4E5B9ull + 0x632BE59BD9B4E019ull;
    if (tag == e) tag += 1;
  };
  const unsigned pwb = WS_PW + ct * (68 * 5120);
  const int grot = 3 * ((idx >> 3) & 1);
  auto gstep = [&](int s) { const int v = s + grot; return v >= 6 ? v - 6 : v; };
  auto cell_dma = [&](int v, int cell) {
    const unsigned so = pwb + (20 + 6 * v + gstep(2 + cell)) * 5120;
#pragma unroll
    for (int j = 0; j < 5; ++j) dma16c(cell ? v * 15360 : CELL0 + v * 5120, (cell ? cell - 1 : 0) * 5120 + j * 1024, lane * 16, sws, so + j * 1024);
  };
  auto ldw = [&](u32x4 (&d)[5], unsigned so) {
#pragma unroll
    for (int n = 0; n < 5; ++n) d[n] = __builtin_amdgcn_raw_buffer_load_b128(rws, lane * 16, so + n * 1024, AUX_SC1);
  };
  const unsigned pscr = PACK_OFF + (w & 3) * 8192;
  const int pu0 = (b - 128) * 4 + w;
  unsigned pra[2], prb[2], pn[2], pb[2];
  auto pack_issue = [&]() {
    const u64 po = wo, pd = wd;
#pragma unroll
    for (int r = 0; r < 2; ++r) {
      const int u = pu0 + 512 * r;
      const int ks = u / 10, cg = u - 10 * ks;
      const unsigned r8 = lane >> 3;
      const u32x4 srd = mk_srd(ks < 20 ? po : pd, ks < 20 ? 640 * 1280 : 1536 * 1280);
      const unsigned base = ks < 16 ? 40 * ks : ks < 20 ? 160 * (ks - 16) + 16 : 32 * (ks - 20);
      const unsigned vr = (ks >= 16 && ks < 20) ? 20 * (r8 >> 2) + (r8 & 3) : r8;
      const unsigned vo = vr * 1280 + (lane & 7) * 16;
#pragma unroll
      for (int p = 0; p < 4; ++p) {
        const unsigned rp = ks < 16 ? 20 * (p >> 1) + 8 * (p & 1) : ks < 20 ? 40 * p : 8 * p;
        dma16(pscr, r * 4096 + p * 1024, vo, srd, u < 680 ? (base + rp) * 1280 + cg * 128 : OOB);
      }
      pra[r] = pscr + r * 4096 + (h * (ks < 16 ? 8 : 4) + q4) * 128 + p4 * 8;
      prb[r] = pra[r] + (ks < 16 ? 512 : 2048);
      pn[r] = 4 * cg; pb[r] = u < 680 ? WS_PW + ks * 5120 : OOB;
    }
    asm volatile("" : "+v"(pra[0]), "+v"(prb[0]), "+v"(pra[1]), "+v"(prb[1]));
    asm volatile("" : "+s"(pn[0]), "+s"(pb[0]), "+s"(pn[1]), "+s"(pb[1]));
  };
  const unsigned smask = w != 4 ? 0u : idx >= 16 ? 0x1040u : 0x1000u;
  auto side_b = [&](int t) {
    if (__builtin_expect(!((smask >> t) & 1), 1)) return;
    if (t == 6) {
      u32x2 tv; tv.x = (unsigned)tag; tv.y = (unsigned)(tag >> 32);
      __builtin_amdgcn_raw_buffer_store_b64(tv, rws, opaque_lane() == 0 ? WS_PKF + (b - 128) * 8 : OOB, 0, AUX_SC1);
    } else {
      dma16c(PKF_OFF, 0, opaque_lane() * 16, sws, WS_PKF);
    }
  };
  auto side_f = [&](int t) {
    {
      if (t == 0) WAITV(0);
      const u32x4 f0 = cat(ldstr(pra[0] + t * 32), ldstr(prb[0] + t * 32)), f1 = cat(ldstr(pra[1] + t * 32), ldstr(prb[1] + t * 32));
      const unsigned n0 = pn[0] + t, c0 = (n0 * 205) >> 10, n1 = pn[1] + t, c1 = (n1 * 205) >> 10;
      __builtin_amdgcn_raw_buffer_store_b128(f0, rws, lane * 16, pb[0] + n0 * 1024 + c0 * (68 * 5120 - 5120), AUX_SC1);
      STPAD();
      __builtin_amdgcn_raw_buffer_store_b128(f1, rws, lane * 16, pb[1] + n1 * 1024 + c1 * (68 * 5120 - 5120), AUX_SC1);
      STPAD();
    }
  };
  auto pk_check = [&]() {
    const u32x4 p0 = lds128(PKF_OFF + lane * 16);
    const unsigned lo = (unsigned)tag, hi = (unsigned)(tag >> 32);
    if (!__all(p0.x == lo && p0.z == lo && p0.y == hi && p0.w == hi)) {
      GAS u64* fp = (GAS u64*)((char*)ws + WS_PKF + opaque_lane() * 8);
#pragma unroll 1
      for (unsigned spin = 0; spin < (1u << 18); ++spin) {
        bool ok = true;
#pragma unroll
        for (int j = 0; j < 2; ++j) ok &= __hip_atomic_load(fp + 64 * j, RLX) == tag;
        if (__all(ok)) break;
        __builtin_amdgcn_s_sleep(2);
      }
    }
    asm volatile("" ::: "memory");
  };
  const int rot = (a + 2 * ((idx >> 3) & 1) + (idx >> 4)) & (ROT - 1);

  const int t0 = 2 * w + min(w, 4);
  float sg0, sg1, sg2;
  auto role = [&](auto RBT) {
    constexpr bool RB = decltype(RBT)::value;
    u32x4 wo_[3][5], wg_[2][5];
    auto tail = [&](int i) {
      if (i == 0) pk_check();
      if (RB) {
        if (i < 2) ldw(wo_[i], pwb + (2 * w + i) * 5120);
        else ldw(wg_[i - 2], pwb + (20 + 6 * w + gstep(i - 2)) * 5120);
        if (i == 1) cell_dma(w - 4, 0);
        if (i == 2) cell_dma(w, 0);
      } else if (i >= 2) {
        ldw(wo_[i - 2], pwb + (2 * w + i - 2) * 5120);
      }
    };
    auto late_w = [&]() {
#pragma unroll
      for (int n = 0; n < 5; ++n) ld128(wo_[2][n], lane * 16, sws, pwb + (16 + w) * 5120 + n * 1024);
#pragma unroll
      for (int j = 0; j < 10; ++j) ld128(wg_[j / 5][j % 5], lane * 16, sws, pwb + (20 + 6 * w + gstep(j / 5)) * 5120 + (j % 5) * 1024);
    };
    if (idx < 16) {
      const int g = gc, hd = w & 3;
      constexpr int par = RB ? 1 : 0;
      using K = P1<true, RB>;
      K k;
      const int q0 = RB ? 12 + 2 * (w - 4) : 3 * w;
      k.xd = K::XB + q0 * 1024; k.wd0 = w * 2048;
      asm volatile("" : "+s"(k.xd), "+s"(k.wd0));
#pragma unroll
      for (int i = 0; i < K::NXP; ++i) {
        const int rho = 8 * (q0 + i) + (lane >> 3);
        const int pp = rho >= 80 ? 1 : 0, u = rho - 80 * pp;
        const int tok = 128 * a - 32 + 2 * u + pp;
        const unsigned v = tok * 1280 + (((lane & 7) ^ ((rho >> 1) & 7)) << 4);
        k.xv[i] = (u >= 9 && tok >= 0) ? v : OOB;
      }
#pragma unroll
      for (int j = 0; j < 2; ++j) {
        const unsigned I = (2 * w + j) * 64 + lane;
        const unsigned kb = (I >= 248) + (I >= 488) + (I >= 736);
        const unsigned i = I - 240 * kb - 8 * ((kb + 1) >> 1);
        const unsigned r = (i * 2185u) >> 16, rem = i - r * 30;
        const unsigned seg = (rem * 6554u) >> 16, ch = rem - seg * 10;
        const unsigned v = (8 * kb + r) * 3840 + seg * 1280 + g * 160 + ch * 16;
        k.wv[j] = i < 240 ? v : OOB;
      }
      asm volatile("" : "+v"(k.xv[0]), "+v"(k.xv[1]), "+v"(k.xv[K::NXP - 1]), "+v"(k.wv[0]), "+v"(k.wv[1]));
      bind();
      STAMP(0);
      k.srdx = mk_srd(x, 2048 * 1280);
      k.srdw = mk_srd(wqkv, 640 * 3840);
      unsigned gv0, gv1;
      asm volatile("global_load_dwordx2 %0, %1, %2 sc1" : "=v"(ev) : "v"(WS_EPOCH), "s"(ws) : "memory");
      asm volatile("buffer_load_ushort %0, %2, %3, 0 offen\n\tbuffer_load_ushort %1, %2, %3, 0 offen offset:1024"
                   : "=&v"(gv0), "=&v"(gv1) : "v"(tid * 2), "s"(mk_srd(gw, 1280)) : "memory");
      STAMP(11);
      k.stage0(rot);
      BAR();
      k.stage1();
      k.xa0 = (K::XB + par * 5 * 2048 + c * 128 + ((h ^ (c >> 1)) << 4)) ^ ((rot & 1) << 6);
      const unsigned kbo = h * 3840 + ((h + 1) >> 1) * 128;
      k.wa0 = kbo + q4 * 480 + (20 * hd + 4 * p4) * 2;
      const bool isgate = p4 == 3;
      k.gmask = isgate ? ~0u : 0u;
      asm volatile("" : "+v"(k.gmask));
      k.wa1 = isgate ? GATE_OFF + (8 * h + q4) * 8 : kbo + q4 * 480 + p4 * 160 + (20 * hd + 16) * 2;
      k.wa2 = k.wa1 + (isgate ? 32 : 1920);
      f32x4 ba, bb, ba1;
#pragma unroll
      for (int r = 0; r < 4; ++r) {
        const int kap = 4 * h + r;
        ba[r] = kap >= c + 9 ? 0.f : -1e30f;
        bb[r] = (kap <= c && kap >= c - 7) ? 0.f : -1e30f;
        ba1[r] = a == 0 ? -1e30f : ba[r];
      }
      unsigned m0mask = h == 0 ? 0xffffffffu : 0u;
      unsigned scw = w * 15360 + 10240 + c * 32 + h * 8, sck = w * 15360 + 10240 + c * 32 + 8,
               sct = w * 15360 + 10240 + (4 * h + q4) * 32 + p4 * 8;
      unsigned ovo = WS_O + (h & 1) * (2 * 20480) + (2 * g + (hd >> 1)) * 1024 + (2 * (hd & 1) + (h >> 1)) * 256 + c * 16;
      unsigned ovt = h == 2 ? WS_O + (32 + g) * 512 + hd * 128 + c * 8 : OOB;
      asm volatile("" : "+v"(ba), "+v"(bb), "+v"(ba1), "+v"(m0mask), "+v"(scw), "+v"(sck), "+v"(sct), "+v"(ovo), "+v"(ovt));
      asm volatile("" : "+v"(k.xa0), "+v"(k.wa0));
      k.zero();
      k.stage2();
      STAMP(12);
      k.template run<RB ? 5 : 0, RB ? 10 : 0>([&]() {
        asm volatile("s_waitcnt vmcnt(%2)" : "+v"(gv0), "+v"(gv1) : "i"(K::VPRO) : "memory");
        u32x2 v; v.x = gv0; v.y = 0;
        *(LAS u32x2*)(GATE_OFF + tid * 8) = v;
        if (!RB && tid < 128) { v.x = gv1; *(LAS u32x2*)(GATE_OFF + 4096 + tid * 8) = v; }
        make_tag(std::integral_constant<int, K::VPRO>{});
        WAITL();
        BAR();
        STAMP(1);
      }, [&](int t) { if (RB) side_b(t); }, tail);
      STAMP(2);
      u32x2 ql[5], kl[5], t2[5], vl[5], tt[5], k4[5];
      unsigned sb16[4];
#pragma unroll
      for (int i = 0; i < 5; ++i) { t2[i] = pk4(k.acc[i][2]); *(LAS u32x2*)(scw + i * 512) = t2[i]; }
#pragma unroll
      for (int i = 0; i < 5; ++i) { if (i) ql[i] = pk4(k.acc[i][0]); kl[i] = pk4(k.acc[i][1]); }
      WAITL();
#pragma unroll
      for (int i = 0; i < 5; ++i) k4[i] = *(const LAS u32x2*)(sck + i * 512);
#pragma unroll
      for (int i = 0; i < 4; ++i) sb16[i] = *(const LAS unsigned*)(sck + (i + 1) * 512 + 16);
#pragma unroll
      for (int i = 0; i < 5; ++i) tt[i] = ldstr(sct + i * 512);
#pragma unroll
      for (int i = 0; i < 5; ++i) vl[i] = pk4(k.acc[i][3]);
      constexpr float SC = 0.22360679774997896f * LOG2E;
      f32x4 sa[4], sb[4];
#pragma unroll
      for (int i = 1; i < 5; ++i) {
        u32x4 qf; qf.x = ql[i].x; qf.y = ql[i].y; qf.z = t2[i].x & m0mask; qf.w = t2[i].y & m0mask;
        sb[i - 1] = mfma(cat(kl[i], k4[i]), qf, bb);
        sa[i - 1] = mfma(cat(kl[i - 1], k4[i - 1]), qf, i == 1 ? ba1 : ba);
      }
      asm volatile("s_nop 7\n\ts_nop 0" : "+v"(sa[0]), "+v"(sa[1]), "+v"(sa[2]), "+v"(sa[3]), "+v"(sb[0]), "+v"(sb[1]), "+v"(sb[2]), "+v"(sb[3]));
      float mx[4], es[4];
#pragma unroll
      for (int i = 0; i < 4; ++i)
        mx[i] = max3(max3(sb[i].x, sb[i].y, sb[i].z), max3(sb[i].w, sa[i].x, sa[i].y), max2(sa[i].z, sa[i].w));
#pragma unroll
      for (int i = 0; i < 4; ++i) {
        auto r = __builtin_amdgcn_permlane16_swap(f2u(mx[i]), f2u(mx[i]), false, false);
        mx[i] = max2(u2f(r[0]), u2f(r[1]));
      }
#pragma unroll
      for (int i = 0; i < 4; ++i) {
        auto r = __builtin_amdgcn_permlane32_swap(f2u(mx[i]), f2u(mx[i]), false, false);
        mx[i] = max2(u2f(r[0]), u2f(r[1])) * SC;
        es[i] = __builtin_amdgcn_exp2f(__builtin_amdgcn_fmed3f(u2f(sb16[i] << 16), -60.f, 60.f) * LOG2E);
      }
      u32x4 pf[4];
#pragma unroll
      for (int i = 0; i < 4; ++i) {
        f32x4 pa, pb;
#pragma unroll
        for (int r = 0; r < 4; ++r) {
          pa[r] = __builtin_amdgcn_exp2f(sa[i][r] * SC - mx[i]);
          pb[r] = __builtin_amdgcn_exp2f(sb[i][r] * SC - mx[i]);
        }
        pf[i] = cat(pk4(pa), pk4(pb));
      }
      const f32x4 z = {0.f, 0.f, 0.f, 0.f};
      const u32x4 ones = {0x3f803f80u, 0x3f803f80u, 0x3f803f80u, 0x3f803f80u};
      f32x4 ls[4], oa[4], ob[4];
#pragma unroll
      for (int i = 0; i < 4; ++i) {
        ls[i] = mfma(ones, pf[i], z);
        oa[i] = mfma(cat(vl[i], vl[i + 1]), pf[i], z);
        ob[i] = mfma(cat(tt[i], tt[i + 1]), pf[i], z);
      }
      const unsigned mt0 = 8 * a + par;
      u32x2 op[4];
#pragma unroll
      for (int i = 0; i < 4; ++i) {
        const float f = es[i] * __builtin_amdgcn_rcpf(ls[i].x);
        op[i] = pk4(oa[i] * f);
        __builtin_amdgcn_raw_buffer_store_b64(pk4(ob[i] * f), rws, ovt, (mt0 + 2 * i) * 20480, AUX_SC1);
        if (i & 1) {
          auto sx = __builtin_amdgcn_permlane16_swap(op[i - 1].x, op[i].x, false, false);
          auto sy = __builtin_amdgcn_permlane16_swap(op[i - 1].y, op[i].y, false, false);
          u32x4 o; o.x = sx[0]; o.y = sy[0]; o.z = sx[1]; o.w = sy[1];
          __builtin_amdgcn_raw_buffer_store_b128(o, rws, ovo, (mt0 + 2 * i - 2) * 20480, AUX_SC1);
          STPAD();
        }
      }
      if (g == 0 && hd == 0) {
        const float e = h == 0 ? es[0] : h == 1 ? es[1] : h == 2 ? es[2] : es[3];
        __builtin_amdgcn_raw_buffer_store_b32(f2u(e), rws, WS_G + h * 128 + c * 4, mt0 * 64, AUX_SC1);
      }
    } else {
      const int cj = gc;
      using K = P1<false, RB>;
      K k;
      const int mh = RB ? (w < 6 ? 1 : 0) : (w >> 1);
      const int up = (w >> 1) & 1, kp = w & 1;
#pragma unroll
      for (int i = 0; i < K::NXP; ++i) {
        const int rho = 8 * (4 * (w & 3) + i) + (lane >> 3);
        const int j = rho >> 4;
        const int tok = 128 * a + 32 * (j >> 1) + 2 * (rho & 15) + (j & 1);
        k.xv[i] = tok * 1280 + (((lane & 7) ^ ((rho >> 1) & 7)) << 4);
      }
      k.xd = K::XB + 4 * (w & 3) * 1024;
      {
        const int r = lane >> 3, p8 = lane & 7;
        const int s = ((r >> 1) & 1) + 2 * kp;
        k.wv[0] = (8 * kp + r) * 3072 + cj * 384 + (((((p8 >> 1) ^ s) << 1) | (p8 & 1)) << 4) + (RB ? 0 : 16 * 3072 + 128);
        k.wv[1] = k.wv[0] + 128; k.wv[2] = k.wv[0] + 256; k.wv[3] = k.wv[0] + 16 * 3072;
      }
      k.wd0 = RB ? (kp * 6 + 3 * up) * 1024 : ((kp + 2) * 6 + 3 * up + 1) * 1024;
      k.wd1 = ((kp + 2) * 6 + 3 * up) * 1024;
      if (RB) asm volatile("" : "+v"(k.xv[0]), "+v"(k.xv[1]), "+v"(k.xv[2]), "+v"(k.xv[3]), "+v"(k.wv[2]), "+v"(k.wv[3]));
      asm volatile("" : "+v"(k.wv[0]), "+v"(k.wv[1]));
      bind();
      STAMP(0);
      const u64 pg = wg, pu = wu;
      k.srdx = mk_srd(x, 2048 * 1280);
      k.srdw = mk_srd(up ? pu : pg, 640 * 3072);
      asm volatile("global_load_dwordx2 %0, %1, %2 sc1" : "=v"(ev) : "v"(WS_EPOCH), "s"(ws) : "memory");
      STAMP(11);
      k.stage0(rot);
      BAR();
      if (!RB) pack_issue();
      k.stage1();
      k.xa0 = (K::XB + mh * 4 * 2048 + c * 128 + ((h ^ (c >> 1)) << 4)) ^ ((rot & 1) << 6);
      {
        const int s = ((q4 >> 1) & 1) + 2 * (h & 1);
        k.wa0 = (h * 6 + (RB ? 2 : (w & 1))) * 1024 + q4 * 128 + (((RB ? 2 * (w & 1) : 0) ^ s) << 5) + p4 * 8;
      }
      asm volatile("" : "+v"(k.xa0), "+v"(k.wa0));
      k.zero();
      k.stage2();
      STAMP(12);
      k.template run<RB ? 5 : 0, RB ? 10 : 0>([&]() {
        make_tag(std::integral_constant<int, RB ? K::VPRO : 8>{});
        BAR();
        STAMP(1);
      }, [&](int t) { if (RB) side_b(t); else side_f(t); }, tail);
      STAMP(2);
      constexpr int NK = RB ? 1 : 2;
      float nl2e = -LOG2E;
      asm volatile("" : "+s"(nl2e));
      const unsigned gso = WS_GU + ((8 * a + 4 * mh) * 48 + 6 * cj + (RB ? 4 + (w & 1) : 2 * (w & 1))) * 1024;
#pragma unroll
      for (int m = 0; m < 4; ++m) {
#pragma unroll
        for (int kk = 0; kk < NK; ++kk) {
          float v[8];
#pragma unroll
          for (int j = 0; j < 8; j += 2) {
            const f32x2 g2 = {k.acc[m][2 * kk + (j >> 2)][j & 3], k.acc[m][2 * kk + (j >> 2)][(j & 3) + 1]};
            const f32x2 u2 = {k.acc[m][2 * NK + 2 * kk + (j >> 2)][j & 3], k.acc[m][2 * NK + 2 * kk + (j >> 2)][(j & 3) + 1]};
            const f32x2 a2 = g2 * nl2e;
            const f32x2 d2 = f32x2{__builtin_amdgcn_exp2f(a2.x), __builtin_amdgcn_exp2f(a2.y)} + 1.f;
            const f32x2 r2 = g2 * u2 * f32x2{__builtin_amdgcn_rcpf(d2.x), __builtin_amdgcn_rcpf(d2.y)};
            v[j] = r2.x; v[j + 1] = r2.y;
          }
          u32x4 o; o.x = pk2(v[0], v[1]); o.y = pk2(v[2], v[3]); o.z = pk2(v[4], v[5]); o.w = pk2(v[6], v[7]);
          __builtin_amdgcn_raw_buffer_store_b128(o, rws, lane * 16, gso + m * (48 * 1024) + kk * 1024, AUX_SC1);
          STPAD();
        }
      }
    }
    SB();
    STAMP(13);
    asm volatile("" : "+v"(wo_[0][0]), "+v"(wo_[0][1]), "+v"(wo_[0][2]), "+v"(wo_[0][3]), "+v"(wo_[0][4]),
                      "+v"(wo_[1][0]), "+v"(wo_[1][1]), "+v"(wo_[1][2]), "+v"(wo_[1][3]), "+v"(wo_[1][4]));
    if (RB) asm volatile("" : "+v"(wg_[0][0]), "+v"(wg_[0][1]), "+v"(wg_[0][2]), "+v"(wg_[0][3]), "+v"(wg_[0][4]),
                              "+v"(wg_[1][0]), "+v"(wg_[1][1]), "+v"(wg_[1][2]), "+v"(wg_[1][3]), "+v"(wg_[1][4]));
    SB();
    STAMP(14);
    if (RB && idx >= 16) { if (PRE > 0) cell_dma(w - 4, 1); if (PRE > 1) cell_dma(w, 1); if (PRE > 2) cell_dma(w - 4, 2); WAITV(5 * PRE); }
    else if (RB || !PREA) WAITV(0);
    else { late_w(); WAITV(15); }
    STAMP(3);
    WAITL();
    BAR();
    if (!RB && w == 0 && lane < 2) {
      GAS u64* fp = (GAS u64*)((char*)ws + (lane ? WS_DONE + b * 8 : WS_FLAG + (a * 16 + (idx < 16 ? 8 : 0) + gc) * 8));
      __hip_atomic_store(fp, tag, RLX);
    }
    STAMP(4);

    SB();
    const unsigned fo = WS_FLAG + ((rb >> 1) * 16 + c) * 8;
    auto wait_flags = [&](u32x2& v, bool need) {
#pragma unroll 1
      for (unsigned spin = 0; spin < (1u << 18); ++spin) {
        if (__all(!need || (v.x == (unsigned)tag && v.y == (unsigned)(tag >> 32)))) break;
        ld64(v, fo, sws, 0);
        asm volatile("s_waitcnt vmcnt(0)" : "+v"(v) :: "memory");
      }
      asm volatile("" ::: "memory");
    };
    u32x2 fv;
    if (RB) {
      if (idx < 16) { if (PRE > 0) cell_dma(w - 4, 1); if (PRE > 1) cell_dma(w, 1); if (PRE > 2) cell_dma(w - 4, 2); }
      if (PRE < 1) cell_dma(w - 4, 1); if (PRE < 2) cell_dma(w, 1); if (PRE < 3) cell_dma(w - 4, 2);
      cell_dma(w, 2);
    } else {
      if (!PREA) late_w();
      if (w == 0) {
        ld64(fv, fo, sws, 0);
        asm volatile("s_waitcnt vmcnt(0)" : "+v"(fv) :: "memory");
        wait_flags(fv, c >= 8);
      }
    }
    if (RB) __builtin_amdgcn_s_setprio(1);
    BAR();
    STAMP(5);
    f32x4 acc[4][5];
    u32x4 ag[6][4];
    auto tk = [](int) {};
    tk(0);
    const unsigned gb = (EXP & 1) ? pwb : WS_GU + (4 * rb * 48 + 6 * w) * 1024;
    auto ldg = [&](int s, int m) { ld128(ag[s][m], lane * 16, sws, gb + gstep(s) * 1024 + m * (48 * 1024)); };
    unsigned ca0 = CELL0 + w * 5120 + lane * 16, ca1 = w * 15360 + lane * 16;
    asm volatile("" : "+v"(ca0), "+v"(ca1));
    {
      using S = Sched<RB>;
      constexpr int NO = S::NO, OB = S::OB;
      u32x4 af[2][4], at[4];
      u32x2 tl[4], th[4];
      const unsigned ob = WS_O + 4 * rb * 20480;
      auto req = [&](auto IT) {
        constexpr int i = IT.value;
        if constexpr (i < 8) ld128(af[i >> 2][i & 3], lane * 16, sws, ob + (i & 3) * 20480 + (2 * w + (i >> 2)) * 1024);
        else if constexpr (i < OB) ld64((i & 1) ? th[(i - 8) >> 1] : tl[(i - 8) >> 1], lane * 8, sws, ob + ((i - 8) >> 1) * 20480 + (16 + w) * 1024 + (i & 1) * 512);
        else if constexpr (i < OB + 29 && (i - OB) % 5 < 4) ldg((i - OB) / 5, (i - OB) % 5);
        else if constexpr (i < OB + 29) dma16c(w * 15360, 10240 + ((i - OB) / 5) * 1024, lane * 16, sws, pwb + (20 + 6 * w + gstep(5)) * 5120 + ((i - OB) / 5) * 1024);
        else ld32(i == OB + 29 ? sg0 : i == OB + 30 ? sg1 : sg2, c * 4, sws, WS_G + (4 * rb + min(t0 + i - OB - 29, 19) / 5) * 64);
      };
      if (RB || w != GPOLL || !GPOLL) sfor<0, S::upto(PL - 1)>(req);
      if (RB) { __builtin_amdgcn_s_setprio(0); WAITV(S::upto(PL - 1)); }
      else if (w == GPOLL) { if (GPOLL) { fv.x = ~(unsigned)tag; fv.y = 0; } wait_flags(fv, c < 8); }
      STAMP(6);
      BAR();
      STAMP(7);
      if (!RB && GPOLL && w == GPOLL) sfor<0, S::upto(PL - 1)>(req);
      tk(1);
      u32x4 wc[5];
      sfor<0, NO + 6>([&](auto ST) {
        constexpr int st = ST.value, s = st - NO;
        if constexpr (st == 2 && !RB) {
          asm volatile("s_waitcnt vmcnt(%8)" : "+v"(tl[0]), "+v"(tl[1]), "+v"(tl[2]), "+v"(tl[3]), "+v"(th[0]), "+v"(th[1]), "+v"(th[2]), "+v"(th[3])
                       : "i"(S::issued(st, 0) - 16) : "memory");
#pragma unroll
          for (int m = 0; m < 4; ++m) at[m] = cat(tl[m], th[m]);
          asm volatile("s_nop 1" : "+v"(at[0]), "+v"(at[1]), "+v"(at[2]), "+v"(at[3]));
        }
        sfor<0, 20>([&](auto IT) {
          constexpr int i = IT.value, m = i / 5, n = i % 5, is = S::issued(st, i);
          if constexpr (WGR == 1 ? n == 0 : i % (5 * WGR) == 0) {
            constexpr int ml = m + WGR - 1;
            if constexpr (WGR == 1) {
              if constexpr (s >= 0)      asm volatile("s_waitcnt vmcnt(%1)" : "+v"(ag[s][m]) : "i"(is - S::gpos(s, m) - 1) : "memory");
              else if constexpr (st < 2) asm volatile("s_waitcnt vmcnt(%1)" : "+v"(af[st][m]) : "i"(is - (4 * st + m) - 1) : "memory");
            } else if constexpr (WGR == 2) {
              if constexpr (s >= 0)      asm volatile("s_waitcnt vmcnt(%2)" : "+v"(ag[s][m]), "+v"(ag[s][ml]) : "i"(is - S::gpos(s, ml) - 1) : "memory");
              else if constexpr (st < 2) asm volatile("s_waitcnt vmcnt(%2)" : "+v"(af[st][m]), "+v"(af[st][ml]) : "i"(is - (4 * st + ml) - 1) : "memory");
            } else {
              if constexpr (s >= 0)      asm volatile("s_waitcnt vmcnt(%4)" : "+v"(ag[s][0]), "+v"(ag[s][1]), "+v"(ag[s][2]), "+v"(ag[s][3]) : "i"(is - S::gpos(s, 3) - 1) : "memory");
              else if constexpr (st < 2) asm volatile("s_waitcnt vmcnt(%4)" : "+v"(af[st][0]), "+v"(af[st][1]), "+v"(af[st][2]), "+v"(af[st][3]) : "i"(is - (4 * st + 3) - 1) : "memory");
            }
          }
          if constexpr (st == 0 && i == 0 && !RB)
            asm volatile("" : "+v"(wo_[2][0]), "+v"(wo_[2][1]), "+v"(wo_[2][2]), "+v"(wo_[2][3]), "+v"(wo_[2][4]),
                              "+v"(wg_[0][0]), "+v"(wg_[0][1]), "+v"(wg_[0][2]), "+v"(wg_[0][3]), "+v"(wg_[0][4]),
                              "+v"(wg_[1][0]), "+v"(wg_[1][1]), "+v"(wg_[1][2]), "+v"(wg_[1][3]), "+v"(wg_[1][4]));
          if constexpr (st == 0)     mul0(acc[m][n], wo_[0][n], af[0][m]);
          else if constexpr (s >= 0) mac(acc[m][n], wg_[s & 1][n], ag[s][m]);
          else if constexpr (st < 2) mac(acc[m][n], wo_[1][n], af[1][m]);
          else                       mac(acc[m][n], wo_[2][n], at[m]);
          SB();
          if constexpr (s == 4 && i == 9) WAITV(is - S::gpos(4, 4) - 1);
          if constexpr (m == (s == 4 ? 2 : 0) && s >= 1 && s < 5) wc[n] = lds128((s > 1 ? ca1 + (s - 2) * 5120 : ca0) + n * 1024);
          if constexpr (S::issued(st, i + 1) > is) req(std::integral_constant<int, is>{});
          SB();
        });
        tk(2 + st);
        if constexpr (s >= 1 && s < 5) {
          asm volatile("" : "+v"(wc[0]), "+v"(wc[1]), "+v"(wc[2]), "+v"(wc[3]), "+v"(wc[4]));
#pragma unroll
          for (int n = 0; n < 5; ++n) wg_[(s + 1) & 1][n] = wc[n];
        }
      });
      asm volatile("s_waitcnt vmcnt(0)" : "+v"(sg0), "+v"(sg1), "+v"(sg2) :: "memory");
    }
    MFMA_DONE();
    STAMP(8);
#pragma unroll
    for (int j = 0; j < 10; ++j) {
      const int ta = j < 4 ? 3 * j : j < 6 ? 6 * j - 22 : 2 * j, tb = j < 4 ? ta + 1 : j < 6 ? ta + 3 : ta + 1;
      *(LAS u32x4*)(ca1 + j * 1024) = cat(pk4(acc[ta / 5][ta % 5]), pk4(acc[tb / 5][tb % 5]));
    }
    tk(12);
  };
  if (w < 4) role(std::false_type{}); else role(std::true_type{});

  if (b == 0 && w == 4) {
    GAS u64* dp = (GAS u64*)((char*)ws_ + WS_DONE + lane * 8);
#pragma unroll 1
    for (unsigned spin = 0; spin < (1u << 18); ++spin) {
      bool ok = true;
#pragma unroll
      for (int j = 0; j < 4; ++j) ok &= __hip_atomic_load(dp + j * 64, RLX) == tag;
      if (__all(ok)) break;
      __builtin_amdgcn_s_sleep(2);
    }
    if (lane == 0) __hip_atomic_store((GAS u64*)((char*)ws_ + WS_EPOCH), tag, RLX);
  }
  sg0 = __builtin_amdgcn_rcpf(1.f + sg0); sg1 = __builtin_amdgcn_rcpf(1.f + sg1); sg2 = __builtin_amdgcn_rcpf(1.f + sg2);
  auto ooff = [&](int tl) {
    const int m = tl / 5, n = tl - 5 * m;
    return (unsigned)((64 * rb + 32 * (m >> 1) + 2 * c + (m & 1)) * 1280 + ct * 160 + n * 32 + h * 8);
  };
  unsigned oo0 = ooff(t0), oo1 = ooff(t0 + 1), oo2 = ooff(t0 + 2);
  asm volatile("" : "+v"(sg0), "+v"(sg1), "+v"(sg2), "+v"(oo0), "+v"(oo1), "+v"(oo2));
  WAITL();
  BAR();
  STAMP(9);
  {
    u32x4 p[8];
    u32x2 p2[8];
    unsigned ra = (w < 4 ? w : w + 2) * 1024 + lane * 16, rb_ = ra + 4 * 15360;
    unsigned rc = (4 + (w >> 1)) * 1024 + (w & 1) * 8 + lane * 16, rd = rc + 4 * 15360;
    asm volatile("" : "+v"(ra), "+v"(rb_), "+v"(rc), "+v"(rd));
#pragma unroll
    for (int v = 0; v < 8; ++v) p[v] = lds128((v < 4 ? ra : rb_) + (v & 3) * 15360);
    if (w < 4) {
#pragma unroll
      for (int v = 0; v < 8; ++v) { p2[v] = *(const LAS u32x2*)((v < 4 ? rc : rd) + (v & 3) * 15360); SB(); }
    }
    SB();
#pragma unroll
    for (int j = 0; j < 3; ++j) {
      if (j < 2 || w < 4) {
        f32x4 s = {0.f, 0.f, 0.f, 0.f};
#pragma unroll
        for (int v = 0; v < 8; ++v) {
          const unsigned px = j == 0 ? p[v].x : j == 1 ? p[v].z : p2[v].x, py = j == 0 ? p[v].y : j == 1 ? p[v].w : p2[v].y;
          const f32x4 q = {u2f(px << 16), u2f(px & 0xffff0000u), u2f(py << 16), u2f(py & 0xffff0000u)};
          s = v ? s + q : q;
        }
        const u32x2 ov = pk4(s * (j == 0 ? sg0 : j == 1 ? sg1 : sg2));
        asm volatile("global_store_dwordx2 %0, %1, %2" OUTFL :: "v"(j == 0 ? oo0 : j == 1 ? oo1 : oo2), "v"(ov), "s"(out_) : "memory");
      }
    }
  }
  STAMP(10);
  if (AMP0 >= 0 && (AMPSEL == 0 || (AMPSEL == 1) == (idx < 16))) {
    const u64 d = amp_t1 - amp_t0, t = __builtin_amdgcn_s_memrealtime();
    while (__builtin_amdgcn_s_memrealtime() - t < d) __builtin_amdgcn_s_sleep(1);
  }
}
}

extern "C" void kernel_launch(void* const* d_in, const int* in_sizes, int n_in, void* d_out, int out_size, void* d_ws,
                              size_t ws_size, hipStream_t stream) {
  fused<<<256, 512, 0, stream>>>(d_in[0], d_in[1], d_in[2], d_in[4], d_in[5], d_in[6], d_in[7], d_out, d_ws);
}
```

```cpp
#include <hip/hip_runtime.h>
#include <hip/hip_bf16.h>
#include <stdint.h>
#include <type_traits>

#define STAMPS 0
#define BEARLY 0
#define CODEPF 1
#define TOUCH_NT ""
#define TOUCH 1
#define EXP 0
#define GLA 4
#define PART16 1
#define PL 2
#define PRE 2
#define OUTFL ""
#define PREA 0
#define WGR 4
#define GPOLL 1
#define ROT 4

#define LAS __attribute__((address_space(3)))
#define GAS __attribute__((address_space(1)))
typedef __bf16 bf16x8 __attribute__((ext_vector_type(8)));
typedef __bf16 bf16x2 __attribute__((ext_vector_type(2)));
typedef float f32x4 __attribute__((ext_vector_type(4)));
typedef float f32x2 __attribute__((ext_vector_type(2)));
typedef unsigned u32x4 __attribute__((ext_vector_type(4)));
typedef unsigned u32x2 __attribute__((ext_vector_type(2)));
typedef short s16x4 __attribute__((ext_vector_type(4)));
typedef unsigned long long u64;

namespace {
constexpr int LDS_BYTES = 163840;
constexpr unsigned GATE_OFF = 110592, PACK_OFF = 122880, PKF_OFF = 162816, CELL0 = 122880;
constexpr unsigned OOB = 0x40000000u;
#define CODE_B 40000
constexpr unsigned CODE_BYTES = CODE_B;
constexpr unsigned WS_FLAG = 0;
constexpr unsigned WS_DONE = 4096;
constexpr unsigned WS_PKF = 8192;
constexpr unsigned WS_EPOCH = 12288;
constexpr unsigned WS_G = 16384;
constexpr unsigned WS_O = 131072;
constexpr unsigned WS_GU = WS_O + 128 * 40 * 512;
constexpr unsigned WS_PW = WS_GU + 128 * 48 * 1024;
constexpr unsigned WS_END = WS_PW + 8 * 68 * 5120;
constexpr unsigned WS_STAMP = 16u << 20;
constexpr int AUX_SC1 = 16;
constexpr float LOG2E = 1.4426950408889634f;

__device__ __forceinline__ u32x4 mk_srd(u64 a, unsigned bytes) {
  u32x4 r; r.x = (unsigned)a; r.y = (unsigned)(a >> 32) & 0xffffu; r.z = bytes; r.w = 0x00020000u; return r;
}
__device__ __forceinline__ void dma16(unsigned lds, unsigned imm, unsigned voff, u32x4 srd, unsigned soff) {
  asm volatile("s_add_u32 m0, %0, %4\n\ts_nop 0\n\tbuffer_load_dwordx4 %1, %2, %3 offen lds"
               :: "s"(lds), "v"(voff), "s"(srd), "s"(soff), "i"(imm) : "memory", "scc");
}
__device__ __forceinline__ void dma16c(unsigned lds, unsigned imm, unsigned voff, u32x4 srd, unsigned soff) {
  asm volatile("s_add_u32 m0, %0, %4\n\ts_nop 0\n\tbuffer_load_dwordx4 %1, %2, %3 offen sc1 lds"
               :: "s"(lds), "v"(voff), "s"(srd), "s"(soff), "i"(imm) : "memory", "scc");
}
__device__ __forceinline__ void ld128(u32x4& d, unsigned voff, u32x4 srd, unsigned soff) {
  asm volatile("buffer_load_dwordx4 %0, %1, %2, %3 offen sc1" : "=v"(d) : "v"(voff), "s"(srd), "s"(soff) : "memory");
}
__device__ __forceinline__ void ld64(u32x2& d, unsigned voff, u32x4 srd, unsigned soff) {
  asm volatile("buffer_load_dwordx2 %0, %1, %2, %3 offen sc1" : "=v"(d) : "v"(voff), "s"(srd), "s"(soff) : "memory");
}
__device__ __forceinline__ void ld32(float& d, unsigned voff, u32x4 srd, unsigned soff) {
  asm volatile("buffer_load_dword %0, %1, %2, %3 offen sc1" : "=v"(d) : "v"(voff), "s"(srd), "s"(soff) : "memory");
}
__device__ __forceinline__ u32x4 lds128(unsigned a) { return *(const LAS u32x4*)a; }
__device__ __forceinline__ u32x2 ldstr(unsigned a) {
  return __builtin_bit_cast(u32x2, __builtin_amdgcn_ds_read_tr16_b64_v4i16((LAS s16x4*)a));
}
__device__ __forceinline__ u32x4 cat(u32x2 a, u32x2 b) { u32x4 r; r.x = a.x; r.y = a.y; r.z = b.x; r.w = b.y; return r; }
__device__ __forceinline__ f32x4 mfma(u32x4 a, u32x4 b, f32x4 c) {
  return __builtin_amdgcn_mfma_f32_16x16x32_bf16(__builtin_bit_cast(bf16x8, a), __builtin_bit_cast(bf16x8, b), c, 0, 0, 0);
}
__device__ __forceinline__ void mac(f32x4& c, u32x4 a, u32x4 b) {
  asm volatile("v_mfma_f32_16x16x32_bf16 %0, %1, %2, %0" : "+v"(c) : "v"(a), "v"(b));
}
__device__ __forceinline__ void mul0(f32x4& c, u32x4 a, u32x4 b) {
  asm volatile("v_mfma_f32_16x16x32_bf16 %0, %1, %2, 0" : "=&v"(c) : "v"(a), "v"(b));
}
__device__ __forceinline__ unsigned pk2(float a, float b) {
  f32x2 t = {a, b};
  return __builtin_bit_cast(unsigned, __builtin_convertvector(t, bf16x2));
}
__device__ __forceinline__ u32x2 pk4(f32x4 v) { u32x2 r; r.x = pk2(v.x, v.y); r.y = pk2(v.z, v.w); return r; }
__device__ __forceinline__ float u2f(unsigned u) { return __builtin_bit_cast(float, u); }
__device__ __forceinline__ unsigned f2u(float f) { return __builtin_bit_cast(unsigned, f); }
#define SB() __builtin_amdgcn_sched_barrier(0)
#define WAITV(n) asm volatile("s_waitcnt vmcnt(%0)" :: "i"(n) : "memory")
#define WAITVL(n) asm volatile("s_waitcnt vmcnt(%0) lgkmcnt(0)" :: "i"(n) : "memory")
#define WAITL() asm volatile("s_waitcnt lgkmcnt(0)" ::: "memory")
#define BAR() asm volatile("s_barrier" ::: "memory")
#define STPAD() do { SB(); asm volatile("s_nop 1" ::: "memory"); SB(); } while (0)
#define MFMA_DONE() do { SB(); asm volatile("s_nop 7\n\ts_nop 1" ::: "memory"); SB(); } while (0)
#define RLX __ATOMIC_RELAXED, __HIP_MEMORY_SCOPE_AGENT
__device__ __forceinline__ unsigned rfl(unsigned v) { return __builtin_amdgcn_readfirstlane(v); }
__device__ __forceinline__ unsigned opaque_lane() { unsigned l = threadIdx.x & 63; asm volatile("" : "+v"(l)); return l; }

#define AMP0 -1
#define AMP1 -1
#define AMPSEL 0
#define STAMP(i) do {} while (0)

template <int I, int N, class F>
__device__ __forceinline__ void sfor(F&& f) {
  if constexpr (I < N) { f(std::integral_constant<int, I>{}); sfor<I + 1, N>(f); }
}
template <bool RB> struct Sched {
  static constexpr int NO = RB ? 2 : 3, OB = RB ? 8 : 16;
  static constexpr int gpos(int s, int m) { return OB + 5 * s + m; }
  static constexpr int upto(int st) { return st < 2 ? 4 * (st + 1) : st < NO ? 16 : st < NO + 5 ? OB + 5 * (st - NO + 1) : st == NO + 5 ? OB + 29 : OB + 32; }
  static constexpr int issued(int st, int i) {
    const int base = upto(st + PL - 1), goal = upto(st + PL), every = goal - base > 5 ? 2 : goal - base > 4 ? 4 : 5;
    int n = base;
    for (int k = 0; k < i; ++k) if (k % every == 1 && n < goal) ++n;
    return n;
  }
};
__device__ __forceinline__ float max3(float a, float b, float c) { float r; asm("v_max3_f32 %0, %1, %2, %3" : "=v"(r) : "v"(a), "v"(b), "v"(c)); return r; }
__device__ __forceinline__ float max2(float a, float b) { float r; asm("v_max_f32 %0, %1, %2" : "=v"(r) : "v"(a), "v"(b)); return r; }

template <bool QKV, bool RB>
struct P1 {
  static constexpr int NM = QKV ? 5 : 4;
  static constexpr int NN = QKV ? 4 : (RB ? 4 : 8);
  static constexpr int NWS = 3;
  static constexpr unsigned WSL = QKV ? 16384 : 24576, XB = NWS * WSL, XSL = QKV ? 20480 : 16384;
  static constexpr unsigned WROW = QKV ? 3840 : 3072;
  static constexpr int NWP = QKV ? 2 : (RB ? 4 : 2);
  static constexpr int NXE = QKV ? (RB ? 1 : 2) : (RB ? 2 : 0), NXO = QKV ? 1 : NXE;
  static constexpr int NXP = NXO + NXE;
  static constexpr int OO = NWP + NXO, OE = NWP + NXE;
  static constexpr bool HAND = !QKV;
  static constexpr int ht = 4;
  f32x4 acc[NM][NN];
  u32x4 srdx, srdw;
  unsigned xv[4], wv[4];
  unsigned xd;
  unsigned wd0, wd1;
  unsigned xa0, wa0, wa1, wa2;
  unsigned gmask;
  __device__ __forceinline__ void tick(int) {}

  unsigned sow, sox, gof;
  unsigned kg, kw, kx, kr;
  template <bool ODD, bool X = true>
  __device__ __forceinline__ void loads(unsigned wwr, unsigned xwr) {
    if (X && NXP) {
#pragma unroll
      for (int i = ODD ? NXE : 0; i < (ODD ? NXP : NXE); ++i) dma16(xwr + xd, i * 1024, xv[i], srdx, sox);
      if (ODD) { kx += 1; sox = (kx ^ (kg >> 1)) * 128; }
    }
    dma16(wwr + wd0, 0, wv[0], srdw, sow);
    dma16(wwr + wd0, 1024, wv[1], srdw, sow);
    if (!QKV && RB) {
      dma16(wwr + wd0, 2048, wv[2], srdw, sow);
      dma16(wwr + wd1, 0, wv[3], srdw, sow);
    }
  }
  __device__ __forceinline__ void other(unsigned wwr) {
    dma16(wwr + wd1, 1024, wv[3], srdw, sow + 128);
    dma16(wwr + wd1, 2048, wv[3], srdw, sow + 256);
  }
  __device__ __forceinline__ void next() { kw += 1; sow = (kw ^ kg) * (32 * WROW); asm volatile("" : "+s"(sow), "+s"(kw)); if (NXP) asm volatile("" : "+s"(sox), "+s"(kx)); }
  __device__ __forceinline__ void stage0(int rot) {
    kg = rot; kw = 0; kx = 0; kr = 0; sow = kg * (32 * WROW); sox = (kg >> 1) * 128; gof = kg * 256;
    if (HAND && !RB) { kw = 3; sow = (3 ^ kg) * (32 * WROW); return; }
#pragma unroll
    for (int i = 0; i < NXE; ++i) dma16(xd, i * 1024, xv[i], srdx, sox);
    loads<true>(0, 0); if (HAND) other(0); next();
  }
  __device__ __forceinline__ void stage1() { if (HAND && !RB) return; loads<false>(WSL, XSL); if (HAND) other(WSL); next(); }
  __device__ __forceinline__ void stage2() { if (HAND && !RB) return; loads<true>(2 * WSL, XSL); if (HAND) other(2 * WSL); next(); }
  static constexpr int VPRO = OE + OO + (HAND ? 4 : 0);
  struct Ad { unsigned x, w[4]; };
  __device__ __forceinline__ void addr(Ad& a, unsigned xb, unsigned wrd) {
    a.x = xb; a.w[0] = wa0 + wrd;
    if (QKV) {
      const unsigned s2 = (gmask & (gof - wrd)) + wrd;
      kr += 1; gof = (kr ^ kg) * 256;
      a.w[1] = wa1 + s2; a.w[2] = wa2 + s2;
      asm volatile("" : "+v"(a.x), "+v"(a.w[0]), "+v"(a.w[1]), "+v"(a.w[2]));
    } else if (RB) {
      a.w[1] = a.w[0] ^ 32;
      asm volatile("" : "+v"(a.x), "+v"(a.w[0]), "+v"(a.w[1]));
    } else {
      a.w[1] = a.w[0] ^ 32; a.w[2] = a.w[0] ^ 64; a.w[3] = a.w[0] ^ 96;
      asm volatile("" : "+v"(a.x), "+v"(a.w[0]), "+v"(a.w[1]), "+v"(a.w[2]), "+v"(a.w[3]));
    }
  }
  __device__ __forceinline__ unsigned wad(const Ad& a, int n, int hi) {
    if (QKV) return n == 2 ? a.w[1 + hi] : a.w[0] + (n == 0 ? 0 : n == 1 ? 160 : 320) + hi * 1920;
    if (RB) return a.w[n & 1] + (n >> 1) * 3072 + hi * 512;
    return a.w[n & 3] + (n >> 2) * 3072 + hi * 512;
  }
  __device__ __forceinline__ void rd_all(u32x4 (&xf)[NM], u32x4 (&wf)[NN], const Ad& a) {
#pragma unroll
    for (int n = 0; n < NN; ++n) wf[n] = cat(ldstr(wad(a, n, 0)), ldstr(wad(a, n, 1)));
#pragma unroll
    for (int m = 0; m < NM; ++m) xf[m] = lds128(a.x + m * 2048);
  }
  template <bool RD = true, class L>
  __device__ __forceinline__ void mm_rd(const u32x4 (&xc)[NM], const u32x4 (&wc)[NN], u32x4 (&xn)[NM], u32x4 (&wn)[NN], const Ad& a, L&& late) {
    u32x2 lo[NN];
    constexpr bool NMAJ = !QKV && !RB;
    constexpr bool EARLY = !QKV && RB && BEARLY;
    if (EARLY && RD) { rd_all(xn, wn, a); SB(); }
    int r = 0;
#pragma unroll
    for (int i = 0; i < NM * NN; ++i) {
      const int m = NMAJ ? i % NM : i / NN, n = NMAJ ? i / NM : i % NN;
      if (QKV && m == 0 && n == 0) continue;
      if (QKV && n == 3) mac(acc[m][3], xc[m], wc[3]);
      else               mac(acc[m][n], wc[n], xc[m]);
      SB();
      if (!RD || EARLY) continue;
      if (NMAJ) {
        if (i < 4) xn[i] = lds128(a.x + i * 2048);
        else if ((i & 3) < 2) {
          const int k = (i >> 2) - 1;
          if (i & 1) wn[k] = cat(lo[k], ldstr(wad(a, k, 1))); else lo[k] = ldstr(wad(a, k, 0));
        } else late(i);
      } else {
        if (r < 2 * NN) {
          const int k = r >> 1;
          if (r & 1) wn[k] = cat(lo[k], ldstr(wad(a, k, 1))); else lo[k] = ldstr(wad(a, k, 0));
        } else if (r < 2 * NN + NM) {
          xn[r - 2 * NN] = lds128(a.x + (r - 2 * NN) * 2048);
        }
        else late(r - 2 * NN - NM);
        ++r;
      }
      SB();
    }
    if (NMAJ && RD) { wn[NN - 1] = cat(ldstr(wad(a, NN - 1, 0)), ldstr(wad(a, NN - 1, 1))); SB(); }
  }
  __device__ __forceinline__ void pin(u32x4 (&xf)[NM], u32x4 (&wf)[NN]) {
    asm volatile("" : "+v"(xf[0]), "+v"(xf[1]), "+v"(xf[2]), "+v"(xf[3]), "+v"(wf[0]), "+v"(wf[1]), "+v"(wf[2]), "+v"(wf[3]));
    if (NM == 5) asm volatile("" : "+v"(xf[NM - 1]));
    if (NN == 8) asm volatile("" : "+v"(wf[NN - 4]), "+v"(wf[NN - 3]), "+v"(wf[NN - 2]), "+v"(wf[NN - 1]));
  }
  __device__ __forceinline__ void zero() {
#pragma unroll
    for (int m = 0; m < NM; ++m)
#pragma unroll
      for (int n = 0; n < NN; ++n) { acc[m][n] = f32x4{0.f, 0.f, 0.f, 0.f}; asm volatile("" : "+v"(acc[m][n])); }
  }
  template <int TL0, int TL1, class F, class G, class H, class U>
  __device__ __forceinline__ void run(F&& pre, G&& extra, H&& tail, U&& gap) {
    pre();
    u32x4 xA[NM], wA[NN], xB[NM], wB[NN];
    Ad a;
    addr(a, xa0, 0);
    rd_all(xA, wA, a);
    if (!RB) __builtin_amdgcn_s_setprio(1);
    unsigned w0 = 0, w1 = WSL, w2 = 2 * WSL;
    unsigned xp = 2 * XSL, x0 = 0, x1 = XSL;
    int t = 0;
    unsigned nw1, nw2, nx1;
    auto adv0 = [&]() { nw1 = w2 + WSL; if (nw1 == NWS * WSL) nw1 = 0; asm volatile("" : "+s"(nw1)); };
    auto adv1 = [&]() { nw2 = nw1 + WSL; if (nw2 == NWS * WSL) nw2 = 0; asm volatile("" : "+s"(nw2)); };
    auto adv2 = [&]() { nx1 = x1 + XSL; if (nx1 == 3 * XSL) nx1 = 0; asm volatile("" : "+s"(nx1)); };
    auto adv3 = [&]() {
      w0 = w2; w1 = nw1; w2 = nw2; xp = x0; x0 = x1; x1 = nx1; t += 2;
      asm volatile("" : "+s"(w0), "+s"(w1), "+s"(w2), "+s"(xp), "+s"(x0), "+s"(x1), "+s"(t));
    };
    auto adv = [&]() { adv0(); adv1(); adv2(); adv3(); };
    asm volatile("" : "+s"(w0), "+s"(w1), "+s"(w2), "+s"(xp), "+s"(x0), "+s"(x1), "+s"(t));
    if (QKV) asm volatile("" : "+s"(gof), "+s"(kr));
    auto none = [](int) {};
    if (RB) {
      auto help = [&](int tt, unsigned wwr) { if (HAND && __builtin_expect(tt < ht, 0)) other(wwr); extra(tt); };
      pin(xA, wA);
      SB(); WAITVL(OO + (HAND ? 2 : 0)); tick(0); BAR(); tick(1); SB();
      loads<false>(w0, xp); help(0, w0); next(); addr(a, (xa0 ^ 64) + x0, w1); SB();
      auto half = [&]() {
        mm_rd(xA, wA, xB, wB, a, [&](int i) {
          if (QKV && i == 0) adv0();
          if (QKV && i == 2) adv1();
          if (QKV && i == 4) adv2();
        });
        pin(xB, wB);
        SB(); WAITVL(OE); tick(2 * t + 2); BAR(); tick(2 * t + 3); SB();
        loads<true>(w1, xp); help(t + 1, w1); next();
        addr(a, xa0 + x1, w2);
        if (QKV) adv3(); else adv();
        SB();
        mm_rd(xB, wB, xA, wA, a, none);
      };
#pragma unroll 1
      do {
        half();
        pin(xA, wA);
        SB(); WAITVL(OO); tick(2 * t); BAR(); tick(2 * t + 1); SB();
        loads<false>(w0, xp); help(t, w0); next(); addr(a, (xa0 ^ 64) + x0, w1); SB();
        if (QKV) asm volatile("" : "+s"(gof), "+s"(kr));
      } while (t < 14);
      half();
    } else {
      addr(a, (xa0 ^ 64) + x0, w1);
      auto trip = [&](auto EARLY) {
        constexpr bool E = EARLY.value;
        pin(xA, wA);
        SB(); if (E) WAITL(); else WAITVL(OO);
        tick(2 * t); BAR(); tick(2 * t + 1); SB();
        mm_rd(xA, wA, xB, wB, a, [&](int i) { if (E) gap(i); });
        if (E) { if (t == 0) WAITV(0); else extra(t); }
        if (!E || t >= ht) loads<false>(w0, xp);
        next(); addr(a, xa0 + x1, w2); adv0(); adv1(); adv2(); SB();
        pin(xB, wB);
        SB(); if (E) WAITL(); else WAITVL(OE);
        tick(2 * t + 2); BAR(); tick(2 * t + 3); SB();
        mm_rd(xB, wB, xA, wA, a, [&](int i) { if (E) gap(i); });
        if (E && t < ht) extra(t + 1); else loads<true>(w1, xp);
        next(); adv3(); addr(a, (xa0 ^ 64) + x0, w1); SB();
        if (QKV) asm volatile("" : "+s"(gof), "+s"(kr));
      };
      if (HAND) {
#pragma unroll 1
        do trip(std::true_type{}); while (t < ht + 2);
      }
#pragma unroll 1
      do trip(std::false_type{}); while (t < 16);
    }
    constexpr unsigned S16 = (16 % NWS) * WSL, S17 = (17 % NWS) * WSL, S18 = (18 % NWS) * WSL, S19 = (19 % NWS) * WSL, X8 = 2 * XSL, X9 = 0;
    pin(xA, wA);
    SB(); WAITVL(OO);
    tick(32); BAR(); tick(33); SB();
    if (RB) { loads<false, false>(S16, 0); tail(0); addr(a, (xa0 ^ 64) + X8, S17); SB(); mm_rd(xA, wA, xB, wB, a, none); }
    else    { mm_rd(xA, wA, xB, wB, a, none); loads<false, false>(S16, 0); tail(0); addr(a, xa0 + X9, S18); SB(); }
    pin(xB, wB);
    SB(); WAITVL(NWP + TL0); tick(34); BAR(); tick(35); SB();
    if (RB) { tail(1); addr(a, xa0 + X9, S18); SB(); mm_rd(xB, wB, xA, wA, a, none); }
    else    { mm_rd(xB, wB, xA, wA, a, none); tail(1); addr(a, (xa0 ^ 64) + X9, S19); SB(); }
    pin(xA, wA);
    SB(); WAITVL(TL0 + TL1); tick(36); BAR(); tick(37); SB();
    if (RB) { tail(2); addr(a, (xa0 ^ 64) + X9, S19); SB(); mm_rd(xA, wA, xB, wB, a, none); }
    else    { mm_rd(xA, wA, xB, wB, a, none); tail(2); SB(); }
    pin(xB, wB);
    SB(); WAITL(); tick(38); BAR(); tick(39); SB();
    if (RB) { tail(3); SB(); mm_rd<false>(xB, wB, xA, wA, a, none); }
    else    { mm_rd<false>(xB, wB, xA, wA, a, none); tail(3); SB(); }
    if (!RB) __builtin_amdgcn_s_setprio(0);
    MFMA_DONE();
    tick(40);
  }
};

__global__ __launch_bounds__(512, 2) void fused(const void* x_, const void* wqkv_, const void* wo_p, const void* wg_p,
                                                const void* wu_p, const void* wd_p, const void* gw_, void* out_, void* ws_) {
  __shared__ __attribute__((aligned(16))) char smem[LDS_BYTES];
  asm volatile("" :: "s"((unsigned)(size_t)(LAS char*)smem));
  const int tid = threadIdx.x, lane = tid & 63;
  const int w = rfl(tid >> 6);
  const int h = lane >> 4, c = lane & 15, q4 = (lane & 15) >> 2, p4 = lane & 3;
  const int b = blockIdx.x, xcd = b & 7, idx = b >> 3;
  const int rh = xcd & 1, cq = xcd >> 1;
  const int a = 8 * rh + (idx & 7);
  const int gc = 2 * cq + ((idx >> 3) & 1);
  const int rb = 4 * xcd + (idx >> 3), ct = idx & 7;
  u64 x, wqkv, wo, wg, wu, wd, gw;
  char* ws;
  __amdgpu_buffer_rsrc_t rws;
  u32x4 sws;
  auto bind = [&]() {
    x = (u64)x_; wqkv = (u64)wqkv_; wo = (u64)wo_p; wg = (u64)wg_p; wu = (u64)wu_p; wd = (u64)wd_p; gw = (u64)gw_;
    u64 pw = (u64)ws_;
    asm volatile("" : "+s"(x), "+s"(wqkv), "+s"(wo), "+s"(wg), "+s"(wu), "+s"(wd), "+s"(gw), "+s"(pw));
    ws = (char*)pw;
    rws = __builtin_amdgcn_make_buffer_rsrc(ws, 0, WS_END, 0x00020000);
    sws = mk_srd(pw, WS_END);
  };
  u64 amp_t0 = 0, amp_t1 = 0;

  if (CODEPF) {
    u64 pc;
    asm volatile("s_getpc_b64 %0" : "=s"(pc));
    asm volatile("s_mov_b32 m0, %0\n\ts_nop 0\n\tbuffer_load_dword %1, %2, 0 offen lds"
                 :: "s"(PKF_OFF), "v"(lane < 6 ? ((idx & 7) * 48 + w * 6 + lane) * 128 : OOB), "s"(mk_srd(pc & ~127ull, CODE_BYTES)) : "memory");
  }
  u32x2 ev;
  u64 tag = 0;
  auto make_tag = [&](auto NV) {
    unsigned lo, hi;
    asm volatile("s_waitcnt vmcnt(%4)\n\tv_readfirstlane_b32 %0, %2\n\tv_readfirstlane_b32 %1, %3"
                 : "=s"(lo), "=s"(hi) : "v"(ev.x), "v"(ev.y), "i"(NV.value) : "memory");
    const u64 e = ((u64)hi << 32) | lo;
    tag = (e ^ 0x9E3779B97F4A7C15ull) * 0xBF58476D1CE4E5B9ull + 0x632BE59BD9B4E019ull;
    if (tag == e) tag += 1;
  };
  const unsigned pwb = WS_PW + ct * (68 * 5120);
  const int grot = 3 * ((idx >> 3) & 1);
  auto gstep = [&](int s) { const int v = s + grot; return v >= 6 ? v - 6 : v; };
  auto cell_dma = [&](int v, int cell) {
    const unsigned so = pwb + (20 + 6 * v + gstep(2 + cell)) * 5120;
#pragma unroll
    for (int j = 0; j < 5; ++j) dma16c(cell ? v * 15360 : CELL0 + v * 5120, (cell ? cell - 1 : 0) * 5120 + j * 1024, lane * 16, sws, so + j * 1024);
  };
  auto ldw = [&](u32x4 (&d)[5], unsigned so) {
#pragma unroll
    for (int n = 0; n < 5; ++n) d[n] = __builtin_amdgcn_raw_buffer_load_b128(rws, lane * 16, so + n * 1024, AUX_SC1);
  };
  const unsigned pscr = PACK_OFF + (w & 3) * 8192;
  const int pu0 = (b - 128) * 4 + w;
  unsigned pra[2], prb[2], pn[2], pb[2];
  auto pack_issue = [&]() {
    const u64 po = wo, pd = wd;
#pragma unroll
    for (int r = 0; r < 2; ++r) {
      const int u = pu0 + 512 * r;
      const int ks = u / 10, cg = u - 10 * ks;
      const unsigned r8 = lane >> 3;
      const u32x4 srd = mk_srd(ks < 20 ? po : pd, ks < 20 ? 640 * 1280 : 1536 * 1280);
      const unsigned base = ks < 16 ? 40 * ks : ks < 20 ? 160 * (ks - 16) + 16 : 32 * (ks - 20);
      const unsigned vr = (ks >= 16 && ks < 20) ? 20 * (r8 >> 2) + (r8 & 3) : r8;
      const unsigned vo = vr * 1280 + (lane & 7) * 16;
#pragma unroll
      for (int p = 0; p < 4; ++p) {
        const unsigned rp = ks < 16 ? 20 * (p >> 1) + 8 * (p & 1) : ks < 20 ? 40 * p : 8 * p;
        dma16(pscr, r * 4096 + p * 1024, vo, srd, u < 680 ? (base + rp) * 1280 + cg * 128 : OOB);
      }
      pra[r] = pscr + r * 4096 + (h * (ks < 16 ? 8 : 4) + q4) * 128 + p4 * 8 - 32;
      prb[r] = pra[r] + (ks < 16 ? 512 : 2048);
      pn[r] = 4 * cg - 1; pb[r] = u < 680 ? WS_PW + ks * 5120 + (4 * cg - 1) * 1024 : OOB;
    }
    asm volatile("" : "+v"(pra[0]), "+v"(prb[0]), "+v"(pra[1]), "+v"(prb[1]));
    asm volatile("" : "+s"(pn[0]), "+s"(pb[0]), "+s"(pn[1]), "+s"(pb[1]));
  };
  const unsigned smask = w != 4 ? 0u : idx >= 16 ? 0x1040u : 0x1000u;
  auto side_b = [&](int t) {
    if (__builtin_expect(!((smask >> t) & 1), 1)) return;
    if (t == 6) {
      u32x2 tv; tv.x = (unsigned)tag; tv.y = (unsigned)(tag >> 32);
      __builtin_amdgcn_raw_buffer_store_b64(tv, rws, opaque_lane() == 0 ? WS_PKF + (b - 128) * 8 : OOB, 0, AUX_SC1);
    } else {
      dma16c(PKF_OFF, 0, opaque_lane() * 16, sws, WS_PKF);
    }
  };
  u32x2 pq[4];
  unsigned ps0, ps1;
  auto pack_gap = [&](int i) {
    if (i == 6)  pq[0] = ldstr(pra[0]);
    if (i == 7)  pq[1] = ldstr(prb[0]);
    if (i == 10) pq[2] = ldstr(pra[1]);
    if (i == 11) pq[3] = ldstr(prb[1]);
    if (i == 14) { pra[0] += 32; prb[0] += 32; asm volatile("" : "+v"(pra[0]), "+v"(prb[0])); }
    if (i == 15) { pra[1] += 32; prb[1] += 32; asm volatile("" : "+v"(pra[1]), "+v"(prb[1])); }
    if (i == 18) { ps0 = (pn[0] * 205) >> 10; asm volatile("" : "+s"(ps0)); }
    if (i == 19) { ps0 = ps0 * (68 * 5120 - 5120) + pb[0]; asm volatile("" : "+s"(ps0)); }
    if (i == 22) { ps1 = (pn[1] * 205) >> 10; asm volatile("" : "+s"(ps1)); }
    if (i == 23) { ps1 = ps1 * (68 * 5120 - 5120) + pb[1]; asm volatile("" : "+s"(ps1)); }
    if (i == 26) { pn[0] += 1; pb[0] += 1024; asm volatile("" : "+s"(pn[0]), "+s"(pb[0])); }
    if (i == 27) { pn[1] += 1; pb[1] += 1024; asm volatile("" : "+s"(pn[1]), "+s"(pb[1])); }
  };
  auto side_f = [&](int) {
    __builtin_amdgcn_raw_buffer_store_b128(cat(pq[0], pq[1]), rws, lane * 16, ps0, AUX_SC1);
    STPAD();
    __builtin_amdgcn_raw_buffer_store_b128(cat(pq[2], pq[3]), rws, lane * 16, ps1, AUX_SC1);
    STPAD();
  };
  auto pk_check = [&]() {
    const u32x4 p0 = lds128(PKF_OFF + lane * 16);
    const unsigned lo = (unsigned)tag, hi = (unsigned)(tag >> 32);
    if (!__all(p0.x == lo && p0.z == lo && p0.y == hi && p0.w == hi)) {
      GAS u64* fp = (GAS u64*)((char*)ws + WS_PKF + opaque_lane() * 8);
#pragma unroll 1
      for (unsigned spin = 0; spin < (1u << 18); ++spin) {
        bool ok = true;
#pragma unroll
        for (int j = 0; j < 2; ++j) ok &= __hip_atomic_load(fp + 64 * j, RLX) == tag;
        if (__all(ok)) break;
        __builtin_amdgcn_s_sleep(2);
      }
    }
    asm volatile("" ::: "memory");
  };
  const int rot = (a + 2 * ((idx >> 3) & 1) + (idx >> 4)) & (ROT - 1);

  const int t0 = 2 * w + min(w, 4);
  float sg0, sg1, sg2;
  auto role = [&](auto RBT) {
    constexpr bool RB = decltype(RBT)::value;
    u32x4 wo_[3][5], wg_[2][5];
    auto tail = [&](int i) {
      if (i == 0) pk_check();
      if (RB) {
        if (i < 2) ldw(wo_[i], pwb + (2 * w + i) * 5120);
        else ldw(wg_[i - 2], pwb + (20 + 6 * w + gstep(i - 2)) * 5120);
        if (i == 1) cell_dma(w - 4, 0);
        if (i == 2) cell_dma(w, 0);
      } else if (i >= 2) {
        ldw(wo_[i - 2], pwb + (2 * w + i - 2) * 5120);
      }
    };
    auto late_w = [&]() {
#pragma unroll
      for (int n = 0; n < 5; ++n) ld128(wo_[2][n], lane * 16, sws, pwb + (16 + w) * 5120 + n * 1024);
#pragma unroll
      for (int j = 0; j < 10; ++j) ld128(wg_[j / 5][j % 5], lane * 16, sws, pwb + (20 + 6 * w + gstep(j / 5)) * 5120 + (j % 5) * 1024);
    };
    if (idx < 16) {
      const int g = gc, hd = w & 3;
      constexpr int par = RB ? 1 : 0;
      using K = P1<true, RB>;
      K k;
      const int q0 = RB ? 12 + 2 * (w - 4) : 3 * w;
      k.xd = K::XB + q0 * 1024; k.wd0 = w * 2048;
      asm volatile("" : "+s"(k.xd), "+s"(k.wd0));
#pragma unroll
      for (int i = 0; i < K::NXP; ++i) {
        const int rho = 8 * (q0 + i) + (lane >> 3);
        const int pp = rho >= 80 ? 1 : 0, u = rho - 80 * pp;
        const int tok = 128 * a - 32 + 2 * u + pp;
        const unsigned v = tok * 1280 + (((lane & 7) ^ ((rho >> 1) & 7)) << 4);
        k.xv[i] = (u >= 9 && tok >= 0) ? v : OOB;
      }
#pragma unroll
      for (int j = 0; j < 2; ++j) {
        const unsigned I = (2 * w + j) * 64 + lane;
        const unsigned kb = (I >= 248) + (I >= 488) + (I >= 736);
        const unsigned i = I - 240 * kb - 8 * ((kb + 1) >> 1);
        const unsigned r = (i * 2185u) >> 16, rem = i - r * 30;
        const unsigned seg = (rem * 6554u) >> 16, ch = rem - seg * 10;
        const unsigned v = (8 * kb + r) * 3840 + seg * 1280 + g * 160 + ch * 16;
        k.wv[j] = i < 240 ? v : OOB;
      }
      asm volatile("" : "+v"(k.xv[0]), "+v"(k.xv[1]), "+v"(k.xv[K::NXP - 1]), "+v"(k.wv[0]), "+v"(k.wv[1]));
      bind();
      STAMP(0);
      k.srdx = mk_srd(x, 2048 * 1280);
      k.srdw = mk_srd(wqkv, 640 * 3840);
      unsigned gv0, gv1;
      asm volatile("global_load_dwordx2 %0, %1, %2 sc1" : "=v"(ev) : "v"(WS_EPOCH), "s"(ws) : "memory");
      asm volatile("buffer_load_ushort %0, %2, %3, 0 offen\n\tbuffer_load_ushort %1, %2, %3, 0 offen offset:1024"
                   : "=&v"(gv0), "=&v"(gv1) : "v"(tid * 2), "s"(mk_srd(gw, 1280)) : "memory");
      STAMP(11);
      k.stage0(rot);
      BAR();
      k.stage1();
      k.xa0 = (K::XB + par * 5 * 2048 + c * 128 + ((h ^ (c >> 1)) << 4)) ^ ((rot & 1) << 6);
      const unsigned kbo = h * 3840 + ((h + 1) >> 1) * 128;
      k.wa0 = kbo + q4 * 480 + (20 * hd + 4 * p4) * 2;
      const bool isgate = p4 == 3;
      k.gmask = isgate ? ~0u : 0u;
      asm volatile("" : "+v"(k.gmask));
      k.wa1 = isgate ? GATE_OFF + (8 * h + q4) * 8 : kbo + q4 * 480 + p4 * 160 + (20 * hd + 16) * 2;
      k.wa2 = k.wa1 + (isgate ? 32 : 1920);
      f32x4 ba, bb, ba1;
#pragma unroll
      for (int r = 0; r < 4; ++r) {
        const int kap = 4 * h + r;
        ba[r] = kap >= c + 9 ? 0.f : -1e30f;
        bb[r] = (kap <= c && kap >= c - 7) ? 0.f : -1e30f;
        ba1[r] = a == 0 ? -1e30f : ba[r];
      }
      unsigned m0mask = h == 0 ? 0xffffffffu : 0u;
      unsigned scw = w * 15360 + 10240 + c * 32 + h * 8, sck = w * 15360 + 10240 + c * 32 + 8,
               sct = w * 15360 + 10240 + (4 * h + q4) * 32 + p4 * 8;
      unsigned ovo = WS_O + (h & 1) * (2 * 20480) + (2 * g + (hd >> 1)) * 1024 + (2 * (hd & 1) + (h >> 1)) * 256 + c * 16;
      unsigned ovt = h == 2 ? WS_O + (32 + g) * 512 + hd * 128 + c * 8 : OOB;
      asm volatile("" : "+v"(ba), "+v"(bb), "+v"(ba1), "+v"(m0mask), "+v"(scw), "+v"(sck), "+v"(sct), "+v"(ovo), "+v"(ovt));
      asm volatile("" : "+v"(k.xa0), "+v"(k.wa0));
      k.zero();
      k.stage2();
      STAMP(12);
      k.template run<RB ? 5 : 0, RB ? 10 : 0>([&]() {
        asm volatile("s_waitcnt vmcnt(%2)" : "+v"(gv0), "+v"(gv1) : "i"(K::VPRO) : "memory");
        u32x2 v; v.x = gv0; v.y = 0;
        *(LAS u32x2*)(GATE_OFF + tid * 8) = v;
        if (!RB && tid < 128) { v.x = gv1; *(LAS u32x2*)(GATE_OFF + 4096 + tid * 8) = v; }
        make_tag(std::integral_constant<int, K::VPRO>{});
        WAITL();
        BAR();
        STAMP(1);
      }, [&](int t) { if (RB) side_b(t); }, tail, [](int) {});
      STAMP(2);
      u32x2 ql[5], kl[5], t2[5], vl[5], tt[5], k4[5];
      unsigned sb16[4];
#pragma unroll
      for (int i = 0; i < 5; ++i) { t2[i] = pk4(k.acc[i][2]); *(LAS u32x2*)(scw + i * 512) = t2[i]; }
#pragma unroll
      for (int i = 0; i < 5; ++i) { if (i) ql[i] = pk4(k.acc[i][0]); kl[i] = pk4(k.acc[i][1]); }
      WAITL();
#pragma unroll
      for (int i = 0; i < 5; ++i) k4[i] = *(const LAS u32x2*)(sck + i * 512);
#pragma unroll
      for (int i = 0; i < 4; ++i) sb16[i] = *(const LAS unsigned*)(sck + (i + 1) * 512 + 16);
#pragma unroll
      for (int i = 0; i < 5; ++i) tt[i] = ldstr(sct + i * 512);
#pragma unroll
      for (int i = 0; i < 5; ++i) vl[i] = pk4(k.acc[i][3]);
      constexpr float SC = 0.22360679774997896f * LOG2E;
      f32x4 sa[4], sb[4];
#pragma unroll
      for (int i = 1; i < 5; ++i) {
        u32x4 qf; qf.x = ql[i].x; qf.y = ql[i].y; qf.z = t2[i].x & m0mask; qf.w = t2[i].y & m0mask;
        sb[i - 1] = mfma(cat(kl[i], k4[i]), qf, bb);
        sa[i - 1] = mfma(cat(kl[i - 1], k4[i - 1]), qf, i == 1 ? ba1 : ba);
      }
      asm volatile("s_nop 7\n\ts_nop 0" : "+v"(sa[0]), "+v"(sa[1]), "+v"(sa[2]), "+v"(sa[3]), "+v"(sb[0]), "+v"(sb[1]), "+v"(sb[2]), "+v"(sb[3]));
      float mx[4], es[4];
#pragma unroll
      for (int i = 0; i < 4; ++i)
        mx[i] = max3(max3(sb[i].x, sb[i].y, sb[i].z), max3(sb[i].w, sa[i].x, sa[i].y), max2(sa[i].z, sa[i].w));
#pragma unroll
      for (int i = 0; i < 4; ++i) {
        auto r = __builtin_amdgcn_permlane16_swap(f2u(mx[i]), f2u(mx[i]), false, false);
        mx[i] = max2(u2f(r[0]), u2f(r[1]));
      }
#pragma unroll
      for (int i = 0; i < 4; ++i) {
        auto r = __builtin_amdgcn_permlane32_swap(f2u(mx[i]), f2u(mx[i]), false, false);
        mx[i] = max2(u2f(r[0]), u2f(r[1])) * SC;
        es[i] = __builtin_amdgcn_exp2f(__builtin_amdgcn_fmed3f(u2f(sb16[i] << 16), -60.f, 60.f) * LOG2E);
      }
      u32x4 pf[4];
#pragma unroll
      for (int i = 0; i < 4; ++i) {
        f32x4 pa, pb;
#pragma unroll
        for (int r = 0; r < 4; ++r) {
          pa[r] = __builtin_amdgcn_exp2f(sa[i][r] * SC - mx[i]);
          pb[r] = __builtin_amdgcn_exp2f(sb[i][r] * SC - mx[i]);
        }
        pf[i] = cat(pk4(pa), pk4(pb));
      }
      const f32x4 z = {0.f, 0.f, 0.f, 0.f};
      const u32x4 ones = {0x3f803f80u, 0x3f803f80u, 0x3f803f80u, 0x3f803f80u};
      f32x4 ls[4], oa[4], ob[4];
#pragma unroll
      for (int i = 0; i < 4; ++i) {
        ls[i] = mfma(ones, pf[i], z);
        oa[i] = mfma(cat(vl[i], vl[i + 1]), pf[i], z);
        ob[i] = mfma(cat(tt[i], tt[i + 1]), pf[i], z);
      }
      const unsigned mt0 = 8 * a + par;
      u32x2 op[4];
#pragma unroll
      for (int i = 0; i < 4; ++i) {
        const float f = es[i] * __builtin_amdgcn_rcpf(ls[i].x);
        op[i] = pk4(oa[i] * f);
        __builtin_amdgcn_raw_buffer_store_b64(pk4(ob[i] * f), rws, ovt, (mt0 + 2 * i) * 20480, AUX_SC1);
        if (i & 1) {
          auto sx = __builtin_amdgcn_permlane16_swap(op[i - 1].x, op[i].x, false, false);
          auto sy = __builtin_amdgcn_permlane16_swap(op[i - 1].y, op[i].y, false, false);
          u32x4 o; o.x = sx[0]; o.y = sy[0]; o.z = sx[1]; o.w = sy[1];
          __builtin_amdgcn_raw_buffer_store_b128(o, rws, ovo, (mt0 + 2 * i - 2) * 20480, AUX_SC1);
          STPAD();
        }
      }
      if (g == 0 && hd == 0) {
        const float e = h == 0 ? es[0] : h == 1 ? es[1] : h == 2 ? es[2] : es[3];
        __builtin_amdgcn_raw_buffer_store_b32(f2u(e), rws, WS_G + h * 128 + c * 4, mt0 * 64, AUX_SC1);
      }
    } else {
      const int cj = gc;
      using K = P1<false, RB>;
      K k;
      const int mh = RB ? (w < 6 ? 1 : 0) : (w >> 1);
      const int up = (w >> 1) & 1, kp = w & 1;
#pragma unroll
      for (int i = 0; i < K::NXP; ++i) {
        const int rho = 8 * (4 * (w & 3) + i) + (lane >> 3);
        const int j = rho >> 4;
        const int tok = 128 * a + 32 * (j >> 1) + 2 * (rho & 15) + (j & 1);
        k.xv[i] = tok * 1280 + (((lane & 7) ^ ((rho >> 1) & 7)) << 4);
      }
      k.xd = K::XB + 4 * (w & 3) * 1024;
      {
        const int r = lane >> 3, p8 = lane & 7;
        const int s = ((r >> 1) & 1) + 2 * kp;
        k.wv[0] = (8 * kp + r) * 3072 + cj * 384 + (((((p8 >> 1) ^ s) << 1) | (p8 & 1)) << 4) + (RB ? 0 : 16 * 3072 + 128);
        k.wv[1] = k.wv[0] + 128; k.wv[2] = k.wv[0] + 256; k.wv[3] = k.wv[0] + 16 * 3072;
      }
      k.wd0 = RB ? (kp * 6 + 3 * up) * 1024 : ((kp + 2) * 6 + 3 * up + 1) * 1024;
      k.wd1 = ((kp + 2) * 6 + 3 * up) * 1024;
      if (RB) asm volatile("" : "+v"(k.xv[0]), "+v"(k.xv[1]), "+v"(k.xv[2]), "+v"(k.xv[3]), "+v"(k.wv[2]), "+v"(k.wv[3]));
      asm volatile("" : "+v"(k.wv[0]), "+v"(k.wv[1]));
      bind();
      STAMP(0);
      const u64 pg = wg, pu = wu;
      k.srdx = mk_srd(x, 2048 * 1280);
      k.srdw = mk_srd(up ? pu : pg, 640 * 3072);
      asm volatile("global_load_dwordx2 %0, %1, %2 sc1" : "=v"(ev) : "v"(WS_EPOCH), "s"(ws) : "memory");
      STAMP(11);
      k.stage0(rot);
      BAR();
      if (!RB) pack_issue();
      k.stage1();
      k.xa0 = (K::XB + mh * 4 * 2048 + c * 128 + ((h ^ (c >> 1)) << 4)) ^ ((rot & 1) << 6);
      {
        const int s = ((q4 >> 1) & 1) + 2 * (h & 1);
        k.wa0 = (h * 6 + (RB ? 2 : (w & 1))) * 1024 + q4 * 128 + (((RB ? 2 * (w & 1) : 0) ^ s) << 5) + p4 * 8;
      }
      asm volatile("" : "+v"(k.xa0), "+v"(k.wa0));
      k.zero();
      k.stage2();
      STAMP(12);
      k.template run<RB ? 5 : 0, RB ? 10 : 0>([&]() {
        make_tag(std::integral_constant<int, RB ? K::VPRO : 8>{});
        BAR();
        STAMP(1);
      }, [&](int t) { if (RB) side_b(t); else side_f(t); }, tail, pack_gap);
      STAMP(2);
      constexpr int NK = RB ? 1 : 2;
      float nl2e = -LOG2E;
      asm volatile("" : "+s"(nl2e));
      const unsigned gso = WS_GU + ((8 * a + 4 * mh) * 48 + 6 * cj + (RB ? 4 + (w & 1) : 2 * (w & 1))) * 1024;
#pragma unroll
      for (int m = 0; m < 4; ++m) {
#pragma unroll
        for (int kk = 0; kk < NK; ++kk) {
          float v[8];
#pragma unroll
          for (int j = 0; j < 8; j += 2) {
            const f32x2 g2 = {k.acc[m][2 * kk + (j >> 2)][j & 3], k.acc[m][2 * kk + (j >> 2)][(j & 3) + 1]};
            const f32x2 u2 = {k.acc[m][2 * NK + 2 * kk + (j >> 2)][j & 3], k.acc[m][2 * NK + 2 * kk + (j >> 2)][(j & 3) + 1]};
            const f32x2 a2 = g2 * nl2e;
            const f32x2 d2 = f32x2{__builtin_amdgcn_exp2f(a2.x), __builtin_amdgcn_exp2f(a2.y)} + 1.f;
            const f32x2 r2 = g2 * u2 * f32x2{__builtin_amdgcn_rcpf(d2.x), __builtin_amdgcn_rcpf(d2.y)};
            v[j] = r2.x; v[j + 1] = r2.y;
          }
          u32x4 o; o.x = pk2(v[0], v[1]); o.y = pk2(v[2], v[3]); o.z = pk2(v[4], v[5]); o.w = pk2(v[6], v[7]);
          __builtin_amdgcn_raw_buffer_store_b128(o, rws, lane * 16, gso + m * (48 * 1024) + kk * 1024, AUX_SC1);
          STPAD();
        }
      }
    }
    SB();
    STAMP(13);
    asm volatile("" : "+v"(wo_[0][0]), "+v"(wo_[0][1]), "+v"(wo_[0][2]), "+v"(wo_[0][3]), "+v"(wo_[0][4]),
                      "+v"(wo_[1][0]), "+v"(wo_[1][1]), "+v"(wo_[1][2]), "+v"(wo_[1][3]), "+v"(wo_[1][4]));
    if (RB) asm volatile("" : "+v"(wg_[0][0]), "+v"(wg_[0][1]), "+v"(wg_[0][2]), "+v"(wg_[0][3]), "+v"(wg_[0][4]),
                              "+v"(wg_[1][0]), "+v"(wg_[1][1]), "+v"(wg_[1][2]), "+v"(wg_[1][3]), "+v"(wg_[1][4]));
    SB();
    STAMP(14);
    if (RB && idx >= 16) { if (PRE > 0) cell_dma(w - 4, 1); if (PRE > 1) cell_dma(w, 1); if (PRE > 2) cell_dma(w - 4, 2); WAITV(5 * PRE); }
    else if (RB || !PREA) WAITV(0);
    else { late_w(); WAITV(15); }
    STAMP(3);
    WAITL();
    BAR();
    if (!RB && w == 0 && lane < 2) {
      GAS u64* fp = (GAS u64*)((char*)ws + (lane ? WS_DONE + b * 8 : WS_FLAG + (a * 16 + (idx < 16 ? 8 : 0) + gc) * 8));
      __hip_atomic_store(fp, tag, RLX);
    }
    STAMP(4);

    SB();
    const unsigned fo = WS_FLAG + ((rb >> 1) * 16 + c) * 8;
    auto wait_flags = [&](u32x2& v, bool need) {
#pragma unroll 1
      for (unsigned spin = 0; spin < (1u << 18); ++spin) {
        if (__all(!need || (v.x == (unsigned)tag && v.y == (unsigned)(tag >> 32)))) break;
        ld64(v, fo, sws, 0);
        asm volatile("s_waitcnt vmcnt(0)" : "+v"(v) :: "memory");
      }
      asm volatile("" ::: "memory");
    };
    u32x2 fv;
    if (RB) {
      if (idx < 16) { if (PRE > 0) cell_dma(w - 4, 1); if (PRE > 1) cell_dma(w, 1); if (PRE > 2) cell_dma(w - 4, 2); }
      if (PRE < 1) cell_dma(w - 4, 1); if (PRE < 2) cell_dma(w, 1); if (PRE < 3) cell_dma(w - 4, 2);
      cell_dma(w, 2);
    } else {
      if (!PREA) late_w();
      if (w == 0) {
        ld64(fv, fo, sws, 0);
        asm volatile("s_waitcnt vmcnt(0)" : "+v"(fv) :: "memory");
        wait_flags(fv, c >= 8);
      }
    }
    if (RB) __builtin_amdgcn_s_setprio(1);
    BAR();
    STAMP(5);
    f32x4 acc[4][5];
    u32x4 ag[6][4];
    auto tk = [](int) {};
    tk(0);
    const unsigned gb = (EXP & 1) ? pwb : WS_GU + (4 * rb * 48 + 6 * w) * 1024;
    auto ldg = [&](int s, int m) { ld128(ag[s][m], lane * 16, sws, gb + gstep(s) * 1024 + m * (48 * 1024)); };
    unsigned ca0 = CELL0 + w * 5120 + lane * 16, ca1 = w * 15360 + lane * 16;
    asm volatile("" : "+v"(ca0), "+v"(ca1));
    {
      using S = Sched<RB>;
      constexpr int NO = S::NO, OB = S::OB;
      u32x4 af[2][4], at[4];
      u32x2 tl[4], th[4];
      const unsigned ob = WS_O + 4 * rb * 20480;
      auto req = [&](auto IT) {
        constexpr int i = IT.value;
        if constexpr (i < 8) ld128(af[i >> 2][i & 3], lane * 16, sws, ob + (i & 3) * 20480 + (2 * w + (i >> 2)) * 1024);
        else if constexpr (i < OB) ld64((i & 1) ? th[(i - 8) >> 1] : tl[(i - 8) >> 1], lane * 8, sws, ob + ((i - 8) >> 1) * 20480 + (16 + w) * 1024 + (i & 1) * 512);
        else if constexpr (i < OB + 29 && (i - OB) % 5 < 4) ldg((i - OB) / 5, (i - OB) % 5);
        else if constexpr (i < OB + 29) dma16c(w * 15360, 10240 + ((i - OB) / 5) * 1024, lane * 16, sws, pwb + (20 + 6 * w + gstep(5)) * 5120 + ((i - OB) / 5) * 1024);
        else ld32(i == OB + 29 ? sg0 : i == OB + 30 ? sg1 : sg2, c * 4, sws, WS_G + (4 * rb + min(t0 + i - OB - 29, 19) / 5) * 64);
      };
      if (RB || w != GPOLL || !GPOLL) sfor<0, S::upto(PL - 1)>(req);
      if (RB) { __builtin_amdgcn_s_setprio(0); WAITV(S::upto(PL - 1)); }
      else if (w == GPOLL) { if (GPOLL) { fv.x = ~(unsigned)tag; fv.y = 0; } wait_flags(fv, c < 8); }
      STAMP(6);
      BAR();
      STAMP(7);
      if (!RB && GPOLL && w == GPOLL) sfor<0, S::upto(PL - 1)>(req);
      tk(1);
      u32x4 wc[5];
      sfor<0, NO + 6>([&](auto ST) {
        constexpr int st = ST.value, s = st - NO;
        if constexpr (st == 2 && !RB) {
          asm volatile("s_waitcnt vmcnt(%8)" : "+v"(tl[0]), "+v"(tl[1]), "+v"(tl[2]), "+v"(tl[3]), "+v"(th[0]), "+v"(th[1]), "+v"(th[2]), "+v"(th[3])
                       : "i"(S::issued(st, 0) - 16) : "memory");
#pragma unroll
          for (int m = 0; m < 4; ++m) at[m] = cat(tl[m], th[m]);
          asm volatile("s_nop 1" : "+v"(at[0]), "+v"(at[1]), "+v"(at[2]), "+v"(at[3]));
        }
        sfor<0, 20>([&](auto IT) {
          constexpr int i = IT.value, m = i / 5, n = i % 5, is = S::issued(st, i);
          if constexpr (WGR == 1 ? n == 0 : i % (5 * WGR) == 0) {
            constexpr int ml = m + WGR - 1;
            if constexpr (WGR == 1) {
              if constexpr (s >= 0)      asm volatile("s_waitcnt vmcnt(%1)" : "+v"(ag[s][m]) : "i"(is - S::gpos(s, m) - 1) : "memory");
              else if constexpr (st < 2) asm volatile("s_waitcnt vmcnt(%1)" : "+v"(af[st][m]) : "i"(is - (4 * st + m) - 1) : "memory");
            } else if constexpr (WGR == 2) {
              if constexpr (s >= 0)      asm volatile("s_waitcnt vmcnt(%2)" : "+v"(ag[s][m]), "+v"(ag[s][ml]) : "i"(is - S::gpos(s, ml) - 1) : "memory");
              else if constexpr (st < 2) asm volatile("s_waitcnt vmcnt(%2)" : "+v"(af[st][m]), "+v"(af[st][ml]) : "i"(is - (4 * st + ml) - 1) : "memory");
            } else {
              if constexpr (s >= 0)      asm volatile("s_waitcnt vmcnt(%4)" : "+v"(ag[s][0]), "+v"(ag[s][1]), "+v"(ag[s][2]), "+v"(ag[s][3]) : "i"(is - S::gpos(s, 3) - 1) : "memory");
              else if constexpr (st < 2) asm volatile("s_waitcnt vmcnt(%4)" : "+v"(af[st][0]), "+v"(af[st][1]), "+v"(af[st][2]), "+v"(af[st][3]) : "i"(is - (4 * st + 3) - 1) : "memory");
            }
          }
          if constexpr (st == 0 && i == 0 && !RB)
            asm volatile("" : "+v"(wo_[2][0]), "+v"(wo_[2][1]), "+v"(wo_[2][2]), "+v"(wo_[2][3]), "+v"(wo_[2][4]),
                              "+v"(wg_[0][0]), "+v"(wg_[0][1]), "+v"(wg_[0][2]), "+v"(wg_[0][3]), "+v"(wg_[0][4]),
                              "+v"(wg_[1][0]), "+v"(wg_[1][1]), "+v"(wg_[1][2]), "+v"(wg_[1][3]), "+v"(wg_[1][4]));
          if constexpr (st == 0)     mul0(acc[m][n], wo_[0][n], af[0][m]);
          else if constexpr (s >= 0) mac(acc[m][n], wg_[s & 1][n], ag[s][m]);
          else if constexpr (st < 2) mac(acc[m][n], wo_[1][n], af[1][m]);
          else                       mac(acc[m][n], wo_[2][n], at[m]);
          SB();
          if constexpr (s == 4 && i == 9) WAITV(is - S::gpos(4, 4) - 1);
          if constexpr (m == (s == 4 ? 2 : 0) && s >= 1 && s < 5) wc[n] = lds128((s > 1 ? ca1 + (s - 2) * 5120 : ca0) + n * 1024);
          if constexpr (S::issued(st, i + 1) > is) req(std::integral_constant<int, is>{});
          SB();
        });
        tk(2 + st);
        if constexpr (s >= 1 && s < 5) {
          asm volatile("" : "+v"(wc[0]), "+v"(wc[1]), "+v"(wc[2]), "+v"(wc[3]), "+v"(wc[4]));
#pragma unroll
          for (int n = 0; n < 5; ++n) wg_[(s + 1) & 1][n] = wc[n];
        }
      });
      asm volatile("s_waitcnt vmcnt(0)" : "+v"(sg0), "+v"(sg1), "+v"(sg2) :: "memory");
    }
    MFMA_DONE();
    STAMP(8);
#pragma unroll
    for (int j = 0; j < 10; ++j) {
      const int ta = j < 4 ? 3 * j : j < 6 ? 6 * j - 22 : 2 * j, tb = j < 4 ? ta + 1 : j < 6 ? ta + 3 : ta + 1;
      *(LAS u32x4*)(ca1 + j * 1024) = cat(pk4(acc[ta / 5][ta % 5]), pk4(acc[tb / 5][tb % 5]));
    }
    tk(12);
  };
  if (w < 4) role(std::false_type{}); else role(std::true_type{});

  if (b == 0 && w == 2) {
    GAS u64* dp = (GAS u64*)((char*)ws_ + WS_DONE + lane * 8);
#pragma unroll 1
    for (unsigned spin = 0; spin < (1u << 18); ++spin) {
      bool ok = true;
#pragma unroll
      for (int j = 0; j < 4; ++j) ok &= __hip_atomic_load(dp + j * 64, RLX) == tag;
      if (__all(ok)) break;
      __builtin_amdgcn_s_sleep(2);
    }
    if (lane == 0) __hip_atomic_store((GAS u64*)((char*)ws_ + WS_EPOCH), tag, RLX);
  }
  sg0 = __builtin_amdgcn_rcpf(1.f + sg0); sg1 = __builtin_amdgcn_rcpf(1.f + sg1); sg2 = __builtin_amdgcn_rcpf(1.f + sg2);
  auto ooff = [&](int tl) {
    const int m = tl / 5, n = tl - 5 * m;
    return (unsigned)((64 * rb + 32 * (m >> 1) + 2 * c + (m & 1)) * 1280 + ct * 160 + n * 32 + h * 8);
  };
  unsigned oo0 = ooff(t0), oo1 = ooff(t0 + 1), oo2 = ooff(t0 + 2);
  asm volatile("" : "+v"(sg0), "+v"(sg1), "+v"(sg2), "+v"(oo0), "+v"(oo1), "+v"(oo2));
  WAITL();
  BAR();
  STAMP(9);
  {
    u32x4 p[8];
    u32x2 p2[8];
    unsigned ra = (w < 4 ? w : w + 2) * 1024 + lane * 16, rb_ = ra + 4 * 15360;
    unsigned rc = (4 + (w >> 1)) * 1024 + (w & 1) * 8 + lane * 16, rd = rc + 4 * 15360;
    asm volatile("" : "+v"(ra), "+v"(rb_), "+v"(rc), "+v"(rd));
#pragma unroll
    for (int v = 0; v < 8; ++v) p[v] = lds128((v < 4 ? ra : rb_) + (v & 3) * 15360);
    if (w < 4) {
#pragma unroll
      for (int v = 0; v < 8; ++v) { p2[v] = *(const LAS u32x2*)((v < 4 ? rc : rd) + (v & 3) * 15360); SB(); }
    }
    SB();
#pragma unroll
    for (int j = 0; j < 3; ++j) {
      if (j < 2 || w < 4) {
        f32x4 s = {0.f, 0.f, 0.f, 0.f};
#pragma unroll
        for (int v = 0; v < 8; ++v) {
          const unsigned px = j == 0 ? p[v].x : j == 1 ? p[v].z : p2[v].x, py = j == 0 ? p[v].y : j == 1 ? p[v].w : p2[v].y;
          const f32x4 q = {u2f(px << 16), u2f(px & 0xffff0000u), u2f(py << 16), u2f(py & 0xffff0000u)};
          s = v ? s + q : q;
        }
        const u32x2 ov = pk4(s * (j == 0 ? sg0 : j == 1 ? sg1 : sg2));
        asm volatile("global_store_dwordx2 %0, %1, %2" OUTFL :: "v"(j == 0 ? oo0 : j == 1 ? oo1 : oo2), "v"(ov), "s"(out_) : "memory");
      }
    }
  }
  STAMP(10);
  if (AMP0 >= 0 && (AMPSEL == 0 || (AMPSEL == 1) == (idx < 16))) {
    const u64 d = amp_t1 - amp_t0, t = __builtin_amdgcn_s_memrealtime();
    while (__builtin_amdgcn_s_memrealtime() - t < d) __builtin_amdgcn_s_sleep(1);
  }
}
}

extern "C" void kernel_launch(void* const* d_in, const int* in_sizes, int n_in, void* d_out, int out_size, void* d_ws,
                              size_t ws_size, hipStream_t stream) {
  fused<<<256, 512, 0, stream>>>(d_in[0], d_in[1], d_in[2], d_in[4], d_in[5], d_in[6], d_in[7], d_out, d_ws);
}
```

```cpp
#include <hip/hip_runtime.h>
#include <hip/hip_bf16.h>
#include <stdint.h>
#include <type_traits>

#define STAMPS 0
#define BEARLY 0
#define CODEPF 1
#define TOUCH_NT ""
#define TOUCH 1
#define EXP 0
#define GLA 4
#define PART16 1
#define PL 2
#define PRE 2
#define OUTFL ""
#define PREA 0
#define WGR 4
#define GPOLL 1
#define ROT 4

#define LAS __attribute__((address_space(3)))
#define GAS __attribute__((address_space(1)))
typedef __bf16 bf16x8 __attribute__((ext_vector_type(8)));
typedef __bf16 bf16x2 __attribute__((ext_vector_type(2)));
typedef float f32x4 __attribute__((ext_vector_type(4)));
typedef float f32x2 __attribute__((ext_vector_type(2)));
typedef unsigned u32x4 __attribute__((ext_vector_type(4)));
typedef unsigned u32x2 __attribute__((ext_vector_type(2)));
typedef short s16x4 __attribute__((ext_vector_type(4)));
typedef unsigned long long u64;

namespace {
constexpr int LDS_BYTES = 163840;
constexpr unsigned GATE_OFF = 110592, PACK_OFF = 122880, PKF_OFF = 162816, CELL0 = 122880;
constexpr unsigned OOB = 0x40000000u;
#define CODE_B 40000
constexpr unsigned CODE_BYTES = CODE_B;
constexpr unsigned WS_FLAG = 0;
constexpr unsigned WS_DONE = 4096;
constexpr unsigned WS_PKF = 8192;
constexpr unsigned WS_EPOCH = 12288;
constexpr unsigned WS_G = 16384;
constexpr unsigned WS_O = 131072;
constexpr unsigned WS_GU = WS_O + 128 * 40 * 512;
constexpr unsigned WS_PW = WS_GU + 128 * 48 * 1024;
constexpr unsigned WS_END = WS_PW + 8 * 68 * 5120;
constexpr unsigned WS_STAMP = 16u << 20;
constexpr int AUX_SC1 = 16;
constexpr float LOG2E = 1.4426950408889634f;

__device__ __forceinline__ u32x4 mk_srd(u64 a, unsigned bytes) {
  u32x4 r; r.x = (unsigned)a; r.y = (unsigned)(a >> 32) & 0xffffu; r.z = bytes; r.w = 0x00020000u; return r;
}
__device__ __forceinline__ void dma16(unsigned lds, unsigned imm, unsigned voff, u32x4 srd, unsigned soff) {
  asm volatile("s_add_u32 m0, %0, %4\n\ts_nop 0\n\tbuffer_load_dwordx4 %1, %2, %3 offen lds"
               :: "s"(lds), "v"(voff), "s"(srd), "s"(soff), "i"(imm) : "memory", "scc");
}
__device__ __forceinline__ void dma16c(unsigned lds, unsigned imm, unsigned voff, u32x4 srd, unsigned soff) {
  asm volatile("s_add_u32 m0, %0, %4\n\ts_nop 0\n\tbuffer_load_dwordx4 %1, %2, %3 offen sc1 lds"
               :: "s"(lds), "v"(voff), "s"(srd), "s"(soff), "i"(imm) : "memory", "scc");
}
__device__ __forceinline__ void ld128(u32x4& d, unsigned voff, u32x4 srd, unsigned soff) {
  asm volatile("buffer_load_dwordx4 %0, %1, %2, %3 offen sc1" : "=v"(d) : "v"(voff), "s"(srd), "s"(soff) : "memory");
}
__device__ __forceinline__ void ld64(u32x2& d, unsigned voff, u32x4 srd, unsigned soff) {
  asm volatile("buffer_load_dwordx2 %0, %1, %2, %3 offen sc1" : "=v"(d) : "v"(voff), "s"(srd), "s"(soff) : "memory");
}
__device__ __forceinline__ void ld32(float& d, unsigned voff, u32x4 srd, unsigned soff) {
  asm volatile("buffer_load_dword %0, %1, %2, %3 offen sc1" : "=v"(d) : "v"(voff), "s"(srd), "s"(soff) : "memory");
}
__device__ __forceinline__ u32x4 lds128(unsigned a) { return *(const LAS u32x4*)a; }
__device__ __forceinline__ u32x2 ldstr(unsigned a) {
  return __builtin_bit_cast(u32x2, __builtin_amdgcn_ds_read_tr16_b64_v4i16((LAS s16x4*)a));
}
__device__ __forceinline__ u32x4 cat(u32x2 a, u32x2 b) { u32x4 r; r.x = a.x; r.y = a.y; r.z = b.x; r.w = b.y; return r; }
__device__ __forceinline__ f32x4 mfma(u32x4 a, u32x4 b, f32x4 c) {
  return __builtin_amdgcn_mfma_f32_16x16x32_bf16(__builtin_bit_cast(bf16x8, a), __builtin_bit_cast(bf16x8, b), c, 0, 0, 0);
}
__device__ __forceinline__ void mac(f32x4& c, u32x4 a, u32x4 b) {
  asm volatile("v_mfma_f32_16x16x32_bf16 %0, %1, %2, %0" : "+v"(c) : "v"(a), "v"(b));
}
__device__ __forceinline__ void mul0(f32x4& c, u32x4 a, u32x4 b) {
  asm volatile("v_mfma_f32_16x16x32_bf16 %0, %1, %2, 0" : "=&v"(c) : "v"(a), "v"(b));
}
__device__ __forceinline__ unsigned pk2(float a, float b) {
  f32x2 t = {a, b};
  return __builtin_bit_cast(unsigned, __builtin_convertvector(t, bf16x2));
}
__device__ __forceinline__ u32x2 pk4(f32x4 v) { u32x2 r; r.x = pk2(v.x, v.y); r.y = pk2(v.z, v.w); return r; }
__device__ __forceinline__ float u2f(unsigned u) { return __builtin_bit_cast(float, u); }
__device__ __forceinline__ unsigned f2u(float f) { return __builtin_bit_cast(unsigned, f); }
#define SB() __builtin_amdgcn_sched_barrier(0)
#define WAITV(n) asm volatile("s_waitcnt vmcnt(%0)" :: "i"(n) : "memory")
#define WAITVL(n) asm volatile("s_waitcnt vmcnt(%0) lgkmcnt(0)" :: "i"(n) : "memory")
#define WAITL() asm volatile("s_waitcnt lgkmcnt(0)" ::: "memory")
#define BAR() asm volatile("s_barrier" ::: "memory")
#define STPAD() do { SB(); asm volatile("s_nop 1" ::: "memory"); SB(); } while (0)
#define MFMA_DONE() do { SB(); asm volatile("s_nop 7\n\ts_nop 1" ::: "memory"); SB(); } while (0)
#define RLX __ATOMIC_RELAXED, __HIP_MEMORY_SCOPE_AGENT
__device__ __forceinline__ unsigned rfl(unsigned v) { return __builtin_amdgcn_readfirstlane(v); }
__device__ __forceinline__ unsigned opaque_lane() { unsigned l = threadIdx.x & 63; asm volatile("" : "+v"(l)); return l; }

#define AMP0 -1
#define AMP1 -1
#define AMPSEL 0
#define STAMP(i) do {} while (0)

template <int I, int N, class F>
__device__ __forceinline__ void sfor(F&& f) {
  if constexpr (I < N) { f(std::integral_constant<int, I>{}); sfor<I + 1, N>(f); }
}
template <bool RB> struct Sched {
  static constexpr int NO = RB ? 2 : 3, OB = RB ? 8 : 16;
  static constexpr int gpos(int s, int m) { return OB + 5 * s + m; }
  static constexpr int upto(int st) { return st < 2 ? 4 * (st + 1) : st < NO ? 16 : st < NO + 5 ? OB + 5 * (st - NO + 1) : st == NO + 5 ? OB + 29 : OB + 32; }
  static constexpr int issued(int st, int i) {
    const int base = upto(st + PL - 1), goal = upto(st + PL), every = goal - base > 5 ? 2 : goal - base > 4 ? 4 : 5;
    int n = base;
    for (int k = 0; k < i; ++k) if (k % every == 1 && n < goal) ++n;
    return n;
  }
};
__device__ __forceinline__ float max3(float a, float b, float c) { float r; asm("v_max3_f32 %0, %1, %2, %3" : "=v"(r) : "v"(a), "v"(b), "v"(c)); return r; }
__device__ __forceinline__ float max2(float a, float b) { float r; asm("v_max_f32 %0, %1, %2" : "=v"(r) : "v"(a), "v"(b)); return r; }

struct Stg {
  u32x4 srdx, srdw;
  unsigned xv[4], wv[4];
  unsigned xd;
  unsigned wd0, wd1;
  unsigned sow, sox;
  unsigned kg, kw, kx;
};
template <bool QKV, bool RB>
struct P1 : Stg {
  static constexpr int NM = QKV ? 5 : 4;
  static constexpr int NN = QKV ? 4 : (RB ? 4 : 8);
  static constexpr int NWS = 3;
  static constexpr unsigned WSL = QKV ? 16384 : 24576, XB = NWS * WSL, XSL = QKV ? 20480 : 16384;
  static constexpr unsigned WROW = QKV ? 3840 : 3072;
  static constexpr int NWP = QKV ? 2 : (RB ? 4 : 2);
  static constexpr int NXE = QKV ? (RB ? 1 : 2) : (RB ? 2 : 0), NXO = QKV ? 1 : NXE;
  static constexpr int NXP = NXO + NXE;
  static constexpr int OO = NWP + NXO, OE = NWP + NXE;
  static constexpr bool HAND = !QKV;
  static constexpr int ht = 4;
  f32x4 acc[NM][NN];
  unsigned xa0, wa0, wa1, wa2;
  unsigned gmask;
  __device__ __forceinline__ void tick(int) {}

  unsigned gof, kr;
  template <bool ODD, bool X = true>
  __device__ __forceinline__ void loads(unsigned wwr, unsigned xwr) {
    if (X && NXP) {
#pragma unroll
      for (int i = ODD ? NXE : 0; i < (ODD ? NXP : NXE); ++i) dma16(xwr + xd, i * 1024, xv[i], srdx, sox);
      if (ODD) { kx += 1; sox = (kx ^ (kg >> 1)) * 128; }
    }
    dma16(wwr + wd0, 0, wv[0], srdw, sow);
    dma16(wwr + wd0, 1024, wv[1], srdw, sow);
    if (!QKV && RB) {
      dma16(wwr + wd0, 2048, wv[2], srdw, sow);
      dma16(wwr + wd1, 0, wv[3], srdw, sow);
    }
  }
  __device__ __forceinline__ void other(unsigned wwr) {
    dma16(wwr + wd1, 1024, wv[3], srdw, sow + 128);
    dma16(wwr + wd1, 2048, wv[3], srdw, sow + 256);
  }
  __device__ __forceinline__ void next() { kw += 1; sow = (kw ^ kg) * (32 * WROW); asm volatile("" : "+s"(sow), "+s"(kw)); if (NXP) asm volatile("" : "+s"(sox), "+s"(kx)); }
  __device__ __forceinline__ void stage0(int rot) {
    kg = rot; kw = 0; kx = 0; sow = kg * (32 * WROW); sox = (kg >> 1) * 128;
    if (HAND && !RB) { kw = 3; sow = (3 ^ kg) * (32 * WROW); return; }
#pragma unroll
    for (int i = 0; i < NXE; ++i) dma16(xd, i * 1024, xv[i], srdx, sox);
    loads<true>(0, 0); if (HAND) other(0); next();
  }
  __device__ __forceinline__ void stage1() { if (HAND && !RB) return; loads<false>(WSL, XSL); if (HAND) other(WSL); next(); }
  __device__ __forceinline__ void stage2() { if (HAND && !RB) return; loads<true>(2 * WSL, XSL); if (HAND) other(2 * WSL); next(); }
  static constexpr int VPRO = OE + OO + (HAND ? 4 : 0);
  struct Ad { unsigned x, w[4]; };
  __device__ __forceinline__ void addr(Ad& a, unsigned xb, unsigned wrd) {
    a.x = xb; a.w[0] = wa0 + wrd;
    if (QKV) {
      const unsigned s2 = (gmask & (gof - wrd)) + wrd;
      kr += 1; gof = (kr ^ kg) * 256;
      a.w[1] = wa1 + s2; a.w[2] = wa2 + s2;
      asm volatile("" : "+v"(a.x), "+v"(a.w[0]), "+v"(a.w[1]), "+v"(a.w[2]));
    } else if (RB) {
      a.w[1] = a.w[0] ^ 32;
      asm volatile("" : "+v"(a.x), "+v"(a.w[0]), "+v"(a.w[1]));
    } else {
      a.w[1] = a.w[0] ^ 32; a.w[2] = a.w[0] ^ 64; a.w[3] = a.w[0] ^ 96;
      asm volatile("" : "+v"(a.x), "+v"(a.w[0]), "+v"(a.w[1]), "+v"(a.w[2]), "+v"(a.w[3]));
    }
  }
  __device__ __forceinline__ unsigned wad(const Ad& a, int n, int hi) {
    if (QKV) return n == 2 ? a.w[1 + hi] : a.w[0] + (n == 0 ? 0 : n == 1 ? 160 : 320) + hi * 1920;
    if (RB) return a.w[n & 1] + (n >> 1) * 3072 + hi * 512;
    return a.w[n & 3] + (n >> 2) * 3072 + hi * 512;
  }
  __device__ __forceinline__ void rd_all(u32x4 (&xf)[NM], u32x4 (&wf)[NN], const Ad& a) {
#pragma unroll
    for (int n = 0; n < NN; ++n) wf[n] = cat(ldstr(wad(a, n, 0)), ldstr(wad(a, n, 1)));
#pragma unroll
    for (int m = 0; m < NM; ++m) xf[m] = lds128(a.x + m * 2048);
  }
  template <bool RD = true, class L>
  __device__ __forceinline__ void mm_rd(const u32x4 (&xc)[NM], const u32x4 (&wc)[NN], u32x4 (&xn)[NM], u32x4 (&wn)[NN], const Ad& a, L&& late) {
    u32x2 lo[NN];
    constexpr bool NMAJ = !QKV && !RB;
    constexpr bool EARLY = !QKV && RB && BEARLY;
    if (EARLY && RD) { rd_all(xn, wn, a); SB(); }
    int r = 0;
#pragma unroll
    for (int i = 0; i < NM * NN; ++i) {
      const int m = NMAJ ? i % NM : i / NN, n = NMAJ ? i / NM : i % NN;
      if (QKV && m == 0 && n == 0) continue;
      if (QKV && n == 3) mac(acc[m][3], xc[m], wc[3]);
      else               mac(acc[m][n], wc[n], xc[m]);
      SB();
      if (!RD || EARLY) continue;
      if (NMAJ) {
        if (i < 4) xn[i] = lds128(a.x + i * 2048);
        else if ((i & 3) < 2) {
          const int k = (i >> 2) - 1;
          if (i & 1) wn[k] = cat(lo[k], ldstr(wad(a, k, 1))); else lo[k] = ldstr(wad(a, k, 0));
        } else late(i);
      } else {
        if (r < 2 * NN) {
          const int k = r >> 1;
          if (r & 1) wn[k] = cat(lo[k], ldstr(wad(a, k, 1))); else lo[k] = ldstr(wad(a, k, 0));
        } else if (r < 2 * NN + NM) {
          xn[r - 2 * NN] = lds128(a.x + (r - 2 * NN) * 2048);
        }
        else late(r - 2 * NN - NM);
        ++r;
      }
      SB();
    }
    if (NMAJ && RD) { wn[NN - 1] = cat(ldstr(wad(a, NN - 1, 0)), ldstr(wad(a, NN - 1, 1))); SB(); }
  }
  __device__ __forceinline__ void pin(u32x4 (&xf)[NM], u32x4 (&wf)[NN]) {
    asm volatile("" : "+v"(xf[0]), "+v"(xf[1]), "+v"(xf[2]), "+v"(xf[3]), "+v"(wf[0]), "+v"(wf[1]), "+v"(wf[2]), "+v"(wf[3]));
    if (NM == 5) asm volatile("" : "+v"(xf[NM - 1]));
    if (NN == 8) asm volatile("" : "+v"(wf[NN - 4]), "+v"(wf[NN - 3]), "+v"(wf[NN - 2]), "+v"(wf[NN - 1]));
  }
  __device__ __forceinline__ void zero() {
#pragma unroll
    for (int m = 0; m < NM; ++m)
#pragma unroll
      for (int n = 0; n < NN; ++n) { acc[m][n] = f32x4{0.f, 0.f, 0.f, 0.f}; asm volatile("" : "+v"(acc[m][n])); }
  }
  template <int TL0, int TL1, class F, class G, class H, class U>
  __device__ __forceinline__ void run(F&& pre, G&& extra, H&& tail, U&& gap) {
    kr = 0; gof = kg * 256;
    pre();
    u32x4 xA[NM], wA[NN], xB[NM], wB[NN];
    Ad a;
    addr(a, xa0, 0);
    rd_all(xA, wA, a);
    if (!RB) __builtin_amdgcn_s_setprio(1);
    unsigned w0 = 0, w1 = WSL, w2 = 2 * WSL;
    unsigned xp = 2 * XSL, x0 = 0, x1 = XSL;
    int t = 0;
    unsigned nw1, nw2, nx1;
    auto adv0 = [&]() { nw1 = w2 + WSL; if (nw1 == NWS * WSL) nw1 = 0; asm volatile("" : "+s"(nw1)); };
    auto adv1 = [&]() { nw2 = nw1 + WSL; if (nw2 == NWS * WSL) nw2 = 0; asm volatile("" : "+s"(nw2)); };
    auto adv2 = [&]() { nx1 = x1 + XSL; if (nx1 == 3 * XSL) nx1 = 0; asm volatile("" : "+s"(nx1)); };
    auto adv3 = [&]() {
      w0 = w2; w1 = nw1; w2 = nw2; xp = x0; x0 = x1; x1 = nx1; t += 2;
      asm volatile("" : "+s"(w0), "+s"(w1), "+s"(w2), "+s"(xp), "+s"(x0), "+s"(x1), "+s"(t));
    };
    auto adv = [&]() { adv0(); adv1(); adv2(); adv3(); };
    asm volatile("" : "+s"(w0), "+s"(w1), "+s"(w2), "+s"(xp), "+s"(x0), "+s"(x1), "+s"(t));
    if (QKV) asm volatile("" : "+s"(gof), "+s"(kr));
    auto none = [](int) {};
    if (RB) {
      auto help = [&](int tt, unsigned wwr) { if (HAND && __builtin_expect(tt < ht, 0)) other(wwr); extra(tt); };
      pin(xA, wA);
      SB(); WAITVL(OO + (HAND ? 2 : 0)); tick(0); BAR(); tick(1); SB();
      loads<false>(w0, xp); help(0, w0); next(); addr(a, (xa0 ^ 64) + x0, w1); SB();
      auto half = [&]() {
        mm_rd(xA, wA, xB, wB, a, [&](int i) {
          if (QKV && i == 0) adv0();
          if (QKV && i == 2) adv1();
          if (QKV && i == 4) adv2();
        });
        pin(xB, wB);
        SB(); WAITVL(OE); tick(2 * t + 2); BAR(); tick(2 * t + 3); SB();
        loads<true>(w1, xp); help(t + 1, w1); next();
        addr(a, xa0 + x1, w2);
        if (QKV) adv3(); else adv();
        SB();
        mm_rd(xB, wB, xA, wA, a, none);
      };
#pragma unroll 1
      do {
        half();
        pin(xA, wA);
        SB(); WAITVL(OO); tick(2 * t); BAR(); tick(2 * t + 1); SB();
        loads<false>(w0, xp); help(t, w0); next(); addr(a, (xa0 ^ 64) + x0, w1); SB();
        if (QKV) asm volatile("" : "+s"(gof), "+s"(kr));
      } while (t < 14);
      half();
    } else {
      addr(a, (xa0 ^ 64) + x0, w1);
      auto trip = [&](auto EARLY) {
        constexpr bool E = EARLY.value;
        pin(xA, wA);
        SB(); if (E) WAITL(); else WAITVL(OO);
        tick(2 * t); BAR(); tick(2 * t + 1); SB();
        mm_rd(xA, wA, xB, wB, a, [&](int i) { if (E) gap(i); });
        if (E) { if (t == 0) WAITV(0); else extra(t); }
        if (!E || t >= ht) loads<false>(w0, xp);
        next(); addr(a, xa0 + x1, w2); adv0(); adv1(); adv2(); SB();
        pin(xB, wB);
        SB(); if (E) WAITL(); else WAITVL(OE);
        tick(2 * t + 2); BAR(); tick(2 * t + 3); SB();
        mm_rd(xB, wB, xA, wA, a, [&](int i) { if (E) gap(i); });
        if (E && t < ht) extra(t + 1); else loads<true>(w1, xp);
        next(); adv3(); addr(a, (xa0 ^ 64) + x0, w1); SB();
        if (QKV) asm volatile("" : "+s"(gof), "+s"(kr));
      };
      if (HAND) {
#pragma unroll 1
        do trip(std::true_type{}); while (t < ht + 2);
      }
#pragma unroll 1
      do trip(std::false_type{}); while (t < 16);
    }
    constexpr unsigned S16 = (16 % NWS) * WSL, S17 = (17 % NWS) * WSL, S18 = (18 % NWS) * WSL, S19 = (19 % NWS) * WSL, X8 = 2 * XSL, X9 = 0;
    pin(xA, wA);
    SB(); WAITVL(OO);
    tick(32); BAR(); tick(33); SB();
    if (RB) { loads<false, false>(S16, 0); tail(0); addr(a, (xa0 ^ 64) + X8, S17); SB(); mm_rd(xA, wA, xB, wB, a, none); }
    else    { mm_rd(xA, wA, xB, wB, a, none); loads<false, false>(S16, 0); tail(0); addr(a, xa0 + X9, S18); SB(); }
    pin(xB, wB);
    SB(); WAITVL(NWP + TL0); tick(34); BAR(); tick(35); SB();
    if (RB) { tail(1); addr(a, xa0 + X9, S18); SB(); mm_rd(xB, wB, xA, wA, a, none); }
    else    { mm_rd(xB, wB, xA, wA, a, none); tail(1); addr(a, (xa0 ^ 64) + X9, S19); SB(); }
    pin(xA, wA);
    SB(); WAITVL(TL0 + TL1); tick(36); BAR(); tick(37); SB();
    if (RB) { tail(2); addr(a, (xa0 ^ 64) + X9, S19); SB(); mm_rd(xA, wA, xB, wB, a, none); }
    else    { mm_rd(xA, wA, xB, wB, a, none); tail(2); SB(); }
    pin(xB, wB);
    SB(); WAITL(); tick(38); BAR(); tick(39); SB();
    if (RB) { tail(3); SB(); mm_rd<false>(xB, wB, xA, wA, a, none); }
    else    { mm_rd<false>(xB, wB, xA, wA, a, none); tail(3); SB(); }
    if (!RB) __builtin_amdgcn_s_setprio(0);
    MFMA_DONE();
    tick(40);
  }
};

__global__ __launch_bounds__(512, 2) void fused(const void* x_, const void* wqkv_, const void* wo_p, const void* wg_p,
                                                const void* wu_p, const void* wd_p, const void* gw_, void* out_, void* ws_) {
  __shared__ __attribute__((aligned(16))) char smem[LDS_BYTES];
  asm volatile("" :: "s"((unsigned)(size_t)(LAS char*)smem));
  const int tid = threadIdx.x, lane = tid & 63;
  const int w = rfl(tid >> 6);
  const int h = lane >> 4, c = lane & 15, q4 = (lane & 15) >> 2, p4 = lane & 3;
  const int b = blockIdx.x, xcd = b & 7, idx = b >> 3;
  const int rh = xcd & 1, cq = xcd >> 1;
  const int a = 8 * rh + (idx & 7);
  const int gc = 2 * cq + ((idx >> 3) & 1);
  const int rb = 4 * xcd + (idx >> 3), ct = idx & 7;
  u64 wo, wd;
  char* ws;
  __amdgpu_buffer_rsrc_t rws;
  u32x4 sws;
  auto bind = [&]() {
    wo = (u64)wo_p; wd = (u64)wd_p;
    u64 pw = (u64)ws_;
    asm volatile("" : "+s"(wo), "+s"(wd), "+s"(pw));
    ws = (char*)pw;
    rws = __builtin_amdgcn_make_buffer_rsrc(ws, 0, WS_END, 0x00020000);
    sws = mk_srd(pw, WS_END);
  };
  u64 amp_t0 = 0, amp_t1 = 0;

  if (CODEPF) {
    u64 pc;
    asm volatile("s_getpc_b64 %0" : "=s"(pc));
    asm volatile("s_mov_b32 m0, %0\n\ts_nop 0\n\tbuffer_load_dword %1, %2, 0 offen lds"
                 :: "s"(PKF_OFF), "v"(lane < 6 ? ((idx & 7) * 48 + w * 6 + lane) * 128 : OOB), "s"(mk_srd(pc & ~127ull, CODE_BYTES)) : "memory");
  }
  u32x2 ev;
  u64 tag = 0;
  auto make_tag = [&](auto NV) {
    unsigned lo, hi;
    asm volatile("s_waitcnt vmcnt(%4)\n\tv_readfirstlane_b32 %0, %2\n\tv_readfirstlane_b32 %1, %3"
                 : "=s"(lo), "=s"(hi) : "v"(ev.x), "v"(ev.y), "i"(NV.value) : "memory");
    const u64 e = ((u64)hi << 32) | lo;
    tag = (e ^ 0x9E3779B97F4A7C15ull) * 0xBF58476D1CE4E5B9ull + 0x632BE59BD9B4E019ull;
    if (tag == e) tag += 1;
  };
  const unsigned pwb = WS_PW + ct * (68 * 5120);
  const int grot = 3 * ((idx >> 3) & 1);
  auto gstep = [&](int s) { const int v = s + grot; return v >= 6 ? v - 6 : v; };
  auto cell_dma = [&](int v, int cell) {
    const unsigned so = pwb + (20 + 6 * v + gstep(2 + cell)) * 5120;
#pragma unroll
    for (int j = 0; j < 5; ++j) dma16c(cell ? v * 15360 : CELL0 + v * 5120, (cell ? cell - 1 : 0) * 5120 + j * 1024, lane * 16, sws, so + j * 1024);
  };
  auto ldw = [&](u32x4 (&d)[5], unsigned so) {
#pragma unroll
    for (int n = 0; n < 5; ++n) d[n] = __builtin_amdgcn_raw_buffer_load_b128(rws, lane * 16, so + n * 1024, AUX_SC1);
  };
  const unsigned pscr = PACK_OFF + (w & 3) * 8192;
  const int pu0 = (b - 128) * 4 + w;
  unsigned pra[2], prb[2], pn[2], pb[2];
  auto pack_issue = [&]() {
    const u64 po = wo, pd = wd;
#pragma unroll
    for (int r = 0; r < 2; ++r) {
      const int u = pu0 + 512 * r;
      const int ks = u / 10, cg = u - 10 * ks;
      const unsigned r8 = lane >> 3;
      const u32x4 srd = mk_srd(ks < 20 ? po : pd, ks < 20 ? 640 * 1280 : 1536 * 1280);
      const unsigned base = ks < 16 ? 40 * ks : ks < 20 ? 160 * (ks - 16) + 16 : 32 * (ks - 20);
      const unsigned vr = (ks >= 16 && ks < 20) ? 20 * (r8 >> 2) + (r8 & 3) : r8;
      const unsigned vo = vr * 1280 + (lane & 7) * 16;
#pragma unroll
      for (int p = 0; p < 4; ++p) {
        const unsigned rp = ks < 16 ? 20 * (p >> 1) + 8 * (p & 1) : ks < 20 ? 40 * p : 8 * p;
        dma16(pscr, r * 4096 + p * 1024, vo, srd, u < 680 ? (base + rp) * 1280 + cg * 128 : OOB);
      }
      pra[r] = pscr + r * 4096 + (h * (ks < 16 ? 8 : 4) + q4) * 128 + p4 * 8 - 32;
      prb[r] = pra[r] + (ks < 16 ? 512 : 2048);
      pn[r] = 4 * cg - 1; pb[r] = u < 680 ? WS_PW + ks * 5120 + (4 * cg - 1) * 1024 : OOB;
    }
    asm volatile("" : "+v"(pra[0]), "+v"(prb[0]), "+v"(pra[1]), "+v"(prb[1]));
    asm volatile("" : "+s"(pn[0]), "+s"(pb[0]), "+s"(pn[1]), "+s"(pb[1]));
  };
  const unsigned smask = w != 4 ? 0u : idx >= 16 ? 0x1040u : 0x1000u;
  auto side_b = [&](int t) {
    if (__builtin_expect(!((smask >> t) & 1), 1)) return;
    if (t == 6) {
      u32x2 tv; tv.x = (unsigned)tag; tv.y = (unsigned)(tag >> 32);
      __builtin_amdgcn_raw_buffer_store_b64(tv, rws, opaque_lane() == 0 ? WS_PKF + (b - 128) * 8 : OOB, 0, AUX_SC1);
    } else {
      dma16c(PKF_OFF, 0, opaque_lane() * 16, sws, WS_PKF);
    }
  };
  u32x2 pq[4];
  unsigned ps0, ps1;
  auto pack_gap = [&](int i) {
    if (i == 6)  pq[0] = ldstr(pra[0]);
    if (i == 7)  pq[1] = ldstr(prb[0]);
    if (i == 10) pq[2] = ldstr(pra[1]);
    if (i == 11) pq[3] = ldstr(prb[1]);
    if (i == 14) { pra[0] += 32; prb[0] += 32; asm volatile("" : "+v"(pra[0]), "+v"(prb[0])); }
    if (i == 15) { pra[1] += 32; prb[1] += 32; asm volatile("" : "+v"(pra[1]), "+v"(prb[1])); }
    if (i == 18) { ps0 = (pn[0] * 205) >> 10; asm volatile("" : "+s"(ps0)); }
    if (i == 19) { ps0 = ps0 * (68 * 5120 - 5120) + pb[0]; asm volatile("" : "+s"(ps0)); }
    if (i == 22) { ps1 = (pn[1] * 205) >> 10; asm volatile("" : "+s"(ps1)); }
    if (i == 23) { ps1 = ps1 * (68 * 5120 - 5120) + pb[1]; asm volatile("" : "+s"(ps1)); }
    if (i == 26) { pn[0] += 1; pb[0] += 1024; asm volatile("" : "+s"(pn[0]), "+s"(pb[0])); }
    if (i == 27) { pn[1] += 1; pb[1] += 1024; asm volatile("" : "+s"(pn[1]), "+s"(pb[1])); }
  };
  auto side_f = [&](int) {
    __builtin_amdgcn_raw_buffer_store_b128(cat(pq[0], pq[1]), rws, lane * 16, ps0, AUX_SC1);
    STPAD();
    __builtin_amdgcn_raw_buffer_store_b128(cat(pq[2], pq[3]), rws, lane * 16, ps1, AUX_SC1);
    STPAD();
  };
  auto pk_check = [&]() {
    const u32x4 p0 = lds128(PKF_OFF + lane * 16);
    const unsigned lo = (unsigned)tag, hi = (unsigned)(tag >> 32);
    if (!__all(p0.x == lo && p0.z == lo && p0.y == hi && p0.w == hi)) {
      GAS u64* fp = (GAS u64*)((char*)ws + WS_PKF + opaque_lane() * 8);
#pragma unroll 1
      for (unsigned spin = 0; spin < (1u << 18); ++spin) {
        bool ok = true;
#pragma unroll
        for (int j = 0; j < 2; ++j) ok &= __hip_atomic_load(fp + 64 * j, RLX) == tag;
        if (__all(ok)) break;
        __builtin_amdgcn_s_sleep(2);
      }
    }
    asm volatile("" ::: "memory");
  };
  const int rot = (a + 2 * ((idx >> 3) & 1) + (idx >> 4)) & (ROT - 1);

  Stg sg_;
  unsigned gv0, gv1;
  auto first = [&](auto QT, auto RT) {
    constexpr bool QKV = QT.value, RB = RT.value;
    using K = P1<QKV, RB>;
    K k;
    k.xv[0] = k.xv[1] = k.xv[2] = k.xv[3] = k.wv[2] = k.wv[3] = OOB; k.wd1 = 0;
    if (QKV) {
      const int q0 = RB ? 12 + 2 * (w - 4) : 3 * w;
      k.xd = K::XB + q0 * 1024; k.wd0 = w * 2048;
      asm volatile("" : "+s"(k.xd), "+s"(k.wd0));
#pragma unroll
      for (int i = 0; i < K::NXP; ++i) {
        const int rho = 8 * (q0 + i) + (lane >> 3);
        const int pp = rho >= 80 ? 1 : 0, u = rho - 80 * pp;
        const int tok = 128 * a - 32 + 2 * u + pp;
        const unsigned v = tok * 1280 + (((lane & 7) ^ ((rho >> 1) & 7)) << 4);
        k.xv[i] = (u >= 9 && tok >= 0) ? v : OOB;
      }
#pragma unroll
      for (int j = 0; j < 2; ++j) {
        const unsigned I = (2 * w + j) * 64 + lane;
        const unsigned kb = (I >= 248) + (I >= 488) + (I >= 736);
        const unsigned i = I - 240 * kb - 8 * ((kb + 1) >> 1);
        const unsigned r = (i * 2185u) >> 16, rem = i - r * 30;
        const unsigned seg = (rem * 6554u) >> 16, ch = rem - seg * 10;
        const unsigned v = (8 * kb + r) * 3840 + seg * 1280 + gc * 160 + ch * 16;
        k.wv[j] = i < 240 ? v : OOB;
      }
    } else {
      const int up = (w >> 1) & 1, kp = w & 1;
#pragma unroll
      for (int i = 0; i < K::NXP; ++i) {
        const int rho = 8 * (4 * (w & 3) + i) + (lane >> 3);
        const int j = rho >> 4;
        const int tok = 128 * a + 32 * (j >> 1) + 2 * (rho & 15) + (j & 1);
        k.xv[i] = tok * 1280 + (((lane & 7) ^ ((rho >> 1) & 7)) << 4);
      }
      k.xd = K::XB + 4 * (w & 3) * 1024;
      const int r = lane >> 3, p8 = lane & 7;
      const int s = ((r >> 1) & 1) + 2 * kp;
      k.wv[0] = (8 * kp + r) * 3072 + gc * 384 + (((((p8 >> 1) ^ s) << 1) | (p8 & 1)) << 4) + (RB ? 0 : 16 * 3072 + 128);
      k.wv[1] = k.wv[0] + 128; k.wv[2] = k.wv[0] + 256; k.wv[3] = k.wv[0] + 16 * 3072;
      k.wd0 = RB ? (kp * 6 + 3 * up) * 1024 : ((kp + 2) * 6 + 3 * up + 1) * 1024;
      k.wd1 = ((kp + 2) * 6 + 3 * up) * 1024;
    }
    asm volatile("" : "+v"(k.xv[0]), "+v"(k.xv[1]), "+v"(k.xv[2]), "+v"(k.xv[3]), "+v"(k.wv[0]), "+v"(k.wv[1]), "+v"(k.wv[2]), "+v"(k.wv[3]));
    u64 px = (u64)x_, pq = (u64)wqkv_, pg = (u64)wg_p, pu = (u64)wu_p;
    asm volatile("" : "+s"(px), "+s"(pq), "+s"(pg), "+s"(pu));
    k.srdx = mk_srd(px, 2048 * 1280);
    k.srdw = QKV ? mk_srd(pq, 640 * 3840) : mk_srd(((w >> 1) & 1) ? pu : pg, 640 * 3072);
    u32x2 e; unsigned g0, g1;
    asm volatile("global_load_dwordx2 %0, %1, %2 sc1" : "=v"(e) : "v"(WS_EPOCH), "s"(ws_) : "memory");
    if (QKV) asm volatile("buffer_load_ushort %0, %2, %3, 0 offen\n\tbuffer_load_ushort %1, %2, %3, 0 offen offset:1024"
                          : "=&v"(g0), "=&v"(g1) : "v"(tid * 2), "s"(mk_srd((u64)gw_, 1280)) : "memory");
    else     asm volatile("" : "=v"(g0), "=v"(g1));
    k.stage0(rot);
    BAR();
    k.stage1();
    sg_ = k; ev = e; gv0 = g0; gv1 = g1;
  };
  if (idx < 16) { if (w < 4) first(std::true_type{}, std::false_type{}); else first(std::true_type{}, std::true_type{}); }
  else          { if (w < 4) first(std::false_type{}, std::false_type{}); else first(std::false_type{}, std::true_type{}); }
  bind();
  STAMP(0);
  STAMP(11);
  const int t0 = 2 * w + min(w, 4);
  float sg0, sg1, sg2;
  auto role = [&](auto RBT) {
    constexpr bool RB = decltype(RBT)::value;
    u32x4 wo_[3][5], wg_[2][5];
    auto tail = [&](int i) {
      if (i == 0) pk_check();
      if (RB) {
        if (i < 2) ldw(wo_[i], pwb + (2 * w + i) * 5120);
        else ldw(wg_[i - 2], pwb + (20 + 6 * w + gstep(i - 2)) * 5120);
        if (i == 1) cell_dma(w - 4, 0);
        if (i == 2) cell_dma(w, 0);
      } else if (i >= 2) {
        ldw(wo_[i - 2], pwb + (2 * w + i - 2) * 5120);
      }
    };
    auto late_w = [&]() {
#pragma unroll
      for (int n = 0; n < 5; ++n) ld128(wo_[2][n], lane * 16, sws, pwb + (16 + w) * 5120 + n * 1024);
#pragma unroll
      for (int j = 0; j < 10; ++j) ld128(wg_[j / 5][j % 5], lane * 16, sws, pwb + (20 + 6 * w + gstep(j / 5)) * 5120 + (j % 5) * 1024);
    };
    if (idx < 16) {
      const int g = gc, hd = w & 3;
      constexpr int par = RB ? 1 : 0;
      using K = P1<true, RB>;
      K k;
      (Stg&)k = sg_;
      k.xa0 = (K::XB + par * 5 * 2048 + c * 128 + ((h ^ (c >> 1)) << 4)) ^ ((rot & 1) << 6);
      const unsigned kbo = h * 3840 + ((h + 1) >> 1) * 128;
      k.wa0 = kbo + q4 * 480 + (20 * hd + 4 * p4) * 2;
      const bool isgate = p4 == 3;
      k.gmask = isgate ? ~0u : 0u;
      asm volatile("" : "+v"(k.gmask));
      k.wa1 = isgate ? GATE_OFF + (8 * h + q4) * 8 : kbo + q4 * 480 + p4 * 160 + (20 * hd + 16) * 2;
      k.wa2 = k.wa1 + (isgate ? 32 : 1920);
      f32x4 ba, bb, ba1;
#pragma unroll
      for (int r = 0; r < 4; ++r) {
        const int kap = 4 * h + r;
        ba[r] = kap >= c + 9 ? 0.f : -1e30f;
        bb[r] = (kap <= c && kap >= c - 7) ? 0.f : -1e30f;
        ba1[r] = a == 0 ? -1e30f : ba[r];
      }
      unsigned m0mask = h == 0 ? 0xffffffffu : 0u;
      unsigned scw = w * 15360 + 10240 + c * 32 + h * 8, sck = w * 15360 + 10240 + c * 32 + 8,
               sct = w * 15360 + 10240 + (4 * h + q4) * 32 + p4 * 8;
      unsigned ovo = WS_O + (h & 1) * (2 * 20480) + (2 * g + (hd >> 1)) * 1024 + (2 * (hd & 1) + (h >> 1)) * 256 + c * 16;
      unsigned ovt = h == 2 ? WS_O + (32 + g) * 512 + hd * 128 + c * 8 : OOB;
      asm volatile("" : "+v"(ba), "+v"(bb), "+v"(ba1), "+v"(m0mask), "+v"(scw), "+v"(sck), "+v"(sct), "+v"(ovo), "+v"(ovt));
      asm volatile("" : "+v"(k.xa0), "+v"(k.wa0));
      k.zero();
      k.stage2();
      STAMP(12);
      k.template run<RB ? 5 : 0, RB ? 10 : 0>([&]() {
        unsigned g0, g1;
        asm volatile("s_waitcnt vmcnt(%4)\n\tv_mov_b32 %0, %2\n\tv_mov_b32 %1, %3" : "=&v"(g0), "=&v"(g1) : "v"(gv0), "v"(gv1), "i"(K::VPRO) : "memory");
        u32x2 v; v.x = g0; v.y = 0;
        *(LAS u32x2*)(GATE_OFF + tid * 8) = v;
        if (!RB && tid < 128) { v.x = g1; *(LAS u32x2*)(GATE_OFF + 4096 + tid * 8) = v; }
        make_tag(std::integral_constant<int, K::VPRO>{});
        WAITL();
        BAR();
        STAMP(1);
      }, [&](int t) { if (RB) side_b(t); }, tail, [](int) {});
      STAMP(2);
      u32x2 ql[5], kl[5], t2[5], vl[5], tt[5], k4[5];
      unsigned sb16[4];
#pragma unroll
      for (int i = 0; i < 5; ++i) { t2[i] = pk4(k.acc[i][2]); *(LAS u32x2*)(scw + i * 512) = t2[i]; }
#pragma unroll
      for (int i = 0; i < 5; ++i) { if (i) ql[i] = pk4(k.acc[i][0]); kl[i] = pk4(k.acc[i][1]); }
      WAITL();
#pragma unroll
      for (int i = 0; i < 5; ++i) k4[i] = *(const LAS u32x2*)(sck + i * 512);
#pragma unroll
      for (int i = 0; i < 4; ++i) sb16[i] = *(const LAS unsigned*)(sck + (i + 1) * 512 + 16);
#pragma unroll
      for (int i = 0; i < 5; ++i) tt[i] = ldstr(sct + i * 512);
#pragma unroll
      for (int i = 0; i < 5; ++i) vl[i] = pk4(k.acc[i][3]);
      constexpr float SC = 0.22360679774997896f * LOG2E;
      f32x4 sa[4], sb[4];
#pragma unroll
      for (int i = 1; i < 5; ++i) {
        u32x4 qf; qf.x = ql[i].x; qf.y = ql[i].y; qf.z = t2[i].x & m0mask; qf.w = t2[i].y & m0mask;
        sb[i - 1] = mfma(cat(kl[i], k4[i]), qf, bb);
        sa[i - 1] = mfma(cat(kl[i - 1], k4[i - 1]), qf, i == 1 ? ba1 : ba);
      }
      asm volatile("s_nop 7\n\ts_nop 0" : "+v"(sa[0]), "+v"(sa[1]), "+v"(sa[2]), "+v"(sa[3]), "+v"(sb[0]), "+v"(sb[1]), "+v"(sb[2]), "+v"(sb[3]));
      float mx[4], es[4];
#pragma unroll
      for (int i = 0; i < 4; ++i)
        mx[i] = max3(max3(sb[i].x, sb[i].y, sb[i].z), max3(sb[i].w, sa[i].x, sa[i].y), max2(sa[i].z, sa[i].w));
#pragma unroll
      for (int i = 0; i < 4; ++i) {
        auto r = __builtin_amdgcn_permlane16_swap(f2u(mx[i]), f2u(mx[i]), false, false);
        mx[i] = max2(u2f(r[0]), u2f(r[1]));
      }
#pragma unroll
      for (int i = 0; i < 4; ++i) {
        auto r = __builtin_amdgcn_permlane32_swap(f2u(mx[i]), f2u(mx[i]), false, false);
        mx[i] = max2(u2f(r[0]), u2f(r[1])) * SC;
        es[i] = __builtin_amdgcn_exp2f(__builtin_amdgcn_fmed3f(u2f(sb16[i] << 16), -60.f, 60.f) * LOG2E);
      }
      u32x4 pf[4];
#pragma unroll
      for (int i = 0; i < 4; ++i) {
        f32x4 pa, pb;
#pragma unroll
        for (int r = 0; r < 4; ++r) {
          pa[r] = __builtin_amdgcn_exp2f(sa[i][r] * SC - mx[i]);
          pb[r] = __builtin_amdgcn_exp2f(sb[i][r] * SC - mx[i]);
        }
        pf[i] = cat(pk4(pa), pk4(pb));
      }
      const f32x4 z = {0.f, 0.f, 0.f, 0.f};
      const u32x4 ones = {0x3f803f80u, 0x3f803f80u, 0x3f803f80u, 0x3f803f80u};
      f32x4 ls[4], oa[4], ob[4];
#pragma unroll
      for (int i = 0; i < 4; ++i) {
        ls[i] = mfma(ones, pf[i], z);
        oa[i] = mfma(cat(vl[i], vl[i + 1]), pf[i], z);
        ob[i] = mfma(cat(tt[i], tt[i + 1]), pf[i], z);
      }
      const unsigned mt0 = 8 * a + par;
      u32x2 op[4];
#pragma unroll
      for (int i = 0; i < 4; ++i) {
        const float f = es[i] * __builtin_amdgcn_rcpf(ls[i].x);
        op[i] = pk4(oa[i] * f);
        __builtin_amdgcn_raw_buffer_store_b64(pk4(ob[i] * f), rws, ovt, (mt0 + 2 * i) * 20480, AUX_SC1);
        if (i & 1) {
          auto sx = __builtin_amdgcn_permlane16_swap(op[i - 1].x, op[i].x, false, false);
          auto sy = __builtin_amdgcn_permlane16_swap(op[i - 1].y, op[i].y, false, false);
          u32x4 o; o.x = sx[0]; o.y = sy[0]; o.z = sx[1]; o.w = sy[1];
          __builtin_amdgcn_raw_buffer_store_b128(o, rws, ovo, (mt0 + 2 * i - 2) * 20480, AUX_SC1);
          STPAD();
        }
      }
      if (g == 0 && hd == 0) {
        const float e = h == 0 ? es[0] : h == 1 ? es[1] : h == 2 ? es[2] : es[3];
        __builtin_amdgcn_raw_buffer_store_b32(f2u(e), rws, WS_G + h * 128 + c * 4, mt0 * 64, AUX_SC1);
      }
    } else {
      const int cj = gc;
      using K = P1<false, RB>;
      K k;
      const int mh = RB ? (w < 6 ? 1 : 0) : (w >> 1);
      (Stg&)k = sg_;
      if (!RB) pack_issue();
      k.xa0 = (K::XB + mh * 4 * 2048 + c * 128 + ((h ^ (c >> 1)) << 4)) ^ ((rot & 1) << 6);
      {
        const int s = ((q4 >> 1) & 1) + 2 * (h & 1);
        k.wa0 = (h * 6 + (RB ? 2 : (w & 1))) * 1024 + q4 * 128 + (((RB ? 2 * (w & 1) : 0) ^ s) << 5) + p4 * 8;
      }
      asm volatile("" : "+v"(k.xa0), "+v"(k.wa0));
      k.zero();
      k.stage2();
      STAMP(12);
      k.template run<RB ? 5 : 0, RB ? 10 : 0>([&]() {
        make_tag(std::integral_constant<int, RB ? K::VPRO : 8>{});
        BAR();
        STAMP(1);
      }, [&](int t) { if (RB) side_b(t); else side_f(t); }, tail, pack_gap);
      STAMP(2);
      constexpr int NK = RB ? 1 : 2;
      float nl2e = -LOG2E;
      asm volatile("" : "+s"(nl2e));
      const unsigned gso = WS_GU + ((8 * a + 4 * mh) * 48 + 6 * cj + (RB ? 4 + (w & 1) : 2 * (w & 1))) * 1024;
#pragma unroll
      for (int m = 0; m < 4; ++m) {
#pragma unroll
        for (int kk = 0; kk < NK; ++kk) {
          float v[8];
#pragma unroll
          for (int j = 0; j < 8; j += 2) {
            const f32x2 g2 = {k.acc[m][2 * kk + (j >> 2)][j & 3], k.acc[m][2 * kk + (j >> 2)][(j & 3) + 1]};
            const f32x2 u2 = {k.acc[m][2 * NK + 2 * kk + (j >> 2)][j & 3], k.acc[m][2 * NK + 2 * kk + (j >> 2)][(j & 3) + 1]};
            const f32x2 a2 = g2 * nl2e;
            const f32x2 d2 = f32x2{__builtin_amdgcn_exp2f(a2.x), __builtin_amdgcn_exp2f(a2.y)} + 1.f;
            const f32x2 r2 = g2 * u2 * f32x2{__builtin_amdgcn_rcpf(d2.x), __builtin_amdgcn_rcpf(d2.y)};
            v[j] = r2.x; v[j + 1] = r2.y;
          }
          u32x4 o; o.x = pk2(v[0], v[1]); o.y = pk2(v[2], v[3]); o.z = pk2(v[4], v[5]); o.w = pk2(v[6], v[7]);
          __builtin_amdgcn_raw_buffer_store_b128(o, rws, lane * 16, gso + m * (48 * 1024) + kk * 1024, AUX_SC1);
          STPAD();
        }
      }
    }
    SB();
    STAMP(13);
    asm volatile("" : "+v"(wo_[0][0]), "+v"(wo_[0][1]), "+v"(wo_[0][2]), "+v"(wo_[0][3]), "+v"(wo_[0][4]),
                      "+v"(wo_[1][0]), "+v"(wo_[1][1]), "+v"(wo_[1][2]), "+v"(wo_[1][3]), "+v"(wo_[1][4]));
    if (RB) asm volatile("" : "+v"(wg_[0][0]), "+v"(wg_[0][1]), "+v"(wg_[0][2]), "+v"(wg_[0][3]), "+v"(wg_[0][4]),
                              "+v"(wg_[1][0]), "+v"(wg_[1][1]), "+v"(wg_[1][2]), "+v"(wg_[1][3]), "+v"(wg_[1][4]));
    SB();
    STAMP(14);
    if (RB && idx >= 16) { if (PRE > 0) cell_dma(w - 4, 1); if (PRE > 1) cell_dma(w, 1); if (PRE > 2) cell_dma(w - 4, 2); WAITV(5 * PRE); }
    else if (RB || !PREA) WAITV(0);
    else { late_w(); WAITV(15); }
    STAMP(3);
    WAITL();
    BAR();
    if (!RB && w == 0 && lane < 2) {
      GAS u64* fp = (GAS u64*)((char*)ws + (lane ? WS_DONE + b * 8 : WS_FLAG + (a * 16 + (idx < 16 ? 8 : 0) + gc) * 8));
      __hip_atomic_store(fp, tag, RLX);
    }
    STAMP(4);

    SB();
    const unsigned fo = WS_FLAG + ((rb >> 1) * 16 + c) * 8;
    auto wait_flags = [&](u32x2& v, bool need) {
#pragma unroll 1
      for (unsigned spin = 0; spin < (1u << 18); ++spin) {
        if (__all(!need || (v.x == (unsigned)tag && v.y == (unsigned)(tag >> 32)))) break;
        ld64(v, fo, sws, 0);
        asm volatile("s_waitcnt vmcnt(0)" : "+v"(v) :: "memory");
      }
      asm volatile("" ::: "memory");
    };
    u32x2 fv;
    if (RB) {
      if (idx < 16) { if (PRE > 0) cell_dma(w - 4, 1); if (PRE > 1) cell_dma(w, 1); if (PRE > 2) cell_dma(w - 4, 2); }
      if (PRE < 1) cell_dma(w - 4, 1); if (PRE < 2) cell_dma(w, 1); if (PRE < 3) cell_dma(w - 4, 2);
      cell_dma(w, 2);
    } else {
      if (!PREA) late_w();
      if (w == 0) {
        ld64(fv, fo, sws, 0);
        asm volatile("s_waitcnt vmcnt(0)" : "+v"(fv) :: "memory");
        wait_flags(fv, c >= 8);
      }
    }
    if (RB) __builtin_amdgcn_s_setprio(1);
    BAR();
    STAMP(5);
    f32x4 acc[4][5];
    u32x4 ag[6][4];
    auto tk = [](int) {};
    tk(0);
    const unsigned gb = (EXP & 1) ? pwb : WS_GU + (4 * rb * 48 + 6 * w) * 1024;
    auto ldg = [&](int s, int m) { ld128(ag[s][m], lane * 16, sws, gb + gstep(s) * 1024 + m * (48 * 1024)); };
    unsigned ca0 = CELL0 + w * 5120 + lane * 16, ca1 = w * 15360 + lane * 16;
    asm volatile("" : "+v"(ca0), "+v"(ca1));
    {
      using S = Sched<RB>;
      constexpr int NO = S::NO, OB = S::OB;
      u32x4 af[2][4], at[4];
      u32x2 tl[4], th[4];
      const unsigned ob = WS_O + 4 * rb * 20480;
      auto req = [&](auto IT) {
        constexpr int i = IT.value;
        if constexpr (i < 8) ld128(af[i >> 2][i & 3], lane * 16, sws, ob + (i & 3) * 20480 + (2 * w + (i >> 2)) * 1024);
        else if constexpr (i < OB) ld64((i & 1) ? th[(i - 8) >> 1] : tl[(i - 8) >> 1], lane * 8, sws, ob + ((i - 8) >> 1) * 20480 + (16 + w) * 1024 + (i & 1) * 512);
        else if constexpr (i < OB + 29 && (i - OB) % 5 < 4) ldg((i - OB) / 5, (i - OB) % 5);
        else if constexpr (i < OB + 29) dma16c(w * 15360, 10240 + ((i - OB) / 5) * 1024, lane * 16, sws, pwb + (20 + 6 * w + gstep(5)) * 5120 + ((i - OB) / 5) * 1024);
        else ld32(i == OB + 29 ? sg0 : i == OB + 30 ? sg1 : sg2, c * 4, sws, WS_G + (4 * rb + min(t0 + i - OB - 29, 19) / 5) * 64);
      };
      if (RB || w != GPOLL || !GPOLL) sfor<0, S::upto(PL - 1)>(req);
      if (RB) { __builtin_amdgcn_s_setprio(0); WAITV(S::upto(PL - 1)); }
      else if (w == GPOLL) { if (GPOLL) { fv.x = ~(unsigned)tag; fv.y = 0; } wait_flags(fv, c < 8); }
      STAMP(6);
      BAR();
      STAMP(7);
      if (!RB && GPOLL && w == GPOLL) sfor<0, S::upto(PL - 1)>(req);
      tk(1);
      u32x4 wc[5];
      sfor<0, NO + 6>([&](auto ST) {
        constexpr int st = ST.value, s = st - NO;
        if constexpr (st == 2 && !RB) {
          asm volatile("s_waitcnt vmcnt(%8)" : "+v"(tl[0]), "+v"(tl[1]), "+v"(tl[2]), "+v"(tl[3]), "+v"(th[0]), "+v"(th[1]), "+v"(th[2]), "+v"(th[3])
                       : "i"(S::issued(st, 0) - 16) : "memory");
#pragma unroll
          for (int m = 0; m < 4; ++m) at[m] = cat(tl[m], th[m]);
          asm volatile("s_nop 1" : "+v"(at[0]), "+v"(at[1]), "+v"(at[2]), "+v"(at[3]));
        }
        sfor<0, 20>([&](auto IT) {
          constexpr int i = IT.value, m = i / 5, n = i % 5, is = S::issued(st, i);
          if constexpr (WGR == 1 ? n == 0 : i % (5 * WGR) == 0) {
            constexpr int ml = m + WGR - 1;
            if constexpr (WGR == 1) {
              if constexpr (s >= 0)      asm volatile("s_waitcnt vmcnt(%1)" : "+v"(ag[s][m]) : "i"(is - S::gpos(s, m) - 1) : "memory");
              else if constexpr (st < 2) asm volatile("s_waitcnt vmcnt(%1)" : "+v"(af[st][m]) : "i"(is - (4 * st + m) - 1) : "memory");
            } else if constexpr (WGR == 2) {
              if constexpr (s >= 0)      asm volatile("s_waitcnt vmcnt(%2)" : "+v"(ag[s][m]), "+v"(ag[s][ml]) : "i"(is - S::gpos(s, ml) - 1) : "memory");
              else if constexpr (st < 2) asm volatile("s_waitcnt vmcnt(%2)" : "+v"(af[st][m]), "+v"(af[st][ml]) : "i"(is - (4 * st + ml) - 1) : "memory");
            } else {
              if constexpr (s >= 0)      asm volatile("s_waitcnt vmcnt(%4)" : "+v"(ag[s][0]), "+v"(ag[s][1]), "+v"(ag[s][2]), "+v"(ag[s][3]) : "i"(is - S::gpos(s, 3) - 1) : "memory");
              else if constexpr (st < 2) asm volatile("s_waitcnt vmcnt(%4)" : "+v"(af[st][0]), "+v"(af[st][1]), "+v"(af[st][2]), "+v"(af[st][3]) : "i"(is - (4 * st + 3) - 1) : "memory");
            }
          }
          if constexpr (st == 0 && i == 0 && !RB)
            asm volatile("" : "+v"(wo_[2][0]), "+v"(wo_[2][1]), "+v"(wo_[2][2]), "+v"(wo_[2][3]), "+v"(wo_[2][4]),
                              "+v"(wg_[0][0]), "+v"(wg_[0][1]), "+v"(wg_[0][2]), "+v"(wg_[0][3]), "+v"(wg_[0][4]),
                              "+v"(wg_[1][0]), "+v"(wg_[1][1]), "+v"(wg_[1][2]), "+v"(wg_[1][3]), "+v"(wg_[1][4]));
          if constexpr (st == 0)     mul0(acc[m][n], wo_[0][n], af[0][m]);
          else if constexpr (s >= 0) mac(acc[m][n], wg_[s & 1][n], ag[s][m]);
          else if constexpr (st < 2) mac(acc[m][n], wo_[1][n], af[1][m]);
          else                       mac(acc[m][n], wo_[2][n], at[m]);
          SB();
          if constexpr (s == 4 && i == 9) WAITV(is - S::gpos(4, 4) - 1);
          if constexpr (m == (s == 4 ? 2 : 0) && s >= 1 && s < 5) wc[n] = lds128((s > 1 ? ca1 + (s - 2) * 5120 : ca0) + n * 1024);
          if constexpr (S::issued(st, i + 1) > is) req(std::integral_constant<int, is>{});
          SB();
        });
        tk(2 + st);
        if constexpr (s >= 1 && s < 5) {
          asm volatile("" : "+v"(wc[0]), "+v"(wc[1]), "+v"(wc[2]), "+v"(wc[3]), "+v"(wc[4]));
#pragma unroll
          for (int n = 0; n < 5; ++n) wg_[(s + 1) & 1][n] = wc[n];
        }
      });
      asm volatile("s_waitcnt vmcnt(0)" : "+v"(sg0), "+v"(sg1), "+v"(sg2) :: "memory");
    }
    MFMA_DONE();
    STAMP(8);
#pragma unroll
    for (int j = 0; j < 10; ++j) {
      const int ta = j < 4 ? 3 * j : j < 6 ? 6 * j - 22 : 2 * j, tb = j < 4 ? ta + 1 : j < 6 ? ta + 3 : ta + 1;
      *(LAS u32x4*)(ca1 + j * 1024) = cat(pk4(acc[ta / 5][ta % 5]), pk4(acc[tb / 5][tb % 5]));
    }
    tk(12);
  };
  if (w < 4) role(std::false_type{}); else role(std::true_type{});

  if (b == 0 && w == 2) {
    GAS u64* dp = (GAS u64*)((char*)ws_ + WS_DONE + lane * 8);
#pragma unroll 1
    for (unsigned spin = 0; spin < (1u << 18); ++spin) {
      bool ok = true;
#pragma unroll
      for (int j = 0; j < 4; ++j) ok &= __hip_atomic_load(dp + j * 64, RLX) == tag;
      if (__all(ok)) break;
      __builtin_amdgcn_s_sleep(2);
    }
    if (lane == 0) __hip_atomic_store((GAS u64*)((char*)ws_ + WS_EPOCH), tag, RLX);
  }
  sg0 = __builtin_amdgcn_rcpf(1.f + sg0); sg1 = __builtin_amdgcn_rcpf(1.f + sg1); sg2 = __builtin_amdgcn_rcpf(1.f + sg2);
  auto ooff = [&](int tl) {
    const int m = tl / 5, n = tl - 5 * m;
    return (unsigned)((64 * rb + 32 * (m >> 1) + 2 * c + (m & 1)) * 1280 + ct * 160 + n * 32 + h * 8);
  };
  unsigned oo0 = ooff(t0), oo1 = ooff(t0 + 1), oo2 = ooff(t0 + 2);
  asm volatile("" : "+v"(sg0), "+v"(sg1), "+v"(sg2), "+v"(oo0), "+v"(oo1), "+v"(oo2));
  WAITL();
  BAR();
  STAMP(9);
  {
    u32x4 p[8];
    u32x2 p2[8];
    unsigned ra = (w < 4 ? w : w + 2) * 1024 + lane * 16, rb_ = ra + 4 * 15360;
    unsigned rc = (4 + (w >> 1)) * 1024 + (w & 1) * 8 + lane * 16, rd = rc + 4 * 15360;
    asm volatile("" : "+v"(ra), "+v"(rb_), "+v"(rc), "+v"(rd));
#pragma unroll
    for (int v = 0; v < 8; ++v) p[v] = lds128((v < 4 ? ra : rb_) + (v & 3) * 15360);
    if (w < 4) {
#pragma unroll
      for (int v = 0; v < 8; ++v) { p2[v] = *(const LAS u32x2*)((v < 4 ? rc : rd) + (v & 3) * 15360); SB(); }
    }
    SB();
#pragma unroll
    for (int j = 0; j < 3; ++j) {
      if (j < 2 || w < 4) {
        f32x4 s = {0.f, 0.f, 0.f, 0.f};
#pragma unroll
        for (int v = 0; v < 8; ++v) {
          const unsigned px = j == 0 ? p[v].x : j == 1 ? p[v].z : p2[v].x, py = j == 0 ? p[v].y : j == 1 ? p[v].w : p2[v].y;
          const f32x4 q = {u2f(px << 16), u2f(px & 0xffff0000u), u2f(py << 16), u2f(py & 0xffff0000u)};
          s = v ? s + q : q;
        }
        const u32x2 ov = pk4(s * (j == 0 ? sg0 : j == 1 ? sg1 : sg2));
        asm volatile("global_store_dwordx2 %0, %1, %2" OUTFL :: "v"(j == 0 ? oo0 : j == 1 ? oo1 : oo2), "v"(ov), "s"(out_) : "memory");
      }
    }
  }
  STAMP(10);
  if (AMP0 >= 0 && (AMPSEL == 0 || (AMPSEL == 1) == (idx < 16))) {
    const u64 d = amp_t1 - amp_t0, t = __builtin_amdgcn_s_memrealtime();
    while (__builtin_amdgcn_s_memrealtime() - t < d) __builtin_amdgcn_s_sleep(1);
  }
}
}

extern "C" void kernel_launch(void* const* d_in, const int* in_sizes, int n_in, void* d_out, int out_size, void* d_ws,
                              size_t ws_size, hipStream_t stream) {
  fused<<<256, 512, 0, stream>>>(d_in[0], d_in[1], d_in[2], d_in[4], d_in[5], d_in[6], d_in[7], d_out, d_ws);
}
```

```cpp
#include <hip/hip_runtime.h>
#include <hip/hip_bf16.h>
#include <stdint.h>
#include <type_traits>

#pragma clang diagnostic ignored "-Wint-to-pointer-cast"

#define LAS __attribute__((address_space(3)))
#define GAS __attribute__((address_space(1)))
typedef __bf16 bf16x8 __attribute__((ext_vector_type(8)));
typedef __bf16 bf16x2 __attribute__((ext_vector_type(2)));
typedef float f32x4 __attribute__((ext_vector_type(4)));
typedef float f32x2 __attribute__((ext_vector_type(2)));
typedef unsigned u32x4 __attribute__((ext_vector_type(4)));
typedef unsigned u32x2 __attribute__((ext_vector_type(2)));
typedef short s16x4 __attribute__((ext_vector_type(4)));
typedef unsigned long long u64;

namespace {
constexpr int LDS_BYTES = 163840;
constexpr unsigned GATE_OFF = 110592, PACK_OFF = 122880, PKF_OFF = 162816, CELL0 = 122880;
constexpr unsigned OOB = 0x40000000u;
constexpr unsigned CODE_BYTES = 40000;
constexpr int PL = 2;
constexpr unsigned WS_FLAG = 0;
constexpr unsigned WS_DONE = 4096;
constexpr unsigned WS_PKF = 8192;
constexpr unsigned WS_EPOCH = 12288;
constexpr unsigned WS_G = 16384;
constexpr unsigned WS_O = 131072;
constexpr unsigned WS_GU = WS_O + 128 * 40 * 512;
constexpr unsigned WS_PW = WS_GU + 128 * 48 * 1024;
constexpr unsigned WS_END = WS_PW + 8 * 68 * 5120;
constexpr int AUX_SC1 = 16;
constexpr float LOG2E = 1.4426950408889634f;

__device__ __forceinline__ u32x4 mk_srd(u64 a, unsigned bytes) {
  u32x4 r; r.x = (unsigned)a; r.y = (unsigned)(a >> 32) & 0xffffu; r.z = bytes; r.w = 0x00020000u; return r;
}
__device__ __forceinline__ void dma16(unsigned lds, unsigned imm, unsigned voff, u32x4 srd, unsigned soff) {
  asm volatile("s_add_u32 m0, %0, %4\n\ts_nop 0\n\tbuffer_load_dwordx4 %1, %2, %3 offen lds"
               :: "s"(lds), "v"(voff), "s"(srd), "s"(soff), "i"(imm) : "memory", "scc");
}
__device__ __forceinline__ void dma16c(unsigned lds, unsigned imm, unsigned voff, u32x4 srd, unsigned soff) {
  asm volatile("s_add_u32 m0, %0, %4\n\ts_nop 0\n\tbuffer_load_dwordx4 %1, %2, %3 offen sc1 lds"
               :: "s"(lds), "v"(voff), "s"(srd), "s"(soff), "i"(imm) : "memory", "scc");
}
__device__ __forceinline__ void ld128(u32x4& d, unsigned voff, u32x4 srd, unsigned soff) {
  asm volatile("buffer_load_dwordx4 %0, %1, %2, %3 offen sc1" : "=v"(d) : "v"(voff), "s"(srd), "s"(soff) : "memory");
}
__device__ __forceinline__ void ld64(u32x2& d, unsigned voff, u32x4 srd, unsigned soff) {
  asm volatile("buffer_load_dwordx2 %0, %1, %2, %3 offen sc1" : "=v"(d) : "v"(voff), "s"(srd), "s"(soff) : "memory");
}
__device__ __forceinline__ void ld32(float& d, unsigned voff, u32x4 srd, unsigned soff) {
  asm volatile("buffer_load_dword %0, %1, %2, %3 offen sc1" : "=v"(d) : "v"(voff), "s"(srd), "s"(soff) : "memory");
}
__device__ __forceinline__ u32x4 lds128(unsigned a) { return *(const LAS u32x4*)a; }
__device__ __forceinline__ u32x2 ldstr(unsigned a) {
  return __builtin_bit_cast(u32x2, __builtin_amdgcn_ds_read_tr16_b64_v4i16((LAS s16x4*)a));
}
__device__ __forceinline__ u32x4 cat(u32x2 a, u32x2 b) { u32x4 r; r.x = a.x; r.y = a.y; r.z = b.x; r.w = b.y; return r; }
__device__ __forceinline__ f32x4 mfma(u32x4 a, u32x4 b, f32x4 c) {
  return __builtin_amdgcn_mfma_f32_16x16x32_bf16(__builtin_bit_cast(bf16x8, a), __builtin_bit_cast(bf16x8, b), c, 0, 0, 0);
}
__device__ __forceinline__ void mac(f32x4& c, u32x4 a, u32x4 b) {
  asm volatile("v_mfma_f32_16x16x32_bf16 %0, %1, %2, %0" : "+v"(c) : "v"(a), "v"(b));
}
__device__ __forceinline__ void mul0(f32x4& c, u32x4 a, u32x4 b) {
  asm volatile("v_mfma_f32_16x16x32_bf16 %0, %1, %2, 0" : "=&v"(c) : "v"(a), "v"(b));
}
__device__ __forceinline__ unsigned pk2(float a, float b) {
  f32x2 t = {a, b};
  return __builtin_bit_cast(unsigned, __builtin_convertvector(t, bf16x2));
}
__device__ __forceinline__ u32x2 pk4(f32x4 v) { u32x2 r; r.x = pk2(v.x, v.y); r.y = pk2(v.z, v.w); return r; }
__device__ __forceinline__ float u2f(unsigned u) { return __builtin_bit_cast(float, u); }
__device__ __forceinline__ unsigned f2u(float f) { return __builtin_bit_cast(unsigned, f); }
#define SB() __builtin_amdgcn_sched_barrier(0)
#define WAITV(n) asm volatile("s_waitcnt vmcnt(%0)" :: "i"(n) : "memory")
#define WAITVL(n) asm volatile("s_waitcnt vmcnt(%0) lgkmcnt(0)" :: "i"(n) : "memory")
#define WAITL() asm volatile("s_waitcnt lgkmcnt(0)" ::: "memory")
#define BAR() asm volatile("s_barrier" ::: "memory")
#define STPAD() do { SB(); asm volatile("s_nop 1" ::: "memory"); SB(); } while (0)
#define MFMA_DONE() do { SB(); asm volatile("s_nop 7\n\ts_nop 1" ::: "memory"); SB(); } while (0)
#define RLX __ATOMIC_RELAXED, __HIP_MEMORY_SCOPE_AGENT
__device__ __forceinline__ unsigned rfl(unsigned v) { return __builtin_amdgcn_readfirstlane(v); }
__device__ __forceinline__ unsigned opaque_lane() { unsigned l = threadIdx.x & 63; asm volatile("" : "+v"(l)); return l; }

template <int I, int N, class F>
__device__ __forceinline__ void sfor(F&& f) {
  if constexpr (I < N) { f(std::integral_constant<int, I>{}); sfor<I + 1, N>(f); }
}
template <bool RB> struct Sched {
  static constexpr int NO = RB ? 2 : 3, OB = RB ? 8 : 16;
  static constexpr int gpos(int s, int m) { return OB + 5 * s + m; }
  static constexpr int upto(int st) { return st < 2 ? 4 * (st + 1) : st < NO ? 16 : st < NO + 5 ? OB + 5 * (st - NO + 1) : st == NO + 5 ? OB + 29 : OB + 32; }
  static constexpr int issued(int st, int i) {
    const int base = upto(st + PL - 1), goal = upto(st + PL), every = goal - base > 5 ? 2 : goal - base > 4 ? 4 : 5;
    int n = base;
    for (int k = 0; k < i; ++k) if (k % every == 1 && n < goal) ++n;
    return n;
  }
};
__device__ __forceinline__ float max3(float a, float b, float c) { float r; asm("v_max3_f32 %0, %1, %2, %3" : "=v"(r) : "v"(a), "v"(b), "v"(c)); return r; }
__device__ __forceinline__ float max2(float a, float b) { float r; asm("v_max_f32 %0, %1, %2" : "=v"(r) : "v"(a), "v"(b)); return r; }

template <bool QKV, bool RB>
struct P1 {
  static constexpr int NM = QKV ? 5 : 4;
  static constexpr int NN = QKV ? 4 : (RB ? 4 : 8);
  static constexpr int NWS = 3;
  static constexpr unsigned WSL = QKV ? 16384 : 24576, XB = NWS * WSL, XSL = QKV ? 20480 : 16384;
  static constexpr unsigned WROW = QKV ? 3840 : 3072;
  static constexpr int NWP = QKV ? 2 : (RB ? 4 : 2);
  static constexpr int NXE = QKV ? (RB ? 1 : 2) : (RB ? 2 : 0), NXO = QKV ? 1 : NXE;
  static constexpr int NXP = NXO + NXE;
  static constexpr int OO = NWP + NXO, OE = NWP + NXE;
  static constexpr bool HAND = !QKV;
  static constexpr int ht = 4;
  f32x4 acc[NM][NN];
  u32x4 srdx, srdw;
  unsigned xv[4], wv[4];
  unsigned xd;
  unsigned wd0, wd1;
  unsigned xa0, wa0, wa1, wa2;
  unsigned gmask;
  unsigned sow, sox, gof;
  unsigned kg, kw, kx, kr;
  template <bool ODD, bool X = true>
  __device__ __forceinline__ void loads(unsigned wwr, unsigned xwr) {
    if (X && NXP) {
#pragma unroll
      for (int i = ODD ? NXE : 0; i < (ODD ? NXP : NXE); ++i) dma16(xwr + xd, i * 1024, xv[i], srdx, sox);
      if (ODD) { kx += 1; sox = (kx ^ (kg >> 1)) * 128; }
    }
    dma16(wwr + wd0, 0, wv[0], srdw, sow);
    dma16(wwr + wd0, 1024, wv[1], srdw, sow);
    if (!QKV && RB) {
      dma16(wwr + wd0, 2048, wv[2], srdw, sow);
      dma16(wwr + wd1, 0, wv[3], srdw, sow);
    }
  }
  __device__ __forceinline__ void other(unsigned wwr) {
    dma16(wwr + wd1, 1024, wv[3], srdw, sow + 128);
    dma16(wwr + wd1, 2048, wv[3], srdw, sow + 256);
  }
  __device__ __forceinline__ void next() { kw += 1; sow = (kw ^ kg) * (32 * WROW); asm volatile("" : "+s"(sow), "+s"(kw)); if (NXP) asm volatile("" : "+s"(sox), "+s"(kx)); }
  __device__ __forceinline__ void stage0(int rot) {
    kg = rot; kw = 0; kx = 0; kr = 0; sow = kg * (32 * WROW); sox = (kg >> 1) * 128; gof = kg * 256;
    if (HAND && !RB) { kw = 3; sow = (3 ^ kg) * (32 * WROW); return; }
#pragma unroll
    for (int i = 0; i < NXE; ++i) dma16(xd, i * 1024, xv[i], srdx, sox);
    loads<true>(0, 0); if (HAND) other(0); next();
  }
  __device__ __forceinline__ void stage1() { if (HAND && !RB) return; loads<false>(WSL, XSL); if (HAND) other(WSL); next(); }
  __device__ __forceinline__ void stage2() { if (HAND && !RB) return; loads<true>(2 * WSL, XSL); if (HAND) other(2 * WSL); next(); }
  static constexpr int VPRO = OE + OO + (HAND ? 4 : 0);
  struct Ad { unsigned x, w[4]; };
  __device__ __forceinline__ void addr(Ad& a, unsigned xb, unsigned wrd) {
    a.x = xb; a.w[0] = wa0 + wrd;
    if (QKV) {
      const unsigned s2 = (gmask & (gof - wrd)) + wrd;
      kr += 1; gof = (kr ^ kg) * 256;
      a.w[1] = wa1 + s2; a.w[2] = wa2 + s2;
      asm volatile("" : "+v"(a.x), "+v"(a.w[0]), "+v"(a.w[1]), "+v"(a.w[2]));
    } else if (RB) {
      a.w[1] = a.w[0] ^ 32;
      asm volatile("" : "+v"(a.x), "+v"(a.w[0]), "+v"(a.w[1]));
    } else {
      a.w[1] = a.w[0] ^ 32; a.w[2] = a.w[0] ^ 64; a.w[3] = a.w[0] ^ 96;
      asm volatile("" : "+v"(a.x), "+v"(a.w[0]), "+v"(a.w[1]), "+v"(a.w[2]), "+v"(a.w[3]));
    }
  }
  __device__ __forceinline__ unsigned wad(const Ad& a, int n, int hi) {
    if (QKV) return n == 2 ? a.w[1 + hi] : a.w[0] + (n == 0 ? 0 : n == 1 ? 160 : 320) + hi * 1920;
    if (RB) return a.w[n & 1] + (n >> 1) * 3072 + hi * 512;
    return a.w[n & 3] + (n >> 2) * 3072 + hi * 512;
  }
  __device__ __forceinline__ void rd_all(u32x4 (&xf)[NM], u32x4 (&wf)[NN], const Ad& a) {
#pragma unroll
    for (int n = 0; n < NN; ++n) wf[n] = cat(ldstr(wad(a, n, 0)), ldstr(wad(a, n, 1)));
#pragma unroll
    for (int m = 0; m < NM; ++m) xf[m] = lds128(a.x + m * 2048);
  }
  template <bool RD = true, class L>
  __device__ __forceinline__ void mm_rd(const u32x4 (&xc)[NM], const u32x4 (&wc)[NN], u32x4 (&xn)[NM], u32x4 (&wn)[NN], const Ad& a, L&& late) {
    u32x2 lo[NN];
    constexpr bool NMAJ = !QKV && !RB;
    int r = 0;
#pragma unroll
    for (int i = 0; i < NM * NN; ++i) {
      const int m = NMAJ ? i % NM : i / NN, n = NMAJ ? i / NM : i % NN;
      if (QKV && m == 0 && n == 0) continue;
      if (QKV && n == 3) mac(acc[m][3], xc[m], wc[3]);
      else               mac(acc[m][n], wc[n], xc[m]);
      SB();
      if (!RD) continue;
      if (NMAJ) {
        if (i < 4) xn[i] = lds128(a.x + i * 2048);
        else if ((i & 3) < 2) {
          const int k = (i >> 2) - 1;
          if (i & 1) wn[k] = cat(lo[k], ldstr(wad(a, k, 1))); else lo[k] = ldstr(wad(a, k, 0));
        } else late(i);
      } else {
        if (r < 2 * NN) {
          const int k = r >> 1;
          if (r & 1) wn[k] = cat(lo[k], ldstr(wad(a, k, 1))); else lo[k] = ldstr(wad(a, k, 0));
        } else if (r < 2 * NN + NM) {
          xn[r - 2 * NN] = lds128(a.x + (r - 2 * NN) * 2048);
        }
        else late(r - 2 * NN - NM);
        ++r;
      }
      SB();
    }
    if (NMAJ && RD) { wn[NN - 1] = cat(ldstr(wad(a, NN - 1, 0)), ldstr(wad(a, NN - 1, 1))); SB(); }
  }
  __device__ __forceinline__ void pin(u32x4 (&xf)[NM], u32x4 (&wf)[NN]) {
    asm volatile("" : "+v"(xf[0]), "+v"(xf[1]), "+v"(xf[2]), "+v"(xf[3]), "+v"(wf[0]), "+v"(wf[1]), "+v"(wf[2]), "+v"(wf[3]));
    if (NM == 5) asm volatile("" : "+v"(xf[NM - 1]));
    if (NN == 8) asm volatile("" : "+v"(wf[NN - 4]), "+v"(wf[NN - 3]), "+v"(wf[NN - 2]), "+v"(wf[NN - 1]));
  }
  __device__ __forceinline__ void zero() {
#pragma unroll
    for (int m = 0; m < NM; ++m)
#pragma unroll
      for (int n = 0; n < NN; ++n) { acc[m][n] = f32x4{0.f, 0.f, 0.f, 0.f}; asm volatile("" : "+v"(acc[m][n])); }
  }
  template <int TL0, int TL1, class F, class G, class H, class U>
  __device__ __forceinline__ void run(F&& pre, G&& extra, H&& tail, U&& gap) {
    pre();
    u32x4 xA[NM], wA[NN], xB[NM], wB[NN];
    Ad a;
    addr(a, xa0, 0);
    rd_all(xA, wA, a);
    if (!RB) __builtin_amdgcn_s_setprio(1);
    unsigned w0 = 0, w1 = WSL, w2 = 2 * WSL;
    unsigned xp = 2 * XSL, x0 = 0, x1 = XSL;
    int t = 0;
    unsigned nw1, nw2, nx1;
    auto adv0 = [&]() { nw1 = w2 + WSL; if (nw1 == NWS * WSL) nw1 = 0; asm volatile("" : "+s"(nw1)); };
    auto adv1 = [&]() { nw2 = nw1 + WSL; if (nw2 == NWS * WSL) nw2 = 0; asm volatile("" : "+s"(nw2)); };
    auto adv2 = [&]() { nx1 = x1 + XSL; if (nx1 == 3 * XSL) nx1 = 0; asm volatile("" : "+s"(nx1)); };
    auto adv3 = [&]() {
      w0 = w2; w1 = nw1; w2 = nw2; xp = x0; x0 = x1; x1 = nx1; t += 2;
      asm volatile("" : "+s"(w0), "+s"(w1), "+s"(w2), "+s"(xp), "+s"(x0), "+s"(x1), "+s"(t));
    };
    auto adv = [&]() { adv0(); adv1(); adv2(); adv3(); };
    asm volatile("" : "+s"(w0), "+s"(w1), "+s"(w2), "+s"(xp), "+s"(x0), "+s"(x1), "+s"(t));
    if (QKV) asm volatile("" : "+s"(gof), "+s"(kr));
    auto none = [](int) {};
    if (RB) {
      auto help = [&](int tt, unsigned wwr) { if (HAND && __builtin_expect(tt < ht, 0)) other(wwr); extra(tt); };
      pin(xA, wA);
      SB(); WAITVL(OO + (HAND ? 2 : 0)); BAR(); SB();
      loads<false>(w0, xp); help(0, w0); next(); addr(a, (xa0 ^ 64) + x0, w1); SB();
      auto half = [&]() {
        mm_rd(xA, wA, xB, wB, a, [&](int i) {
          if (QKV && i == 0) adv0();
          if (QKV && i == 2) adv1();
          if (QKV && i == 4) adv2();
        });
        pin(xB, wB);
        SB(); WAITVL(OE); BAR(); SB();
        loads<true>(w1, xp); help(t + 1, w1); next();
        addr(a, xa0 + x1, w2);
        if (QKV) adv3(); else adv();
        SB();
        mm_rd(xB, wB, xA, wA, a, none);
      };
#pragma unroll 1
      do {
        half();
        pin(xA, wA);
        SB(); WAITVL(OO); BAR(); SB();
        loads<false>(w0, xp); help(t, w0); next(); addr(a, (xa0 ^ 64) + x0, w1); SB();
        if (QKV) asm volatile("" : "+s"(gof), "+s"(kr));
      } while (t < 14);
      half();
    } else {
      addr(a, (xa0 ^ 64) + x0, w1);
      auto trip = [&](auto EARLY) {
        constexpr bool E = EARLY.value;
        pin(xA, wA);
        SB(); if (E) WAITL(); else WAITVL(OO);
        BAR(); SB();
        mm_rd(xA, wA, xB, wB, a, [&](int i) { if (E) gap(i); });
        if (E) { if (t == 0) WAITV(0); else extra(t); }
        if (!E || t >= ht) loads<false>(w0, xp);
        next(); addr(a, xa0 + x1, w2); adv0(); adv1(); adv2(); SB();
        pin(xB, wB);
        SB(); if (E) WAITL(); else WAITVL(OE);
        BAR(); SB();
        mm_rd(xB, wB, xA, wA, a, [&](int i) { if (E) gap(i); });
        if (E && t < ht) extra(t + 1); else loads<true>(w1, xp);
        next(); adv3(); addr(a, (xa0 ^ 64) + x0, w1); SB();
        if (QKV) asm volatile("" : "+s"(gof), "+s"(kr));
      };
      if (HAND) {
#pragma unroll 1
        do trip(std::true_type{}); while (t < ht + 2);
      }
#pragma unroll 1
      do trip(std::false_type{}); while (t < 16);
    }
    constexpr unsigned S16 = (16 % NWS) * WSL, S17 = (17 % NWS) * WSL, S18 = (18 % NWS) * WSL, S19 = (19 % NWS) * WSL, X8 = 2 * XSL, X9 = 0;
    pin(xA, wA);
    SB(); WAITVL(OO);
    BAR(); SB();
    if (RB) { loads<false, false>(S16, 0); tail(0); addr(a, (xa0 ^ 64) + X8, S17); SB(); mm_rd(xA, wA, xB, wB, a, none); }
    else    { mm_rd(xA, wA, xB, wB, a, none); loads<false, false>(S16, 0); tail(0); addr(a, xa0 + X9, S18); SB(); }
    pin(xB, wB);
    SB(); WAITVL(NWP + TL0); BAR(); SB();
    if (RB) { tail(1); addr(a, xa0 + X9, S18); SB(); mm_rd(xB, wB, xA, wA, a, none); }
    else    { mm_rd(xB, wB, xA, wA, a, none); tail(1); addr(a, (xa0 ^ 64) + X9, S19); SB(); }
    pin(xA, wA);
    SB(); WAITVL(TL0 + TL1); BAR(); SB();
    if (RB) { tail(2); addr(a, (xa0 ^ 64) + X9, S19); SB(); mm_rd(xA, wA, xB, wB, a, none); }
    else    { mm_rd(xA, wA, xB, wB, a, none); tail(2); SB(); }
    pin(xB, wB);
    SB(); WAITL(); BAR(); SB();
    if (RB) { tail(3); SB(); mm_rd<false>(xB, wB, xA, wA, a, none); }
    else    { mm_rd<false>(xB, wB, xA, wA, a, none); tail(3); SB(); }
    if (!RB) __builtin_amdgcn_s_setprio(0);
    MFMA_DONE();
  }
};

__global__ __launch_bounds__(512, 2) void fused(const void* x_, const void* wqkv_, const void* wo_p, const void* wg_p,
                                                const void* wu_p, const void* wd_p, const void* gw_, void* out_, void* ws_) {
  __shared__ __attribute__((aligned(16))) char smem[LDS_BYTES];
  asm volatile("" :: "s"((unsigned)(size_t)(LAS char*)smem));
  const int tid = threadIdx.x, lane = tid & 63;
  const int w = rfl(tid >> 6);
  const int h = lane >> 4, c = lane & 15, q4 = (lane & 15) >> 2, p4 = lane & 3;
  const int b = blockIdx.x, xcd = b & 7, idx = b >> 3;
  const int rh = xcd & 1, cq = xcd >> 1;
  const int a = 8 * rh + (idx & 7);
  const int gc = 2 * cq + ((idx >> 3) & 1);
  const int rb = 4 * xcd + (idx >> 3), ct = idx & 7;
  u64 x, wqkv, wo, wg, wu, wd, gw;
  char* ws;
  __amdgpu_buffer_rsrc_t rws;
  u32x4 sws;
  auto bind = [&]() {
    x = (u64)x_; wqkv = (u64)wqkv_; wo = (u64)wo_p; wg = (u64)wg_p; wu = (u64)wu_p; wd = (u64)wd_p; gw = (u64)gw_;
    u64 pw = (u64)ws_;
    asm volatile("" : "+s"(x), "+s"(wqkv), "+s"(wo), "+s"(wg), "+s"(wu), "+s"(wd), "+s"(gw), "+s"(pw));
    ws = (char*)pw;
    rws = __builtin_amdgcn_make_buffer_rsrc(ws, 0, WS_END, 0x00020000);
    sws = mk_srd(pw, WS_END);
  };

  {
    u64 pc;
    asm volatile("s_getpc_b64 %0" : "=s"(pc));
    asm volatile("s_mov_b32 m0, %0\n\ts_nop 0\n\tbuffer_load_dword %1, %2, 0 offen lds"
                 :: "s"(PKF_OFF), "v"(lane < 6 ? ((idx & 7) * 48 + w * 6 + lane) * 128 : OOB), "s"(mk_srd(pc & ~127ull, CODE_BYTES)) : "memory");
  }
  u32x2 ev;
  u64 tag = 0;
  auto make_tag = [&](auto NV) {
    unsigned lo, hi;
    asm volatile("s_waitcnt vmcnt(%4)\n\tv_readfirstlane_b32 %0, %2\n\tv_readfirstlane_b32 %1, %3"
                 : "=s"(lo), "=s"(hi) : "v"(ev.x), "v"(ev.y), "i"(NV.value) : "memory");
    const u64 e = ((u64)hi << 32) | lo;
    tag = (e ^ 0x9E3779B97F4A7C15ull) * 0xBF58476D1CE4E5B9ull + 0x632BE59BD9B4E019ull;
    if (tag == e) tag += 1;
  };
  const unsigned pwb = WS_PW + ct * (68 * 5120);
  const int grot = 3 * ((idx >> 3) & 1);
  auto gstep = [&](int s) { const int v = s + grot; return v >= 6 ? v - 6 : v; };
  auto cell_dma = [&](int v, int cell) {
    const unsigned so = pwb + (20 + 6 * v + gstep(2 + cell)) * 5120;
#pragma unroll
    for (int j = 0; j < 5; ++j) dma16c(cell ? v * 15360 : CELL0 + v * 5120, (cell ? cell - 1 : 0) * 5120 + j * 1024, lane * 16, sws, so + j * 1024);
  };
  auto ldw = [&](u32x4 (&d)[5], unsigned so) {
#pragma unroll
    for (int n = 0; n < 5; ++n) d[n] = __builtin_amdgcn_raw_buffer_load_b128(rws, lane * 16, so + n * 1024, AUX_SC1);
  };
  const unsigned pscr = PACK_OFF + (w & 3) * 8192;
  const int pu0 = (b - 128) * 4 + w;
  unsigned pra[2], prb[2], pn[2], pb[2];
  auto pack_issue = [&]() {
    const u64 po = wo, pd = wd;
#pragma unroll
    for (int r = 0; r < 2; ++r) {
      const int u = pu0 + 512 * r;
      const int ks = u / 10, cg = u - 10 * ks;
      const unsigned r8 = lane >> 3;
      const u32x4 srd = mk_srd(ks < 20 ? po : pd, ks < 20 ? 640 * 1280 : 1536 * 1280);
      const unsigned base = ks < 16 ? 40 * ks : ks < 20 ? 160 * (ks - 16) + 16 : 32 * (ks - 20);
      const unsigned vr = (ks >= 16 && ks < 20) ? 20 * (r8 >> 2) + (r8 & 3) : r8;
      const unsigned vo = vr * 1280 + (lane & 7) * 16;
#pragma unroll
      for (int p = 0; p < 4; ++p) {
        const unsigned rp = ks < 16 ? 20 * (p >> 1) + 8 * (p & 1) : ks < 20 ? 40 * p : 8 * p;
        dma16(pscr, r * 4096 + p * 1024, vo, srd, u < 680 ? (base + rp) * 1280 + cg * 128 : OOB);
      }
      pra[r] = pscr + r * 4096 + (h * (ks < 16 ? 8 : 4) + q4) * 128 + p4 * 8 - 32;
      prb[r] = pra[r] + (ks < 16 ? 512 : 2048);
      pn[r] = 4 * cg - 1; pb[r] = u < 680 ? WS_PW + ks * 5120 + (4 * cg - 1) * 1024 : OOB;
    }
    asm volatile("" : "+v"(pra[0]), "+v"(prb[0]), "+v"(pra[1]), "+v"(prb[1]));
    asm volatile("" : "+s"(pn[0]), "+s"(pb[0]), "+s"(pn[1]), "+s"(pb[1]));
  };
  const unsigned smask = w != 4 ? 0u : idx >= 16 ? 0x1040u : 0x1000u;
  auto side_b = [&](int t) {
    if (__builtin_expect(!((smask >> t) & 1), 1)) return;
    if (t == 6) {
      u32x2 tv; tv.x = (unsigned)tag; tv.y = (unsigned)(tag >> 32);
      __builtin_amdgcn_raw_buffer_store_b64(tv, rws, opaque_lane() == 0 ? WS_PKF + (b - 128) * 8 : OOB, 0, AUX_SC1);
    } else {
      dma16c(PKF_OFF, 0, opaque_lane() * 16, sws, WS_PKF);
    }
  };
  u32x2 pq[4];
  unsigned ps0, ps1;
  auto pack_gap = [&](int i) {
    if (i == 6)  pq[0] = ldstr(pra[0]);
    if (i == 7)  pq[1] = ldstr(prb[0]);
    if (i == 10) pq[2] = ldstr(pra[1]);
    if (i == 11) pq[3] = ldstr(prb[1]);
    if (i == 14) { pra[0] += 32; prb[0] += 32; asm volatile("" : "+v"(pra[0]), "+v"(prb[0])); }
    if (i == 15) { pra[1] += 32; prb[1] += 32; asm volatile("" : "+v"(pra[1]), "+v"(prb[1])); }
    if (i == 18) { ps0 = (pn[0] * 205) >> 10; asm volatile("" : "+s"(ps0)); }
    if (i == 19) { ps0 = ps0 * (68 * 5120 - 5120) + pb[0]; asm volatile("" : "+s"(ps0)); }
    if (i == 22) { ps1 = (pn[1] * 205) >> 10; asm volatile("" : "+s"(ps1)); }
    if (i == 23) { ps1 = ps1 * (68 * 5120 - 5120) + pb[1]; asm volatile("" : "+s"(ps1)); }
    if (i == 26) { pn[0] += 1; pb[0] += 1024; asm volatile("" : "+s"(pn[0]), "+s"(pb[0])); }
    if (i == 27) { pn[1] += 1; pb[1] += 1024; asm volatile("" : "+s"(pn[1]), "+s"(pb[1])); }
  };
  auto side_f = [&](int) {
    __builtin_amdgcn_raw_buffer_store_b128(cat(pq[0], pq[1]), rws, lane * 16, ps0, AUX_SC1);
    STPAD();
    __builtin_amdgcn_raw_buffer_store_b128(cat(pq[2], pq[3]), rws, lane * 16, ps1, AUX_SC1);
    STPAD();
  };
  auto pk_check = [&]() {
    const u32x4 p0 = lds128(PKF_OFF + lane * 16);
    const unsigned lo = (unsigned)tag, hi = (unsigned)(tag >> 32);
    if (!__all(p0.x == lo && p0.z == lo && p0.y == hi && p0.w == hi)) {
      GAS u64* fp = (GAS u64*)((char*)ws + WS_PKF + opaque_lane() * 8);
#pragma unroll 1
      for (unsigned spin = 0; spin < (1u << 18); ++spin) {
        bool ok = true;
#pragma unroll
        for (int j = 0; j < 2; ++j) ok &= __hip_atomic_load(fp + 64 * j, RLX) == tag;
        if (__all(ok)) break;
        __builtin_amdgcn_s_sleep(2);
      }
    }
    asm volatile("" ::: "memory");
  };
  const int rot = (a + 2 * ((idx >> 3) & 1) + (idx >> 4)) & 3;

  const int t0 = 2 * w + min(w, 4);
  float sg0, sg1, sg2;
  auto role = [&](auto RBT) {
    constexpr bool RB = decltype(RBT)::value;
    u32x4 wo_[3][5], wg_[2][5];
    auto tail = [&](int i) {
      if (i == 0) pk_check();
      if (RB) {
        if (i < 2) ldw(wo_[i], pwb + (2 * w + i) * 5120);
        else ldw(wg_[i - 2], pwb + (20 + 6 * w + gstep(i - 2)) * 5120);
        if (i == 1) cell_dma(w - 4, 0);
        if (i == 2) cell_dma(w, 0);
      } else if (i >= 2) {
        ldw(wo_[i - 2], pwb + (2 * w + i - 2) * 5120);
      }
    };
    auto late_w = [&]() {
#pragma unroll
      for (int n = 0; n < 5; ++n) ld128(wo_[2][n], lane * 16, sws, pwb + (16 + w) * 5120 + n * 1024);
#pragma unroll
      for (int j = 0; j < 10; ++j) ld128(wg_[j / 5][j % 5], lane * 16, sws, pwb + (20 + 6 * w + gstep(j / 5)) * 5120 + (j % 5) * 1024);
    };
    if (idx < 16) {
      const int g = gc, hd = w & 3;
      constexpr int par = RB ? 1 : 0;
      using K = P1<true, RB>;
      K k;
      const int q0 = RB ? 12 + 2 * (w - 4) : 3 * w;
      k.xd = K::XB + q0 * 1024; k.wd0 = w * 2048;
      asm volatile("" : "+s"(k.xd), "+s"(k.wd0));
#pragma unroll
      for (int i = 0; i < K::NXP; ++i) {
        const int rho = 8 * (q0 + i) + (lane >> 3);
        const int pp = rho >= 80 ? 1 : 0, u = rho - 80 * pp;
        const int tok = 128 * a - 32 + 2 * u + pp;
        const unsigned v = tok * 1280 + (((lane & 7) ^ ((rho >> 1) & 7)) << 4);
        k.xv[i] = (u >= 9 && tok >= 0) ? v : OOB;
      }
#pragma unroll
      for (int j = 0; j < 2; ++j) {
        const unsigned I = (2 * w + j) * 64 + lane;
        const unsigned kb = (I >= 248) + (I >= 488) + (I >= 736);
        const unsigned i = I - 240 * kb - 8 * ((kb + 1) >> 1);
        const unsigned r = (i * 2185u) >> 16, rem = i - r * 30;
        const unsigned seg = (rem * 6554u) >> 16, ch = rem - seg * 10;
        const unsigned v = (8 * kb + r) * 3840 + seg * 1280 + g * 160 + ch * 16;
        k.wv[j] = i < 240 ? v : OOB;
      }
      asm volatile("" : "+v"(k.xv[0]), "+v"(k.xv[1]), "+v"(k.xv[K::NXP - 1]), "+v"(k.wv[0]), "+v"(k.wv[1]));
      bind();
      k.srdx = mk_srd(x, 2048 * 1280);
      k.srdw = mk_srd(wqkv, 640 * 3840);
      unsigned gv0, gv1;
      asm volatile("global_load_dwordx2 %0, %1, %2 sc1" : "=v"(ev) : "v"(WS_EPOCH), "s"(ws) : "memory");
      asm volatile("buffer_load_ushort %0, %2, %3, 0 offen\n\tbuffer_load_ushort %1, %2, %3, 0 offen offset:1024"
                   : "=&v"(gv0), "=&v"(gv1) : "v"(tid * 2), "s"(mk_srd(gw, 1280)) : "memory");
      k.stage0(rot);
      BAR();
      k.stage1();
      k.xa0 = (K::XB + par * 5 * 2048 + c * 128 + ((h ^ (c >> 1)) << 4)) ^ ((rot & 1) << 6);
      const unsigned kbo = h * 3840 + ((h + 1) >> 1) * 128;
      k.wa0 = kbo + q4 * 480 + (20 * hd + 4 * p4) * 2;
      const bool isgate = p4 == 3;
      k.gmask = isgate ? ~0u : 0u;
      asm volatile("" : "+v"(k.gmask));
      k.wa1 = isgate ? GATE_OFF + (8 * h + q4) * 8 : kbo + q4 * 480 + p4 * 160 + (20 * hd + 16) * 2;
      k.wa2 = k.wa1 + (isgate ? 32 : 1920);
      f32x4 ba, bb, ba1;
#pragma unroll
      for (int r = 0; r < 4; ++r) {
        const int kap = 4 * h + r;
        ba[r] = kap >= c + 9 ? 0.f : -1e30f;
        bb[r] = (kap <= c && kap >= c - 7) ? 0.f : -1e30f;
        ba1[r] = a == 0 ? -1e30f : ba[r];
      }
      unsigned m0mask = h == 0 ? 0xffffffffu : 0u;
      unsigned scw = w * 15360 + 10240 + c * 32 + h * 8, sck = w * 15360 + 10240 + c * 32 + 8,
               sct = w * 15360 + 10240 + (4 * h + q4) * 32 + p4 * 8;
      unsigned ovo = WS_O + (h & 1) * (2 * 20480) + (2 * g + (hd >> 1)) * 1024 + (2 * (hd & 1) + (h >> 1)) * 256 + c * 16;
      unsigned ovt = h == 2 ? WS_O + (32 + g) * 512 + hd * 128 + c * 8 : OOB;
      asm volatile("" : "+v"(ba), "+v"(bb), "+v"(ba1), "+v"(m0mask), "+v"(scw), "+v"(sck), "+v"(sct), "+v"(ovo), "+v"(ovt));
      asm volatile("" : "+v"(k.xa0), "+v"(k.wa0));
      k.zero();
      k.stage2();
      k.template run<RB ? 5 : 0, RB ? 10 : 0>([&]() {
        asm volatile("s_waitcnt vmcnt(%2)" : "+v"(gv0), "+v"(gv1) : "i"(K::VPRO) : "memory");
        u32x2 v; v.x = gv0; v.y = 0;
        *(LAS u32x2*)(GATE_OFF + tid * 8) = v;
        if (!RB && tid < 128) { v.x = gv1; *(LAS u32x2*)(GATE_OFF + 4096 + tid * 8) = v; }
        make_tag(std::integral_constant<int, K::VPRO>{});
        WAITL();
        BAR();
      }, [&](int t) { if (RB) side_b(t); }, tail, [](int) {});
      u32x2 ql[5], kl[5], t2[5], vl[5], tt[5], k4[5];
      unsigned sb16[4];
#pragma unroll
      for (int i = 0; i < 5; ++i) { t2[i] = pk4(k.acc[i][2]); *(LAS u32x2*)(scw + i * 512) = t2[i]; }
#pragma unroll
      for (int i = 0; i < 5; ++i) { if (i) ql[i] = pk4(k.acc[i][0]); kl[i] = pk4(k.acc[i][1]); }
      WAITL();
#pragma unroll
      for (int i = 0; i < 5; ++i) k4[i] = *(const LAS u32x2*)(sck + i * 512);
#pragma unroll
      for (int i = 0; i < 4; ++i) sb16[i] = *(const LAS unsigned*)(sck + (i + 1) * 512 + 16);
#pragma unroll
      for (int i = 0; i < 5; ++i) tt[i] = ldstr(sct + i * 512);
#pragma unroll
      for (int i = 0; i < 5; ++i) vl[i] = pk4(k.acc[i][3]);
      constexpr float SC = 0.22360679774997896f * LOG2E;
      f32x4 sa[4], sb[4];
#pragma unroll
      for (int i = 1; i < 5; ++i) {
        u32x4 qf; qf.x = ql[i].x; qf.y = ql[i].y; qf.z = t2[i].x & m0mask; qf.w = t2[i].y & m0mask;
        sb[i - 1] = mfma(cat(kl[i], k4[i]), qf, bb);
        sa[i - 1] = mfma(cat(kl[i - 1], k4[i - 1]), qf, i == 1 ? ba1 : ba);
      }
      asm volatile("s_nop 7\n\ts_nop 0" : "+v"(sa[0]), "+v"(sa[1]), "+v"(sa[2]), "+v"(sa[3]), "+v"(sb[0]), "+v"(sb[1]), "+v"(sb[2]), "+v"(sb[3]));
      float mx[4], es[4];
#pragma unroll
      for (int i = 0; i < 4; ++i)
        mx[i] = max3(max3(sb[i].x, sb[i].y, sb[i].z), max3(sb[i].w, sa[i].x, sa[i].y), max2(sa[i].z, sa[i].w));
#pragma unroll
      for (int i = 0; i < 4; ++i) {
        auto r = __builtin_amdgcn_permlane16_swap(f2u(mx[i]), f2u(mx[i]), false, false);
        mx[i] = max2(u2f(r[0]), u2f(r[1]));
      }
#pragma unroll
      for (int i = 0; i < 4; ++i) {
        auto r = __builtin_amdgcn_permlane32_swap(f2u(mx[i]), f2u(mx[i]), false, false);
        mx[i] = max2(u2f(r[0]), u2f(r[1])) * SC;
        es[i] = __builtin_amdgcn_exp2f(__builtin_amdgcn_fmed3f(u2f(sb16[i] << 16), -60.f, 60.f) * LOG2E);
      }
      u32x4 pf[4];
#pragma unroll
      for (int i = 0; i < 4; ++i) {
        f32x4 pa, pb;
#pragma unroll
        for (int r = 0; r < 4; ++r) {
          pa[r] = __builtin_amdgcn_exp2f(sa[i][r] * SC - mx[i]);
          pb[r] = __builtin_amdgcn_exp2f(sb[i][r] * SC - mx[i]);
        }
        pf[i] = cat(pk4(pa), pk4(pb));
      }
      const f32x4 z = {0.f, 0.f, 0.f, 0.f};
      const u32x4 ones = {0x3f803f80u, 0x3f803f80u, 0x3f803f80u, 0x3f803f80u};
      f32x4 ls[4], oa[4], ob[4];
#pragma unroll
      for (int i = 0; i < 4; ++i) {
        ls[i] = mfma(ones, pf[i], z);
        oa[i] = mfma(cat(vl[i], vl[i + 1]), pf[i], z);
        ob[i] = mfma(cat(tt[i], tt[i + 1]), pf[i], z);
      }
      const unsigned mt0 = 8 * a + par;
      u32x2 op[4];
#pragma unroll
      for (int i = 0; i < 4; ++i) {
        const float f = es[i] * __builtin_amdgcn_rcpf(ls[i].x);
        op[i] = pk4(oa[i] * f);
        __builtin_amdgcn_raw_buffer_store_b64(pk4(ob[i] * f), rws, ovt, (mt0 + 2 * i) * 20480, AUX_SC1);
        if (i & 1) {
          auto sx = __builtin_amdgcn_permlane16_swap(op[i - 1].x, op[i].x, false, false);
          auto sy = __builtin_amdgcn_permlane16_swap(op[i - 1].y, op[i].y, false, false);
          u32x4 o; o.x = sx[0]; o.y = sy[0]; o.z = sx[1]; o.w = sy[1];
          __builtin_amdgcn_raw_buffer_store_b128(o, rws, ovo, (mt0 + 2 * i - 2) * 20480, AUX_SC1);
          STPAD();
        }
      }
      if (g == 0 && hd == 0) {
        const float e = h == 0 ? es[0] : h == 1 ? es[1] : h == 2 ? es[2] : es[3];
        __builtin_amdgcn_raw_buffer_store_b32(f2u(e), rws, WS_G + h * 128 + c * 4, mt0 * 64, AUX_SC1);
      }
    } else {
      const int cj = gc;
      using K = P1<false, RB>;
      K k;
      const int mh = RB ? (w < 6 ? 1 : 0) : (w >> 1);
      const int up = (w >> 1) & 1, kp = w & 1;
#pragma unroll
      for (int i = 0; i < K::NXP; ++i) {
        const int rho = 8 * (4 * (w & 3) + i) + (lane >> 3);
        const int j = rho >> 4;
        const int tok = 128 * a + 32 * (j >> 1) + 2 * (rho & 15) + (j & 1);
        k.xv[i] = tok * 1280 + (((lane & 7) ^ ((rho >> 1) & 7)) << 4);
      }
      k.xd = K::XB + 4 * (w & 3) * 1024;
      {
        const int r = lane >> 3, p8 = lane & 7;
        const int s = ((r >> 1) & 1) + 2 * kp;
        k.wv[0] = (8 * kp + r) * 3072 + cj * 384 + (((((p8 >> 1) ^ s) << 1) | (p8 & 1)) << 4) + (RB ? 0 : 16 * 3072 + 128);
        k.wv[1] = k.wv[0] + 128; k.wv[2] = k.wv[0] + 256; k.wv[3] = k.wv[0] + 16 * 3072;
      }
      k.wd0 = RB ? (kp * 6 + 3 * up) * 1024 : ((kp + 2) * 6 + 3 * up + 1) * 1024;
      k.wd1 = ((kp + 2) * 6 + 3 * up) * 1024;
      if (RB) asm volatile("" : "+v"(k.xv[0]), "+v"(k.xv[1]), "+v"(k.xv[2]), "+v"(k.xv[3]), "+v"(k.wv[2]), "+v"(k.wv[3]));
      asm volatile("" : "+v"(k.wv[0]), "+v"(k.wv[1]));
      bind();
      const u64 pg = wg, pu = wu;
      k.srdx = mk_srd(x, 2048 * 1280);
      k.srdw = mk_srd(up ? pu : pg, 640 * 3072);
      asm volatile("global_load_dwordx2 %0, %1, %2 sc1" : "=v"(ev) : "v"(WS_EPOCH), "s"(ws) : "memory");
      k.stage0(rot);
      BAR();
      if (!RB) pack_issue();
      k.stage1();
      k.xa0 = (K::XB + mh * 4 * 2048 + c * 128 + ((h ^ (c >> 1)) << 4)) ^ ((rot & 1) << 6);
      {
        const int s = ((q4 >> 1) & 1) + 2 * (h & 1);
        k.wa0 = (h * 6 + (RB ? 2 : (w & 1))) * 1024 + q4 * 128 + (((RB ? 2 * (w & 1) : 0) ^ s) << 5) + p4 * 8;
      }
      asm volatile("" : "+v"(k.xa0), "+v"(k.wa0));
      k.zero();
      k.stage2();
      k.template run<RB ? 5 : 0, RB ? 10 : 0>([&]() {
        make_tag(std::integral_constant<int, RB ? K::VPRO : 8>{});
        BAR();
      }, [&](int t) { if (RB) side_b(t); else side_f(t); }, tail, pack_gap);
      constexpr int NK = RB ? 1 : 2;
      float nl2e = -LOG2E;
      asm volatile("" : "+s"(nl2e));
      const unsigned gso = WS_GU + ((8 * a + 4 * mh) * 48 + 6 * cj + (RB ? 4 + (w & 1) : 2 * (w & 1))) * 1024;
#pragma unroll
      for (int m = 0; m < 4; ++m) {
#pragma unroll
        for (int kk = 0; kk < NK; ++kk) {
          float v[8];
#pragma unroll
          for (int j = 0; j < 8; j += 2) {
            const f32x2 g2 = {k.acc[m][2 * kk + (j >> 2)][j & 3], k.acc[m][2 * kk + (j >> 2)][(j & 3) + 1]};
            const f32x2 u2 = {k.acc[m][2 * NK + 2 * kk + (j >> 2)][j & 3], k.acc[m][2 * NK + 2 * kk + (j >> 2)][(j & 3) + 1]};
            const f32x2 a2 = g2 * nl2e;
            const f32x2 d2 = f32x2{__builtin_amdgcn_exp2f(a2.x), __builtin_amdgcn_exp2f(a2.y)} + 1.f;
            const f32x2 r2 = g2 * u2 * f32x2{__builtin_amdgcn_rcpf(d2.x), __builtin_amdgcn_rcpf(d2.y)};
            v[j] = r2.x; v[j + 1] = r2.y;
          }
          u32x4 o; o.x = pk2(v[0], v[1]); o.y = pk2(v[2], v[3]); o.z = pk2(v[4], v[5]); o.w = pk2(v[6], v[7]);
          __builtin_amdgcn_raw_buffer_store_b128(o, rws, lane * 16, gso + m * (48 * 1024) + kk * 1024, AUX_SC1);
          STPAD();
        }
      }
    }
    SB();
    asm volatile("" : "+v"(wo_[0][0]), "+v"(wo_[0][1]), "+v"(wo_[0][2]), "+v"(wo_[0][3]), "+v"(wo_[0][4]),
                      "+v"(wo_[1][0]), "+v"(wo_[1][1]), "+v"(wo_[1][2]), "+v"(wo_[1][3]), "+v"(wo_[1][4]));
    if (RB) asm volatile("" : "+v"(wg_[0][0]), "+v"(wg_[0][1]), "+v"(wg_[0][2]), "+v"(wg_[0][3]), "+v"(wg_[0][4]),
                              "+v"(wg_[1][0]), "+v"(wg_[1][1]), "+v"(wg_[1][2]), "+v"(wg_[1][3]), "+v"(wg_[1][4]));
    SB();
    if (RB && idx >= 16) { cell_dma(w - 4, 1); cell_dma(w, 1); WAITV(10); }
    else WAITV(0);
    WAITL();
    BAR();
    if (!RB && w == 0 && lane < 2) {
      GAS u64* fp = (GAS u64*)((char*)ws + (lane ? WS_DONE + b * 8 : WS_FLAG + (a * 16 + (idx < 16 ? 8 : 0) + gc) * 8));
      __hip_atomic_store(fp, tag, RLX);
    }

    SB();
    const unsigned fo = WS_FLAG + ((rb >> 1) * 16 + c) * 8;
    auto wait_flags = [&](u32x2& v, bool need) {
#pragma unroll 1
      for (unsigned spin = 0; spin < (1u << 18); ++spin) {
        if (__all(!need || (v.x == (unsigned)tag && v.y == (unsigned)(tag >> 32)))) break;
        ld64(v, fo, sws, 0);
        asm volatile("s_waitcnt vmcnt(0)" : "+v"(v) :: "memory");
      }
      asm volatile("" ::: "memory");
    };
    u32x2 fv;
    if (RB) {
      if (idx < 16) { cell_dma(w - 4, 1); cell_dma(w, 1); }
      cell_dma(w - 4, 2);
      cell_dma(w, 2);
    } else {
      late_w();
      if (w == 0) {
        ld64(fv, fo, sws, 0);
        asm volatile("s_waitcnt vmcnt(0)" : "+v"(fv) :: "memory");
        wait_flags(fv, c >= 8);
      }
    }
    if (RB) __builtin_amdgcn_s_setprio(1);
    BAR();
    f32x4 acc[4][5];
    u32x4 ag[6][4];
    const unsigned gb = WS_GU + (4 * rb * 48 + 6 * w) * 1024;
    auto ldg = [&](int s, int m) { ld128(ag[s][m], lane * 16, sws, gb + gstep(s) * 1024 + m * (48 * 1024)); };
    unsigned ca0 = CELL0 + w * 5120 + lane * 16, ca1 = w * 15360 + lane * 16;
    asm volatile("" : "+v"(ca0), "+v"(ca1));
    {
      using S = Sched<RB>;
      constexpr int NO = S::NO, OB = S::OB;
      u32x4 af[2][4], at[4];
      u32x2 tl[4], th[4];
      const unsigned ob = WS_O + 4 * rb * 20480;
      auto req = [&](auto IT) {
        constexpr int i = IT.value;
        if constexpr (i < 8) ld128(af[i >> 2][i & 3], lane * 16, sws, ob + (i & 3) * 20480 + (2 * w + (i >> 2)) * 1024);
        else if constexpr (i < OB) ld64((i & 1) ? th[(i - 8) >> 1] : tl[(i - 8) >> 1], lane * 8, sws, ob + ((i - 8) >> 1) * 20480 + (16 + w) * 1024 + (i & 1) * 512);
        else if constexpr (i < OB + 29 && (i - OB) % 5 < 4) ldg((i - OB) / 5, (i - OB) % 5);
        else if constexpr (i < OB + 29) dma16c(w * 15360, 10240 + ((i - OB) / 5) * 1024, lane * 16, sws, pwb + (20 + 6 * w + gstep(5)) * 5120 + ((i - OB) / 5) * 1024);
        else ld32(i == OB + 29 ? sg0 : i == OB + 30 ? sg1 : sg2, c * 4, sws, WS_G + (4 * rb + min(t0 + i - OB - 29, 19) / 5) * 64);
      };
      if (RB || w != 1) sfor<0, S::upto(PL - 1)>(req);
      if (RB) { __builtin_amdgcn_s_setprio(0); WAITV(S::upto(PL - 1)); }
      else if (w == 1) { fv.x = ~(unsigned)tag; fv.y = 0; wait_flags(fv, c < 8); }
      BAR();
      if (!RB && w == 1) sfor<0, S::upto(PL - 1)>(req);
      u32x4 wc[5];
      sfor<0, NO + 6>([&](auto ST) {
        constexpr int st = ST.value, s = st - NO;
        if constexpr (st == 2 && !RB) {
          asm volatile("s_waitcnt vmcnt(%8)" : "+v"(tl[0]), "+v"(tl[1]), "+v"(tl[2]), "+v"(tl[3]), "+v"(th[0]), "+v"(th[1]), "+v"(th[2]), "+v"(th[3])
                       : "i"(S::issued(st, 0) - 16) : "memory");
#pragma unroll
          for (int m = 0; m < 4; ++m) at[m] = cat(tl[m], th[m]);
          asm volatile("s_nop 1" : "+v"(at[0]), "+v"(at[1]), "+v"(at[2]), "+v"(at[3]));
        }
        sfor<0, 20>([&](auto IT) {
          constexpr int i = IT.value, m = i / 5, n = i % 5, is = S::issued(st, i);
          if constexpr (i == 0) {
            if constexpr (s >= 0)      asm volatile("s_waitcnt vmcnt(%4)" : "+v"(ag[s][0]), "+v"(ag[s][1]), "+v"(ag[s][2]), "+v"(ag[s][3]) : "i"(is - S::gpos(s, 3) - 1) : "memory");
            else if constexpr (st < 2) asm volatile("s_waitcnt vmcnt(%4)" : "+v"(af[st][0]), "+v"(af[st][1]), "+v"(af[st][2]), "+v"(af[st][3]) : "i"(is - (4 * st + 3) - 1) : "memory");
          }
          if constexpr (st == 0 && i == 0 && !RB)
            asm volatile("" : "+v"(wo_[2][0]), "+v"(wo_[2][1]), "+v"(wo_[2][2]), "+v"(wo_[2][3]), "+v"(wo_[2][4]),
                              "+v"(wg_[0][0]), "+v"(wg_[0][1]), "+v"(wg_[0][2]), "+v"(wg_[0][3]), "+v"(wg_[0][4]),
                              "+v"(wg_[1][0]), "+v"(wg_[1][1]), "+v"(wg_[1][2]), "+v"(wg_[1][3]), "+v"(wg_[1][4]));
          if constexpr (st == 0)     mul0(acc[m][n], wo_[0][n], af[0][m]);
          else if constexpr (s >= 0) mac(acc[m][n], wg_[s & 1][n], ag[s][m]);
          else if constexpr (st < 2) mac(acc[m][n], wo_[1][n], af[1][m]);
          else                       mac(acc[m][n], wo_[2][n], at[m]);
          SB();
          if constexpr (s == 4 && i == 9) WAITV(is - S::gpos(4, 4) - 1);
          if constexpr (m == (s == 4 ? 2 : 0) && s >= 1 && s < 5) wc[n] = lds128((s > 1 ? ca1 + (s - 2) * 5120 : ca0) + n * 1024);
          if constexpr (S::issued(st, i + 1) > is) req(std::integral_constant<int, is>{});
          SB();
        });
        if constexpr (s >= 1 && s < 5) {
          asm volatile("" : "+v"(wc[0]), "+v"(wc[1]), "+v"(wc[2]), "+v"(wc[3]), "+v"(wc[4]));
#pragma unroll
          for (int n = 0; n < 5; ++n) wg_[(s + 1) & 1][n] = wc[n];
        }
      });
      asm volatile("s_waitcnt vmcnt(0)" : "+v"(sg0), "+v"(sg1), "+v"(sg2) :: "memory");
    }
    MFMA_DONE();
#pragma unroll
    for (int j = 0; j < 10; ++j) {
      const int ta = j < 4 ? 3 * j : j < 6 ? 6 * j - 22 : 2 * j, tb = j < 4 ? ta + 1 : j < 6 ? ta + 3 : ta + 1;
      *(LAS u32x4*)(ca1 + j * 1024) = cat(pk4(acc[ta / 5][ta % 5]), pk4(acc[tb / 5][tb % 5]));
    }
  };
  if (w < 4) role(std::false_type{}); else role(std::true_type{});

  if (b == 0 && w == 2) {
    GAS u64* dp = (GAS u64*)((char*)ws_ + WS_DONE + lane * 8);
#pragma unroll 1
    for (unsigned spin = 0; spin < (1u << 18); ++spin) {
      bool ok = true;
#pragma unroll
      for (int j = 0; j < 4; ++j) ok &= __hip_atomic_load(dp + j * 64, RLX) == tag;
      if (__all(ok)) break;
      __builtin_amdgcn_s_sleep(2);
    }
    if (lane == 0) __hip_atomic_store((GAS u64*)((char*)ws_ + WS_EPOCH), tag, RLX);
  }
  sg0 = __builtin_amdgcn_rcpf(1.f + sg0); sg1 = __builtin_amdgcn_rcpf(1.f + sg1); sg2 = __builtin_amdgcn_rcpf(1.f + sg2);
  auto ooff = [&](int tl) {
    const int m = tl / 5, n = tl - 5 * m;
    return (unsigned)((64 * rb + 32 * (m >> 1) + 2 * c + (m & 1)) * 1280 + ct * 160 + n * 32 + h * 8);
  };
  unsigned oo0 = ooff(t0), oo1 = ooff(t0 + 1), oo2 = ooff(t0 + 2);
  asm volatile("" : "+v"(sg0), "+v"(sg1), "+v"(sg2), "+v"(oo0), "+v"(oo1), "+v"(oo2));
  WAITL();
  BAR();
  {
    u32x4 p[8];
    u32x2 p2[8];
    unsigned ra = (w < 4 ? w : w + 2) * 1024 + lane * 16, rb_ = ra + 4 * 15360;
    unsigned rc = (4 + (w >> 1)) * 1024 + (w & 1) * 8 + lane * 16, rd = rc + 4 * 15360;
    asm volatile("" : "+v"(ra), "+v"(rb_), "+v"(rc), "+v"(rd));
#pragma unroll
    for (int v = 0; v < 8; ++v) p[v] = lds128((v < 4 ? ra : rb_) + (v & 3) * 15360);
    if (w < 4) {
#pragma unroll
      for (int v = 0; v < 8; ++v) { p2[v] = *(const LAS u32x2*)((v < 4 ? rc : rd) + (v & 3) * 15360); SB(); }
    }
    SB();
#pragma unroll
    for (int j = 0; j < 3; ++j) {
      if (j < 2 || w < 4) {
        f32x4 s = {0.f, 0.f, 0.f, 0.f};
#pragma unroll
        for (int v = 0; v < 8; ++v) {
          const unsigned px = j == 0 ? p[v].x : j == 1 ? p[v].z : p2[v].x, py = j == 0 ? p[v].y : j == 1 ? p[v].w : p2[v].y;
          const f32x4 q = {u2f(px << 16), u2f(px & 0xffff0000u), u2f(py << 16), u2f(py & 0xffff0000u)};
          s = v ? s + q : q;
        }
        const u32x2 ov = pk4(s * (j == 0 ? sg0 : j == 1 ? sg1 : sg2));
        asm volatile("global_store_dwordx2 %0, %1, %2" :: "v"(j == 0 ? oo0 : j == 1 ? oo1 : oo2), "v"(ov), "s"(out_) : "memory");
      }
    }
  }
}
}

extern "C" void kernel_launch(void* const* d_in, const int* in_sizes, int n_in, void* d_out, int out_size, void* d_ws,
                              size_t ws_size, hipStream_t stream) {
  fused<<<256, 512, 0, stream>>>(d_in[0], d_in[1], d_in[2], d_in[4], d_in[5], d_in[6], d_in[7], d_out, d_ws);
}
```

```cpp
#include <hip/hip_runtime.h>
#include <hip/hip_bf16.h>
#include <stdint.h>
#include <type_traits>

#define STAMPS 0
#define BEARLY 0
#define CODEPF 1
#define TOUCH_NT ""
#define TOUCH 1
#define EXP 0
#define GLA 4
#define PART16 1
#define PL 2
#define PRE 2
#define OUTFL ""
#define PREA 0
#define WGR 4
#define GPOLL 1
#define ROT 4

#define LAS __attribute__((address_space(3)))
#define GAS __attribute__((address_space(1)))
typedef __bf16 bf16x8 __attribute__((ext_vector_type(8)));
typedef __bf16 bf16x2 __attribute__((ext_vector_type(2)));
typedef float f32x4 __attribute__((ext_vector_type(4)));
typedef float f32x2 __attribute__((ext_vector_type(2)));
typedef unsigned u32x4 __attribute__((ext_vector_type(4)));
typedef unsigned u32x2 __attribute__((ext_vector_type(2)));
typedef short s16x4 __attribute__((ext_vector_type(4)));
typedef unsigned long long u64;

namespace {
constexpr int LDS_BYTES = 163840;
constexpr unsigned GATE_OFF = 110592, PACK_OFF = 122880, PKF_OFF = 162816, CELL0 = 122880;
constexpr unsigned OOB = 0x40000000u;
#define CODE_B 40000
constexpr unsigned CODE_BYTES = CODE_B;
constexpr unsigned WS_FLAG = 0;
constexpr unsigned WS_DONE = 4096;
constexpr unsigned WS_PKF = 8192;
constexpr unsigned WS_EPOCH = 12288;
constexpr unsigned WS_G = 16384;
constexpr unsigned WS_O = 131072;
constexpr unsigned WS_GU = WS_O + 128 * 40 * 512;
constexpr unsigned WS_PW = WS_GU + 128 * 48 * 1024;
constexpr unsigned WS_END = WS_PW + 8 * 68 * 5120;
constexpr unsigned WS_STAMP = 16u << 20;
constexpr int AUX_SC1 = 16;
constexpr float LOG2E = 1.4426950408889634f;

__device__ __forceinline__ u32x4 mk_srd(u64 a, unsigned bytes) {
  u32x4 r; r.x = (unsigned)a; r.y = (unsigned)(a >> 32) & 0xffffu; r.z = bytes; r.w = 0x00020000u; return r;
}
__device__ __forceinline__ void dma16(unsigned lds, unsigned imm, unsigned voff, u32x4 srd, unsigned soff) {
  asm volatile("s_add_u32 m0, %0, %4\n\ts_nop 0\n\tbuffer_load_dwordx4 %1, %2, %3 offen lds"
               :: "s"(lds), "v"(voff), "s"(srd), "s"(soff), "i"(imm) : "memory", "scc");
}
__device__ __forceinline__ void dma16c(unsigned lds, unsigned imm, unsigned voff, u32x4 srd, unsigned soff) {
  asm volatile("s_add_u32 m0, %0, %4\n\ts_nop 0\n\tbuffer_load_dwordx4 %1, %2, %3 offen sc1 lds"
               :: "s"(lds), "v"(voff), "s"(srd), "s"(soff), "i"(imm) : "memory", "scc");
}
__device__ __forceinline__ void ld128(u32x4& d, unsigned voff, u32x4 srd, unsigned soff) {
  asm volatile("buffer_load_dwordx4 %0, %1, %2, %3 offen sc1" : "=v"(d) : "v"(voff), "s"(srd), "s"(soff) : "memory");
}
__device__ __forceinline__ void ld64(u32x2& d, unsigned voff, u32x4 srd, unsigned soff) {
  asm volatile("buffer_load_dwordx2 %0, %1, %2, %3 offen sc1" : "=v"(d) : "v"(voff), "s"(srd), "s"(soff) : "memory");
}
__device__ __forceinline__ void ld32(float& d, unsigned voff, u32x4 srd, unsigned soff) {
  asm volatile("buffer_load_dword %0, %1, %2, %3 offen sc1" : "=v"(d) : "v"(voff), "s"(srd), "s"(soff) : "memory");
}
__device__ __forceinline__ u32x4 lds128(unsigned a) { return *(const LAS u32x4*)a; }
__device__ __forceinline__ u32x2 ldstr(unsigned a) {
  return __builtin_bit_cast(u32x2, __builtin_amdgcn_ds_read_tr16_b64_v4i16((LAS s16x4*)a));
}
__device__ __forceinline__ u32x4 cat(u32x2 a, u32x2 b) { u32x4 r; r.x = a.x; r.y = a.y; r.z = b.x; r.w = b.y; return r; }
__device__ __forceinline__ f32x4 mfma(u32x4 a, u32x4 b, f32x4 c) {
  return __builtin_amdgcn_mfma_f32_16x16x32_bf16(__builtin_bit_cast(bf16x8, a), __builtin_bit_cast(bf16x8, b), c, 0, 0, 0);
}
__device__ __forceinline__ void mac(f32x4& c, u32x4 a, u32x4 b) {
  asm volatile("v_mfma_f32_16x16x32_bf16 %0, %1, %2, %0" : "+v"(c) : "v"(a), "v"(b));
}
__device__ __forceinline__ void mul0(f32x4& c, u32x4 a, u32x4 b) {
  asm volatile("v_mfma_f32_16x16x32_bf16 %0, %1, %2, 0" : "=&v"(c) : "v"(a), "v"(b));
}
__device__ __forceinline__ unsigned pk2(float a, float b) {
  f32x2 t = {a, b};
  return __builtin_bit_cast(unsigned, __builtin_convertvector(t, bf16x2));
}
__device__ __forceinline__ u32x2 pk4(f32x4 v) { u32x2 r; r.x = pk2(v.x, v.y); r.y = pk2(v.z, v.w); return r; }
__device__ __forceinline__ float u2f(unsigned u) { return __builtin_bit_cast(float, u); }
__device__ __forceinline__ unsigned f2u(float f) { return __builtin_bit_cast(unsigned, f); }
#define SB() __builtin_amdgcn_sched_barrier(0)
#define WAITV(n) asm volatile("s_waitcnt vmcnt(%0)" :: "i"(n) : "memory")
#define WAITVL(n) asm volatile("s_waitcnt vmcnt(%0) lgkmcnt(0)" :: "i"(n) : "memory")
#define WAITL() asm volatile("s_waitcnt lgkmcnt(0)" ::: "memory")
#define BAR() asm volatile("s_barrier" ::: "memory")
#define STPAD() do { SB(); asm volatile("s_nop 1" ::: "memory"); SB(); } while (0)
#define MFMA_DONE() do { SB(); asm volatile("s_nop 7\n\ts_nop 1" ::: "memory"); SB(); } while (0)
#define RLX __ATOMIC_RELAXED, __HIP_MEMORY_SCOPE_AGENT
__device__ __forceinline__ unsigned rfl(unsigned v) { return __builtin_amdgcn_readfirstlane(v); }
__device__ __forceinline__ unsigned opaque_lane() { unsigned l = threadIdx.x & 63; asm volatile("" : "+v"(l)); return l; }

#define AMP0 -1
#define AMP1 -1
#define AMPSEL 0
#define STAMP(i) do {} while (0)

template <int I, int N, class F>
__device__ __forceinline__ void sfor(F&& f) {
  if constexpr (I < N) { f(std::integral_constant<int, I>{}); sfor<I + 1, N>(f); }
}
template <bool RB> struct Sched {
  static constexpr int NO = RB ? 2 : 3, OB = RB ? 8 : 16;
  static constexpr int gpos(int s, int m) { return OB + 5 * s + m; }
  static constexpr int upto(int st) { return st < 2 ? 4 * (st + 1) : st < NO ? 16 : st < NO + 5 ? OB + 5 * (st - NO + 1) : st == NO + 5 ? OB + 29 : OB + 32; }
  static constexpr int issued(int st, int i) {
    const int base = upto(st + PL - 1), goal = upto(st + PL), every = goal - base > 5 ? 2 : goal - base > 4 ? 4 : 5;
    int n = base;
    for (int k = 0; k < i; ++k) if (k % every == 1 && n < goal) ++n;
    return n;
  }
};
__device__ __forceinline__ float max3(float a, float b, float c) { float r; asm("v_max3_f32 %0, %1, %2, %3" : "=v"(r) : "v"(a), "v"(b), "v"(c)); return r; }
__device__ __forceinline__ float max2(float a, float b) { float r; asm("v_max_f32 %0, %1, %2" : "=v"(r) : "v"(a), "v"(b)); return r; }

template <bool QKV, bool RB>
struct P1 {
  static constexpr int NM = QKV ? 5 : 4;
  static constexpr int NN = QKV ? 4 : (RB ? 4 : 8);
  static constexpr int NWS = 3;
  static constexpr unsigned WSL = QKV ? 16384 : 24576, XB = NWS * WSL, XSL = QKV ? 20480 : 16384;
  static constexpr unsigned WROW = QKV ? 3840 : 3072;
  static constexpr int NWP = QKV ? 2 : (RB ? 4 : 2);
  static constexpr int NXE = QKV ? (RB ? 1 : 2) : (RB ? 2 : 0), NXO = QKV ? 1 : NXE;
  static constexpr int NXP = NXO + NXE;
  static constexpr int OO = NWP + NXO, OE = NWP + NXE;
  static constexpr bool HAND = !QKV;
  static constexpr int ht = 4;
  f32x4 acc[NM][NN];
  u32x4 srdx, srdw;
  unsigned xv[4], wv[4];
  unsigned xd;
  unsigned wd0, wd1;
  unsigned xa0, wa0, wa1, wa2;
  unsigned gmask;
  __device__ __forceinline__ void tick(int) {}

  unsigned sow, sox, gof;
  unsigned kg, kw, kx, kr;
  template <bool ODD, bool X = true>
  __device__ __forceinline__ void loads(unsigned wwr, unsigned xwr) {
    if (X && NXP) {
#pragma unroll
      for (int i = ODD ? NXE : 0; i < (ODD ? NXP : NXE); ++i) dma16(xwr + xd, i * 1024, xv[i], srdx, sox);
      if (ODD) { kx += 1; sox = (kx ^ (kg >> 1)) * 128; }
    }
    dma16(wwr + wd0, 0, wv[0], srdw, sow);
    dma16(wwr + wd0, 1024, wv[1], srdw, sow);
    if (!QKV && RB) {
      dma16(wwr + wd0, 2048, wv[2], srdw, sow);
      dma16(wwr + wd1, 0, wv[3], srdw, sow);
    }
  }
  __device__ __forceinline__ void other(unsigned wwr) {
    dma16(wwr + wd1, 1024, wv[3], srdw, sow + 128);
    dma16(wwr + wd1, 2048, wv[3], srdw, sow + 256);
  }
  __device__ __forceinline__ void next() { kw += 1; sow = (kw ^ kg) * (32 * WROW); asm volatile("" : "+s"(sow), "+s"(kw)); if (NXP) asm volatile("" : "+s"(sox), "+s"(kx)); }
  __device__ __forceinline__ void stage0(int rot) {
    kg = rot; kw = 0; kx = 0; kr = 0; sow = kg * (32 * WROW); sox = (kg >> 1) * 128; gof = kg * 256;
    if (HAND && !RB) { kw = 3; sow = (3 ^ kg) * (32 * WROW); return; }
#pragma unroll
    for (int i = 0; i < NXE; ++i) dma16(xd, i * 1024, xv[i], srdx, sox);
    loads<true>(0, 0); if (HAND) other(0); next();
  }
  __device__ __forceinline__ void stage1() { if (HAND && !RB) return; loads<false>(WSL, XSL); if (HAND) other(WSL); next(); }
  __device__ __forceinline__ void stage2() { if (HAND && !RB) return; loads<true>(2 * WSL, XSL); if (HAND) other(2 * WSL); next(); }
  static constexpr int VPRO = OE + OO + (HAND ? 4 : 0);
  struct Ad { unsigned x, w[4]; };
  __device__ __forceinline__ void addr(Ad& a, unsigned xb, unsigned wrd) {
    a.x = xb; a.w[0] = wa0 + wrd;
    if (QKV) {
      const unsigned s2 = (gmask & (gof - wrd)) + wrd;
      kr += 1; gof = (kr ^ kg) * 256;
      a.w[1] = wa1 + s2; a.w[2] = wa2 + s2;
      asm volatile("" : "+v"(a.x), "+v"(a.w[0]), "+v"(a.w[1]), "+v"(a.w[2]));
    } else if (RB) {
      a.w[1] = a.w[0] ^ 32;
      asm volatile("" : "+v"(a.x), "+v"(a.w[0]), "+v"(a.w[1]));
    } else {
      a.w[1] = a.w[0] ^ 32; a.w[2] = a.w[0] ^ 64; a.w[3] = a.w[0] ^ 96;
      asm volatile("" : "+v"(a.x), "+v"(a.w[0]), "+v"(a.w[1]), "+v"(a.w[2]), "+v"(a.w[3]));
    }
  }
  __device__ __forceinline__ unsigned wad(const Ad& a, int n, int hi) {
    if (QKV) return n == 2 ? a.w[1 + hi] : a.w[0] + (n == 0 ? 0 : n == 1 ? 160 : 320) + hi * 1920;
    if (RB) return a.w[n & 1] + (n >> 1) * 3072 + hi * 512;
    return a.w[n & 3] + (n >> 2) * 3072 + hi * 512;
  }
  __device__ __forceinline__ void rd_all(u32x4 (&xf)[NM], u32x4 (&wf)[NN], const Ad& a) {
#pragma unroll
    for (int n = 0; n < NN; ++n) wf[n] = cat(ldstr(wad(a, n, 0)), ldstr(wad(a, n, 1)));
#pragma unroll
    for (int m = 0; m < NM; ++m) xf[m] = lds128(a.x + m * 2048);
  }
  template <bool RD = true, class L>
  __device__ __forceinline__ void mm_rd(const u32x4 (&xc)[NM], const u32x4 (&wc)[NN], u32x4 (&xn)[NM], u32x4 (&wn)[NN], const Ad& a, L&& late) {
    u32x2 lo[NN];
    constexpr bool NMAJ = !QKV && !RB;
    constexpr bool EARLY = !QKV && RB && BEARLY;
    if (EARLY && RD) { rd_all(xn, wn, a); SB(); }
    int r = 0;
#pragma unroll
    for (int i = 0; i < NM * NN; ++i) {
      const int m = NMAJ ? i % NM : i / NN, n = NMAJ ? i / NM : i % NN;
      if (QKV && m == 0 && n == 0) continue;
      if (QKV && n == 3) mac(acc[m][3], xc[m], wc[3]);
      else               mac(acc[m][n], wc[n], xc[m]);
      SB();
      if (!RD || EARLY) continue;
      if (NMAJ) {
        if (i < 4) xn[i] = lds128(a.x + i * 2048);
        else if ((i & 3) < 2) {
          const int k = (i >> 2) - 1;
          if (i & 1) wn[k] = cat(lo[k], ldstr(wad(a, k, 1))); else lo[k] = ldstr(wad(a, k, 0));
        } else late(i);
      } else {
        if (r < 2 * NN) {
          const int k = r >> 1;
          if (r & 1) wn[k] = cat(lo[k], ldstr(wad(a, k, 1))); else lo[k] = ldstr(wad(a, k, 0));
        } else if (r < 2 * NN + NM) {
          xn[r - 2 * NN] = lds128(a.x + (r - 2 * NN) * 2048);
        }
        else late(r - 2 * NN - NM);
        ++r;
      }
      SB();
    }
    if (NMAJ && RD) { wn[NN - 1] = cat(ldstr(wad(a, NN - 1, 0)), ldstr(wad(a, NN - 1, 1))); SB(); }
  }
  __device__ __forceinline__ void pin(u32x4 (&xf)[NM], u32x4 (&wf)[NN]) {
    asm volatile("" : "+v"(xf[0]), "+v"(xf[1]), "+v"(xf[2]), "+v"(xf[3]), "+v"(wf[0]), "+v"(wf[1]), "+v"(wf[2]), "+v"(wf[3]));
    if (NM == 5) asm volatile("" : "+v"(xf[NM - 1]));
    if (NN == 8) asm volatile("" : "+v"(wf[NN - 4]), "+v"(wf[NN - 3]), "+v"(wf[NN - 2]), "+v"(wf[NN - 1]));
  }
  __device__ __forceinline__ void zero() {
#pragma unroll
    for (int m = 0; m < NM; ++m)
#pragma unroll
      for (int n = 0; n < NN; ++n) { acc[m][n] = f32x4{0.f, 0.f, 0.f, 0.f}; asm volatile("" : "+v"(acc[m][n])); }
  }
  template <int TL0, int TL1, class F, class G, class H, class U>
  __device__ __forceinline__ void run(F&& pre, G&& extra, H&& tail, U&& gap) {
    pre();
    u32x4 xA[NM], wA[NN], xB[NM], wB[NN];
    Ad a;
    addr(a, xa0, 0);
    rd_all(xA, wA, a);
    if (!RB) __builtin_amdgcn_s_setprio(1);
    unsigned w0 = 0, w1 = WSL, w2 = 2 * WSL;
    unsigned xp = 2 * XSL, x0 = 0, x1 = XSL;
    int t = 0;
    unsigned nw1, nw2, nx1;
    auto adv0 = [&]() { nw1 = w2 + WSL; if (nw1 == NWS * WSL) nw1 = 0; asm volatile("" : "+s"(nw1)); };
    auto adv1 = [&]() { nw2 = nw1 + WSL; if (nw2 == NWS * WSL) nw2 = 0; asm volatile("" : "+s"(nw2)); };
    auto adv2 = [&]() { nx1 = x1 + XSL; if (nx1 == 3 * XSL) nx1 = 0; asm volatile("" : "+s"(nx1)); };
    auto adv3 = [&]() {
      w0 = w2; w1 = nw1; w2 = nw2; xp = x0; x0 = x1; x1 = nx1; t += 2;
      asm volatile("" : "+s"(w0), "+s"(w1), "+s"(w2), "+s"(xp), "+s"(x0), "+s"(x1), "+s"(t));
    };
    auto adv = [&]() { adv0(); adv1(); adv2(); adv3(); };
    asm volatile("" : "+s"(w0), "+s"(w1), "+s"(w2), "+s"(xp), "+s"(x0), "+s"(x1), "+s"(t));
    if (QKV) asm volatile("" : "+s"(gof), "+s"(kr));
    auto none = [](int) {};
    if (RB) {
      auto help = [&](int tt, unsigned wwr) { if (HAND && __builtin_expect(tt < ht, 0)) other(wwr); extra(tt); };
      pin(xA, wA);
      SB(); WAITVL(OO + (HAND ? 2 : 0)); tick(0); BAR(); tick(1); SB();
      loads<false>(w0, xp); help(0, w0); next(); addr(a, (xa0 ^ 64) + x0, w1); SB();
      auto half = [&]() {
        mm_rd(xA, wA, xB, wB, a, [&](int i) {
          if (QKV && i == 0) adv0();
          if (QKV && i == 2) adv1();
          if (QKV && i == 4) adv2();
        });
        pin(xB, wB);
        SB(); WAITVL(OE); tick(2 * t + 2); BAR(); tick(2 * t + 3); SB();
        loads<true>(w1, xp); help(t + 1, w1); next();
        addr(a, xa0 + x1, w2);
        if (QKV) adv3(); else adv();
        SB();
        mm_rd(xB, wB, xA, wA, a, none);
      };
#pragma unroll 1
      do {
        half();
        pin(xA, wA);
        SB(); WAITVL(OO); tick(2 * t); BAR(); tick(2 * t + 1); SB();
        loads<false>(w0, xp); help(t, w0); next(); addr(a, (xa0 ^ 64) + x0, w1); SB();
        if (QKV) asm volatile("" : "+s"(gof), "+s"(kr));
      } while (t < 14);
      half();
    } else {
      addr(a, (xa0 ^ 64) + x0, w1);
      auto trip = [&](auto EARLY) {
        constexpr bool E = EARLY.value;
        pin(xA, wA);
        SB(); if (E) WAITL(); else WAITVL(OO);
        tick(2 * t); BAR(); tick(2 * t + 1); SB();
        mm_rd(xA, wA, xB, wB, a, [&](int i) { if (E) gap(i); });
        if (E) { if (t == 0) WAITV(0); else extra(t); }
        if (!E || t >= ht) loads<false>(w0, xp);
        next(); addr(a, xa0 + x1, w2); adv0(); adv1(); adv2(); SB();
        pin(xB, wB);
        SB(); if (E) WAITL(); else WAITVL(OE);
        tick(2 * t + 2); BAR(); tick(2 * t + 3); SB();
        mm_rd(xB, wB, xA, wA, a, [&](int i) { if (E) gap(i); });
        if (E && t < ht) extra(t + 1); else loads<true>(w1, xp);
        next(); adv3(); addr(a, (xa0 ^ 64) + x0, w1); SB();
        if (QKV) asm volatile("" : "+s"(gof), "+s"(kr));
      };
      if (HAND) {
#pragma unroll 1
        do trip(std::true_type{}); while (t < ht + 2);
      }
#pragma unroll 1
      do trip(std::false_type{}); while (t < 16);
    }
    constexpr unsigned S16 = (16 % NWS) * WSL, S17 = (17 % NWS) * WSL, S18 = (18 % NWS) * WSL, S19 = (19 % NWS) * WSL, X8 = 2 * XSL, X9 = 0;
    pin(xA, wA);
    SB(); WAITVL(OO);
    tick(32); BAR(); tick(33); SB();
    if (RB) { loads<false, false>(S16, 0); tail(0); addr(a, (xa0 ^ 64) + X8, S17); SB(); mm_rd(xA, wA, xB, wB, a, none); }
    else    { mm_rd(xA, wA, xB, wB, a, none); loads<false, false>(S16, 0); tail(0); addr(a, xa0 + X9, S18); SB(); }
    pin(xB, wB);
    SB(); WAITVL(NWP + TL0); tick(34); BAR(); tick(35); SB();
    if (RB) { tail(1); addr(a, xa0 + X9, S18); SB(); mm_rd(xB, wB, xA, wA, a, none); }
    else    { mm_rd(xB, wB, xA, wA, a, none); tail(1); addr(a, (xa0 ^ 64) + X9, S19); SB(); }
    pin(xA, wA);
    SB(); WAITVL(TL0 + TL1); tick(36); BAR(); tick(37); SB();
    if (RB) { tail(2); addr(a, (xa0 ^ 64) + X9, S19); SB(); mm_rd(xA, wA, xB, wB, a, none); }
    else    { mm_rd(xA, wA, xB, wB, a, none); tail(2); SB(); }
    pin(xB, wB);
    SB(); WAITL(); tick(38); BAR(); tick(39); SB();
    if (RB) { tail(3); SB(); mm_rd<false>(xB, wB, xA, wA, a, none); }
    else    { mm_rd<false>(xB, wB, xA, wA, a, none); tail(3); SB(); }
    if (!RB) __builtin_amdgcn_s_setprio(0);
    MFMA_DONE();
    tick(40);
  }
};

__global__ __launch_bounds__(512, 2) void fused(const void* x_, const void* wqkv_, const void* wo_p, const void* wg_p,
                                                const void* wu_p, const void* wd_p, const void* gw_, void* out_, void* ws_) {
  __shared__ __attribute__((aligned(16))) char smem[LDS_BYTES];
  asm volatile("" :: "s"((unsigned)(size_t)(LAS char*)smem));
  const int tid = threadIdx.x, lane = tid & 63;
  const int w = rfl(tid >> 6);
  const int h = lane >> 4, c = lane & 15, q4 = (lane & 15) >> 2, p4 = lane & 3;
  const int b = blockIdx.x, xcd = b & 7, idx = b >> 3;
  const int rh = xcd & 1, cq = xcd >> 1;
  const int a = 8 * rh + (idx & 7);
  const int gc = 2 * cq + ((idx >> 3) & 1);
  const int rb = 4 * xcd + (idx >> 3), ct = idx & 7;
  u64 x, wqkv, wo, wg, wu, wd, gw;
  char* ws;
  __amdgpu_buffer_rsrc_t rws;
  u32x4 sws;
  auto bind = [&]() {
    x = (u64)x_; wqkv = (u64)wqkv_; wo = (u64)wo_p; wg = (u64)wg_p; wu = (u64)wu_p; wd = (u64)wd_p; gw = (u64)gw_;
    u64 pw = (u64)ws_;
    asm volatile("" : "+s"(x), "+s"(wqkv), "+s"(wo), "+s"(wg), "+s"(wu), "+s"(wd), "+s"(gw), "+s"(pw));
    ws = (char*)pw;
    rws = __builtin_amdgcn_make_buffer_rsrc(ws, 0, WS_END, 0x00020000);
    sws = mk_srd(pw, WS_END);
  };
  u64 amp_t0 = 0, amp_t1 = 0;

  if (CODEPF) {
    u64 pc;
    asm volatile("s_getpc_b64 %0" : "=s"(pc));
    asm volatile("s_mov_b32 m0, %0\n\ts_nop 0\n\tbuffer_load_dword %1, %2, 0 offen lds"
                 :: "s"(PKF_OFF), "v"(lane < 6 ? ((idx & 7) * 48 + w * 6 + lane) * 128 : OOB), "s"(mk_srd(pc & ~127ull, CODE_BYTES)) : "memory");
  }
  u32x2 ev;
  u64 tag = 0;
  auto make_tag = [&](auto NV) {
    unsigned lo, hi;
    asm volatile("s_waitcnt vmcnt(%4)\n\tv_readfirstlane_b32 %0, %2\n\tv_readfirstlane_b32 %1, %3"
                 : "=s"(lo), "=s"(hi) : "v"(ev.x), "v"(ev.y), "i"(NV.value) : "memory");
    const u64 e = ((u64)hi << 32) | lo;
    tag = (e ^ 0x9E3779B97F4A7C15ull) * 0xBF58476D1CE4E5B9ull + 0x632BE59BD9B4E019ull;
    if (tag == e) tag += 1;
  };
  const unsigned pwb = WS_PW + ct * (68 * 5120);
  const int grot = 3 * ((idx >> 3) & 1);
  auto gstep = [&](int s) { const int v = s + grot; return v >= 6 ? v - 6 : v; };
  auto cell_dma = [&](int v, int cell) {
    const unsigned so = pwb + (20 + 6 * v + gstep(2 + cell)) * 5120;
#pragma unroll
    for (int j = 0; j < 5; ++j) dma16c(cell ? v * 15360 : CELL0 + v * 5120, (cell ? cell - 1 : 0) * 5120 + j * 1024, lane * 16, sws, so + j * 1024);
  };
  auto ldw = [&](u32x4 (&d)[5], unsigned so) {
#pragma unroll
    for (int n = 0; n < 5; ++n) d[n] = __builtin_amdgcn_raw_buffer_load_b128(rws, lane * 16, so + n * 1024, AUX_SC1);
  };
  const unsigned pscr = PACK_OFF + (w & 3) * 8192;
  const int pu0 = (b - 128) * 4 + w;
  unsigned pra[2], prb[2], pn[2], pb[2];
  auto pack_issue = [&]() {
    const u64 po = wo, pd = wd;
#pragma unroll
    for (int r = 0; r < 2; ++r) {
      const int u = pu0 + 512 * r;
      const int ks = u / 10, cg = u - 10 * ks;
      const unsigned r8 = lane >> 3;
      const u32x4 srd = mk_srd(ks < 20 ? po : pd, ks < 20 ? 640 * 1280 : 1536 * 1280);
      const unsigned base = ks < 16 ? 40 * ks : ks < 20 ? 160 * (ks - 16) + 16 : 32 * (ks - 20);
      const unsigned vr = (ks >= 16 && ks < 20) ? 20 * (r8 >> 2) + (r8 & 3) : r8;
      const unsigned vo = vr * 1280 + (lane & 7) * 16;
#pragma unroll
      for (int p = 0; p < 4; ++p) {
        const unsigned rp = ks < 16 ? 20 * (p >> 1) + 8 * (p & 1) : ks < 20 ? 40 * p : 8 * p;
        dma16(pscr, r * 4096 + p * 1024, vo, srd, u < 680 ? (base + rp) * 1280 + cg * 128 : OOB);
      }
      pra[r] = pscr + r * 4096 + (h * (ks < 16 ? 8 : 4) + q4) * 128 + p4 * 8 - 32;
      prb[r] = pra[r] + (ks < 16 ? 512 : 2048);
      pn[r] = 4 * cg - 1; pb[r] = u < 680 ? WS_PW + ks * 5120 + (4 * cg - 1) * 1024 : OOB;
    }
    asm volatile("" : "+v"(pra[0]), "+v"(prb[0]), "+v"(pra[1]), "+v"(prb[1]));
    asm volatile("" : "+s"(pn[0]), "+s"(pb[0]), "+s"(pn[1]), "+s"(pb[1]));
  };
  const unsigned smask = w != 4 ? 0u : idx >= 16 ? 0x1040u : 0x1000u;
  auto side_b = [&](int t) {
    if (__builtin_expect(!((smask >> t) & 1), 1)) return;
    if (t == 6) {
      u32x2 tv; tv.x = (unsigned)tag; tv.y = (unsigned)(tag >> 32);
      __builtin_amdgcn_raw_buffer_store_b64(tv, rws, opaque_lane() == 0 ? WS_PKF + (b - 128) * 8 : OOB, 0, AUX_SC1);
    } else {
      dma16c(PKF_OFF, 0, opaque_lane() * 16, sws, WS_PKF);
    }
  };
  u32x2 pq[4];
  unsigned ps0, ps1;
  auto pack_gap = [&](int i) {
    if (i == 6)  pq[0] = ldstr(pra[0]);
    if (i == 7)  pq[1] = ldstr(prb[0]);
    if (i == 10) pq[2] = ldstr(pra[1]);
    if (i == 11) pq[3] = ldstr(prb[1]);
    if (i == 14) { pra[0] += 32; prb[0] += 32; asm volatile("" : "+v"(pra[0]), "+v"(prb[0])); }
    if (i == 15) { pra[1] += 32; prb[1] += 32; asm volatile("" : "+v"(pra[1]), "+v"(prb[1])); }
    if (i == 18) { ps0 = (pn[0] * 205) >> 10; asm volatile("" : "+s"(ps0)); }
    if (i == 19) { ps0 = ps0 * (68 * 5120 - 5120) + pb[0]; asm volatile("" : "+s"(ps0)); }
    if (i == 22) { ps1 = (pn[1] * 205) >> 10; asm volatile("" : "+s"(ps1)); }
    if (i == 23) { ps1 = ps1 * (68 * 5120 - 5120) + pb[1]; asm volatile("" : "+s"(ps1)); }
    if (i == 26) { pn[0] += 1; pb[0] += 1024; asm volatile("" : "+s"(pn[0]), "+s"(pb[0])); }
    if (i == 27) { pn[1] += 1; pb[1] += 1024; asm volatile("" : "+s"(pn[1]), "+s"(pb[1])); }
  };
  auto side_f = [&](int) {
    __builtin_amdgcn_raw_buffer_store_b128(cat(pq[0], pq[1]), rws, lane * 16, ps0, AUX_SC1);
    STPAD();
    __builtin_amdgcn_raw_buffer_store_b128(cat(pq[2], pq[3]), rws, lane * 16, ps1, AUX_SC1);
    STPAD();
  };
  auto pk_check = [&]() {
    const u32x4 p0 = lds128(PKF_OFF + lane * 16);
    const unsigned lo = (unsigned)tag, hi = (unsigned)(tag >> 32);
    if (!__all(p0.x == lo && p0.z == lo && p0.y == hi && p0.w == hi)) {
      GAS u64* fp = (GAS u64*)((char*)ws + WS_PKF + opaque_lane() * 8);
#pragma unroll 1
      for (unsigned spin = 0; spin < (1u << 18); ++spin) {
        bool ok = true;
#pragma unroll
        for (int j = 0; j < 2; ++j) ok &= __hip_atomic_load(fp + 64 * j, RLX) == tag;
        if (__all(ok)) break;
        __builtin_amdgcn_s_sleep(2);
      }
    }
    asm volatile("" ::: "memory");
  };
  const int rot = (a + 2 * ((idx >> 3) & 1) + (idx >> 4)) & (ROT - 1);

  const int t0 = 2 * w + min(w, 4);
  float sg0, sg1, sg2;
  auto role = [&](auto RBT) {
    constexpr bool RB = decltype(RBT)::value;
    u32x4 wo_[3][5], wg_[2][5];
    auto tail = [&](int i) {
      if (i == 0) pk_check();
      if (RB) {
        if (i == 1) { ldw(wo_[0], pwb + 2 * w * 5120); ldw(wo_[1], pwb + (2 * w + 1) * 5120); }
        if (i >= 2) ldw(wg_[i - 2], pwb + (20 + 6 * w + gstep(i - 2)) * 5120);
        if (i == 2) cell_dma(w - 4, 0);
        if (i == 3) cell_dma(w, 0);
      } else if (i >= 2) {
        ldw(wo_[i - 2], pwb + (2 * w + i - 2) * 5120);
      }
    };
    auto late_w = [&]() {
#pragma unroll
      for (int n = 0; n < 5; ++n) ld128(wo_[2][n], lane * 16, sws, pwb + (16 + w) * 5120 + n * 1024);
#pragma unroll
      for (int j = 0; j < 10; ++j) ld128(wg_[j / 5][j % 5], lane * 16, sws, pwb + (20 + 6 * w + gstep(j / 5)) * 5120 + (j % 5) * 1024);
    };
    if (idx < 16) {
      const int g = gc, hd = w & 3;
      constexpr int par = RB ? 1 : 0;
      using K = P1<true, RB>;
      K k;
      const int q0 = RB ? 12 + 2 * (w - 4) : 3 * w;
      k.xd = K::XB + q0 * 1024; k.wd0 = w * 2048;
      asm volatile("" : "+s"(k.xd), "+s"(k.wd0));
#pragma unroll
      for (int i = 0; i < K::NXP; ++i) {
        const int rho = 8 * (q0 + i) + (lane >> 3);
        const int pp = rho >= 80 ? 1 : 0, u = rho - 80 * pp;
        const int tok = 128 * a - 32 + 2 * u + pp;
        const unsigned v = tok * 1280 + (((lane & 7) ^ ((rho >> 1) & 7)) << 4);
        k.xv[i] = (u >= 9 && tok >= 0) ? v : OOB;
      }
#pragma unroll
      for (int j = 0; j < 2; ++j) {
        const unsigned I = (2 * w + j) * 64 + lane;
        const unsigned kb = (I >= 248) + (I >= 488) + (I >= 736);
        const unsigned i = I - 240 * kb - 8 * ((kb + 1) >> 1);
        const unsigned r = (i * 2185u) >> 16, rem = i - r * 30;
        const unsigned seg = (rem * 6554u) >> 16, ch = rem - seg * 10;
        const unsigned v = (8 * kb + r) * 3840 + seg * 1280 + g * 160 + ch * 16;
        k.wv[j] = i < 240 ? v : OOB;
      }
      asm volatile("" : "+v"(k.xv[0]), "+v"(k.xv[1]), "+v"(k.xv[K::NXP - 1]), "+v"(k.wv[0]), "+v"(k.wv[1]));
      bind();
      STAMP(0);
      k.srdx = mk_srd(x, 2048 * 1280);
      k.srdw = mk_srd(wqkv, 640 * 3840);
      unsigned gv0, gv1;
      asm volatile("global_load_dwordx2 %0, %1, %2 sc1" : "=v"(ev) : "v"(WS_EPOCH), "s"(ws) : "memory");
      asm volatile("buffer_load_ushort %0, %2, %3, 0 offen\n\tbuffer_load_ushort %1, %2, %3, 0 offen offset:1024"
                   : "=&v"(gv0), "=&v"(gv1) : "v"(tid * 2), "s"(mk_srd(gw, 1280)) : "memory");
      STAMP(11);
      k.stage0(rot);
      BAR();
      k.stage1();
      k.xa0 = (K::XB + par * 5 * 2048 + c * 128 + ((h ^ (c >> 1)) << 4)) ^ ((rot & 1) << 6);
      const unsigned kbo = h * 3840 + ((h + 1) >> 1) * 128;
      k.wa0 = kbo + q4 * 480 + (20 * hd + 4 * p4) * 2;
      const bool isgate = p4 == 3;
      k.gmask = isgate ? ~0u : 0u;
      asm volatile("" : "+v"(k.gmask));
      k.wa1 = isgate ? GATE_OFF + (8 * h + q4) * 8 : kbo + q4 * 480 + p4 * 160 + (20 * hd + 16) * 2;
      k.wa2 = k.wa1 + (isgate ? 32 : 1920);
      f32x4 ba, bb, ba1;
#pragma unroll
      for (int r = 0; r < 4; ++r) {
        const int kap = 4 * h + r;
        ba[r] = kap >= c + 9 ? 0.f : -1e30f;
        bb[r] = (kap <= c && kap >= c - 7) ? 0.f : -1e30f;
        ba1[r] = a == 0 ? -1e30f : ba[r];
      }
      unsigned m0mask = h == 0 ? 0xffffffffu : 0u;
      unsigned scw = w * 15360 + 10240 + c * 32 + h * 8, sck = w * 15360 + 10240 + c * 32 + 8,
               sct = w * 15360 + 10240 + (4 * h + q4) * 32 + p4 * 8;
      unsigned ovo = WS_O + (h & 1) * (2 * 20480) + (2 * g + (hd >> 1)) * 1024 + (2 * (hd & 1) + (h >> 1)) * 256 + c * 16;
      unsigned ovt = h == 2 ? WS_O + (32 + g) * 512 + hd * 128 + c * 8 : OOB;
      asm volatile("" : "+v"(ba), "+v"(bb), "+v"(ba1), "+v"(m0mask), "+v"(scw), "+v"(sck), "+v"(sct), "+v"(ovo), "+v"(ovt));
      asm volatile("" : "+v"(k.xa0), "+v"(k.wa0));
      k.zero();
      k.stage2();
      STAMP(12);
      k.template run<0, RB ? 10 : 0>([&]() {
        asm volatile("s_waitcnt vmcnt(%2)" : "+v"(gv0), "+v"(gv1) : "i"(K::VPRO) : "memory");
        u32x2 v; v.x = gv0; v.y = 0;
        *(LAS u32x2*)(GATE_OFF + tid * 8) = v;
        if (!RB && tid < 128) { v.x = gv1; *(LAS u32x2*)(GATE_OFF + 4096 + tid * 8) = v; }
        make_tag(std::integral_constant<int, K::VPRO>{});
        WAITL();
        BAR();
        STAMP(1);
      }, [&](int t) { if (RB) side_b(t); }, tail, [](int) {});
      STAMP(2);
      u32x2 ql[5], kl[5], t2[5], vl[5], tt[5], k4[5];
      unsigned sb16[4];
#pragma unroll
      for (int i = 0; i < 5; ++i) { t2[i] = pk4(k.acc[i][2]); *(LAS u32x2*)(scw + i * 512) = t2[i]; }
#pragma unroll
      for (int i = 0; i < 5; ++i) { if (i) ql[i] = pk4(k.acc[i][0]); kl[i] = pk4(k.acc[i][1]); }
      WAITL();
#pragma unroll
      for (int i = 0; i < 5; ++i) k4[i] = *(const LAS u32x2*)(sck + i * 512);
#pragma unroll
      for (int i = 0; i < 4; ++i) sb16[i] = *(const LAS unsigned*)(sck + (i + 1) * 512 + 16);
#pragma unroll
      for (int i = 0; i < 5; ++i) tt[i] = ldstr(sct + i * 512);
#pragma unroll
      for (int i = 0; i < 5; ++i) vl[i] = pk4(k.acc[i][3]);
      constexpr float SC = 0.22360679774997896f * LOG2E;
      f32x4 sa[4], sb[4];
#pragma unroll
      for (int i = 1; i < 5; ++i) {
        u32x4 qf; qf.x = ql[i].x; qf.y = ql[i].y; qf.z = t2[i].x & m0mask; qf.w = t2[i].y & m0mask;
        sb[i - 1] = mfma(cat(kl[i], k4[i]), qf, bb);
        sa[i - 1] = mfma(cat(kl[i - 1], k4[i - 1]), qf, i == 1 ? ba1 : ba);
      }
      asm volatile("s_nop 7\n\ts_nop 0" : "+v"(sa[0]), "+v"(sa[1]), "+v"(sa[2]), "+v"(sa[3]), "+v"(sb[0]), "+v"(sb[1]), "+v"(sb[2]), "+v"(sb[3]));
      float mx[4], es[4];
#pragma unroll
      for (int i = 0; i < 4; ++i)
        mx[i] = max3(max3(sb[i].x, sb[i].y, sb[i].z), max3(sb[i].w, sa[i].x, sa[i].y), max2(sa[i].z, sa[i].w));
#pragma unroll
      for (int i = 0; i < 4; ++i) {
        auto r = __builtin_amdgcn_permlane16_swap(f2u(mx[i]), f2u(mx[i]), false, false);
        mx[i] = max2(u2f(r[0]), u2f(r[1]));
      }
#pragma unroll
      for (int i = 0; i < 4; ++i) {
        auto r = __builtin_amdgcn_permlane32_swap(f2u(mx[i]), f2u(mx[i]), false, false);
        mx[i] = max2(u2f(r[0]), u2f(r[1])) * SC;
        es[i] = __builtin_amdgcn_exp2f(__builtin_amdgcn_fmed3f(u2f(sb16[i] << 16), -60.f, 60.f) * LOG2E);
      }
      u32x4 pf[4];
#pragma unroll
      for (int i = 0; i < 4; ++i) {
        f32x4 pa, pb;
#pragma unroll
        for (int r = 0; r < 4; ++r) {
          pa[r] = __builtin_amdgcn_exp2f(sa[i][r] * SC - mx[i]);
          pb[r] = __builtin_amdgcn_exp2f(sb[i][r] * SC - mx[i]);
        }
        pf[i] = cat(pk4(pa), pk4(pb));
      }
      const f32x4 z = {0.f, 0.f, 0.f, 0.f};
      const u32x4 ones = {0x3f803f80u, 0x3f803f80u, 0x3f803f80u, 0x3f803f80u};
      f32x4 ls[4], oa[4], ob[4];
#pragma unroll
      for (int i = 0; i < 4; ++i) {
        ls[i] = mfma(ones, pf[i], z);
        oa[i] = mfma(cat(vl[i], vl[i + 1]), pf[i], z);
        ob[i] = mfma(cat(tt[i], tt[i + 1]), pf[i], z);
      }
      const unsigned mt0 = 8 * a + par;
      u32x2 op[4];
#pragma unroll
      for (int i = 0; i < 4; ++i) {
        const float f = es[i] * __builtin_amdgcn_rcpf(ls[i].x);
        op[i] = pk4(oa[i] * f);
        __builtin_amdgcn_raw_buffer_store_b64(pk4(ob[i] * f), rws, ovt, (mt0 + 2 * i) * 20480, AUX_SC1);
        if (i & 1) {
          auto sx = __builtin_amdgcn_permlane16_swap(op[i - 1].x, op[i].x, false, false);
          auto sy = __builtin_amdgcn_permlane16_swap(op[i - 1].y, op[i].y, false, false);
          u32x4 o; o.x = sx[0]; o.y = sy[0]; o.z = sx[1]; o.w = sy[1];
          __builtin_amdgcn_raw_buffer_store_b128(o, rws, ovo, (mt0 + 2 * i - 2) * 20480, AUX_SC1);
          STPAD();
        }
      }
      if (g == 0 && hd == 0) {
        const float e = h == 0 ? es[0] : h == 1 ? es[1] : h == 2 ? es[2] : es[3];
        __builtin_amdgcn_raw_buffer_store_b32(f2u(e), rws, WS_G + h * 128 + c * 4, mt0 * 64, AUX_SC1);
      }
    } else {
      const int cj = gc;
      using K = P1<false, RB>;
      K k;
      const int mh = RB ? (w < 6 ? 1 : 0) : (w >> 1);
      const int up = (w >> 1) & 1, kp = w & 1;
#pragma unroll
      for (int i = 0; i < K::NXP; ++i) {
        const int rho = 8 * (4 * (w & 3) + i) + (lane >> 3);
        const int j = rho >> 4;
        const int tok = 128 * a + 32 * (j >> 1) + 2 * (rho & 15) + (j & 1);
        k.xv[i] = tok * 1280 + (((lane & 7) ^ ((rho >> 1) & 7)) << 4);
      }
      k.xd = K::XB + 4 * (w & 3) * 1024;
      {
        const int r = lane >> 3, p8 = lane & 7;
        const int s = ((r >> 1) & 1) + 2 * kp;
        k.wv[0] = (8 * kp + r) * 3072 + cj * 384 + (((((p8 >> 1) ^ s) << 1) | (p8 & 1)) << 4) + (RB ? 0 : 16 * 3072 + 128);
        k.wv[1] = k.wv[0] + 128; k.wv[2] = k.wv[0] + 256; k.wv[3] = k.wv[0] + 16 * 3072;
      }
      k.wd0 = RB ? (kp * 6 + 3 * up) * 1024 : ((kp + 2) * 6 + 3 * up + 1) * 1024;
      k.wd1 = ((kp + 2) * 6 + 3 * up) * 1024;
      if (RB) asm volatile("" : "+v"(k.xv[0]), "+v"(k.xv[1]), "+v"(k.xv[2]), "+v"(k.xv[3]), "+v"(k.wv[2]), "+v"(k.wv[3]));
      asm volatile("" : "+v"(k.wv[0]), "+v"(k.wv[1]));
      bind();
      STAMP(0);
      const u64 pg = wg, pu = wu;
      k.srdx = mk_srd(x, 2048 * 1280);
      k.srdw = mk_srd(up ? pu : pg, 640 * 3072);
      asm volatile("global_load_dwordx2 %0, %1, %2 sc1" : "=v"(ev) : "v"(WS_EPOCH), "s"(ws) : "memory");
      STAMP(11);
      k.stage0(rot);
      BAR();
      if (!RB) pack_issue();
      k.stage1();
      k.xa0 = (K::XB + mh * 4 * 2048 + c * 128 + ((h ^ (c >> 1)) << 4)) ^ ((rot & 1) << 6);
      {
        const int s = ((q4 >> 1) & 1) + 2 * (h & 1);
        k.wa0 = (h * 6 + (RB ? 2 : (w & 1))) * 1024 + q4 * 128 + (((RB ? 2 * (w & 1) : 0) ^ s) << 5) + p4 * 8;
      }
      asm volatile("" : "+v"(k.xa0), "+v"(k.wa0));
      k.zero();
      k.stage2();
      STAMP(12);
      k.template run<0, RB ? 10 : 0>([&]() {
        make_tag(std::integral_constant<int, RB ? K::VPRO : 8>{});
        BAR();
        STAMP(1);
      }, [&](int t) { if (RB) side_b(t); else side_f(t); }, tail, pack_gap);
      STAMP(2);
      constexpr int NK = RB ? 1 : 2;
      float nl2e = -LOG2E;
      asm volatile("" : "+s"(nl2e));
      const unsigned gso = WS_GU + ((8 * a + 4 * mh) * 48 + 6 * cj + (RB ? 4 + (w & 1) : 2 * (w & 1))) * 1024;
#pragma unroll
      for (int m = 0; m < 4; ++m) {
#pragma unroll
        for (int kk = 0; kk < NK; ++kk) {
          float v[8];
#pragma unroll
          for (int j = 0; j < 8; j += 2) {
            const f32x2 g2 = {k.acc[m][2 * kk + (j >> 2)][j & 3], k.acc[m][2 * kk + (j >> 2)][(j & 3) + 1]};
            const f32x2 u2 = {k.acc[m][2 * NK + 2 * kk + (j >> 2)][j & 3], k.acc[m][2 * NK + 2 * kk + (j >> 2)][(j & 3) + 1]};
            const f32x2 a2 = g2 * nl2e;
            const f32x2 d2 = f32x2{__builtin_amdgcn_exp2f(a2.x), __builtin_amdgcn_exp2f(a2.y)} + 1.f;
            const f32x2 r2 = g2 * u2 * f32x2{__builtin_amdgcn_rcpf(d2.x), __builtin_amdgcn_rcpf(d2.y)};
            v[j] = r2.x; v[j + 1] = r2.y;
          }
          u32x4 o; o.x = pk2(v[0], v[1]); o.y = pk2(v[2], v[3]); o.z = pk2(v[4], v[5]); o.w = pk2(v[6], v[7]);
          __builtin_amdgcn_raw_buffer_store_b128(o, rws, lane * 16, gso + m * (48 * 1024) + kk * 1024, AUX_SC1);
          STPAD();
        }
      }
    }
    SB();
    STAMP(13);
    asm volatile("" : "+v"(wo_[0][0]), "+v"(wo_[0][1]), "+v"(wo_[0][2]), "+v"(wo_[0][3]), "+v"(wo_[0][4]),
                      "+v"(wo_[1][0]), "+v"(wo_[1][1]), "+v"(wo_[1][2]), "+v"(wo_[1][3]), "+v"(wo_[1][4]));
    if (RB) asm volatile("" : "+v"(wg_[0][0]), "+v"(wg_[0][1]), "+v"(wg_[0][2]), "+v"(wg_[0][3]), "+v"(wg_[0][4]),
                              "+v"(wg_[1][0]), "+v"(wg_[1][1]), "+v"(wg_[1][2]), "+v"(wg_[1][3]), "+v"(wg_[1][4]));
    SB();
    STAMP(14);
    if (RB && idx >= 16) { if (PRE > 0) cell_dma(w - 4, 1); if (PRE > 1) cell_dma(w, 1); if (PRE > 2) cell_dma(w - 4, 2); WAITV(5 * PRE); }
    else if (RB || !PREA) WAITV(0);
    else { late_w(); WAITV(15); }
    STAMP(3);
    WAITL();
    BAR();
    if (!RB && w == 0 && lane < 2) {
      GAS u64* fp = (GAS u64*)((char*)ws + (lane ? WS_DONE + b * 8 : WS_FLAG + (a * 16 + (idx < 16 ? 8 : 0) + gc) * 8));
      __hip_atomic_store(fp, tag, RLX);
    }
    STAMP(4);

    SB();
    const unsigned fo = WS_FLAG + ((rb >> 1) * 16 + c) * 8;
    auto wait_flags = [&](u32x2& v, bool need) {
#pragma unroll 1
      for (unsigned spin = 0; spin < (1u << 18); ++spin) {
        if (__all(!need || (v.x == (unsigned)tag && v.y == (unsigned)(tag >> 32)))) break;
        ld64(v, fo, sws, 0);
        asm volatile("s_waitcnt vmcnt(0)" : "+v"(v) :: "memory");
      }
      asm volatile("" ::: "memory");
    };
    u32x2 fv;
    if (RB) {
      if (idx < 16) { if (PRE > 0) cell_dma(w - 4, 1); if (PRE > 1) cell_dma(w, 1); if (PRE > 2) cell_dma(w - 4, 2); }
      if (PRE < 1) cell_dma(w - 4, 1); if (PRE < 2) cell_dma(w, 1); if (PRE < 3) cell_dma(w - 4, 2);
      cell_dma(w, 2);
    } else {
      if (!PREA) late_w();
      if (w == 0) {
        ld64(fv, fo, sws, 0);
        asm volatile("s_waitcnt vmcnt(0)" : "+v"(fv) :: "memory");
        wait_flags(fv, c >= 8);
      }
    }
    if (RB) __builtin_amdgcn_s_setprio(1);
    BAR();
    STAMP(5);
    f32x4 acc[4][5];
    u32x4 ag[6][4];
    auto tk = [](int) {};
    tk(0);
    const unsigned gb = (EXP & 1) ? pwb : WS_GU + (4 * rb * 48 + 6 * w) * 1024;
    auto ldg = [&](int s, int m) { ld128(ag[s][m], lane * 16, sws, gb + gstep(s) * 1024 + m * (48 * 1024)); };
    unsigned ca0 = CELL0 + w * 5120 + lane * 16, ca1 = w * 15360 + lane * 16;
    asm volatile("" : "+v"(ca0), "+v"(ca1));
    {
      using S = Sched<RB>;
      constexpr int NO = S::NO, OB = S::OB;
      u32x4 af[2][4], at[4];
      u32x2 tl[4], th[4];
      const unsigned ob = WS_O + 4 * rb * 20480;
      auto req = [&](auto IT) {
        constexpr int i = IT.value;
        if constexpr (i < 8) ld128(af[i >> 2][i & 3], lane * 16, sws, ob + (i & 3) * 20480 + (2 * w + (i >> 2)) * 1024);
        else if constexpr (i < OB) ld64((i & 1) ? th[(i - 8) >> 1] : tl[(i - 8) >> 1], lane * 8, sws, ob + ((i - 8) >> 1) * 20480 + (16 + w) * 1024 + (i & 1) * 512);
        else if constexpr (i < OB + 29 && (i - OB) % 5 < 4) ldg((i - OB) / 5, (i - OB) % 5);
        else if constexpr (i < OB + 29) dma16c(w * 15360, 10240 + ((i - OB) / 5) * 1024, lane * 16, sws, pwb + (20 + 6 * w + gstep(5)) * 5120 + ((i - OB) / 5) * 1024);
        else ld32(i == OB + 29 ? sg0 : i == OB + 30 ? sg1 : sg2, c * 4, sws, WS_G + (4 * rb + min(t0 + i - OB - 29, 19) / 5) * 64);
      };
      if (RB || w != GPOLL || !GPOLL) sfor<0, S::upto(PL - 1)>(req);
      if (RB) { __builtin_amdgcn_s_setprio(0); WAITV(S::upto(PL - 1)); }
      else if (w == GPOLL) { if (GPOLL) { fv.x = ~(unsigned)tag; fv.y = 0; } wait_flags(fv, c < 8); }
      STAMP(6);
      BAR();
      STAMP(7);
      if (!RB && GPOLL && w == GPOLL) sfor<0, S::upto(PL - 1)>(req);
      tk(1);
      u32x4 wc[5];
      sfor<0, NO + 6>([&](auto ST) {
        constexpr int st = ST.value, s = st - NO;
        if constexpr (st == 2 && !RB) {
          asm volatile("s_waitcnt vmcnt(%8)" : "+v"(tl[0]), "+v"(tl[1]), "+v"(tl[2]), "+v"(tl[3]), "+v"(th[0]), "+v"(th[1]), "+v"(th[2]), "+v"(th[3])
                       : "i"(S::issued(st, 0) - 16) : "memory");
#pragma unroll
          for (int m = 0; m < 4; ++m) at[m] = cat(tl[m], th[m]);
          asm volatile("s_nop 1" : "+v"(at[0]), "+v"(at[1]), "+v"(at[2]), "+v"(at[3]));
        }
        sfor<0, 20>([&](auto IT) {
          constexpr int i = IT.value, m = i / 5, n = i % 5, is = S::issued(st, i);
          if constexpr (WGR == 1 ? n == 0 : i % (5 * WGR) == 0) {
            constexpr int ml = m + WGR - 1;
            if constexpr (WGR == 1) {
              if constexpr (s >= 0)      asm volatile("s_waitcnt vmcnt(%1)" : "+v"(ag[s][m]) : "i"(is - S::gpos(s, m) - 1) : "memory");
              else if constexpr (st < 2) asm volatile("s_waitcnt vmcnt(%1)" : "+v"(af[st][m]) : "i"(is - (4 * st + m) - 1) : "memory");
            } else if constexpr (WGR == 2) {
              if constexpr (s >= 0)      asm volatile("s_waitcnt vmcnt(%2)" : "+v"(ag[s][m]), "+v"(ag[s][ml]) : "i"(is - S::gpos(s, ml) - 1) : "memory");
              else if constexpr (st < 2) asm volatile("s_waitcnt vmcnt(%2)" : "+v"(af[st][m]), "+v"(af[st][ml]) : "i"(is - (4 * st + ml) - 1) : "memory");
            } else {
              if constexpr (s >= 0)      asm volatile("s_waitcnt vmcnt(%4)" : "+v"(ag[s][0]), "+v"(ag[s][1]), "+v"(ag[s][2]), "+v"(ag[s][3]) : "i"(is - S::gpos(s, 3) - 1) : "memory");
              else if constexpr (st < 2) asm volatile("s_waitcnt vmcnt(%4)" : "+v"(af[st][0]), "+v"(af[st][1]), "+v"(af[st][2]), "+v"(af[st][3]) : "i"(is - (4 * st + 3) - 1) : "memory");
            }
          }
          if constexpr (st == 0 && i == 0 && !RB)
            asm volatile("" : "+v"(wo_[2][0]), "+v"(wo_[2][1]), "+v"(wo_[2][2]), "+v"(wo_[2][3]), "+v"(wo_[2][4]),
                              "+v"(wg_[0][0]), "+v"(wg_[0][1]), "+v"(wg_[0][2]), "+v"(wg_[0][3]), "+v"(wg_[0][4]),
                              "+v"(wg_[1][0]), "+v"(wg_[1][1]), "+v"(wg_[1][2]), "+v"(wg_[1][3]), "+v"(wg_[1][4]));
          if constexpr (st == 0)     mul0(acc[m][n], wo_[0][n], af[0][m]);
          else if constexpr (s >= 0) mac(acc[m][n], wg_[s & 1][n], ag[s][m]);
          else if constexpr (st < 2) mac(acc[m][n], wo_[1][n], af[1][m]);
          else                       mac(acc[m][n], wo_[2][n], at[m]);
          SB();
          if constexpr (s == 4 && i == 9) WAITV(is - S::gpos(4, 4) - 1);
          if constexpr (m == (s == 4 ? 2 : 0) && s >= 1 && s < 5) wc[n] = lds128((s > 1 ? ca1 + (s - 2) * 5120 : ca0) + n * 1024);
          if constexpr (S::issued(st, i + 1) > is) req(std::integral_constant<int, is>{});
          SB();
        });
        tk(2 + st);
        if constexpr (s >= 1 && s < 5) {
          asm volatile("" : "+v"(wc[0]), "+v"(wc[1]), "+v"(wc[2]), "+v"(wc[3]), "+v"(wc[4]));
#pragma unroll
          for (int n = 0; n < 5; ++n) wg_[(s + 1) & 1][n] = wc[n];
        }
      });
      asm volatile("s_waitcnt vmcnt(0)" : "+v"(sg0), "+v"(sg1), "+v"(sg2) :: "memory");
    }
    MFMA_DONE();
    STAMP(8);
#pragma unroll
    for (int j = 0; j < 10; ++j) {
      const int ta = j < 4 ? 3 * j : j < 6 ? 6 * j - 22 : 2 * j, tb = j < 4 ? ta + 1 : j < 6 ? ta + 3 : ta + 1;
      *(LAS u32x4*)(ca1 + j * 1024) = cat(pk4(acc[ta / 5][ta % 5]), pk4(acc[tb / 5][tb % 5]));
    }
    tk(12);
  };
  if (w < 4) role(std::false_type{}); else role(std::true_type{});

  if (b == 0 && w == 2) {
    GAS u64* dp = (GAS u64*)((char*)ws_ + WS_DONE + lane * 8);
#pragma unroll 1
    for (unsigned spin = 0; spin < (1u << 18); ++spin) {
      bool ok = true;
#pragma unroll
      for (int j = 0; j < 4; ++j) ok &= __hip_atomic_load(dp + j * 64, RLX) == tag;
      if (__all(ok)) break;
      __builtin_amdgcn_s_sleep(2);
    }
    if (lane == 0) __hip_atomic_store((GAS u64*)((char*)ws_ + WS_EPOCH), tag, RLX);
  }
  sg0 = __builtin_amdgcn_rcpf(1.f + sg0); sg1 = __builtin_amdgcn_rcpf(1.f + sg1); sg2 = __builtin_amdgcn_rcpf(1.f + sg2);
  auto ooff = [&](int tl) {
    const int m = tl / 5, n = tl - 5 * m;
    return (unsigned)((64 * rb + 32 * (m >> 1) + 2 * c + (m & 1)) * 1280 + ct * 160 + n * 32 + h * 8);
  };
  unsigned oo0 = ooff(t0), oo1 = ooff(t0 + 1), oo2 = ooff(t0 + 2);
  asm volatile("" : "+v"(sg0), "+v"(sg1), "+v"(sg2), "+v"(oo0), "+v"(oo1), "+v"(oo2));
  WAITL();
  BAR();
  STAMP(9);
  {
    u32x4 p[8];
    u32x2 p2[8];
    unsigned ra = (w < 4 ? w : w + 2) * 1024 + lane * 16, rb_ = ra + 4 * 15360;
    unsigned rc = (4 + (w >> 1)) * 1024 + (w & 1) * 8 + lane * 16, rd = rc + 4 * 15360;
    asm volatile("" : "+v"(ra), "+v"(rb_), "+v"(rc), "+v"(rd));
#pragma unroll
    for (int v = 0; v < 8; ++v) p[v] = lds128((v < 4 ? ra : rb_) + (v & 3) * 15360);
    if (w < 4) {
#pragma unroll
      for (int v = 0; v < 8; ++v) { p2[v] = *(const LAS u32x2*)((v < 4 ? rc : rd) + (v & 3) * 15360); SB(); }
    }
    SB();
#pragma unroll
    for (int j = 0; j < 3; ++j) {
      if (j < 2 || w < 4) {
        f32x4 s = {0.f, 0.f, 0.f, 0.f};
#pragma unroll
        for (int v = 0; v < 8; ++v) {
          const unsigned px = j == 0 ? p[v].x : j == 1 ? p[v].z : p2[v].x, py = j == 0 ? p[v].y : j == 1 ? p[v].w : p2[v].y;
          const f32x4 q = {u2f(px << 16), u2f(px & 0xffff0000u), u2f(py << 16), u2f(py & 0xffff0000u)};
          s = v ? s + q : q;
        }
        const u32x2 ov = pk4(s * (j == 0 ? sg0 : j == 1 ? sg1 : sg2));
        asm volatile("global_store_dwordx2 %0, %1, %2" OUTFL :: "v"(j == 0 ? oo0 : j == 1 ? oo1 : oo2), "v"(ov), "s"(out_) : "memory");
      }
    }
  }
  STAMP(10);
  if (AMP0 >= 0 && (AMPSEL == 0 || (AMPSEL == 1) == (idx < 16))) {
    const u64 d = amp_t1 - amp_t0, t = __builtin_amdgcn_s_memrealtime();
    while (__builtin_amdgcn_s_memrealtime() - t < d) __builtin_amdgcn_s_sleep(1);
  }
}
}

extern "C" void kernel_launch(void* const* d_in, const int* in_sizes, int n_in, void* d_out, int out_size, void* d_ws,
                              size_t ws_size, hipStream_t stream) {
  fused<<<256, 512, 0, stream>>>(d_in[0], d_in[1], d_in[2], d_in[4], d_in[5], d_in[6], d_in[7], d_out, d_ws);
}
```

```cpp
#include <hip/hip_runtime.h>
#include <hip/hip_bf16.h>
#include <stdint.h>
#include <type_traits>

#define STAMPS 0
#define BEARLY 0
#define CODEPF 1
#define TOUCH_NT ""
#define TOUCH 1
#define EXP 0
#define GLA 4
#define PART16 1
#define PL 2
#define PRE 2
#define OUTFL ""
#define PREA 0
#define WGR 4
#define GPOLL 1
#define ROT 4

#define LAS __attribute__((address_space(3)))
#define GAS __attribute__((address_space(1)))
typedef __bf16 bf16x8 __attribute__((ext_vector_type(8)));
typedef __bf16 bf16x2 __attribute__((ext_vector_type(2)));
typedef float f32x4 __attribute__((ext_vector_type(4)));
typedef float f32x2 __attribute__((ext_vector_type(2)));
typedef unsigned u32x4 __attribute__((ext_vector_type(4)));
typedef unsigned u32x2 __attribute__((ext_vector_type(2)));
typedef short s16x4 __attribute__((ext_vector_type(4)));
typedef unsigned long long u64;

namespace {
constexpr int LDS_BYTES = 163840;
constexpr unsigned GATE_OFF = 110592, PACK_OFF = 122880, PKF_OFF = 162816, CELL0 = 122880;
constexpr unsigned OOB = 0x40000000u;
#define CODE_B 40000
constexpr unsigned CODE_BYTES = CODE_B;
constexpr unsigned WS_FLAG = 0;
constexpr unsigned WS_DONE = 4096;
constexpr unsigned WS_PKF = 8192;
constexpr unsigned WS_EPOCH = 12288;
constexpr unsigned WS_G = 16384;
constexpr unsigned WS_O = 131072;
constexpr unsigned WS_GU = WS_O + 128 * 40 * 512;
constexpr unsigned WS_PW = WS_GU + 128 * 48 * 1024;
constexpr unsigned WS_END = WS_PW + 8 * 68 * 5120;
constexpr unsigned WS_STAMP = 16u << 20;
constexpr int AUX_SC1 = 16;
constexpr float LOG2E = 1.4426950408889634f;

__device__ __forceinline__ u32x4 mk_srd(u64 a, unsigned bytes) {
  u32x4 r; r.x = (unsigned)a; r.y = (unsigned)(a >> 32) & 0xffffu; r.z = bytes; r.w = 0x00020000u; return r;
}
__device__ __forceinline__ void dma16(unsigned lds, unsigned imm, unsigned voff, u32x4 srd, unsigned soff) {
  asm volatile("s_add_u32 m0, %0, %4\n\ts_nop 0\n\tbuffer_load_dwordx4 %1, %2, %3 offen lds"
               :: "s"(lds), "v"(voff), "s"(srd), "s"(soff), "i"(imm) : "memory", "scc");
}
__device__ __forceinline__ void dma16c(unsigned lds, unsigned imm, unsigned voff, u32x4 srd, unsigned soff) {
  asm volatile("s_add_u32 m0, %0, %4\n\ts_nop 0\n\tbuffer_load_dwordx4 %1, %2, %3 offen sc1 lds"
               :: "s"(lds), "v"(voff), "s"(srd), "s"(soff), "i"(imm) : "memory", "scc");
}
__device__ __forceinline__ void ld128(u32x4& d, unsigned voff, u32x4 srd, unsigned soff) {
  asm volatile("buffer_load_dwordx4 %0, %1, %2, %3 offen sc1" : "=v"(d) : "v"(voff), "s"(srd), "s"(soff) : "memory");
}
__device__ __forceinline__ void ld64(u32x2& d, unsigned voff, u32x4 srd, unsigned soff) {
  asm volatile("buffer_load_dwordx2 %0, %1, %2, %3 offen sc1" : "=v"(d) : "v"(voff), "s"(srd), "s"(soff) : "memory");
}
__device__ __forceinline__ void ld32(float& d, unsigned voff, u32x4 srd, unsigned soff) {
  asm volatile("buffer_load_dword %0, %1, %2, %3 offen sc1" : "=v"(d) : "v"(voff), "s"(srd), "s"(soff) : "memory");
}
__device__ __forceinline__ u32x4 lds128(unsigned a) { return *(const LAS u32x4*)a; }
__device__ __forceinline__ u32x2 ldstr(unsigned a) {
  return __builtin_bit_cast(u32x2, __builtin_amdgcn_ds_read_tr16_b64_v4i16((LAS s16x4*)a));
}
__device__ __forceinline__ u32x4 cat(u32x2 a, u32x2 b) { u32x4 r; r.x = a.x; r.y = a.y; r.z = b.x; r.w = b.y; return r; }
__device__ __forceinline__ f32x4 mfma(u32x4 a, u32x4 b, f32x4 c) {
  return __builtin_amdgcn_mfma_f32_16x16x32_bf16(__builtin_bit_cast(bf16x8, a), __builtin_bit_cast(bf16x8, b), c, 0, 0, 0);
}
__device__ __forceinline__ void mac(f32x4& c, u32x4 a, u32x4 b) {
  asm volatile("v_mfma_f32_16x16x32_bf16 %0, %1, %2, %0" : "+v"(c) : "v"(a), "v"(b));
}
__device__ __forceinline__ void mul0(f32x4& c, u32x4 a, u32x4 b) {
  asm volatile("v_mfma_f32_16x16x32_bf16 %0, %1, %2, 0" : "=&v"(c) : "v"(a), "v"(b));
}
__device__ __forceinline__ unsigned pk2(float a, float b) {
  f32x2 t = {a, b};
  return __builtin_bit_cast(unsigned, __builtin_convertvector(t, bf16x2));
}
__device__ __forceinline__ u32x2 pk4(f32x4 v) { u32x2 r; r.x = pk2(v.x, v.y); r.y = pk2(v.z, v.w); return r; }
__device__ __forceinline__ float u2f(unsigned u) { return __builtin_bit_cast(float, u); }
__device__ __forceinline__ unsigned f2u(float f) { return __builtin_bit_cast(unsigned, f); }
#define SB() __builtin_amdgcn_sched_barrier(0)
#define WAITV(n) asm volatile("s_waitcnt vmcnt(%0)" :: "i"(n) : "memory")
#define WAITVL(n) asm volatile("s_waitcnt vmcnt(%0) lgkmcnt(0)" :: "i"(n) : "memory")
#define WAITL() asm volatile("s_waitcnt lgkmcnt(0)" ::: "memory")
#define BAR() asm volatile("s_barrier" ::: "memory")
#define STPAD() do { SB(); asm volatile("s_nop 1" ::: "memory"); SB(); } while (0)
#define MFMA_DONE() do { SB(); asm volatile("s_nop 7\n\ts_nop 1" ::: "memory"); SB(); } while (0)
#define RLX __ATOMIC_RELAXED, __HIP_MEMORY_SCOPE_AGENT
__device__ __forceinline__ unsigned rfl(unsigned v) { return __builtin_amdgcn_readfirstlane(v); }
__device__ __forceinline__ unsigned opaque_lane() { unsigned l = threadIdx.x & 63; asm volatile("" : "+v"(l)); return l; }

#define AMP0 -1
#define AMP1 -1
#define AMPSEL 0
#define STAMP(i) do {} while (0)

template <int I, int N, class F>
__device__ __forceinline__ void sfor(F&& f) {
  if constexpr (I < N) { f(std::integral_constant<int, I>{}); sfor<I + 1, N>(f); }
}
template <bool RB> struct Sched {
  static constexpr int NO = RB ? 2 : 3, OB = RB ? 8 : 16;
  static constexpr int gpos(int s, int m) { return OB + 5 * s + m; }
  static constexpr int upto(int st) { return st < 2 ? 4 * (st + 1) : st < NO ? 16 : st < NO + 5 ? OB + 5 * (st - NO + 1) : st == NO + 5 ? OB + 29 : OB + 32; }
  static constexpr int issued(int st, int i) {
    const int base = upto(st + PL - 1), goal = upto(st + PL), every = goal - base > 5 ? 2 : goal - base > 4 ? 4 : 5;
    int n = base;
    for (int k = 0; k < i; ++k) if (k % every == 1 && n < goal) ++n;
    return n;
  }
};
__device__ __forceinline__ float max3(float a, float b, float c) { float r; asm("v_max3_f32 %0, %1, %2, %3" : "=v"(r) : "v"(a), "v"(b), "v"(c)); return r; }
__device__ __forceinline__ float max2(float a, float b) { float r; asm("v_max_f32 %0, %1, %2" : "=v"(r) : "v"(a), "v"(b)); return r; }

template <bool QKV, bool RB>
struct P1 {
  static constexpr int NM = QKV ? 5 : 4;
  static constexpr int NN = QKV ? 4 : (RB ? 4 : 8);
  static constexpr int NWS = 3;
  static constexpr unsigned WSL = QKV ? 16384 : 24576, XB = NWS * WSL, XSL = QKV ? 20480 : 16384;
  static constexpr unsigned WROW = QKV ? 3840 : 3072;
  static constexpr int NWP = QKV ? 2 : (RB ? 4 : 2);
  static constexpr int NXE = QKV ? (RB ? 1 : 2) : (RB ? 2 : 0), NXO = QKV ? 1 : NXE;
  static constexpr int NXP = NXO + NXE;
  static constexpr int OO = NWP + NXO, OE = NWP + NXE;
  static constexpr bool HAND = !QKV;
  static constexpr int ht = 4;
  f32x4 acc[NM][NN];
  u32x4 srdx, srdw;
  unsigned xv[4], wv[4];
  unsigned xd;
  unsigned wd0, wd1;
  unsigned xa0, wa0, wa1, wa2;
  unsigned gmask;
  __device__ __forceinline__ void tick(int) {}

  unsigned sow, sox, gof;
  unsigned kg, kw, kx, kr;
  template <bool ODD, bool X = true>
  __device__ __forceinline__ void loads(unsigned wwr, unsigned xwr) {
    if (X && NXP) {
#pragma unroll
      for (int i = ODD ? NXE : 0; i < (ODD ? NXP : NXE); ++i) dma16(xwr + xd, i * 1024, xv[i], srdx, sox);
      if (ODD) { kx += 1; sox = (kx ^ ((kx < 8 ? kg : kg & 3) >> 1)) * 128; }
    }
    dma16(wwr + wd0, 0, wv[0], srdw, sow);
    dma16(wwr + wd0, 1024, wv[1], srdw, sow);
    if (!QKV && RB) {
      dma16(wwr + wd0, 2048, wv[2], srdw, sow);
      dma16(wwr + wd1, 0, wv[3], srdw, sow);
    }
  }
  __device__ __forceinline__ void other(unsigned wwr) {
    dma16(wwr + wd1, 1024, wv[3], srdw, sow + 128);
    dma16(wwr + wd1, 2048, wv[3], srdw, sow + 256);
  }
  __device__ __forceinline__ void next() { kw += 1; sow = (kw ^ (kw < 16 ? kg : kg & 3)) * (32 * WROW); asm volatile("" : "+s"(sow), "+s"(kw)); if (NXP) asm volatile("" : "+s"(sox), "+s"(kx)); }
  __device__ __forceinline__ void stage0(int rot) {
    kg = rot; kw = 0; kx = 0; kr = 0; sow = kg * (32 * WROW); sox = (kg >> 1) * 128; gof = kg * 256;
    if (HAND && !RB) { kw = 3; sow = (3 ^ kg) * (32 * WROW); return; }
#pragma unroll
    for (int i = 0; i < NXE; ++i) dma16(xd, i * 1024, xv[i], srdx, sox);
    loads<true>(0, 0); if (HAND) other(0); next();
  }
  __device__ __forceinline__ void stage1() { if (HAND && !RB) return; loads<false>(WSL, XSL); if (HAND) other(WSL); next(); }
  __device__ __forceinline__ void stage2() { if (HAND && !RB) return; loads<true>(2 * WSL, XSL); if (HAND) other(2 * WSL); next(); }
  static constexpr int VPRO = OE + OO + (HAND ? 4 : 0);
  struct Ad { unsigned x, w[4]; };
  __device__ __forceinline__ void addr(Ad& a, unsigned xb, unsigned wrd) {
    a.x = xb; a.w[0] = wa0 + wrd;
    if (QKV) {
      const unsigned s2 = (gmask & (gof - wrd)) + wrd;
      kr += 1; gof = (kr ^ (kr < 16 ? kg : kg & 3)) * 256;
      a.w[1] = wa1 + s2; a.w[2] = wa2 + s2;
      asm volatile("" : "+v"(a.x), "+v"(a.w[0]), "+v"(a.w[1]), "+v"(a.w[2]));
    } else if (RB) {
      a.w[1] = a.w[0] ^ 32;
      asm volatile("" : "+v"(a.x), "+v"(a.w[0]), "+v"(a.w[1]));
    } else {
      a.w[1] = a.w[0] ^ 32; a.w[2] = a.w[0] ^ 64; a.w[3] = a.w[0] ^ 96;
      asm volatile("" : "+v"(a.x), "+v"(a.w[0]), "+v"(a.w[1]), "+v"(a.w[2]), "+v"(a.w[3]));
    }
  }
  __device__ __forceinline__ unsigned wad(const Ad& a, int n, int hi) {
    if (QKV) return n == 2 ? a.w[1 + hi] : a.w[0] + (n == 0 ? 0 : n == 1 ? 160 : 320) + hi * 1920;
    if (RB) return a.w[n & 1] + (n >> 1) * 3072 + hi * 512;
    return a.w[n & 3] + (n >> 2) * 3072 + hi * 512;
  }
  __device__ __forceinline__ void rd_all(u32x4 (&xf)[NM], u32x4 (&wf)[NN], const Ad& a) {
#pragma unroll
    for (int n = 0; n < NN; ++n) wf[n] = cat(ldstr(wad(a, n, 0)), ldstr(wad(a, n, 1)));
#pragma unroll
    for (int m = 0; m < NM; ++m) xf[m] = lds128(a.x + m * 2048);
  }
  template <bool RD = true, class L>
  __device__ __forceinline__ void mm_rd(const u32x4 (&xc)[NM], const u32x4 (&wc)[NN], u32x4 (&xn)[NM], u32x4 (&wn)[NN], const Ad& a, L&& late) {
    u32x2 lo[NN];
    constexpr bool NMAJ = !QKV && !RB;
    constexpr bool EARLY = !QKV && RB && BEARLY;
    if (EARLY && RD) { rd_all(xn, wn, a); SB(); }
    int r = 0;
#pragma unroll
    for (int i = 0; i < NM * NN; ++i) {
      const int m = NMAJ ? i % NM : i / NN, n = NMAJ ? i / NM : i % NN;
      if (QKV && m == 0 && n == 0) continue;
      if (QKV && n == 3) mac(acc[m][3], xc[m], wc[3]);
      else               mac(acc[m][n], wc[n], xc[m]);
      SB();
      if (!RD || EARLY) continue;
      if (NMAJ) {
        if (i < 4) xn[i] = lds128(a.x + i * 2048);
        else if ((i & 3) < 2) {
          const int k = (i >> 2) - 1;
          if (i & 1) wn[k] = cat(lo[k], ldstr(wad(a, k, 1))); else lo[k] = ldstr(wad(a, k, 0));
        } else late(i);
      } else {
        if (r < 2 * NN) {
          const int k = r >> 1;
          if (r & 1) wn[k] = cat(lo[k], ldstr(wad(a, k, 1))); else lo[k] = ldstr(wad(a, k, 0));
        } else if (r < 2 * NN + NM) {
          xn[r - 2 * NN] = lds128(a.x + (r - 2 * NN) * 2048);
        }
        else late(r - 2 * NN - NM);
        ++r;
      }
      SB();
    }
    if (NMAJ && RD) { wn[NN - 1] = cat(ldstr(wad(a, NN - 1, 0)), ldstr(wad(a, NN - 1, 1))); SB(); }
  }
  __device__ __forceinline__ void pin(u32x4 (&xf)[NM], u32x4 (&wf)[NN]) {
    asm volatile("" : "+v"(xf[0]), "+v"(xf[1]), "+v"(xf[2]), "+v"(xf[3]), "+v"(wf[0]), "+v"(wf[1]), "+v"(wf[2]), "+v"(wf[3]));
    if (NM == 5) asm volatile("" : "+v"(xf[NM - 1]));
    if (NN == 8) asm volatile("" : "+v"(wf[NN - 4]), "+v"(wf[NN - 3]), "+v"(wf[NN - 2]), "+v"(wf[NN - 1]));
  }
  __device__ __forceinline__ void zero() {
#pragma unroll
    for (int m = 0; m < NM; ++m)
#pragma unroll
      for (int n = 0; n < NN; ++n) { acc[m][n] = f32x4{0.f, 0.f, 0.f, 0.f}; asm volatile("" : "+v"(acc[m][n])); }
  }
  template <int TL0, int TL1, class F, class G, class H, class U>
  __device__ __forceinline__ void run(F&& pre, G&& extra, H&& tail, U&& gap) {
    pre();
    u32x4 xA[NM], wA[NN], xB[NM], wB[NN];
    Ad a;
    addr(a, xa0, 0);
    rd_all(xA, wA, a);
    if (!RB) __builtin_amdgcn_s_setprio(1);
    unsigned w0 = 0, w1 = WSL, w2 = 2 * WSL;
    unsigned xp = 2 * XSL, x0 = 0, x1 = XSL;
    int t = 0;
    unsigned nw1, nw2, nx1;
    auto adv0 = [&]() { nw1 = w2 + WSL; if (nw1 == NWS * WSL) nw1 = 0; asm volatile("" : "+s"(nw1)); };
    auto adv1 = [&]() { nw2 = nw1 + WSL; if (nw2 == NWS * WSL) nw2 = 0; asm volatile("" : "+s"(nw2)); };
    auto adv2 = [&]() { nx1 = x1 + XSL; if (nx1 == 3 * XSL) nx1 = 0; asm volatile("" : "+s"(nx1)); };
    auto adv3 = [&]() {
      w0 = w2; w1 = nw1; w2 = nw2; xp = x0; x0 = x1; x1 = nx1; t += 2;
      asm volatile("" : "+s"(w0), "+s"(w1), "+s"(w2), "+s"(xp), "+s"(x0), "+s"(x1), "+s"(t));
    };
    auto adv = [&]() { adv0(); adv1(); adv2(); adv3(); };
    asm volatile("" : "+s"(w0), "+s"(w1), "+s"(w2), "+s"(xp), "+s"(x0), "+s"(x1), "+s"(t));
    if (QKV) asm volatile("" : "+s"(gof), "+s"(kr));
    auto none = [](int) {};
    if (RB) {
      auto help = [&](int tt, unsigned wwr) { if (HAND && __builtin_expect(tt < ht, 0)) other(wwr); extra(tt); };
      pin(xA, wA);
      SB(); WAITVL(OO + (HAND ? 2 : 0)); tick(0); BAR(); tick(1); SB();
      loads<false>(w0, xp); help(0, w0); next(); addr(a, (xa0 ^ 64) + x0, w1); SB();
      auto half = [&]() {
        mm_rd(xA, wA, xB, wB, a, [&](int i) {
          if (QKV && i == 0) adv0();
          if (QKV && i == 2) adv1();
          if (QKV && i == 4) adv2();
        });
        pin(xB, wB);
        SB(); WAITVL(OE); tick(2 * t + 2); BAR(); tick(2 * t + 3); SB();
        loads<true>(w1, xp); help(t + 1, w1); next();
        addr(a, xa0 + x1, w2);
        if (QKV) adv3(); else adv();
        SB();
        mm_rd(xB, wB, xA, wA, a, none);
      };
#pragma unroll 1
      do {
        half();
        pin(xA, wA);
        SB(); WAITVL(OO); tick(2 * t); BAR(); tick(2 * t + 1); SB();
        loads<false>(w0, xp); help(t, w0); next(); addr(a, (xa0 ^ 64) + x0, w1); SB();
        if (QKV) asm volatile("" : "+s"(gof), "+s"(kr));
      } while (t < 14);
      half();
    } else {
      addr(a, (xa0 ^ 64) + x0, w1);
      auto trip = [&](auto EARLY) {
        constexpr bool E = EARLY.value;
        pin(xA, wA);
        SB(); if (E) WAITL(); else WAITVL(OO);
        tick(2 * t); BAR(); tick(2 * t + 1); SB();
        mm_rd(xA, wA, xB, wB, a, [&](int i) { if (E) gap(i); });
        if (E) { if (t == 0) WAITV(0); else extra(t); }
        if (!E || t >= ht) loads<false>(w0, xp);
        next(); addr(a, xa0 + x1, w2); adv0(); adv1(); adv2(); SB();
        pin(xB, wB);
        SB(); if (E) WAITL(); else WAITVL(OE);
        tick(2 * t + 2); BAR(); tick(2 * t + 3); SB();
        mm_rd(xB, wB, xA, wA, a, [&](int i) { if (E) gap(i); });
        if (E && t < ht) extra(t + 1); else loads<true>(w1, xp);
        next(); adv3(); addr(a, (xa0 ^ 64) + x0, w1); SB();
        if (QKV) asm volatile("" : "+s"(gof), "+s"(kr));
      };
      if (HAND) {
#pragma unroll 1
        do trip(std::true_type{}); while (t < ht + 2);
      }
#pragma unroll 1
      do trip(std::false_type{}); while (t < 16);
    }
    constexpr unsigned S16 = (16 % NWS) * WSL, S17 = (17 % NWS) * WSL, S18 = (18 % NWS) * WSL, S19 = (19 % NWS) * WSL, X8 = 2 * XSL, X9 = 0;
    pin(xA, wA);
    SB(); WAITVL(OO);
    tick(32); BAR(); tick(33); SB();
    if (RB) { loads<false, false>(S16, 0); tail(0); addr(a, (xa0 ^ 64) + X8, S17); SB(); mm_rd(xA, wA, xB, wB, a, none); }
    else    { mm_rd(xA, wA, xB, wB, a, none); loads<false, false>(S16, 0); tail(0); addr(a, xa0 + X9, S18); SB(); }
    pin(xB, wB);
    SB(); WAITVL(NWP + TL0); tick(34); BAR(); tick(35); SB();
    if (RB) { tail(1); addr(a, xa0 + X9, S18); SB(); mm_rd(xB, wB, xA, wA, a, none); }
    else    { mm_rd(xB, wB, xA, wA, a, none); tail(1); addr(a, (xa0 ^ 64) + X9, S19); SB(); }
    pin(xA, wA);
    SB(); WAITVL(TL0 + TL1); tick(36); BAR(); tick(37); SB();
    if (RB) { tail(2); addr(a, (xa0 ^ 64) + X9, S19); SB(); mm_rd(xA, wA, xB, wB, a, none); }
    else    { mm_rd(xA, wA, xB, wB, a, none); tail(2); SB(); }
    pin(xB, wB);
    SB(); WAITL(); tick(38); BAR(); tick(39); SB();
    if (RB) { tail(3); SB(); mm_rd<false>(xB, wB, xA, wA, a, none); }
    else    { mm_rd<false>(xB, wB, xA, wA, a, none); tail(3); SB(); }
    if (!RB) __builtin_amdgcn_s_setprio(0);
    MFMA_DONE();
    tick(40);
  }
};

__global__ __launch_bounds__(512, 2) void fused(const void* x_, const void* wqkv_, const void* wo_p, const void* wg_p,
                                                const void* wu_p, const void* wd_p, const void* gw_, void* out_, void* ws_) {
  __shared__ __attribute__((aligned(16))) char smem[LDS_BYTES];
  asm volatile("" :: "s"((unsigned)(size_t)(LAS char*)smem));
  const int tid = threadIdx.x, lane = tid & 63;
  const int w = rfl(tid >> 6);
  const int h = lane >> 4, c = lane & 15, q4 = (lane & 15) >> 2, p4 = lane & 3;
  const int b = blockIdx.x, xcd = b & 7, idx = b >> 3;
  const int rh = xcd & 1, cq = xcd >> 1;
  const int a = 8 * rh + (idx & 7);
  const int gc = 2 * cq + ((idx >> 3) & 1);
  const int rb = 4 * xcd + (idx >> 3), ct = idx & 7;
  u64 x, wqkv, wo, wg, wu, wd, gw;
  char* ws;
  __amdgpu_buffer_rsrc_t rws;
  u32x4 sws;
  auto bind = [&]() {
    x = (u64)x_; wqkv = (u64)wqkv_; wo = (u64)wo_p; wg = (u64)wg_p; wu = (u64)wu_p; wd = (u64)wd_p; gw = (u64)gw_;
    u64 pw = (u64)ws_;
    asm volatile("" : "+s"(x), "+s"(wqkv), "+s"(wo), "+s"(wg), "+s"(wu), "+s"(wd), "+s"(gw), "+s"(pw));
    ws = (char*)pw;
    rws = __builtin_amdgcn_make_buffer_rsrc(ws, 0, WS_END, 0x00020000);
    sws = mk_srd(pw, WS_END);
  };
  u64 amp_t0 = 0, amp_t1 = 0;

  if (CODEPF) {
    u64 pc;
    asm volatile("s_getpc_b64 %0" : "=s"(pc));
    asm volatile("s_mov_b32 m0, %0\n\ts_nop 0\n\tbuffer_load_dword %1, %2, 0 offen lds"
                 :: "s"(PKF_OFF), "v"(lane < 6 ? ((idx & 7) * 48 + w * 6 + lane) * 128 : OOB), "s"(mk_srd(pc & ~127ull, CODE_BYTES)) : "memory");
  }
  u32x2 ev;
  u64 tag = 0;
  auto make_tag = [&](auto NV) {
    unsigned lo, hi;
    asm volatile("s_waitcnt vmcnt(%4)\n\tv_readfirstlane_b32 %0, %2\n\tv_readfirstlane_b32 %1, %3"
                 : "=s"(lo), "=s"(hi) : "v"(ev.x), "v"(ev.y), "i"(NV.value) : "memory");
    const u64 e = ((u64)hi << 32) | lo;
    tag = (e ^ 0x9E3779B97F4A7C15ull) * 0xBF58476D1CE4E5B9ull + 0x632BE59BD9B4E019ull;
    if (tag == e) tag += 1;
  };
  const unsigned pwb = WS_PW + ct * (68 * 5120);
  const int grot = 3 * ((idx >> 3) & 1);
  auto gstep = [&](int s) { const int v = s + grot; return v >= 6 ? v - 6 : v; };
  auto cell_dma = [&](int v, int cell) {
    const unsigned so = pwb + (20 + 6 * v + gstep(2 + cell)) * 5120;
#pragma unroll
    for (int j = 0; j < 5; ++j) dma16c(cell ? v * 15360 : CELL0 + v * 5120, (cell ? cell - 1 : 0) * 5120 + j * 1024, lane * 16, sws, so + j * 1024);
  };
  auto ldw = [&](u32x4 (&d)[5], unsigned so) {
#pragma unroll
    for (int n = 0; n < 5; ++n) d[n] = __builtin_amdgcn_raw_buffer_load_b128(rws, lane * 16, so + n * 1024, AUX_SC1);
  };
  const unsigned pscr = PACK_OFF + (w & 3) * 8192;
  const int pu0 = (b - 128) * 4 + w;
  unsigned pra[2], prb[2], pn[2], pb[2];
  auto pack_issue = [&]() {
    const u64 po = wo, pd = wd;
#pragma unroll
    for (int r = 0; r < 2; ++r) {
      const int u = pu0 + 512 * r;
      const int ks = u / 10, cg = u - 10 * ks;
      const unsigned r8 = lane >> 3;
      const u32x4 srd = mk_srd(ks < 20 ? po : pd, ks < 20 ? 640 * 1280 : 1536 * 1280);
      const unsigned base = ks < 16 ? 40 * ks : ks < 20 ? 160 * (ks - 16) + 16 : 32 * (ks - 20);
      const unsigned vr = (ks >= 16 && ks < 20) ? 20 * (r8 >> 2) + (r8 & 3) : r8;
      const unsigned vo = vr * 1280 + (lane & 7) * 16;
#pragma unroll
      for (int p = 0; p < 4; ++p) {
        const unsigned rp = ks < 16 ? 20 * (p >> 1) + 8 * (p & 1) : ks < 20 ? 40 * p : 8 * p;
        dma16(pscr, r * 4096 + p * 1024, vo, srd, u < 680 ? (base + rp) * 1280 + cg * 128 : OOB);
      }
      pra[r] = pscr + r * 4096 + (h * (ks < 16 ? 8 : 4) + q4) * 128 + p4 * 8 - 32;
      prb[r] = pra[r] + (ks < 16 ? 512 : 2048);
      pn[r] = 4 * cg - 1; pb[r] = u < 680 ? WS_PW + ks * 5120 + (4 * cg - 1) * 1024 : OOB;
    }
    asm volatile("" : "+v"(pra[0]), "+v"(prb[0]), "+v"(pra[1]), "+v"(prb[1]));
    asm volatile("" : "+s"(pn[0]), "+s"(pb[0]), "+s"(pn[1]), "+s"(pb[1]));
  };
  const unsigned smask = w != 4 ? 0u : idx >= 16 ? 0x1040u : 0x1000u;
  auto side_b = [&](int t) {
    if (__builtin_expect(!((smask >> t) & 1), 1)) return;
    if (t == 6) {
      u32x2 tv; tv.x = (unsigned)tag; tv.y = (unsigned)(tag >> 32);
      __builtin_amdgcn_raw_buffer_store_b64(tv, rws, opaque_lane() == 0 ? WS_PKF + (b - 128) * 8 : OOB, 0, AUX_SC1);
    } else {
      dma16c(PKF_OFF, 0, opaque_lane() * 16, sws, WS_PKF);
    }
  };
  u32x2 pq[4];
  unsigned ps0, ps1;
  auto pack_gap = [&](int i) {
    if (i == 6)  pq[0] = ldstr(pra[0]);
    if (i == 7)  pq[1] = ldstr(prb[0]);
    if (i == 10) pq[2] = ldstr(pra[1]);
    if (i == 11) pq[3] = ldstr(prb[1]);
    if (i == 14) { pra[0] += 32; prb[0] += 32; asm volatile("" : "+v"(pra[0]), "+v"(prb[0])); }
    if (i == 15) { pra[1] += 32; prb[1] += 32; asm volatile("" : "+v"(pra[1]), "+v"(prb[1])); }
    if (i == 18) { ps0 = (pn[0] * 205) >> 10; asm volatile("" : "+s"(ps0)); }
    if (i == 19) { ps0 = ps0 * (68 * 5120 - 5120) + pb[0]; asm volatile("" : "+s"(ps0)); }
    if (i == 22) { ps1 = (pn[1] * 205) >> 10; asm volatile("" : "+s"(ps1)); }
    if (i == 23) { ps1 = ps1 * (68 * 5120 - 5120) + pb[1]; asm volatile("" : "+s"(ps1)); }
    if (i == 26) { pn[0] += 1; pb[0] += 1024; asm volatile("" : "+s"(pn[0]), "+s"(pb[0])); }
    if (i == 27) { pn[1] += 1; pb[1] += 1024; asm volatile("" : "+s"(pn[1]), "+s"(pb[1])); }
  };
  auto side_f = [&](int) {
    __builtin_amdgcn_raw_buffer_store_b128(cat(pq[0], pq[1]), rws, lane * 16, ps0, AUX_SC1);
    STPAD();
    __builtin_amdgcn_raw_buffer_store_b128(cat(pq[2], pq[3]), rws, lane * 16, ps1, AUX_SC1);
    STPAD();
  };
  auto pk_check = [&]() {
    const u32x4 p0 = lds128(PKF_OFF + lane * 16);
    const unsigned lo = (unsigned)tag, hi = (unsigned)(tag >> 32);
    if (!__all(p0.x == lo && p0.z == lo && p0.y == hi && p0.w == hi)) {
      GAS u64* fp = (GAS u64*)((char*)ws + WS_PKF + opaque_lane() * 8);
#pragma unroll 1
      for (unsigned spin = 0; spin < (1u << 18); ++spin) {
        bool ok = true;
#pragma unroll
        for (int j = 0; j < 2; ++j) ok &= __hip_atomic_load(fp + 64 * j, RLX) == tag;
        if (__all(ok)) break;
        __builtin_amdgcn_s_sleep(2);
      }
    }
    asm volatile("" ::: "memory");
  };
  const int rot = (a + 4 * ((idx >> 3) & 1) + 2 * (idx >> 4)) & 7;

  const int t0 = 2 * w + min(w, 4);
  float sg0, sg1, sg2;
  auto role = [&](auto RBT) {
    constexpr bool RB = decltype(RBT)::value;
    u32x4 wo_[3][5], wg_[2][5];
    auto tail = [&](int i) {
      if (i == 0) pk_check();
      if (RB) {
        if (i == 1) { ldw(wo_[0], pwb + 2 * w * 5120); ldw(wo_[1], pwb + (2 * w + 1) * 5120); }
        if (i >= 2) ldw(wg_[i - 2], pwb + (20 + 6 * w + gstep(i - 2)) * 5120);
        if (i == 2) cell_dma(w - 4, 0);
        if (i == 3) cell_dma(w, 0);
      } else if (i >= 2) {
        ldw(wo_[i - 2], pwb + (2 * w + i - 2) * 5120);
      }
    };
    auto late_w = [&]() {
#pragma unroll
      for (int n = 0; n < 5; ++n) ld128(wo_[2][n], lane * 16, sws, pwb + (16 + w) * 5120 + n * 1024);
#pragma unroll
      for (int j = 0; j < 10; ++j) ld128(wg_[j / 5][j % 5], lane * 16, sws, pwb + (20 + 6 * w + gstep(j / 5)) * 5120 + (j % 5) * 1024);
    };
    if (idx < 16) {
      const int g = gc, hd = w & 3;
      constexpr int par = RB ? 1 : 0;
      using K = P1<true, RB>;
      K k;
      const int q0 = RB ? 12 + 2 * (w - 4) : 3 * w;
      k.xd = K::XB + q0 * 1024; k.wd0 = w * 2048;
      asm volatile("" : "+s"(k.xd), "+s"(k.wd0));
#pragma unroll
      for (int i = 0; i < K::NXP; ++i) {
        const int rho = 8 * (q0 + i) + (lane >> 3);
        const int pp = rho >= 80 ? 1 : 0, u = rho - 80 * pp;
        const int tok = 128 * a - 32 + 2 * u + pp;
        const unsigned v = tok * 1280 + (((lane & 7) ^ ((rho >> 1) & 7)) << 4);
        k.xv[i] = (u >= 9 && tok >= 0) ? v : OOB;
      }
#pragma unroll
      for (int j = 0; j < 2; ++j) {
        const unsigned I = (2 * w + j) * 64 + lane;
        const unsigned kb = (I >= 248) + (I >= 488) + (I >= 736);
        const unsigned i = I - 240 * kb - 8 * ((kb + 1) >> 1);
        const unsigned r = (i * 2185u) >> 16, rem = i - r * 30;
        const unsigned seg = (rem * 6554u) >> 16, ch = rem - seg * 10;
        const unsigned v = (8 * kb + r) * 3840 + seg * 1280 + g * 160 + ch * 16;
        k.wv[j] = i < 240 ? v : OOB;
      }
      asm volatile("" : "+v"(k.xv[0]), "+v"(k.xv[1]), "+v"(k.xv[K::NXP - 1]), "+v"(k.wv[0]), "+v"(k.wv[1]));
      bind();
      STAMP(0);
      k.srdx = mk_srd(x, 2048 * 1280);
      k.srdw = mk_srd(wqkv, 640 * 3840);
      unsigned gv0, gv1;
      asm volatile("global_load_dwordx2 %0, %1, %2 sc1" : "=v"(ev) : "v"(WS_EPOCH), "s"(ws) : "memory");
      asm volatile("buffer_load_ushort %0, %2, %3, 0 offen\n\tbuffer_load_ushort %1, %2, %3, 0 offen offset:1024"
                   : "=&v"(gv0), "=&v"(gv1) : "v"(tid * 2), "s"(mk_srd(gw, 1280)) : "memory");
      STAMP(11);
      k.stage0(rot);
      BAR();
      k.stage1();
      k.xa0 = (K::XB + par * 5 * 2048 + c * 128 + ((h ^ (c >> 1)) << 4)) ^ ((rot & 1) << 6);
      const unsigned kbo = h * 3840 + ((h + 1) >> 1) * 128;
      k.wa0 = kbo + q4 * 480 + (20 * hd + 4 * p4) * 2;
      const bool isgate = p4 == 3;
      k.gmask = isgate ? ~0u : 0u;
      asm volatile("" : "+v"(k.gmask));
      k.wa1 = isgate ? GATE_OFF + (8 * h + q4) * 8 : kbo + q4 * 480 + p4 * 160 + (20 * hd + 16) * 2;
      k.wa2 = k.wa1 + (isgate ? 32 : 1920);
      f32x4 ba, bb, ba1;
#pragma unroll
      for (int r = 0; r < 4; ++r) {
        const int kap = 4 * h + r;
        ba[r] = kap >= c + 9 ? 0.f : -1e30f;
        bb[r] = (kap <= c && kap >= c - 7) ? 0.f : -1e30f;
        ba1[r] = a == 0 ? -1e30f : ba[r];
      }
      unsigned m0mask = h == 0 ? 0xffffffffu : 0u;
      unsigned scw = w * 15360 + 10240 + c * 32 + h * 8, sck = w * 15360 + 10240 + c * 32 + 8,
               sct = w * 15360 + 10240 + (4 * h + q4) * 32 + p4 * 8;
      unsigned ovo = WS_O + (h & 1) * (2 * 20480) + (2 * g + (hd >> 1)) * 1024 + (2 * (hd & 1) + (h >> 1)) * 256 + c * 16;
      unsigned ovt = h == 2 ? WS_O + (32 + g) * 512 + hd * 128 + c * 8 : OOB;
      asm volatile("" : "+v"(ba), "+v"(bb), "+v"(ba1), "+v"(m0mask), "+v"(scw), "+v"(sck), "+v"(sct), "+v"(ovo), "+v"(ovt));
      asm volatile("" : "+v"(k.xa0), "+v"(k.wa0));
      k.zero();
      k.stage2();
      STAMP(12);
      k.template run<0, RB ? 10 : 0>([&]() {
        asm volatile("s_waitcnt vmcnt(%2)" : "+v"(gv0), "+v"(gv1) : "i"(K::VPRO) : "memory");
        u32x2 v; v.x = gv0; v.y = 0;
        *(LAS u32x2*)(GATE_OFF + tid * 8) = v;
        if (!RB && tid < 128) { v.x = gv1; *(LAS u32x2*)(GATE_OFF + 4096 + tid * 8) = v; }
        make_tag(std::integral_constant<int, K::VPRO>{});
        WAITL();
        BAR();
        STAMP(1);
      }, [&](int t) { if (RB) side_b(t); }, tail, [](int) {});
      STAMP(2);
      u32x2 ql[5], kl[5], t2[5], vl[5], tt[5], k4[5];
      unsigned sb16[4];
#pragma unroll
      for (int i = 0; i < 5; ++i) { t2[i] = pk4(k.acc[i][2]); *(LAS u32x2*)(scw + i * 512) = t2[i]; }
#pragma unroll
      for (int i = 0; i < 5; ++i) { if (i) ql[i] = pk4(k.acc[i][0]); kl[i] = pk4(k.acc[i][1]); }
      WAITL();
#pragma unroll
      for (int i = 0; i < 5; ++i) k4[i] = *(const LAS u32x2*)(sck + i * 512);
#pragma unroll
      for (int i = 0; i < 4; ++i) sb16[i] = *(const LAS unsigned*)(sck + (i + 1) * 512 + 16);
#pragma unroll
      for (int i = 0; i < 5; ++i) tt[i] = ldstr(sct + i * 512);
#pragma unroll
      for (int i = 0; i < 5; ++i) vl[i] = pk4(k.acc[i][3]);
      constexpr float SC = 0.22360679774997896f * LOG2E;
      f32x4 sa[4], sb[4];
#pragma unroll
      for (int i = 1; i < 5; ++i) {
        u32x4 qf; qf.x = ql[i].x; qf.y = ql[i].y; qf.z = t2[i].x & m0mask; qf.w = t2[i].y & m0mask;
        sb[i - 1] = mfma(cat(kl[i], k4[i]), qf, bb);
        sa[i - 1] = mfma(cat(kl[i - 1], k4[i - 1]), qf, i == 1 ? ba1 : ba);
      }
      asm volatile("s_nop 7\n\ts_nop 0" : "+v"(sa[0]), "+v"(sa[1]), "+v"(sa[2]), "+v"(sa[3]), "+v"(sb[0]), "+v"(sb[1]), "+v"(sb[2]), "+v"(sb[3]));
      float mx[4], es[4];
#pragma unroll
      for (int i = 0; i < 4; ++i)
        mx[i] = max3(max3(sb[i].x, sb[i].y, sb[i].z), max3(sb[i].w, sa[i].x, sa[i].y), max2(sa[i].z, sa[i].w));
#pragma unroll
      for (int i = 0; i < 4; ++i) {
        auto r = __builtin_amdgcn_permlane16_swap(f2u(mx[i]), f2u(mx[i]), false, false);
        mx[i] = max2(u2f(r[0]), u2f(r[1]));
      }
#pragma unroll
      for (int i = 0; i < 4; ++i) {
        auto r = __builtin_amdgcn_permlane32_swap(f2u(mx[i]), f2u(mx[i]), false, false);
        mx[i] = max2(u2f(r[0]), u2f(r[1])) * SC;
        es[i] = __builtin_amdgcn_exp2f(__builtin_amdgcn_fmed3f(u2f(sb16[i] << 16), -60.f, 60.f) * LOG2E);
      }
      u32x4 pf[4];
#pragma unroll
      for (int i = 0; i < 4; ++i) {
        f32x4 pa, pb;
#pragma unroll
        for (int r = 0; r < 4; ++r) {
          pa[r] = __builtin_amdgcn_exp2f(sa[i][r] * SC - mx[i]);
          pb[r] = __builtin_amdgcn_exp2f(sb[i][r] * SC - mx[i]);
        }
        pf[i] = cat(pk4(pa), pk4(pb));
      }
      const f32x4 z = {0.f, 0.f, 0.f, 0.f};
      const u32x4 ones = {0x3f803f80u, 0x3f803f80u, 0x3f803f80u, 0x3f803f80u};
      f32x4 ls[4], oa[4], ob[4];
#pragma unroll
      for (int i = 0; i < 4; ++i) {
        ls[i] = mfma(ones, pf[i], z);
        oa[i] = mfma(cat(vl[i], vl[i + 1]), pf[i], z);
        ob[i] = mfma(cat(tt[i], tt[i + 1]), pf[i], z);
      }
      const unsigned mt0 = 8 * a + par;
      u32x2 op[4];
#pragma unroll
      for (int i = 0; i < 4; ++i) {
        const float f = es[i] * __builtin_amdgcn_rcpf(ls[i].x);
        op[i] = pk4(oa[i] * f);
        __builtin_amdgcn_raw_buffer_store_b64(pk4(ob[i] * f), rws, ovt, (mt0 + 2 * i) * 20480, AUX_SC1);
        if (i & 1) {
          auto sx = __builtin_amdgcn_permlane16_swap(op[i - 1].x, op[i].x, false, false);
          auto sy = __builtin_amdgcn_permlane16_swap(op[i - 1].y, op[i].y, false, false);
          u32x4 o; o.x = sx[0]; o.y = sy[0]; o.z = sx[1]; o.w = sy[1];
          __builtin_amdgcn_raw_buffer_store_b128(o, rws, ovo, (mt0 + 2 * i - 2) * 20480, AUX_SC1);
          STPAD();
        }
      }
      if (g == 0 && hd == 0) {
        const float e = h == 0 ? es[0] : h == 1 ? es[1] : h == 2 ? es[2] : es[3];
        __builtin_amdgcn_raw_buffer_store_b32(f2u(e), rws, WS_G + h * 128 + c * 4, mt0 * 64, AUX_SC1);
      }
    } else {
      const int cj = gc;
      using K = P1<false, RB>;
      K k;
      const int mh = RB ? (w < 6 ? 1 : 0) : (w >> 1);
      const int up = (w >> 1) & 1, kp = w & 1;
#pragma unroll
      for (int i = 0; i < K::NXP; ++i) {
        const int rho = 8 * (4 * (w & 3) + i) + (lane >> 3);
        const int j = rho >> 4;
        const int tok = 128 * a + 32 * (j >> 1) + 2 * (rho & 15) + (j & 1);
        k.xv[i] = tok * 1280 + (((lane & 7) ^ ((rho >> 1) & 7)) << 4);
      }
      k.xd = K::XB + 4 * (w & 3) * 1024;
      {
        const int r = lane >> 3, p8 = lane & 7;
        const int s = ((r >> 1) & 1) + 2 * kp;
        k.wv[0] = (8 * kp + r) * 3072 + cj * 384 + (((((p8 >> 1) ^ s) << 1) | (p8 & 1)) << 4) + (RB ? 0 : 16 * 3072 + 128);
        k.wv[1] = k.wv[0] + 128; k.wv[2] = k.wv[0] + 256; k.wv[3] = k.wv[0] + 16 * 3072;
      }
      k.wd0 = RB ? (kp * 6 + 3 * up) * 1024 : ((kp + 2) * 6 + 3 * up + 1) * 1024;
      k.wd1 = ((kp + 2) * 6 + 3 * up) * 1024;
      if (RB) asm volatile("" : "+v"(k.xv[0]), "+v"(k.xv[1]), "+v"(k.xv[2]), "+v"(k.xv[3]), "+v"(k.wv[2]), "+v"(k.wv[3]));
      asm volatile("" : "+v"(k.wv[0]), "+v"(k.wv[1]));
      bind();
      STAMP(0);
      const u64 pg = wg, pu = wu;
      k.srdx = mk_srd(x, 2048 * 1280);
      k.srdw = mk_srd(up ? pu : pg, 640 * 3072);
      asm volatile("global_load_dwordx2 %0, %1, %2 sc1" : "=v"(ev) : "v"(WS_EPOCH), "s"(ws) : "memory");
      STAMP(11);
      k.stage0(rot);
      BAR();
      if (!RB) pack_issue();
      k.stage1();
      k.xa0 = (K::XB + mh * 4 * 2048 + c * 128 + ((h ^ (c >> 1)) << 4)) ^ ((rot & 1) << 6);
      {
        const int s = ((q4 >> 1) & 1) + 2 * (h & 1);
        k.wa0 = (h * 6 + (RB ? 2 : (w & 1))) * 1024 + q4 * 128 + (((RB ? 2 * (w & 1) : 0) ^ s) << 5) + p4 * 8;
      }
      asm volatile("" : "+v"(k.xa0), "+v"(k.wa0));
      k.zero();
      k.stage2();
      STAMP(12);
      k.template run<0, RB ? 10 : 0>([&]() {
        make_tag(std::integral_constant<int, RB ? K::VPRO : 8>{});
        BAR();
        STAMP(1);
      }, [&](int t) { if (RB) side_b(t); else side_f(t); }, tail, pack_gap);
      STAMP(2);
      constexpr int NK = RB ? 1 : 2;
      float nl2e = -LOG2E;
      asm volatile("" : "+s"(nl2e));
      const unsigned gso = WS_GU + ((8 * a + 4 * mh) * 48 + 6 * cj + (RB ? 4 + (w & 1) : 2 * (w & 1))) * 1024;
#pragma unroll
      for (int m = 0; m < 4; ++m) {
#pragma unroll
        for (int kk = 0; kk < NK; ++kk) {
          float v[8];
#pragma unroll
          for (int j = 0; j < 8; j += 2) {
            const f32x2 g2 = {k.acc[m][2 * kk + (j >> 2)][j & 3], k.acc[m][2 * kk + (j >> 2)][(j & 3) + 1]};
            const f32x2 u2 = {k.acc[m][2 * NK + 2 * kk + (j >> 2)][j & 3], k.acc[m][2 * NK + 2 * kk + (j >> 2)][(j & 3) + 1]};
            const f32x2 a2 = g2 * nl2e;
            const f32x2 d2 = f32x2{__builtin_amdgcn_exp2f(a2.x), __builtin_amdgcn_exp2f(a2.y)} + 1.f;
            const f32x2 r2 = g2 * u2 * f32x2{__builtin_amdgcn_rcpf(d2.x), __builtin_amdgcn_rcpf(d2.y)};
            v[j] = r2.x; v[j + 1] = r2.y;
          }
          u32x4 o; o.x = pk2(v[0], v[1]); o.y = pk2(v[2], v[3]); o.z = pk2(v[4], v[5]); o.w = pk2(v[6], v[7]);
          __builtin_amdgcn_raw_buffer_store_b128(o, rws, lane * 16, gso + m * (48 * 1024) + kk * 1024, AUX_SC1);
          STPAD();
        }
      }
    }
    SB();
    STAMP(13);
    asm volatile("" : "+v"(wo_[0][0]), "+v"(wo_[0][1]), "+v"(wo_[0][2]), "+v"(wo_[0][3]), "+v"(wo_[0][4]),
                      "+v"(wo_[1][0]), "+v"(wo_[1][1]), "+v"(wo_[1][2]), "+v"(wo_[1][3]), "+v"(wo_[1][4]));
    if (RB) asm volatile("" : "+v"(wg_[0][0]), "+v"(wg_[0][1]), "+v"(wg_[0][2]), "+v"(wg_[0][3]), "+v"(wg_[0][4]),
                              "+v"(wg_[1][0]), "+v"(wg_[1][1]), "+v"(wg_[1][2]), "+v"(wg_[1][3]), "+v"(wg_[1][4]));
    SB();
    STAMP(14);
    if (RB && idx >= 16) { if (PRE > 0) cell_dma(w - 4, 1); if (PRE > 1) cell_dma(w, 1); if (PRE > 2) cell_dma(w - 4, 2); WAITV(5 * PRE); }
    else if (RB || !PREA) WAITV(0);
    else { late_w(); WAITV(15); }
    STAMP(3);
    WAITL();
    BAR();
    if (!RB && w == 0 && lane < 2) {
      GAS u64* fp = (GAS u64*)((char*)ws + (lane ? WS_DONE + b * 8 : WS_FLAG + (a * 16 + (idx < 16 ? 8 : 0) + gc) * 8));
      __hip_atomic_store(fp, tag, RLX);
    }
    STAMP(4);

    SB();
    const unsigned fo = WS_FLAG + ((rb >> 1) * 16 + c) * 8;
    auto wait_flags = [&](u32x2& v, bool need) {
#pragma unroll 1
      for (unsigned spin = 0; spin < (1u << 18); ++spin) {
        if (__all(!need || (v.x == (unsigned)tag && v.y == (unsigned)(tag >> 32)))) break;
        ld64(v, fo, sws, 0);
        asm volatile("s_waitcnt vmcnt(0)" : "+v"(v) :: "memory");
      }
      asm volatile("" ::: "memory");
    };
    u32x2 fv;
    if (RB) {
      if (idx < 16) { if (PRE > 0) cell_dma(w - 4, 1); if (PRE > 1) cell_dma(w, 1); if (PRE > 2) cell_dma(w - 4, 2); }
      if (PRE < 1) cell_dma(w - 4, 1); if (PRE < 2) cell_dma(w, 1); if (PRE < 3) cell_dma(w - 4, 2);
      cell_dma(w, 2);
    } else {
      if (!PREA) late_w();
      if (w == 0) {
        ld64(fv, fo, sws, 0);
        asm volatile("s_waitcnt vmcnt(0)" : "+v"(fv) :: "memory");
        wait_flags(fv, c >= 8);
      }
    }
    if (RB) __builtin_amdgcn_s_setprio(1);
    BAR();
    STAMP(5);
    f32x4 acc[4][5];
    u32x4 ag[6][4];
    auto tk = [](int) {};
    tk(0);
    const unsigned gb = (EXP & 1) ? pwb : WS_GU + (4 * rb * 48 + 6 * w) * 1024;
    auto ldg = [&](int s, int m) { ld128(ag[s][m], lane * 16, sws, gb + gstep(s) * 1024 + m * (48 * 1024)); };
    unsigned ca0 = CELL0 + w * 5120 + lane * 16, ca1 = w * 15360 + lane * 16;
    asm volatile("" : "+v"(ca0), "+v"(ca1));
    {
      using S = Sched<RB>;
      constexpr int NO = S::NO, OB = S::OB;
      u32x4 af[2][4], at[4];
      u32x2 tl[4], th[4];
      const unsigned ob = WS_O + 4 * rb * 20480;
      auto req = [&](auto IT) {
        constexpr int i = IT.value;
        if constexpr (i < 8) ld128(af[i >> 2][i & 3], lane * 16, sws, ob + (i & 3) * 20480 + (2 * w + (i >> 2)) * 1024);
        else if constexpr (i < OB) ld64((i & 1) ? th[(i - 8) >> 1] : tl[(i - 8) >> 1], lane * 8, sws, ob + ((i - 8) >> 1) * 20480 + (16 + w) * 1024 + (i & 1) * 512);
        else if constexpr (i < OB + 29 && (i - OB) % 5 < 4) ldg((i - OB) / 5, (i - OB) % 5);
        else if constexpr (i < OB + 29) dma16c(w * 15360, 10240 + ((i - OB) / 5) * 1024, lane * 16, sws, pwb + (20 + 6 * w + gstep(5)) * 5120 + ((i - OB) / 5) * 1024);
        else ld32(i == OB + 29 ? sg0 : i == OB + 30 ? sg1 : sg2, c * 4, sws, WS_G + (4 * rb + min(t0 + i - OB - 29, 19) / 5) * 64);
      };
      if (RB || w != GPOLL || !GPOLL) sfor<0, S::upto(PL - 1)>(req);
      if (RB) { __builtin_amdgcn_s_setprio(0); WAITV(S::upto(PL - 1)); }
      else if (w == GPOLL) { if (GPOLL) { fv.x = ~(unsigned)tag; fv.y = 0; } wait_flags(fv, c < 8); }
      STAMP(6);
      BAR();
      STAMP(7);
      if (!RB && GPOLL && w == GPOLL) sfor<0, S::upto(PL - 1)>(req);
      tk(1);
      u32x4 wc[5];
      sfor<0, NO + 6>([&](auto ST) {
        constexpr int st = ST.value, s = st - NO;
        if constexpr (st == 2 && !RB) {
          asm volatile("s_waitcnt vmcnt(%8)" : "+v"(tl[0]), "+v"(tl[1]), "+v"(tl[2]), "+v"(tl[3]), "+v"(th[0]), "+v"(th[1]), "+v"(th[2]), "+v"(th[3])
                       : "i"(S::issued(st, 0) - 16) : "memory");
#pragma unroll
          for (int m = 0; m < 4; ++m) at[m] = cat(tl[m], th[m]);
          asm volatile("s_nop 1" : "+v"(at[0]), "+v"(at[1]), "+v"(at[2]), "+v"(at[3]));
        }
        sfor<0, 20>([&](auto IT) {
          constexpr int i = IT.value, m = i / 5, n = i % 5, is = S::issued(st, i);
          if constexpr (WGR == 1 ? n == 0 : i % (5 * WGR) == 0) {
            constexpr int ml = m + WGR - 1;
            if constexpr (WGR == 1) {
              if constexpr (s >= 0)      asm volatile("s_waitcnt vmcnt(%1)" : "+v"(ag[s][m]) : "i"(is - S::gpos(s, m) - 1) : "memory");
              else if constexpr (st < 2) asm volatile("s_waitcnt vmcnt(%1)" : "+v"(af[st][m]) : "i"(is - (4 * st + m) - 1) : "memory");
            } else if constexpr (WGR == 2) {
              if constexpr (s >= 0)      asm volatile("s_waitcnt vmcnt(%2)" : "+v"(ag[s][m]), "+v"(ag[s][ml]) : "i"(is - S::gpos(s, ml) - 1) : "memory");
              else if constexpr (st < 2) asm volatile("s_waitcnt vmcnt(%2)" : "+v"(af[st][m]), "+v"(af[st][ml]) : "i"(is - (4 * st + ml) - 1) : "memory");
            } else {
              if constexpr (s >= 0)      asm volatile("s_waitcnt vmcnt(%4)" : "+v"(ag[s][0]), "+v"(ag[s][1]), "+v"(ag[s][2]), "+v"(ag[s][3]) : "i"(is - S::gpos(s, 3) - 1) : "memory");
              else if constexpr (st < 2) asm volatile("s_waitcnt vmcnt(%4)" : "+v"(af[st][0]), "+v"(af[st][1]), "+v"(af[st][2]), "+v"(af[st][3]) : "i"(is - (4 * st + 3) - 1) : "memory");
            }
          }
          if constexpr (st == 0 && i == 0 && !RB)
            asm volatile("" : "+v"(wo_[2][0]), "+v"(wo_[2][1]), "+v"(wo_[2][2]), "+v"(wo_[2][3]), "+v"(wo_[2][4]),
                              "+v"(wg_[0][0]), "+v"(wg_[0][1]), "+v"(wg_[0][2]), "+v"(wg_[0][3]), "+v"(wg_[0][4]),
                              "+v"(wg_[1][0]), "+v"(wg_[1][1]), "+v"(wg_[1][2]), "+v"(wg_[1][3]), "+v"(wg_[1][4]));
          if constexpr (st == 0)     mul0(acc[m][n], wo_[0][n], af[0][m]);
          else if constexpr (s >= 0) mac(acc[m][n], wg_[s & 1][n], ag[s][m]);
          else if constexpr (st < 2) mac(acc[m][n], wo_[1][n], af[1][m]);
          else                       mac(acc[m][n], wo_[2][n], at[m]);
          SB();
          if constexpr (s == 4 && i == 9) WAITV(is - S::gpos(4, 4) - 1);
          if constexpr (m == (s == 4 ? 2 : 0) && s >= 1 && s < 5) wc[n] = lds128((s > 1 ? ca1 + (s - 2) * 5120 : ca0) + n * 1024);
          if constexpr (S::issued(st, i + 1) > is) req(std::integral_constant<int, is>{});
          SB();
        });
        tk(2 + st);
        if constexpr (s >= 1 && s < 5) {
          asm volatile("" : "+v"(wc[0]), "+v"(wc[1]), "+v"(wc[2]), "+v"(wc[3]), "+v"(wc[4]));
#pragma unroll
          for (int n = 0; n < 5; ++n) wg_[(s + 1) & 1][n] = wc[n];
        }
      });
      asm volatile("s_waitcnt vmcnt(0)" : "+v"(sg0), "+v"(sg1), "+v"(sg2) :: "memory");
    }
    MFMA_DONE();
    STAMP(8);
#pragma unroll
    for (int j = 0; j < 10; ++j) {
      const int ta = j < 4 ? 3 * j : j < 6 ? 6 * j - 22 : 2 * j, tb = j < 4 ? ta + 1 : j < 6 ? ta + 3 : ta + 1;
      *(LAS u32x4*)(ca1 + j * 1024) = cat(pk4(acc[ta / 5][ta % 5]), pk4(acc[tb / 5][tb % 5]));
    }
    tk(12);
  };
  if (w < 4) role(std::false_type{}); else role(std::true_type{});

  if (b == 0 && w == 2) {
    GAS u64* dp = (GAS u64*)((char*)ws_ + WS_DONE + lane * 8);
#pragma unroll 1
    for (unsigned spin = 0; spin < (1u << 18); ++spin) {
      bool ok = true;
#pragma unroll
      for (int j = 0; j < 4; ++j) ok &= __hip_atomic_load(dp + j * 64, RLX) == tag;
      if (__all(ok)) break;
      __builtin_amdgcn_s_sleep(2);
    }
    if (lane == 0) __hip_atomic_store((GAS u64*)((char*)ws_ + WS_EPOCH), tag, RLX);
  }
  sg0 = __builtin_amdgcn_rcpf(1.f + sg0); sg1 = __builtin_amdgcn_rcpf(1.f + sg1); sg2 = __builtin_amdgcn_rcpf(1.f + sg2);
  auto ooff = [&](int tl) {
    const int m = tl / 5, n = tl - 5 * m;
    return (unsigned)((64 * rb + 32 * (m >> 1) + 2 * c + (m & 1)) * 1280 + ct * 160 + n * 32 + h * 8);
  };
  unsigned oo0 = ooff(t0), oo1 = ooff(t0 + 1), oo2 = ooff(t0 + 2);
  asm volatile("" : "+v"(sg0), "+v"(sg1), "+v"(sg2), "+v"(oo0), "+v"(oo1), "+v"(oo2));
  WAITL();
  BAR();
  STAMP(9);
  {
    u32x4 p[8];
    u32x2 p2[8];
    unsigned ra = (w < 4 ? w : w + 2) * 1024 + lane * 16, rb_ = ra + 4 * 15360;
    unsigned rc = (4 + (w >> 1)) * 1024 + (w & 1) * 8 + lane * 16, rd = rc + 4 * 15360;
    asm volatile("" : "+v"(ra), "+v"(rb_), "+v"(rc), "+v"(rd));
#pragma unroll
    for (int v = 0; v < 8; ++v) p[v] = lds128((v < 4 ? ra : rb_) + (v & 3) * 15360);
    if (w < 4) {
#pragma unroll
      for (int v = 0; v < 8; ++v) { p2[v] = *(const LAS u32x2*)((v < 4 ? rc : rd) + (v & 3) * 15360); SB(); }
    }
    SB();
#pragma unroll
    for (int j = 0; j < 3; ++j) {
      if (j < 2 || w < 4) {
        f32x4 s = {0.f, 0.f, 0.f, 0.f};
#pragma unroll
        for (int v = 0; v < 8; ++v) {
          const unsigned px = j == 0 ? p[v].x : j == 1 ? p[v].z : p2[v].x, py = j == 0 ? p[v].y : j == 1 ? p[v].w : p2[v].y;
          const f32x4 q = {u2f(px << 16), u2f(px & 0xffff0000u), u2f(py << 16), u2f(py & 0xffff0000u)};
          s = v ? s + q : q;
        }
        const u32x2 ov = pk4(s * (j == 0 ? sg0 : j == 1 ? sg1 : sg2));
        asm volatile("global_store_dwordx2 %0, %1, %2" OUTFL :: "v"(j == 0 ? oo0 : j == 1 ? oo1 : oo2), "v"(ov), "s"(out_) : "memory");
      }
    }
  }
  STAMP(10);
  if (AMP0 >= 0 && (AMPSEL == 0 || (AMPSEL == 1) == (idx < 16))) {
    const u64 d = amp_t1 - amp_t0, t = __builtin_amdgcn_s_memrealtime();
    while (__builtin_amdgcn_s_memrealtime() - t < d) __builtin_amdgcn_s_sleep(1);
  }
}
}

extern "C" void kernel_launch(void* const* d_in, const int* in_sizes, int n_in, void* d_out, int out_size, void* d_ws,
                              size_t ws_size, hipStream_t stream) {
  fused<<<256, 512, 0, stream>>>(d_in[0], d_in[1], d_in[2], d_in[4], d_in[5], d_in[6], d_in[7], d_out, d_ws);
}
```

```cpp
#include <hip/hip_runtime.h>
#include <hip/hip_bf16.h>
#include <stdint.h>
#include <type_traits>

#define STAMPS 0
#define BEARLY 0
#define CODEPF 1
#define TOUCH_NT ""
#define TOUCH 1
#define EXP 0
#define GLA 4
#define PART16 1
#define PL 2
#define PRE 2
#define OUTFL ""
#define PREA 0
#define WGR 4
#define GPOLL 1
#define ROT 4

#define LAS __attribute__((address_space(3)))
#define GAS __attribute__((address_space(1)))
typedef __bf16 bf16x8 __attribute__((ext_vector_type(8)));
typedef __bf16 bf16x2 __attribute__((ext_vector_type(2)));
typedef float f32x4 __attribute__((ext_vector_type(4)));
typedef float f32x2 __attribute__((ext_vector_type(2)));
typedef unsigned u32x4 __attribute__((ext_vector_type(4)));
typedef unsigned u32x2 __attribute__((ext_vector_type(2)));
typedef short s16x4 __attribute__((ext_vector_type(4)));
typedef unsigned long long u64;

namespace {
constexpr int LDS_BYTES = 163840;
constexpr unsigned GATE_OFF = 110592, PACK_OFF = 122880, PKF_OFF = 162816, CELL0 = 122880;
constexpr unsigned OOB = 0x40000000u;
#define CODE_B 40000
constexpr unsigned CODE_BYTES = CODE_B;
constexpr unsigned WS_FLAG = 0;
constexpr unsigned WS_DONE = 4096;
constexpr unsigned WS_PKF = 8192;
constexpr unsigned WS_EPOCH = 12288;
constexpr unsigned WS_G = 16384;
constexpr unsigned WS_O = 131072;
constexpr unsigned WS_GU = WS_O + 128 * 40 * 512;
constexpr unsigned WS_PW = WS_GU + 128 * 48 * 1024;
constexpr unsigned WS_END = WS_PW + 8 * 68 * 5120;
constexpr unsigned WS_STAMP = 16u << 20;
constexpr int AUX_SC1 = 16;
constexpr float LOG2E = 1.4426950408889634f;

__device__ __forceinline__ u32x4 mk_srd(u64 a, unsigned bytes) {
  u32x4 r; r.x = (unsigned)a; r.y = (unsigned)(a >> 32) & 0xffffu; r.z = bytes; r.w = 0x00020000u; return r;
}
__device__ __forceinline__ void dma16(unsigned lds, unsigned imm, unsigned voff, u32x4 srd, unsigned soff) {
  asm volatile("s_add_u32 m0, %0, %4\n\ts_nop 0\n\tbuffer_load_dwordx4 %1, %2, %3 offen lds"
               :: "s"(lds), "v"(voff), "s"(srd), "s"(soff), "i"(imm) : "memory", "scc");
}
__device__ __forceinline__ void dma16c(unsigned lds, unsigned imm, unsigned voff, u32x4 srd, unsigned soff) {
  asm volatile("s_add_u32 m0, %0, %4\n\ts_nop 0\n\tbuffer_load_dwordx4 %1, %2, %3 offen sc1 lds"
               :: "s"(lds), "v"(voff), "s"(srd), "s"(soff), "i"(imm) : "memory", "scc");
}
__device__ __forceinline__ void ld128(u32x4& d, unsigned voff, u32x4 srd, unsigned soff) {
  asm volatile("buffer_load_dwordx4 %0, %1, %2, %3 offen sc1" : "=v"(d) : "v"(voff), "s"(srd), "s"(soff) : "memory");
}
__device__ __forceinline__ void ld64(u32x2& d, unsigned voff, u32x4 srd, unsigned soff) {
  asm volatile("buffer_load_dwordx2 %0, %1, %2, %3 offen sc1" : "=v"(d) : "v"(voff), "s"(srd), "s"(soff) : "memory");
}
__device__ __forceinline__ void ld32(float& d, unsigned voff, u32x4 srd, unsigned soff) {
  asm volatile("buffer_load_dword %0, %1, %2, %3 offen sc1" : "=v"(d) : "v"(voff), "s"(srd), "s"(soff) : "memory");
}
__device__ __forceinline__ u32x4 lds128(unsigned a) { return *(const LAS u32x4*)a; }
__device__ __forceinline__ u32x2 ldstr(unsigned a) {
  return __builtin_bit_cast(u32x2, __builtin_amdgcn_ds_read_tr16_b64_v4i16((LAS s16x4*)a));
}
__device__ __forceinline__ u32x4 cat(u32x2 a, u32x2 b) { u32x4 r; r.x = a.x; r.y = a.y; r.z = b.x; r.w = b.y; return r; }
__device__ __forceinline__ f32x4 mfma(u32x4 a, u32x4 b, f32x4 c) {
  return __builtin_amdgcn_mfma_f32_16x16x32_bf16(__builtin_bit_cast(bf16x8, a), __builtin_bit_cast(bf16x8, b), c, 0, 0, 0);
}
__device__ __forceinline__ void mac(f32x4& c, u32x4 a, u32x4 b) {
  asm volatile("v_mfma_f32_16x16x32_bf16 %0, %1, %2, %0" : "+v"(c) : "v"(a), "v"(b));
}
__device__ __forceinline__ void mul0(f32x4& c, u32x4 a, u32x4 b) {
  asm volatile("v_mfma_f32_16x16x32_bf16 %0, %1, %2, 0" : "=&v"(c) : "v"(a), "v"(b));
}
__device__ __forceinline__ unsigned pk2(float a, float b) {
  f32x2 t = {a, b};
  return __builtin_bit_cast(unsigned, __builtin_convertvector(t, bf16x2));
}
__device__ __forceinline__ u32x2 pk4(f32x4 v) { u32x2 r; r.x = pk2(v.x, v.y); r.y = pk2(v.z, v.w); return r; }
__device__ __forceinline__ float u2f(unsigned u) { return __builtin_bit_cast(float, u); }
__device__ __forceinline__ unsigned f2u(float f) { return __builtin_bit_cast(unsigned, f); }
#define SB() __builtin_amdgcn_sched_barrier(0)
#define WAITV(n) asm volatile("s_waitcnt vmcnt(%0)" :: "i"(n) : "memory")
#define WAITVL(n) asm volatile("s_waitcnt vmcnt(%0) lgkmcnt(0)" :: "i"(n) : "memory")
#define WAITL() asm volatile("s_waitcnt lgkmcnt(0)" ::: "memory")
#define BAR() asm volatile("s_barrier" ::: "memory")
#define STPAD() do { SB(); asm volatile("s_nop 1" ::: "memory"); SB(); } while (0)
#define MFMA_DONE() do { SB(); asm volatile("s_nop 7\n\ts_nop 1" ::: "memory"); SB(); } while (0)
#define RLX __ATOMIC_RELAXED, __HIP_MEMORY_SCOPE_AGENT
__device__ __forceinline__ unsigned rfl(unsigned v) { return __builtin_amdgcn_readfirstlane(v); }
__device__ __forceinline__ unsigned opaque_lane() { unsigned l = threadIdx.x & 63; asm volatile("" : "+v"(l)); return l; }

#define AMP0 -1
#define AMP1 -1
#define AMPSEL 0
#define STAMP(i) do {} while (0)

template <int I, int N, class F>
__device__ __forceinline__ void sfor(F&& f) {
  if constexpr (I < N) { f(std::integral_constant<int, I>{}); sfor<I + 1, N>(f); }
}
template <bool RB> struct Sched {
  static constexpr int NO = RB ? 2 : 3, OB = RB ? 8 : 16;
  static constexpr int gpos(int s, int m) { return OB + 5 * s + m; }
  static constexpr int upto(int st) { return st < 2 ? 4 * (st + 1) : st < NO ? 16 : st < NO + 5 ? OB + 5 * (st - NO + 1) : st == NO + 5 ? OB + 29 : OB + 32; }
  static constexpr int issued(int st, int i) {
    const int base = upto(st + PL - 1), goal = upto(st + PL), every = goal - base > 5 ? 2 : goal - base > 4 ? 4 : 5;
    int n = base;
    for (int k = 0; k < i; ++k) if (k % every == 1 && n < goal) ++n;
    return n;
  }
};
__device__ __forceinline__ float max3(float a, float b, float c) { float r; asm("v_max3_f32 %0, %1, %2, %3" : "=v"(r) : "v"(a), "v"(b), "v"(c)); return r; }
__device__ __forceinline__ float max2(float a, float b) { float r; asm("v_max_f32 %0, %1, %2" : "=v"(r) : "v"(a), "v"(b)); return r; }

template <bool QKV, bool RB>
struct P1 {
  static constexpr int NM = QKV ? 5 : 4;
  static constexpr int NN = QKV ? 4 : (RB ? 4 : 8);
  static constexpr int NWS = 3;
  static constexpr unsigned WSL = QKV ? 16384 : 24576, XB = NWS * WSL, XSL = QKV ? 20480 : 16384;
  static constexpr unsigned WROW = QKV ? 3840 : 3072;
  static constexpr int NWP = QKV ? 2 : (RB ? 4 : 2);
  static constexpr int NXE = QKV ? (RB ? 1 : 2) : (RB ? 2 : 0), NXO = QKV ? 1 : NXE;
  static constexpr int NXP = NXO + NXE;
  static constexpr int OO = NWP + NXO, OE = NWP + NXE;
  static constexpr bool HAND = !QKV;
  static constexpr int ht = 4;
  f32x4 acc[NM][NN];
  u32x4 srdx, srdw;
  unsigned xv[4], wv[4];
  unsigned xd;
  unsigned wd0, wd1;
  unsigned xa0, wa0, wa1, wa2;
  unsigned gmask;
  __device__ __forceinline__ void tick(int) {}

  unsigned sow, sox, gof;
  unsigned kg, kw, kx, kr;
  template <bool ODD, bool X = true>
  __device__ __forceinline__ void loads(unsigned wwr, unsigned xwr) {
    if (X && NXP) {
#pragma unroll
      for (int i = ODD ? NXE : 0; i < (ODD ? NXP : NXE); ++i) dma16(xwr + xd, i * 1024, xv[i], srdx, sox);
      if (ODD) { kx += 1; sox = (kx ^ (kg >> 1)) * 128; }
    }
    dma16(wwr + wd0, 0, wv[0], srdw, sow);
    dma16(wwr + wd0, 1024, wv[1], srdw, sow);
    if (!QKV && RB) {
      dma16(wwr + wd0, 2048, wv[2], srdw, sow);
      dma16(wwr + wd1, 0, wv[3], srdw, sow);
    }
  }
  __device__ __forceinline__ void other(unsigned wwr) {
    dma16(wwr + wd1, 1024, wv[3], srdw, sow + 128);
    dma16(wwr + wd1, 2048, wv[3], srdw, sow + 256);
  }
  __device__ __forceinline__ void next() { kw += 1; sow = (kw ^ kg) * (32 * WROW); asm volatile("" : "+s"(sow), "+s"(kw)); if (NXP) asm volatile("" : "+s"(sox), "+s"(kx)); }
  __device__ __forceinline__ void stage0(int rot) {
    kg = rot; kw = 0; kx = 0; kr = 0; sow = kg * (32 * WROW); sox = (kg >> 1) * 128; gof = kg * 256;
    if (HAND && !RB) { kw = 3; sow = (3 ^ kg) * (32 * WROW); return; }
#pragma unroll
    for (int i = 0; i < NXE; ++i) dma16(xd, i * 1024, xv[i], srdx, sox);
    loads<true>(0, 0); if (HAND) other(0); next();
  }
  __device__ __forceinline__ void stage1() { if (HAND && !RB) return; loads<false>(WSL, XSL); if (HAND) other(WSL); next(); }
  __device__ __forceinline__ void stage2() { if (HAND && !RB) return; loads<true>(2 * WSL, XSL); if (HAND) other(2 * WSL); next(); }
  static constexpr int VPRO = OE + OO + (HAND ? 4 : 0);
  struct Ad { unsigned x, w[4]; };
  __device__ __forceinline__ void addr(Ad& a, unsigned xb, unsigned wrd) {
    a.x = xb; a.w[0] = wa0 + wrd;
    if (QKV) {
      const unsigned s2 = (gmask & (gof - wrd)) + wrd;
      kr += 1; gof = (kr ^ kg) * 256;
      a.w[1] = wa1 + s2; a.w[2] = wa2 + s2;
      asm volatile("" : "+v"(a.x), "+v"(a.w[0]), "+v"(a.w[1]), "+v"(a.w[2]));
    } else if (RB) {
      a.w[1] = a.w[0] ^ 32;
      asm volatile("" : "+v"(a.x), "+v"(a.w[0]), "+v"(a.w[1]));
    } else {
      a.w[1] = a.w[0] ^ 32; a.w[2] = a.w[0] ^ 64; a.w[3] = a.w[0] ^ 96;
      asm volatile("" : "+v"(a.x), "+v"(a.w[0]), "+v"(a.w[1]), "+v"(a.w[2]), "+v"(a.w[3]));
    }
  }
  __device__ __forceinline__ unsigned wad(const Ad& a, int n, int hi) {
    if (QKV) return n == 2 ? a.w[1 + hi] : a.w[0] + (n == 0 ? 0 : n == 1 ? 160 : 320) + hi * 1920;
    if (RB) return a.w[n & 1] + (n >> 1) * 3072 + hi * 512;
    return a.w[n & 3] + (n >> 2) * 3072 + hi * 512;
  }
  __device__ __forceinline__ void rd_all(u32x4 (&xf)[NM], u32x4 (&wf)[NN], const Ad& a) {
#pragma unroll
    for (int n = 0; n < NN; ++n) wf[n] = cat(ldstr(wad(a, n, 0)), ldstr(wad(a, n, 1)));
#pragma unroll
    for (int m = 0; m < NM; ++m) xf[m] = lds128(a.x + m * 2048);
  }
  template <bool RD = true, class L>
  __device__ __forceinline__ void mm_rd(const u32x4 (&xc)[NM], const u32x4 (&wc)[NN], u32x4 (&xn)[NM], u32x4 (&wn)[NN], const Ad& a, L&& late) {
    u32x2 lo[NN];
    constexpr bool NMAJ = !QKV && !RB;
    constexpr bool EARLY = !QKV && RB && BEARLY;
    if (EARLY && RD) { rd_all(xn, wn, a); SB(); }
    int r = 0;
#pragma unroll
    for (int i = 0; i < NM * NN; ++i) {
      const int m = NMAJ ? i % NM : i / NN, n = NMAJ ? i / NM : i % NN;
      if (QKV && m == 0 && n == 0) continue;
      if (QKV && n == 3) mac(acc[m][3], xc[m], wc[3]);
      else               mac(acc[m][n], wc[n], xc[m]);
      SB();
      if (!RD || EARLY) continue;
      if (NMAJ) {
        if (i < 4) xn[i] = lds128(a.x + i * 2048);
        else if ((i & 3) < 2) {
          const int k = (i >> 2) - 1;
          if (i & 1) wn[k] = cat(lo[k], ldstr(wad(a, k, 1))); else lo[k] = ldstr(wad(a, k, 0));
        } else late(i);
      } else {
        if (r < 2 * NN) {
          const int k = r >> 1;
          if (r & 1) wn[k] = cat(lo[k], ldstr(wad(a, k, 1))); else lo[k] = ldstr(wad(a, k, 0));
        } else if (r < 2 * NN + NM) {
          xn[r - 2 * NN] = lds128(a.x + (r - 2 * NN) * 2048);
        }
        else late(r - 2 * NN - NM);
        ++r;
      }
      SB();
    }
    if (NMAJ && RD) { wn[NN - 1] = cat(ldstr(wad(a, NN - 1, 0)), ldstr(wad(a, NN - 1, 1))); SB(); }
  }
  __device__ __forceinline__ void pin(u32x4 (&xf)[NM], u32x4 (&wf)[NN]) {
    asm volatile("" : "+v"(xf[0]), "+v"(xf[1]), "+v"(xf[2]), "+v"(xf[3]), "+v"(wf[0]), "+v"(wf[1]), "+v"(wf[2]), "+v"(wf[3]));
    if (NM == 5) asm volatile("" : "+v"(xf[NM - 1]));
    if (NN == 8) asm volatile("" : "+v"(wf[NN - 4]), "+v"(wf[NN - 3]), "+v"(wf[NN - 2]), "+v"(wf[NN - 1]));
  }
  __device__ __forceinline__ void zero() {
#pragma unroll
    for (int m = 0; m < NM; ++m)
#pragma unroll
      for (int n = 0; n < NN; ++n) { acc[m][n] = f32x4{0.f, 0.f, 0.f, 0.f}; asm volatile("" : "+v"(acc[m][n])); }
  }
  template <int TL0, int TL1, class F, class G, class H, class U>
  __device__ __forceinline__ void run(F&& pre, G&& extra, H&& tail, U&& gap) {
    pre();
    u32x4 xA[NM], wA[NN], xB[NM], wB[NN];
    Ad a;
    addr(a, xa0, 0);
    rd_all(xA, wA, a);
    if (!RB) __builtin_amdgcn_s_setprio(1);
    unsigned w0 = 0, w1 = WSL, w2 = 2 * WSL;
    unsigned xp = 2 * XSL, x0 = 0, x1 = XSL;
    int t = 0;
    unsigned nw1, nw2, nx1;
    auto adv0 = [&]() { nw1 = w2 + WSL; if (nw1 == NWS * WSL) nw1 = 0; asm volatile("" : "+s"(nw1)); };
    auto adv1 = [&]() { nw2 = nw1 + WSL; if (nw2 == NWS * WSL) nw2 = 0; asm volatile("" : "+s"(nw2)); };
    auto adv2 = [&]() { nx1 = x1 + XSL; if (nx1 == 3 * XSL) nx1 = 0; asm volatile("" : "+s"(nx1)); };
    auto adv3 = [&]() {
      w0 = w2; w1 = nw1; w2 = nw2; xp = x0; x0 = x1; x1 = nx1; t += 2;
      asm volatile("" : "+s"(w0), "+s"(w1), "+s"(w2), "+s"(xp), "+s"(x0), "+s"(x1), "+s"(t));
    };
    auto adv = [&]() { adv0(); adv1(); adv2(); adv3(); };
    asm volatile("" : "+s"(w0), "+s"(w1), "+s"(w2), "+s"(xp), "+s"(x0), "+s"(x1), "+s"(t));
    if (QKV) asm volatile("" : "+s"(gof), "+s"(kr));
    auto none = [](int) {};
    if (RB) {
      auto help = [&](int tt, unsigned wwr) { if (HAND && __builtin_expect(tt < ht, 0)) other(wwr); extra(tt); };
      pin(xA, wA);
      SB(); WAITVL(OO + (HAND ? 2 : 0)); tick(0); BAR(); tick(1); SB();
      loads<false>(w0, xp); help(0, w0); next(); addr(a, (xa0 ^ 64) + x0, w1); SB();
      auto half = [&]() {
        mm_rd(xA, wA, xB, wB, a, [&](int i) {
          if (QKV && i == 0) adv0();
          if (QKV && i == 2) adv1();
          if (QKV && i == 4) adv2();
        });
        pin(xB, wB);
        SB(); WAITVL(OE); tick(2 * t + 2); BAR(); tick(2 * t + 3); SB();
        loads<true>(w1, xp); help(t + 1, w1); next();
        addr(a, xa0 + x1, w2);
        if (QKV) adv3(); else adv();
        SB();
        mm_rd(xB, wB, xA, wA, a, none);
      };
#pragma unroll 1
      do {
        half();
        pin(xA, wA);
        SB(); WAITVL(OO); tick(2 * t); BAR(); tick(2 * t + 1); SB();
        loads<false>(w0, xp); help(t, w0); next(); addr(a, (xa0 ^ 64) + x0, w1); SB();
        if (QKV) asm volatile("" : "+s"(gof), "+s"(kr));
      } while (t < 14);
      half();
    } else {
      addr(a, (xa0 ^ 64) + x0, w1);
      auto trip = [&](auto EARLY) {
        constexpr bool E = EARLY.value;
        pin(xA, wA);
        SB(); if (E) WAITL(); else WAITVL(OO);
        tick(2 * t); BAR(); tick(2 * t + 1); SB();
        mm_rd(xA, wA, xB, wB, a, [&](int i) { if (E) gap(i); });
        if (E) { if (t == 0) WAITV(0); else extra(t); }
        if (!E || t >= ht) loads<false>(w0, xp);
        next(); addr(a, xa0 + x1, w2); adv0(); adv1(); adv2(); SB();
        pin(xB, wB);
        SB(); if (E) WAITL(); else WAITVL(OE);
        tick(2 * t + 2); BAR(); tick(2 * t + 3); SB();
        mm_rd(xB, wB, xA, wA, a, [&](int i) { if (E) gap(i); });
        if (E && t < ht) extra(t + 1); else loads<true>(w1, xp);
        next(); adv3(); addr(a, (xa0 ^ 64) + x0, w1); SB();
        if (QKV) asm volatile("" : "+s"(gof), "+s"(kr));
      };
      if (HAND) {
#pragma unroll 1
        do trip(std::true_type{}); while (t < ht + 2);
      }
#pragma unroll 1
      do trip(std::false_type{}); while (t < 16);
    }
    constexpr unsigned S16 = (16 % NWS) * WSL, S17 = (17 % NWS) * WSL, S18 = (18 % NWS) * WSL, S19 = (19 % NWS) * WSL, X8 = 2 * XSL, X9 = 0;
    pin(xA, wA);
    SB(); WAITVL(OO);
    tick(32); BAR(); tick(33); SB();
    if (RB) { loads<false, false>(S16, 0); tail(0); addr(a, (xa0 ^ 64) + X8, S17); SB(); mm_rd(xA, wA, xB, wB, a, none); }
    else    { mm_rd(xA, wA, xB, wB, a, none); loads<false, false>(S16, 0); tail(0); addr(a, xa0 + X9, S18); SB(); }
    pin(xB, wB);
    SB(); WAITVL(NWP + TL0); tick(34); BAR(); tick(35); SB();
    if (RB) { tail(1); addr(a, xa0 + X9, S18); SB(); mm_rd(xB, wB, xA, wA, a, none); }
    else    { mm_rd(xB, wB, xA, wA, a, none); tail(1); addr(a, (xa0 ^ 64) + X9, S19); SB(); }
    pin(xA, wA);
    SB(); WAITVL(TL0 + TL1); tick(36); BAR(); tick(37); SB();
    if (RB) { tail(2); addr(a, (xa0 ^ 64) + X9, S19); SB(); mm_rd(xA, wA, xB, wB, a, none); }
    else    { mm_rd(xA, wA, xB, wB, a, none); tail(2); SB(); }
    pin(xB, wB);
    SB(); WAITL(); tick(38); BAR(); tick(39); SB();
    if (RB) { tail(3); SB(); mm_rd<false>(xB, wB, xA, wA, a, none); }
    else    { mm_rd<false>(xB, wB, xA, wA, a, none); tail(3); SB(); }
    if (!RB) __builtin_amdgcn_s_setprio(0);
    MFMA_DONE();
    tick(40);
  }
};

__global__ __launch_bounds__(512, 2) void fused(const void* x_, const void* wqkv_, const void* wo_p, const void* wg_p,
                                                const void* wu_p, const void* wd_p, const void* gw_, void* out_, void* ws_) {
  __shared__ __attribute__((aligned(16))) char smem[LDS_BYTES];
  asm volatile("" :: "s"((unsigned)(size_t)(LAS char*)smem));
  const int tid = threadIdx.x, lane = tid & 63;
  const int w = rfl(tid >> 6);
  const int h = lane >> 4, c = lane & 15, q4 = (lane & 15) >> 2, p4 = lane & 3;
  const int b = blockIdx.x, xcd = b & 7, idx = b >> 3;
  const int rh = xcd & 1, cq = xcd >> 1;
  const int a = 8 * rh + (idx & 7);
  const int gc = 2 * cq + ((idx >> 3) & 1);
  const int rb = 4 * xcd + (idx >> 3), ct = idx & 7;
  u64 x, wqkv, wo, wg, wu, wd, gw;
  char* ws;
  __amdgpu_buffer_rsrc_t rws;
  u32x4 sws;
  auto bind = [&]() {
    x = (u64)x_; wqkv = (u64)wqkv_; wo = (u64)wo_p; wg = (u64)wg_p; wu = (u64)wu_p; wd = (u64)wd_p; gw = (u64)gw_;
    u64 pw = (u64)ws_;
    asm volatile("" : "+s"(x), "+s"(wqkv), "+s"(wo), "+s"(wg), "+s"(wu), "+s"(wd), "+s"(gw), "+s"(pw));
    ws = (char*)pw;
    rws = __builtin_amdgcn_make_buffer_rsrc(ws, 0, WS_END, 0x00020000);
    sws = mk_srd(pw, WS_END);
  };
  u64 amp_t0 = 0, amp_t1 = 0;

  if (CODEPF) {
    u64 pc;
    asm volatile("s_getpc_b64 %0" : "=s"(pc));
    asm volatile("s_mov_b32 m0, %0\n\ts_nop 0\n\tbuffer_load_dword %1, %2, 0 offen lds"
                 :: "s"(PKF_OFF), "v"(lane < 6 ? ((idx & 7) * 48 + w * 6 + lane) * 128 : OOB), "s"(mk_srd(pc & ~127ull, CODE_BYTES)) : "memory");
  }
  u32x2 ev;
  u64 tag = 0;
  auto make_tag = [&](auto NV) {
    unsigned lo, hi;
    asm volatile("s_waitcnt vmcnt(%4)\n\tv_readfirstlane_b32 %0, %2\n\tv_readfirstlane_b32 %1, %3"
                 : "=s"(lo), "=s"(hi) : "v"(ev.x), "v"(ev.y), "i"(NV.value) : "memory");
    const u64 e = ((u64)hi << 32) | lo;
    tag = (e ^ 0x9E3779B97F4A7C15ull) * 0xBF58476D1CE4E5B9ull + 0x632BE59BD9B4E019ull;
    if (tag == e) tag += 1;
  };
  const unsigned pwb = WS_PW + ct * (68 * 5120);
  const int grot = 3 * ((idx >> 3) & 1);
  auto gstep = [&](int s) { const int v = s + grot; return v >= 6 ? v - 6 : v; };
  auto cell_dma = [&](int v, int cell) {
    const unsigned so = pwb + (20 + 6 * v + gstep(2 + cell)) * 5120;
#pragma unroll
    for (int j = 0; j < 5; ++j) dma16c(cell ? v * 15360 : CELL0 + v * 5120, (cell ? cell - 1 : 0) * 5120 + j * 1024, lane * 16, sws, so + j * 1024);
  };
  auto ldw = [&](u32x4 (&d)[5], unsigned so) {
#pragma unroll
    for (int n = 0; n < 5; ++n) d[n] = __builtin_amdgcn_raw_buffer_load_b128(rws, lane * 16, so + n * 1024, AUX_SC1);
  };
  const unsigned pscr = PACK_OFF + (w & 3) * 8192;
  const int pu0 = (b - 128) * 4 + w;
  unsigned pra[2], prb[2], pn[2], pb[2];
  auto pack_issue = [&]() {
    const u64 po = wo, pd = wd;
#pragma unroll
    for (int r = 0; r < 2; ++r) {
      const int u = pu0 + 512 * r;
      const int ks = u / 10, cg = u - 10 * ks;
      const unsigned r8 = lane >> 3;
      const u32x4 srd = mk_srd(ks < 20 ? po : pd, ks < 20 ? 640 * 1280 : 1536 * 1280);
      const unsigned base = ks < 16 ? 40 * ks : ks < 20 ? 160 * (ks - 16) + 16 : 32 * (ks - 20);
      const unsigned vr = (ks >= 16 && ks < 20) ? 20 * (r8 >> 2) + (r8 & 3) : r8;
      const unsigned vo = vr * 1280 + (lane & 7) * 16;
#pragma unroll
      for (int p = 0; p < 4; ++p) {
        const unsigned rp = ks < 16 ? 20 * (p >> 1) + 8 * (p & 1) : ks < 20 ? 40 * p : 8 * p;
        dma16(pscr, r * 4096 + p * 1024, vo, srd, u < 680 ? (base + rp) * 1280 + cg * 128 : OOB);
      }
      pra[r] = pscr + r * 4096 + (h * (ks < 16 ? 8 : 4) + q4) * 128 + p4 * 8 - 32;
      prb[r] = pra[r] + (ks < 16 ? 512 : 2048);
      pn[r] = 4 * cg - 1; pb[r] = u < 680 ? WS_PW + ks * 5120 + (4 * cg - 1) * 1024 : OOB;
    }
    asm volatile("" : "+v"(pra[0]), "+v"(prb[0]), "+v"(pra[1]), "+v"(prb[1]));
    asm volatile("" : "+s"(pn[0]), "+s"(pb[0]), "+s"(pn[1]), "+s"(pb[1]));
  };
  const unsigned smask = w != 4 ? 0u : idx >= 16 ? 0x1040u : 0x1000u;
  auto side_b = [&](int t) {
    if (__builtin_expect(!((smask >> t) & 1), 1)) return;
    if (t == 6) {
      u32x2 tv; tv.x = (unsigned)tag; tv.y = (unsigned)(tag >> 32);
      __builtin_amdgcn_raw_buffer_store_b64(tv, rws, opaque_lane() == 0 ? WS_PKF + (b - 128) * 8 : OOB, 0, AUX_SC1);
    } else {
      dma16c(PKF_OFF, 0, opaque_lane() * 16, sws, WS_PKF);
    }
  };
  u32x2 pq[4];
  unsigned ps0, ps1;
  auto pack_gap = [&](int i) {
    if (i == 6)  pq[0] = ldstr(pra[0]);
    if (i == 7)  pq[1] = ldstr(prb[0]);
    if (i == 10) pq[2] = ldstr(pra[1]);
    if (i == 11) pq[3] = ldstr(prb[1]);
    if (i == 14) { pra[0] += 32; prb[0] += 32; asm volatile("" : "+v"(pra[0]), "+v"(prb[0])); }
    if (i == 15) { pra[1] += 32; prb[1] += 32; asm volatile("" : "+v"(pra[1]), "+v"(prb[1])); }
    if (i == 18) { ps0 = (pn[0] * 205) >> 10; asm volatile("" : "+s"(ps0)); }
    if (i == 19) { ps0 = ps0 * (68 * 5120 - 5120) + pb[0]; asm volatile("" : "+s"(ps0)); }
    if (i == 22) { ps1 = (pn[1] * 205) >> 10; asm volatile("" : "+s"(ps1)); }
    if (i == 23) { ps1 = ps1 * (68 * 5120 - 5120) + pb[1]; asm volatile("" : "+s"(ps1)); }
    if (i == 26) { pn[0] += 1; pb[0] += 1024; asm volatile("" : "+s"(pn[0]), "+s"(pb[0])); }
    if (i == 27) { pn[1] += 1; pb[1] += 1024; asm volatile("" : "+s"(pn[1]), "+s"(pb[1])); }
  };
  auto side_f = [&](int) {
    __builtin_amdgcn_raw_buffer_store_b128(cat(pq[0], pq[1]), rws, lane * 16, ps0, AUX_SC1);
    STPAD();
    __builtin_amdgcn_raw_buffer_store_b128(cat(pq[2], pq[3]), rws, lane * 16, ps1, AUX_SC1);
    STPAD();
  };
  auto pk_check = [&]() {
    const u32x4 p0 = lds128(PKF_OFF + lane * 16);
    const unsigned lo = (unsigned)tag, hi = (unsigned)(tag >> 32);
    if (!__all(p0.x == lo && p0.z == lo && p0.y == hi && p0.w == hi)) {
      GAS u64* fp = (GAS u64*)((char*)ws + WS_PKF + opaque_lane() * 8);
#pragma unroll 1
      for (unsigned spin = 0; spin < (1u << 18); ++spin) {
        bool ok = true;
#pragma unroll
        for (int j = 0; j < 2; ++j) ok &= __hip_atomic_load(fp + 64 * j, RLX) == tag;
        if (__all(ok)) break;
        __builtin_amdgcn_s_sleep(2);
      }
    }
    asm volatile("" ::: "memory");
  };
  const int rot = (a + 2 * ((idx >> 3) & 1) + (idx >> 4)) & (ROT - 1);

  const int t0 = 2 * w + min(w, 4);
  float sg0, sg1, sg2;
  auto role = [&](auto RBT) {
    constexpr bool RB = decltype(RBT)::value;
    u32x4 wo_[3][5], wg_[2][5];
    auto tail = [&](int i) {
      if (i == 0) pk_check();
      if (RB) {
        if (i == 1) { ldw(wo_[0], pwb + 2 * w * 5120); ldw(wo_[1], pwb + (2 * w + 1) * 5120); }
        if (i == 2) { ldw(wg_[0], pwb + (20 + 6 * w + gstep(0)) * 5120); ldw(wg_[1], pwb + (20 + 6 * w + gstep(1)) * 5120); }
        if (i == 3) { cell_dma(w - 4, 0); cell_dma(w, 0); }
      } else if (i >= 2) {
        ldw(wo_[i - 2], pwb + (2 * w + i - 2) * 5120);
      }
    };
    auto late_w = [&]() {
#pragma unroll
      for (int n = 0; n < 5; ++n) ld128(wo_[2][n], lane * 16, sws, pwb + (16 + w) * 5120 + n * 1024);
#pragma unroll
      for (int j = 0; j < 10; ++j) ld128(wg_[j / 5][j % 5], lane * 16, sws, pwb + (20 + 6 * w + gstep(j / 5)) * 5120 + (j % 5) * 1024);
    };
    if (idx < 16) {
      const int g = gc, hd = w & 3;
      constexpr int par = RB ? 1 : 0;
      using K = P1<true, RB>;
      K k;
      const int q0 = RB ? 12 + 2 * (w - 4) : 3 * w;
      k.xd = K::XB + q0 * 1024; k.wd0 = w * 2048;
      asm volatile("" : "+s"(k.xd), "+s"(k.wd0));
#pragma unroll
      for (int i = 0; i < K::NXP; ++i) {
        const int rho = 8 * (q0 + i) + (lane >> 3);
        const int pp = rho >= 80 ? 1 : 0, u = rho - 80 * pp;
        const int tok = 128 * a - 32 + 2 * u + pp;
        const unsigned v = tok * 1280 + (((lane & 7) ^ ((rho >> 1) & 7)) << 4);
        k.xv[i] = (u >= 9 && tok >= 0) ? v : OOB;
      }
#pragma unroll
      for (int j = 0; j < 2; ++j) {
        const unsigned I = (2 * w + j) * 64 + lane;
        const unsigned kb = (I >= 248) + (I >= 488) + (I >= 736);
        const unsigned i = I - 240 * kb - 8 * ((kb + 1) >> 1);
        const unsigned r = (i * 2185u) >> 16, rem = i - r * 30;
        const unsigned seg = (rem * 6554u) >> 16, ch = rem - seg * 10;
        const unsigned v = (8 * kb + r) * 3840 + seg * 1280 + g * 160 + ch * 16;
        k.wv[j] = i < 240 ? v : OOB;
      }
      asm volatile("" : "+v"(k.xv[0]), "+v"(k.xv[1]), "+v"(k.xv[K::NXP - 1]), "+v"(k.wv[0]), "+v"(k.wv[1]));
      bind();
      STAMP(0);
      k.srdx = mk_srd(x, 2048 * 1280);
      k.srdw = mk_srd(wqkv, 640 * 3840);
      unsigned gv0, gv1;
      asm volatile("global_load_dwordx2 %0, %1, %2 sc1" : "=v"(ev) : "v"(WS_EPOCH), "s"(ws) : "memory");
      asm volatile("buffer_load_ushort %0, %2, %3, 0 offen\n\tbuffer_load_ushort %1, %2, %3, 0 offen offset:1024"
                   : "=&v"(gv0), "=&v"(gv1) : "v"(tid * 2), "s"(mk_srd(gw, 1280)) : "memory");
      STAMP(11);
      k.stage0(rot);
      BAR();
      k.stage1();
      k.xa0 = (K::XB + par * 5 * 2048 + c * 128 + ((h ^ (c >> 1)) << 4)) ^ ((rot & 1) << 6);
      const unsigned kbo = h * 3840 + ((h + 1) >> 1) * 128;
      k.wa0 = kbo + q4 * 480 + (20 * hd + 4 * p4) * 2;
      const bool isgate = p4 == 3;
      k.gmask = isgate ? ~0u : 0u;
      asm volatile("" : "+v"(k.gmask));
      k.wa1 = isgate ? GATE_OFF + (8 * h + q4) * 8 : kbo + q4 * 480 + p4 * 160 + (20 * hd + 16) * 2;
      k.wa2 = k.wa1 + (isgate ? 32 : 1920);
      f32x4 ba, bb, ba1;
#pragma unroll
      for (int r = 0; r < 4; ++r) {
        const int kap = 4 * h + r;
        ba[r] = kap >= c + 9 ? 0.f : -1e30f;
        bb[r] = (kap <= c && kap >= c - 7) ? 0.f : -1e30f;
        ba1[r] = a == 0 ? -1e30f : ba[r];
      }
      unsigned m0mask = h == 0 ? 0xffffffffu : 0u;
      unsigned scw = w * 15360 + 10240 + c * 32 + h * 8, sck = w * 15360 + 10240 + c * 32 + 8,
               sct = w * 15360 + 10240 + (4 * h + q4) * 32 + p4 * 8;
      unsigned ovo = WS_O + (h & 1) * (2 * 20480) + (2 * g + (hd >> 1)) * 1024 + (2 * (hd & 1) + (h >> 1)) * 256 + c * 16;
      unsigned ovt = h == 2 ? WS_O + (32 + g) * 512 + hd * 128 + c * 8 : OOB;
      asm volatile("" : "+v"(ba), "+v"(bb), "+v"(ba1), "+v"(m0mask), "+v"(scw), "+v"(sck), "+v"(sct), "+v"(ovo), "+v"(ovt));
      asm volatile("" : "+v"(k.xa0), "+v"(k.wa0));
      k.zero();
      k.stage2();
      STAMP(12);
      k.template run<0, RB ? 10 : 0>([&]() {
        asm volatile("s_waitcnt vmcnt(%2)" : "+v"(gv0), "+v"(gv1) : "i"(K::VPRO) : "memory");
        u32x2 v; v.x = gv0; v.y = 0;
        *(LAS u32x2*)(GATE_OFF + tid * 8) = v;
        if (!RB && tid < 128) { v.x = gv1; *(LAS u32x2*)(GATE_OFF + 4096 + tid * 8) = v; }
        make_tag(std::integral_constant<int, K::VPRO>{});
        WAITL();
        BAR();
        STAMP(1);
      }, [&](int t) { if (RB) side_b(t); }, tail, [](int) {});
      STAMP(2);
      u32x2 ql[5], kl[5], t2[5], vl[5], tt[5], k4[5];
      unsigned sb16[4];
#pragma unroll
      for (int i = 0; i < 5; ++i) { t2[i] = pk4(k.acc[i][2]); *(LAS u32x2*)(scw + i * 512) = t2[i]; }
#pragma unroll
      for (int i = 0; i < 5; ++i) { if (i) ql[i] = pk4(k.acc[i][0]); kl[i] = pk4(k.acc[i][1]); }
      WAITL();
#pragma unroll
      for (int i = 0; i < 5; ++i) k4[i] = *(const LAS u32x2*)(sck + i * 512);
#pragma unroll
      for (int i = 0; i < 4; ++i) sb16[i] = *(const LAS unsigned*)(sck + (i + 1) * 512 + 16);
#pragma unroll
      for (int i = 0; i < 5; ++i) tt[i] = ldstr(sct + i * 512);
#pragma unroll
      for (int i = 0; i < 5; ++i) vl[i] = pk4(k.acc[i][3]);
      constexpr float SC = 0.22360679774997896f * LOG2E;
      f32x4 sa[4], sb[4];
#pragma unroll
      for (int i = 1; i < 5; ++i) {
        u32x4 qf; qf.x = ql[i].x; qf.y = ql[i].y; qf.z = t2[i].x & m0mask; qf.w = t2[i].y & m0mask;
        sb[i - 1] = mfma(cat(kl[i], k4[i]), qf, bb);
        sa[i - 1] = mfma(cat(kl[i - 1], k4[i - 1]), qf, i == 1 ? ba1 : ba);
      }
      asm volatile("s_nop 7\n\ts_nop 0" : "+v"(sa[0]), "+v"(sa[1]), "+v"(sa[2]), "+v"(sa[3]), "+v"(sb[0]), "+v"(sb[1]), "+v"(sb[2]), "+v"(sb[3]));
      float mx[4], es[4];
#pragma unroll
      for (int i = 0; i < 4; ++i)
        mx[i] = max3(max3(sb[i].x, sb[i].y, sb[i].z), max3(sb[i].w, sa[i].x, sa[i].y), max2(sa[i].z, sa[i].w));
#pragma unroll
      for (int i = 0; i < 4; ++i) {
        auto r = __builtin_amdgcn_permlane16_swap(f2u(mx[i]), f2u(mx[i]), false, false);
        mx[i] = max2(u2f(r[0]), u2f(r[1]));
      }
#pragma unroll
      for (int i = 0; i < 4; ++i) {
        auto r = __builtin_amdgcn_permlane32_swap(f2u(mx[i]), f2u(mx[i]), false, false);
        mx[i] = max2(u2f(r[0]), u2f(r[1])) * SC;
        es[i] = __builtin_amdgcn_exp2f(__builtin_amdgcn_fmed3f(u2f(sb16[i] << 16), -60.f, 60.f) * LOG2E);
      }
      u32x4 pf[4];
#pragma unroll
      for (int i = 0; i < 4; ++i) {
        f32x4 pa, pb;
#pragma unroll
        for (int r = 0; r < 4; ++r) {
          pa[r] = __builtin_amdgcn_exp2f(sa[i][r] * SC - mx[i]);
          pb[r] = __builtin_amdgcn_exp2f(sb[i][r] * SC - mx[i]);
        }
        pf[i] = cat(pk4(pa), pk4(pb));
      }
      const f32x4 z = {0.f, 0.f, 0.f, 0.f};
      const u32x4 ones = {0x3f803f80u, 0x3f803f80u, 0x3f803f80u, 0x3f803f80u};
      f32x4 ls[4], oa[4], ob[4];
#pragma unroll
      for (int i = 0; i < 4; ++i) {
        ls[i] = mfma(ones, pf[i], z);
        oa[i] = mfma(cat(vl[i], vl[i + 1]), pf[i], z);
        ob[i] = mfma(cat(tt[i], tt[i + 1]), pf[i], z);
      }
      const unsigned mt0 = 8 * a + par;
      u32x2 op[4];
#pragma unroll
      for (int i = 0; i < 4; ++i) {
        const float f = es[i] * __builtin_amdgcn_rcpf(ls[i].x);
        op[i] = pk4(oa[i] * f);
        __builtin_amdgcn_raw_buffer_store_b64(pk4(ob[i] * f), rws, ovt, (mt0 + 2 * i) * 20480, AUX_SC1);
        if (i & 1) {
          auto sx = __builtin_amdgcn_permlane16_swap(op[i - 1].x, op[i].x, false, false);
          auto sy = __builtin_amdgcn_permlane16_swap(op[i - 1].y, op[i].y, false, false);
          u32x4 o; o.x = sx[0]; o.y = sy[0]; o.z = sx[1]; o.w = sy[1];
          __builtin_amdgcn_raw_buffer_store_b128(o, rws, ovo, (mt0 + 2 * i - 2) * 20480, AUX_SC1);
          STPAD();
        }
      }
      if (g == 0 && hd == 0) {
        const float e = h == 0 ? es[0] : h == 1 ? es[1] : h == 2 ? es[2] : es[3];
        __builtin_amdgcn_raw_buffer_store_b32(f2u(e), rws, WS_G + h * 128 + c * 4, mt0 * 64, AUX_SC1);
      }
    } else {
      const int cj = gc;
      using K = P1<false, RB>;
      K k;
      const int mh = RB ? (w < 6 ? 1 : 0) : (w >> 1);
      const int up = (w >> 1) & 1, kp = w & 1;
#pragma unroll
      for (int i = 0; i < K::NXP; ++i) {
        const int rho = 8 * (4 * (w & 3) + i) + (lane >> 3);
        const int j = rho >> 4;
        const int tok = 128 * a + 32 * (j >> 1) + 2 * (rho & 15) + (j & 1);
        k.xv[i] = tok * 1280 + (((lane & 7) ^ ((rho >> 1) & 7)) << 4);
      }
      k.xd = K::XB + 4 * (w & 3) * 1024;
      {
        const int r = lane >> 3, p8 = lane & 7;
        const int s = ((r >> 1) & 1) + 2 * kp;
        k.wv[0] = (8 * kp + r) * 3072 + cj * 384 + (((((p8 >> 1) ^ s) << 1) | (p8 & 1)) << 4) + (RB ? 0 : 16 * 3072 + 128);
        k.wv[1] = k.wv[0] + 128; k.wv[2] = k.wv[0] + 256; k.wv[3] = k.wv[0] + 16 * 3072;
      }
      k.wd0 = RB ? (kp * 6 + 3 * up) * 1024 : ((kp + 2) * 6 + 3 * up + 1) * 1024;
      k.wd1 = ((kp + 2) * 6 + 3 * up) * 1024;
      if (RB) asm volatile("" : "+v"(k.xv[0]), "+v"(k.xv[1]), "+v"(k.xv[2]), "+v"(k.xv[3]), "+v"(k.wv[2]), "+v"(k.wv[3]));
      asm volatile("" : "+v"(k.wv[0]), "+v"(k.wv[1]));
      bind();
      STAMP(0);
      const u64 pg = wg, pu = wu;
      k.srdx = mk_srd(x, 2048 * 1280);
      k.srdw = mk_srd(up ? pu : pg, 640 * 3072);
      asm volatile("global_load_dwordx2 %0, %1, %2 sc1" : "=v"(ev) : "v"(WS_EPOCH), "s"(ws) : "memory");
      STAMP(11);
      k.stage0(rot);
      BAR();
      if (!RB) pack_issue();
      k.stage1();
      k.xa0 = (K::XB + mh * 4 * 2048 + c * 128 + ((h ^ (c >> 1)) << 4)) ^ ((rot & 1) << 6);
      {
        const int s = ((q4 >> 1) & 1) + 2 * (h & 1);
        k.wa0 = (h * 6 + (RB ? 2 : (w & 1))) * 1024 + q4 * 128 + (((RB ? 2 * (w & 1) : 0) ^ s) << 5) + p4 * 8;
      }
      asm volatile("" : "+v"(k.xa0), "+v"(k.wa0));
      k.zero();
      k.stage2();
      STAMP(12);
      k.template run<0, RB ? 10 : 0>([&]() {
        make_tag(std::integral_constant<int, RB ? K::VPRO : 8>{});
        BAR();
        STAMP(1);
      }, [&](int t) { if (RB) side_b(t); else side_f(t); }, tail, pack_gap);
      STAMP(2);
      constexpr int NK = RB ? 1 : 2;
      float nl2e = -LOG2E;
      asm volatile("" : "+s"(nl2e));
      const unsigned gso = WS_GU + ((8 * a + 4 * mh) * 48 + 6 * cj + (RB ? 4 + (w & 1) : 2 * (w & 1))) * 1024;
#pragma unroll
      for (int m = 0; m < 4; ++m) {
#pragma unroll
        for (int kk = 0; kk < NK; ++kk) {
          float v[8];
#pragma unroll
          for (int j = 0; j < 8; j += 2) {
            const f32x2 g2 = {k.acc[m][2 * kk + (j >> 2)][j & 3], k.acc[m][2 * kk + (j >> 2)][(j & 3) + 1]};
            const f32x2 u2 = {k.acc[m][2 * NK + 2 * kk + (j >> 2)][j & 3], k.acc[m][2 * NK + 2 * kk + (j >> 2)][(j & 3) + 1]};
            const f32x2 a2 = g2 * nl2e;
            const f32x2 d2 = f32x2{__builtin_amdgcn_exp2f(a2.x), __builtin_amdgcn_exp2f(a2.y)} + 1.f;
            const f32x2 r2 = g2 * u2 * f32x2{__builtin_amdgcn_rcpf(d2.x), __builtin_amdgcn_rcpf(d2.y)};
            v[j] = r2.x; v[j + 1] = r2.y;
          }
          u32x4 o; o.x = pk2(v[0], v[1]); o.y = pk2(v[2], v[3]); o.z = pk2(v[4], v[5]); o.w = pk2(v[6], v[7]);
          __builtin_amdgcn_raw_buffer_store_b128(o, rws, lane * 16, gso + m * (48 * 1024) + kk * 1024, AUX_SC1);
          STPAD();
        }
      }
    }
    SB();
    STAMP(13);
    asm volatile("" : "+v"(wo_[0][0]), "+v"(wo_[0][1]), "+v"(wo_[0][2]), "+v"(wo_[0][3]), "+v"(wo_[0][4]),
                      "+v"(wo_[1][0]), "+v"(wo_[1][1]), "+v"(wo_[1][2]), "+v"(wo_[1][3]), "+v"(wo_[1][4]));
    if (RB) asm volatile("" : "+v"(wg_[0][0]), "+v"(wg_[0][1]), "+v"(wg_[0][2]), "+v"(wg_[0][3]), "+v"(wg_[0][4]),
                              "+v"(wg_[1][0]), "+v"(wg_[1][1]), "+v"(wg_[1][2]), "+v"(wg_[1][3]), "+v"(wg_[1][4]));
    SB();
    STAMP(14);
    if (RB && idx >= 16) { if (PRE > 0) cell_dma(w - 4, 1); if (PRE > 1) cell_dma(w, 1); if (PRE > 2) cell_dma(w - 4, 2); WAITV(5 * PRE); }
    else if (RB || !PREA) WAITV(0);
    else { late_w(); WAITV(15); }
    STAMP(3);
    WAITL();
    BAR();
    if (!RB && w == 0 && lane < 2) {
      GAS u64* fp = (GAS u64*)((char*)ws + (lane ? WS_DONE + b * 8 : WS_FLAG + (a * 16 + (idx < 16 ? 8 : 0) + gc) * 8));
      __hip_atomic_store(fp, tag, RLX);
    }
    STAMP(4);

    SB();
    const unsigned fo = WS_FLAG + ((rb >> 1) * 16 + c) * 8;
    auto wait_flags = [&](u32x2& v, bool need) {
#pragma unroll 1
      for (unsigned spin = 0; spin < (1u << 18); ++spin) {
        if (__all(!need || (v.x == (unsigned)tag && v.y == (unsigned)(tag >> 32)))) break;
        ld64(v, fo, sws, 0);
        asm volatile("s_waitcnt vmcnt(0)" : "+v"(v) :: "memory");
      }
      asm volatile("" ::: "memory");
    };
    u32x2 fv;
    if (RB) {
      if (idx < 16) { if (PRE > 0) cell_dma(w - 4, 1); if (PRE > 1) cell_dma(w, 1); if (PRE > 2) cell_dma(w - 4, 2); }
      if (PRE < 1) cell_dma(w - 4, 1); if (PRE < 2) cell_dma(w, 1); if (PRE < 3) cell_dma(w - 4, 2);
      cell_dma(w, 2);
    } else {
      if (!PREA) late_w();
      if (w == 0) {
        ld64(fv, fo, sws, 0);
        asm volatile("s_waitcnt vmcnt(0)" : "+v"(fv) :: "memory");
        wait_flags(fv, c >= 8);
      }
    }
    if (RB) __builtin_amdgcn_s_setprio(1);
    BAR();
    STAMP(5);
    f32x4 acc[4][5];
    u32x4 ag[6][4];
    auto tk = [](int) {};
    tk(0);
    const unsigned gb = (EXP & 1) ? pwb : WS_GU + (4 * rb * 48 + 6 * w) * 1024;
    auto ldg = [&](int s, int m) { ld128(ag[s][m], lane * 16, sws, gb + gstep(s) * 1024 + m * (48 * 1024)); };
    unsigned ca0 = CELL0 + w * 5120 + lane * 16, ca1 = w * 15360 + lane * 16;
    asm volatile("" : "+v"(ca0), "+v"(ca1));
    {
      using S = Sched<RB>;
      constexpr int NO = S::NO, OB = S::OB;
      u32x4 af[2][4], at[4];
      u32x2 tl[4], th[4];
      const unsigned ob = WS_O + 4 * rb * 20480;
      auto req = [&](auto IT) {
        constexpr int i = IT.value;
        if constexpr (i < 8) ld128(af[i >> 2][i & 3], lane * 16, sws, ob + (i & 3) * 20480 + (2 * w + (i >> 2)) * 1024);
        else if constexpr (i < OB) ld64((i & 1) ? th[(i - 8) >> 1] : tl[(i - 8) >> 1], lane * 8, sws, ob + ((i - 8) >> 1) * 20480 + (16 + w) * 1024 + (i & 1) * 512);
        else if constexpr (i < OB + 29 && (i - OB) % 5 < 4) ldg((i - OB) / 5, (i - OB) % 5);
        else if constexpr (i < OB + 29) dma16c(w * 15360, 10240 + ((i - OB) / 5) * 1024, lane * 16, sws, pwb + (20 + 6 * w + gstep(5)) * 5120 + ((i - OB) / 5) * 1024);
        else ld32(i == OB + 29 ? sg0 : i == OB + 30 ? sg1 : sg2, c * 4, sws, WS_G + (4 * rb + min(t0 + i - OB - 29, 19) / 5) * 64);
      };
      if (RB || w != GPOLL || !GPOLL) sfor<0, S::upto(PL - 1)>(req);
      if (RB) { __builtin_amdgcn_s_setprio(0); WAITV(S::upto(PL - 1)); }
      else if (w == GPOLL) { if (GPOLL) { fv.x = ~(unsigned)tag; fv.y = 0; } wait_flags(fv, c < 8); }
      STAMP(6);
      BAR();
      STAMP(7);
      if (!RB && GPOLL && w == GPOLL) sfor<0, S::upto(PL - 1)>(req);
      tk(1);
      u32x4 wc[5];
      sfor<0, NO + 6>([&](auto ST) {
        constexpr int st = ST.value, s = st - NO;
        if constexpr (st == 2 && !RB) {
          asm volatile("s_waitcnt vmcnt(%8)" : "+v"(tl[0]), "+v"(tl[1]), "+v"(tl[2]), "+v"(tl[3]), "+v"(th[0]), "+v"(th[1]), "+v"(th[2]), "+v"(th[3])
                       : "i"(S::issued(st, 0) - 16) : "memory");
#pragma unroll
          for (int m = 0; m < 4; ++m) at[m] = cat(tl[m], th[m]);
          asm volatile("s_nop 1" : "+v"(at[0]), "+v"(at[1]), "+v"(at[2]), "+v"(at[3]));
        }
        sfor<0, 20>([&](auto IT) {
          constexpr int i = IT.value, m = i / 5, n = i % 5, is = S::issued(st, i);
          if constexpr (WGR == 1 ? n == 0 : i % (5 * WGR) == 0) {
            constexpr int ml = m + WGR - 1;
            if constexpr (WGR == 1) {
              if constexpr (s >= 0)      asm volatile("s_waitcnt vmcnt(%1)" : "+v"(ag[s][m]) : "i"(is - S::gpos(s, m) - 1) : "memory");
              else if constexpr (st < 2) asm volatile("s_waitcnt vmcnt(%1)" : "+v"(af[st][m]) : "i"(is - (4 * st + m) - 1) : "memory");
            } else if constexpr (WGR == 2) {
              if constexpr (s >= 0)      asm volatile("s_waitcnt vmcnt(%2)" : "+v"(ag[s][m]), "+v"(ag[s][ml]) : "i"(is - S::gpos(s, ml) - 1) : "memory");
              else if constexpr (st < 2) asm volatile("s_waitcnt vmcnt(%2)" : "+v"(af[st][m]), "+v"(af[st][ml]) : "i"(is - (4 * st + ml) - 1) : "memory");
            } else {
              if constexpr (s >= 0)      asm volatile("s_waitcnt vmcnt(%4)" : "+v"(ag[s][0]), "+v"(ag[s][1]), "+v"(ag[s][2]), "+v"(ag[s][3]) : "i"(is - S::gpos(s, 3) - 1) : "memory");
              else if constexpr (st < 2) asm volatile("s_waitcnt vmcnt(%4)" : "+v"(af[st][0]), "+v"(af[st][1]), "+v"(af[st][2]), "+v"(af[st][3]) : "i"(is - (4 * st + 3) - 1) : "memory");
            }
          }
          if constexpr (st == 0 && i == 0 && !RB)
            asm volatile("" : "+v"(wo_[2][0]), "+v"(wo_[2][1]), "+v"(wo_[2][2]), "+v"(wo_[2][3]), "+v"(wo_[2][4]),
                              "+v"(wg_[0][0]), "+v"(wg_[0][1]), "+v"(wg_[0][2]), "+v"(wg_[0][3]), "+v"(wg_[0][4]),
                              "+v"(wg_[1][0]), "+v"(wg_[1][1]), "+v"(wg_[1][2]), "+v"(wg_[1][3]), "+v"(wg_[1][4]));
          if constexpr (st == 0)     mul0(acc[m][n], wo_[0][n], af[0][m]);
          else if constexpr (s >= 0) mac(acc[m][n], wg_[s & 1][n], ag[s][m]);
          else if constexpr (st < 2) mac(acc[m][n], wo_[1][n], af[1][m]);
          else                       mac(acc[m][n], wo_[2][n], at[m]);
          SB();
          if constexpr (s == 4 && i == 9) WAITV(is - S::gpos(4, 4) - 1);
          if constexpr (m == (s == 4 ? 2 : 0) && s >= 1 && s < 5) wc[n] = lds128((s > 1 ? ca1 + (s - 2) * 5120 : ca0) + n * 1024);
          if constexpr (S::issued(st, i + 1) > is) req(std::integral_constant<int, is>{});
          SB();
        });
        tk(2 + st);
        if constexpr (s >= 1 && s < 5) {
          asm volatile("" : "+v"(wc[0]), "+v"(wc[1]), "+v"(wc[2]), "+v"(wc[3]), "+v"(wc[4]));
#pragma unroll
          for (int n = 0; n < 5; ++n) wg_[(s + 1) & 1][n] = wc[n];
        }
      });
      asm volatile("s_waitcnt vmcnt(0)" : "+v"(sg0), "+v"(sg1), "+v"(sg2) :: "memory");
    }
    MFMA_DONE();
    STAMP(8);
#pragma unroll
    for (int j = 0; j < 10; ++j) {
      const int ta = j < 4 ? 3 * j : j < 6 ? 6 * j - 22 : 2 * j, tb = j < 4 ? ta + 1 : j < 6 ? ta + 3 : ta + 1;
      *(LAS u32x4*)(ca1 + j * 1024) = cat(pk4(acc[ta / 5][ta % 5]), pk4(acc[tb / 5][tb % 5]));
    }
    tk(12);
  };
  if (w < 4) role(std::false_type{}); else role(std::true_type{});

  if (b == 0 && w == 2) {
    GAS u64* dp = (GAS u64*)((char*)ws_ + WS_DONE + lane * 8);
#pragma unroll 1
    for (unsigned spin = 0; spin < (1u << 18); ++spin) {
      bool ok = true;
#pragma unroll
      for (int j = 0; j < 4; ++j) ok &= __hip_atomic_load(dp + j * 64, RLX) == tag;
      if (__all(ok)) break;
      __builtin_amdgcn_s_sleep(2);
    }
    if (lane == 0) __hip_atomic_store((GAS u64*)((char*)ws_ + WS_EPOCH), tag, RLX);
  }
  sg0 = __builtin_amdgcn_rcpf(1.f + sg0); sg1 = __builtin_amdgcn_rcpf(1.f + sg1); sg2 = __builtin_amdgcn_rcpf(1.f + sg2);
  auto ooff = [&](int tl) {
    const int m = tl / 5, n = tl - 5 * m;
    return (unsigned)((64 * rb + 32 * (m >> 1) + 2 * c + (m & 1)) * 1280 + ct * 160 + n * 32 + h * 8);
  };
  unsigned oo0 = ooff(t0), oo1 = ooff(t0 + 1), oo2 = ooff(t0 + 2);
  asm volatile("" : "+v"(sg0), "+v"(sg1), "+v"(sg2), "+v"(oo0), "+v"(oo1), "+v"(oo2));
  WAITL();
  BAR();
  STAMP(9);
  {
    u32x4 p[8];
    u32x2 p2[8];
    unsigned ra = (w < 4 ? w : w + 2) * 1024 + lane * 16, rb_ = ra + 4 * 15360;
    unsigned rc = (4 + (w >> 1)) * 1024 + (w & 1) * 8 + lane * 16, rd = rc + 4 * 15360;
    asm volatile("" : "+v"(ra), "+v"(rb_), "+v"(rc), "+v"(rd));
#pragma unroll
    for (int v = 0; v < 8; ++v) p[v] = lds128((v < 4 ? ra : rb_) + (v & 3) * 15360);
    if (w < 4) {
#pragma unroll
      for (int v = 0; v < 8; ++v) { p2[v] = *(const LAS u32x2*)((v < 4 ? rc : rd) + (v & 3) * 15360); SB(); }
    }
    SB();
#pragma unroll
    for (int j = 0; j < 3; ++j) {
      if (j < 2 || w < 4) {
        f32x4 s = {0.f, 0.f, 0.f, 0.f};
#pragma unroll
        for (int v = 0; v < 8; ++v) {
          const unsigned px = j == 0 ? p[v].x : j == 1 ? p[v].z : p2[v].x, py = j == 0 ? p[v].y : j == 1 ? p[v].w : p2[v].y;
          const f32x4 q = {u2f(px << 16), u2f(px & 0xffff0000u), u2f(py << 16), u2f(py & 0xffff0000u)};
          s = v ? s + q : q;
        }
        const u32x2 ov = pk4(s * (j == 0 ? sg0 : j == 1 ? sg1 : sg2));
        asm volatile("global_store_dwordx2 %0, %1, %2" OUTFL :: "v"(j == 0 ? oo0 : j == 1 ? oo1 : oo2), "v"(ov), "s"(out_) : "memory");
      }
    }
  }
  STAMP(10);
  if (AMP0 >= 0 && (AMPSEL == 0 || (AMPSEL == 1) == (idx < 16))) {
    const u64 d = amp_t1 - amp_t0, t = __builtin_amdgcn_s_memrealtime();
    while (__builtin_amdgcn_s_memrealtime() - t < d) __builtin_amdgcn_s_sleep(1);
  }
}
}

extern "C" void kernel_launch(void* const* d_in, const int* in_sizes, int n_in, void* d_out, int out_size, void* d_ws,
                              size_t ws_size, hipStream_t stream) {
  fused<<<256, 512, 0, stream>>>(d_in[0], d_in[1], d_in[2], d_in[4], d_in[5], d_in[6], d_in[7], d_out, d_ws);
}
```

```cpp
#include <hip/hip_runtime.h>
#include <hip/hip_bf16.h>
#include <stdint.h>
#include <type_traits>

#define STAMPS 0
#define BEARLY 0
#define CODEPF 1
#define TOUCH_NT ""
#define TOUCH 1
#define EXP 0
#define GLA 4
#define PART16 1
#define PL 2
#define PRE 2
#define OUTFL ""
#define PREA 0
#define WGR 4
#define GPOLL 1
#define ROT 4

#define LAS __attribute__((address_space(3)))
#define GAS __attribute__((address_space(1)))
typedef __bf16 bf16x8 __attribute__((ext_vector_type(8)));
typedef __bf16 bf16x2 __attribute__((ext_vector_type(2)));
typedef float f32x4 __attribute__((ext_vector_type(4)));
typedef float f32x2 __attribute__((ext_vector_type(2)));
typedef unsigned u32x4 __attribute__((ext_vector_type(4)));
typedef unsigned u32x2 __attribute__((ext_vector_type(2)));
typedef short s16x4 __attribute__((ext_vector_type(4)));
typedef unsigned long long u64;

namespace {
constexpr int LDS_BYTES = 163840;
constexpr unsigned GATE_OFF = 110592, PACK_OFF = 122880, PKF_OFF = 162816, CELL0 = 122880;
constexpr unsigned OOB = 0x40000000u;
#define CODE_B 40000
constexpr unsigned CODE_BYTES = CODE_B;
constexpr unsigned WS_FLAG = 0;
constexpr unsigned WS_DONE = 4096;
constexpr unsigned WS_PKF = 8192;
constexpr unsigned WS_EPOCH = 12288;
constexpr unsigned WS_G = 16384;
constexpr unsigned WS_O = 131072;
constexpr unsigned WS_GU = WS_O + 128 * 40 * 512;
constexpr unsigned WS_PW = WS_GU + 128 * 48 * 1024;
constexpr unsigned WS_END = WS_PW + 8 * 68 * 5120;
constexpr unsigned WS_STAMP = 16u << 20;
constexpr int AUX_SC1 = 16;
constexpr float LOG2E = 1.4426950408889634f;

__device__ __forceinline__ u32x4 mk_srd(u64 a, unsigned bytes) {
  u32x4 r; r.x = (unsigned)a; r.y = (unsigned)(a >> 32) & 0xffffu; r.z = bytes; r.w = 0x00020000u; return r;
}
__device__ __forceinline__ void dma16(unsigned lds, unsigned imm, unsigned voff, u32x4 srd, unsigned soff) {
  asm volatile("s_add_u32 m0, %0, %4\n\ts_nop 0\n\tbuffer_load_dwordx4 %1, %2, %3 offen lds"
               :: "s"(lds), "v"(voff), "s"(srd), "s"(soff), "i"(imm) : "memory", "scc");
}
__device__ __forceinline__ void dma16c(unsigned lds, unsigned imm, unsigned voff, u32x4 srd, unsigned soff) {
  asm volatile("s_add_u32 m0, %0, %4\n\ts_nop 0\n\tbuffer_load_dwordx4 %1, %2, %3 offen sc1 lds"
               :: "s"(lds), "v"(voff), "s"(srd), "s"(soff), "i"(imm) : "memory", "scc");
}
__device__ __forceinline__ void ld128(u32x4& d, unsigned voff, u32x4 srd, unsigned soff) {
  asm volatile("buffer_load_dwordx4 %0, %1, %2, %3 offen sc1" : "=v"(d) : "v"(voff), "s"(srd), "s"(soff) : "memory");
}
__device__ __forceinline__ void ld64(u32x2& d, unsigned voff, u32x4 srd, unsigned soff) {
  asm volatile("buffer_load_dwordx2 %0, %1, %2, %3 offen sc1" : "=v"(d) : "v"(voff), "s"(srd), "s"(soff) : "memory");
}
__device__ __forceinline__ void ld32(float& d, unsigned voff, u32x4 srd, unsigned soff) {
  asm volatile("buffer_load_dword %0, %1, %2, %3 offen sc1" : "=v"(d) : "v"(voff), "s"(srd), "s"(soff) : "memory");
}
__device__ __forceinline__ u32x4 lds128(unsigned a) { return *(const LAS u32x4*)a; }
__device__ __forceinline__ u32x2 ldstr(unsigned a) {
  return __builtin_bit_cast(u32x2, __builtin_amdgcn_ds_read_tr16_b64_v4i16((LAS s16x4*)a));
}
__device__ __forceinline__ u32x4 cat(u32x2 a, u32x2 b) { u32x4 r; r.x = a.x; r.y = a.y; r.z = b.x; r.w = b.y; return r; }
__device__ __forceinline__ f32x4 mfma(u32x4 a, u32x4 b, f32x4 c) {
  return __builtin_amdgcn_mfma_f32_16x16x32_bf16(__builtin_bit_cast(bf16x8, a), __builtin_bit_cast(bf16x8, b), c, 0, 0, 0);
}
__device__ __forceinline__ void mac(f32x4& c, u32x4 a, u32x4 b) {
  asm volatile("v_mfma_f32_16x16x32_bf16 %0, %1, %2, %0" : "+v"(c) : "v"(a), "v"(b));
}
__device__ __forceinline__ void mul0(f32x4& c, u32x4 a, u32x4 b) {
  asm volatile("v_mfma_f32_16x16x32_bf16 %0, %1, %2, 0" : "=&v"(c) : "v"(a), "v"(b));
}
__device__ __forceinline__ unsigned pk2(float a, float b) {
  f32x2 t = {a, b};
  return __builtin_bit_cast(unsigned, __builtin_convertvector(t, bf16x2));
}
__device__ __forceinline__ u32x2 pk4(f32x4 v) { u32x2 r; r.x = pk2(v.x, v.y); r.y = pk2(v.z, v.w); return r; }
__device__ __forceinline__ float u2f(unsigned u) { return __builtin_bit_cast(float, u); }
__device__ __forceinline__ unsigned f2u(float f) { return __builtin_bit_cast(unsigned, f); }
#define SB() __builtin_amdgcn_sched_barrier(0)
#define WAITV(n) asm volatile("s_waitcnt vmcnt(%0)" :: "i"(n) : "memory")
#define WAITVL(n) asm volatile("s_waitcnt vmcnt(%0) lgkmcnt(0)" :: "i"(n) : "memory")
#define WAITL() asm volatile("s_waitcnt lgkmcnt(0)" ::: "memory")
#define BAR() asm volatile("s_barrier" ::: "memory")
#define STPAD() do { SB(); asm volatile("s_nop 1" ::: "memory"); SB(); } while (0)
#define MFMA_DONE() do { SB(); asm volatile("s_nop 7\n\ts_nop 1" ::: "memory"); SB(); } while (0)
#define RLX __ATOMIC_RELAXED, __HIP_MEMORY_SCOPE_AGENT
__device__ __forceinline__ unsigned rfl(unsigned v) { return __builtin_amdgcn_readfirstlane(v); }
__device__ __forceinline__ unsigned opaque_lane() { unsigned l = threadIdx.x & 63; asm volatile("" : "+v"(l)); return l; }

#define AMP0 -1
#define AMP1 -1
#define AMPSEL 0
#define STAMP(i) do {} while (0)

template <int I, int N, class F>
__device__ __forceinline__ void sfor(F&& f) {
  if constexpr (I < N) { f(std::integral_constant<int, I>{}); sfor<I + 1, N>(f); }
}
template <bool RB> struct Sched {
  static constexpr int NO = RB ? 2 : 3, OB = RB ? 8 : 16;
  static constexpr int gpos(int s, int m) { return OB + 5 * s + m; }
  static constexpr int upto(int st) { return st < 2 ? 4 * (st + 1) : st < NO ? 16 : st < NO + 5 ? OB + 5 * (st - NO + 1) : st == NO + 5 ? OB + 29 : OB + 32; }
  static constexpr int issued(int st, int i) {
    const int base = upto(st + PL - 1), goal = upto(st + PL), every = goal - base > 5 ? 2 : goal - base > 4 ? 4 : 5;
    int n = base;
    for (int k = 0; k < i; ++k) if (k % every == 1 && n < goal) ++n;
    return n;
  }
};
__device__ __forceinline__ float max3(float a, float b, float c) { float r; asm("v_max3_f32 %0, %1, %2, %3" : "=v"(r) : "v"(a), "v"(b), "v"(c)); return r; }
__device__ __forceinline__ float max2(float a, float b) { float r; asm("v_max_f32 %0, %1, %2" : "=v"(r) : "v"(a), "v"(b)); return r; }

template <bool QKV, bool RB>
struct P1 {
  static constexpr int NM = QKV ? 5 : 4;
  static constexpr int NN = QKV ? 4 : (RB ? 4 : 8);
  static constexpr int NWS = 3;
  static constexpr unsigned WSL = QKV ? 16384 : 24576, XB = NWS * WSL, XSL = QKV ? 20480 : 16384;
  static constexpr unsigned WROW = QKV ? 3840 : 3072;
  static constexpr int NWP = QKV ? 2 : (RB ? 4 : 2);
  static constexpr int NXE = QKV ? (RB ? 1 : 2) : (RB ? 2 : 0), NXO = QKV ? 1 : NXE;
  static constexpr int NXP = NXO + NXE;
  static constexpr int OO = NWP + NXO, OE = NWP + NXE;
  static constexpr bool HAND = !QKV;
  static constexpr int ht = 4;
  f32x4 acc[NM][NN];
  u32x4 srdx, srdw;
  unsigned xv[4], wv[4];
  unsigned xd;
  unsigned wd0, wd1;
  unsigned xa0, wa0, wa1, wa2;
  unsigned gmask;
  __device__ __forceinline__ void tick(int) {}

  unsigned sow, sox, gof;
  unsigned kg, kw, kx, kr;
  template <bool ODD, bool X = true>
  __device__ __forceinline__ void loads(unsigned wwr, unsigned xwr) {
    if (X && NXP) {
#pragma unroll
      for (int i = ODD ? NXE : 0; i < (ODD ? NXP : NXE); ++i) dma16(xwr + xd, i * 1024, xv[i], srdx, sox);
      if (ODD) { kx += 1; sox = (kx ^ (kg >> 1)) * 128; }
    }
    dma16(wwr + wd0, 0, wv[0], srdw, sow);
    dma16(wwr + wd0, 1024, wv[1], srdw, sow);
    if (!QKV && RB) {
      dma16(wwr + wd0, 2048, wv[2], srdw, sow);
      dma16(wwr + wd1, 0, wv[3], srdw, sow);
    }
  }
  __device__ __forceinline__ void other(unsigned wwr) {
    dma16(wwr + wd1, 1024, wv[3], srdw, sow + 128);
    dma16(wwr + wd1, 2048, wv[3], srdw, sow + 256);
  }
  __device__ __forceinline__ void next() { kw += 1; sow = (kw ^ kg) * (32 * WROW); asm volatile("" : "+s"(sow), "+s"(kw)); if (NXP) asm volatile("" : "+s"(sox), "+s"(kx)); }
  __device__ __forceinline__ void stage0(int rot) {
    kg = rot; kw = 0; kx = 0; kr = 0; sow = kg * (32 * WROW); sox = (kg >> 1) * 128; gof = kg * 256;
    if (HAND && !RB) { kw = 3; sow = (3 ^ kg) * (32 * WROW); return; }
#pragma unroll
    for (int i = 0; i < NXE; ++i) dma16(xd, i * 1024, xv[i], srdx, sox);
    loads<true>(0, 0); if (HAND) other(0); next();
  }
  __device__ __forceinline__ void stage1() { if (HAND && !RB) return; loads<false>(WSL, XSL); if (HAND) other(WSL); next(); }
  __device__ __forceinline__ void stage2() { if (HAND && !RB) return; loads<true>(2 * WSL, XSL); if (HAND) other(2 * WSL); next(); }
  static constexpr int VPRO = OE + OO + (HAND ? 4 : 0);
  struct Ad { unsigned x, w[4]; };
  __device__ __forceinline__ void addr(Ad& a, unsigned xb, unsigned wrd) {
    a.x = xb; a.w[0] = wa0 + wrd;
    if (QKV) {
      const unsigned s2 = (gmask & (gof - wrd)) + wrd;
      kr += 1; gof = (kr ^ kg) * 256;
      a.w[1] = wa1 + s2; a.w[2] = wa2 + s2;
      asm volatile("" : "+v"(a.x), "+v"(a.w[0]), "+v"(a.w[1]), "+v"(a.w[2]));
    } else if (RB) {
      a.w[1] = a.w[0] ^ 32;
      asm volatile("" : "+v"(a.x), "+v"(a.w[0]), "+v"(a.w[1]));
    } else {
      a.w[1] = a.w[0] ^ 32; a.w[2] = a.w[0] ^ 64; a.w[3] = a.w[0] ^ 96;
      asm volatile("" : "+v"(a.x), "+v"(a.w[0]), "+v"(a.w[1]), "+v"(a.w[2]), "+v"(a.w[3]));
    }
  }
  __device__ __forceinline__ unsigned wad(const Ad& a, int n, int hi) {
    if (QKV) return n == 2 ? a.w[1 + hi] : a.w[0] + (n == 0 ? 0 : n == 1 ? 160 : 320) + hi * 1920;
    if (RB) return a.w[n & 1] + (n >> 1) * 3072 + hi * 512;
    return a.w[n & 3] + (n >> 2) * 3072 + hi * 512;
  }
  __device__ __forceinline__ void rd_all(u32x4 (&xf)[NM], u32x4 (&wf)[NN], const Ad& a) {
#pragma unroll
    for (int n = 0; n < NN; ++n) wf[n] = cat(ldstr(wad(a, n, 0)), ldstr(wad(a, n, 1)));
#pragma unroll
    for (int m = 0; m < NM; ++m) xf[m] = lds128(a.x + m * 2048);
  }
  template <bool RD = true, class L>
  __device__ __forceinline__ void mm_rd(const u32x4 (&xc)[NM], const u32x4 (&wc)[NN], u32x4 (&xn)[NM], u32x4 (&wn)[NN], const Ad& a, L&& late) {
    u32x2 lo[NN];
    constexpr bool NMAJ = !QKV && !RB;
    constexpr bool EARLY = !QKV && RB && BEARLY;
    if (EARLY && RD) { rd_all(xn, wn, a); SB(); }
    int r = 0;
#pragma unroll
    for (int i = 0; i < NM * NN; ++i) {
      const int m = NMAJ ? i % NM : i / NN, n = NMAJ ? i / NM : i % NN;
      if (QKV && m == 0 && n == 0) continue;
      if (QKV && n == 3) mac(acc[m][3], xc[m], wc[3]);
      else               mac(acc[m][n], wc[n], xc[m]);
      SB();
      if (!RD || EARLY) continue;
      if (NMAJ) {
        if (i < 4) xn[i] = lds128(a.x + i * 2048);
        else if ((i & 3) < 2) {
          const int k = (i >> 2) - 1;
          if (i & 1) wn[k] = cat(lo[k], ldstr(wad(a, k, 1))); else lo[k] = ldstr(wad(a, k, 0));
        } else late(i);
      } else {
        if (r < 2 * NN) {
          const int k = r >> 1;
          if (r & 1) wn[k] = cat(lo[k], ldstr(wad(a, k, 1))); else lo[k] = ldstr(wad(a, k, 0));
        } else if (r < 2 * NN + NM) {
          xn[r - 2 * NN] = lds128(a.x + (r - 2 * NN) * 2048);
        }
        else late(r - 2 * NN - NM);
        ++r;
      }
      SB();
    }
    if (NMAJ && RD) { wn[NN - 1] = cat(ldstr(wad(a, NN - 1, 0)), ldstr(wad(a, NN - 1, 1))); SB(); }
  }
  __device__ __forceinline__ void pin(u32x4 (&xf)[NM], u32x4 (&wf)[NN]) {
    asm volatile("" : "+v"(xf[0]), "+v"(xf[1]), "+v"(xf[2]), "+v"(xf[3]), "+v"(wf[0]), "+v"(wf[1]), "+v"(wf[2]), "+v"(wf[3]));
    if (NM == 5) asm volatile("" : "+v"(xf[NM - 1]));
    if (NN == 8) asm volatile("" : "+v"(wf[NN - 4]), "+v"(wf[NN - 3]), "+v"(wf[NN - 2]), "+v"(wf[NN - 1]));
  }
  __device__ __forceinline__ void zero() {
#pragma unroll
    for (int m = 0; m < NM; ++m)
#pragma unroll
      for (int n = 0; n < NN; ++n) { acc[m][n] = f32x4{0.f, 0.f, 0.f, 0.f}; asm volatile("" : "+v"(acc[m][n])); }
  }
  template <int TL0, int TL1, class F, class G, class H, class U>
  __device__ __forceinline__ void run(F&& pre, G&& extra, H&& tail, U&& gap) {
    pre();
    u32x4 xA[NM], wA[NN], xB[NM], wB[NN];
    Ad a;
    addr(a, xa0, 0);
    rd_all(xA, wA, a);
    if (!RB) __builtin_amdgcn_s_setprio(1);
    unsigned w0 = 0, w1 = WSL, w2 = 2 * WSL;
    unsigned xp = 2 * XSL, x0 = 0, x1 = XSL;
    int t = 0;
    unsigned nw1, nw2, nx1;
    auto adv0 = [&]() { nw1 = w2 + WSL; if (nw1 == NWS * WSL) nw1 = 0; asm volatile("" : "+s"(nw1)); };
    auto adv1 = [&]() { nw2 = nw1 + WSL; if (nw2 == NWS * WSL) nw2 = 0; asm volatile("" : "+s"(nw2)); };
    auto adv2 = [&]() { nx1 = x1 + XSL; if (nx1 == 3 * XSL) nx1 = 0; asm volatile("" : "+s"(nx1)); };
    auto adv3 = [&]() {
      w0 = w2; w1 = nw1; w2 = nw2; xp = x0; x0 = x1; x1 = nx1; t += 2;
      asm volatile("" : "+s"(w0), "+s"(w1), "+s"(w2), "+s"(xp), "+s"(x0), "+s"(x1), "+s"(t));
    };
    auto adv = [&]() { adv0(); adv1(); adv2(); adv3(); };
    asm volatile("" : "+s"(w0), "+s"(w1), "+s"(w2), "+s"(xp), "+s"(x0), "+s"(x1), "+s"(t));
    if (QKV) asm volatile("" : "+s"(gof), "+s"(kr));
    auto none = [](int) {};
    if (RB) {
      auto help = [&](int tt, unsigned wwr) { if (HAND && __builtin_expect(tt < ht, 0)) other(wwr); extra(tt); };
      pin(xA, wA);
      SB(); WAITVL(OO + (HAND ? 2 : 0)); tick(0); BAR(); tick(1); SB();
      loads<false>(w0, xp); help(0, w0); next(); addr(a, (xa0 ^ 64) + x0, w1); SB();
      auto half = [&]() {
        mm_rd(xA, wA, xB, wB, a, [&](int i) {
          if (QKV && i == 0) adv0();
          if (QKV && i == 2) adv1();
          if (QKV && i == 4) adv2();
        });
        pin(xB, wB);
        SB(); WAITVL(OE); tick(2 * t + 2); BAR(); tick(2 * t + 3); SB();
        loads<true>(w1, xp); help(t + 1, w1); next();
        addr(a, xa0 + x1, w2);
        if (QKV) adv3(); else adv();
        SB();
        mm_rd(xB, wB, xA, wA, a, none);
      };
#pragma unroll 1
      do {
        half();
        pin(xA, wA);
        SB(); WAITVL(OO); tick(2 * t); BAR(); tick(2 * t + 1); SB();
        loads<false>(w0, xp); help(t, w0); next(); addr(a, (xa0 ^ 64) + x0, w1); SB();
        if (QKV) asm volatile("" : "+s"(gof), "+s"(kr));
      } while (t < 14);
      half();
    } else {
      addr(a, (xa0 ^ 64) + x0, w1);
      auto trip = [&](auto EARLY) {
        constexpr bool E = EARLY.value;
        pin(xA, wA);
        SB(); if (E) WAITL(); else WAITVL(OO);
        tick(2 * t); BAR(); tick(2 * t + 1); SB();
        mm_rd(xA, wA, xB, wB, a, [&](int i) { if (E) gap(i); });
        if (E) { if (t == 0) WAITV(0); else extra(t); }
        if (!E || t >= ht) loads<false>(w0, xp);
        next(); addr(a, xa0 + x1, w2); adv0(); adv1(); adv2(); SB();
        pin(xB, wB);
        SB(); if (E) WAITL(); else WAITVL(OE);
        tick(2 * t + 2); BAR(); tick(2 * t + 3); SB();
        mm_rd(xB, wB, xA, wA, a, [&](int i) { if (E) gap(i); });
        if (E && t < ht) extra(t + 1); else loads<true>(w1, xp);
        next(); adv3(); addr(a, (xa0 ^ 64) + x0, w1); SB();
        if (QKV) asm volatile("" : "+s"(gof), "+s"(kr));
      };
      if (HAND) {
#pragma unroll 1
        do trip(std::true_type{}); while (t < ht + 2);
      }
#pragma unroll 1
      do trip(std::false_type{}); while (t < 16);
    }
    constexpr unsigned S16 = (16 % NWS) * WSL, S17 = (17 % NWS) * WSL, S18 = (18 % NWS) * WSL, S19 = (19 % NWS) * WSL, X8 = 2 * XSL, X9 = 0;
    pin(xA, wA);
    SB(); WAITVL(OO);
    tick(32); BAR(); tick(33); SB();
    if (RB) { loads<false, false>(S16, 0); tail(0); addr(a, (xa0 ^ 64) + X8, S17); SB(); mm_rd(xA, wA, xB, wB, a, none); }
    else    { mm_rd(xA, wA, xB, wB, a, none); loads<false, false>(S16, 0); tail(0); addr(a, xa0 + X9, S18); SB(); }
    pin(xB, wB);
    SB(); WAITVL(NWP + TL0); tick(34); BAR(); tick(35); SB();
    if (RB) { tail(1); addr(a, xa0 + X9, S18); SB(); mm_rd(xB, wB, xA, wA, a, none); }
    else    { mm_rd(xB, wB, xA, wA, a, none); tail(1); addr(a, (xa0 ^ 64) + X9, S19); SB(); }
    pin(xA, wA);
    SB(); WAITVL(TL0 + TL1); tick(36); BAR(); tick(37); SB();
    if (RB) { tail(2); addr(a, (xa0 ^ 64) + X9, S19); SB(); mm_rd(xA, wA, xB, wB, a, none); }
    else    { mm_rd(xA, wA, xB, wB, a, none); tail(2); SB(); }
    pin(xB, wB);
    SB(); WAITL(); tick(38); BAR(); tick(39); SB();
    if (RB) { tail(3); SB(); mm_rd<false>(xB, wB, xA, wA, a, none); }
    else    { mm_rd<false>(xB, wB, xA, wA, a, none); tail(3); SB(); }
    if (!RB) __builtin_amdgcn_s_setprio(0);
    MFMA_DONE();
    tick(40);
  }
};

__global__ __launch_bounds__(512, 2) void fused(const void* x_, const void* wqkv_, const void* wo_p, const void* wg_p,
                                                const void* wu_p, const void* wd_p, const void* gw_, void* out_, void* ws_) {
  __shared__ __attribute__((aligned(16))) char smem[LDS_BYTES];
  asm volatile("" :: "s"((unsigned)(size_t)(LAS char*)smem));
  const int tid = threadIdx.x, lane = tid & 63;
  const int w = rfl(tid >> 6);
  const int h = lane >> 4, c = lane & 15, q4 = (lane & 15) >> 2, p4 = lane & 3;
  const int b = blockIdx.x, xcd = b & 7, idx = b >> 3;
  const int rh = xcd & 1, cq = xcd >> 1;
  const int a = 8 * rh + (idx & 7);
  const int gc = 2 * cq + ((idx >> 3) & 1);
  const int rb = 4 * xcd + (idx >> 3), ct = idx & 7;
  u64 x, wqkv, wo, wg, wu, wd, gw;
  char* ws;
  __amdgpu_buffer_rsrc_t rws;
  u32x4 sws;
  auto bind = [&]() {
    x = (u64)x_; wqkv = (u64)wqkv_; wo = (u64)wo_p; wg = (u64)wg_p; wu = (u64)wu_p; wd = (u64)wd_p; gw = (u64)gw_;
    u64 pw = (u64)ws_;
    asm volatile("" : "+s"(x), "+s"(wqkv), "+s"(wo), "+s"(wg), "+s"(wu), "+s"(wd), "+s"(gw), "+s"(pw));
    ws = (char*)pw;
    rws = __builtin_amdgcn_make_buffer_rsrc(ws, 0, WS_END, 0x00020000);
    sws = mk_srd(pw, WS_END);
  };
  u64 amp_t0 = 0, amp_t1 = 0;

  if (CODEPF) {
    u64 pc;
    asm volatile("s_getpc_b64 %0" : "=s"(pc));
    asm volatile("s_mov_b32 m0, %0\n\ts_nop 0\n\tbuffer_load_dword %1, %2, 0 offen lds"
                 :: "s"(PKF_OFF), "v"(lane < 6 ? ((idx & 7) * 48 + w * 6 + lane) * 128 : OOB), "s"(mk_srd(pc & ~127ull, CODE_BYTES)) : "memory");
  }
  u32x2 ev;
  u64 tag = 0;
  auto make_tag = [&](auto NV) {
    unsigned lo, hi;
    asm volatile("s_waitcnt vmcnt(%4)\n\tv_readfirstlane_b32 %0, %2\n\tv_readfirstlane_b32 %1, %3"
                 : "=s"(lo), "=s"(hi) : "v"(ev.x), "v"(ev.y), "i"(NV.value) : "memory");
    const u64 e = ((u64)hi << 32) | lo;
    tag = (e ^ 0x9E3779B97F4A7C15ull) * 0xBF58476D1CE4E5B9ull + 0x632BE59BD9B4E019ull;
    if (tag == e) tag += 1;
  };
  const unsigned pwb = WS_PW + ct * (68 * 5120);
  const int grot = 3 * ((idx >> 3) & 1);
  auto gstep = [&](int s) { const int v = s + grot; return v >= 6 ? v - 6 : v; };
  auto cell_dma = [&](int v, int cell) {
    const unsigned so = pwb + (20 + 6 * v + gstep(2 + cell)) * 5120;
#pragma unroll
    for (int j = 0; j < 5; ++j) dma16c(cell ? v * 15360 : CELL0 + v * 5120, (cell ? cell - 1 : 0) * 5120 + j * 1024, lane * 16, sws, so + j * 1024);
  };
  auto ldw = [&](u32x4 (&d)[5], unsigned so) {
#pragma unroll
    for (int n = 0; n < 5; ++n) d[n] = __builtin_amdgcn_raw_buffer_load_b128(rws, lane * 16, so + n * 1024, AUX_SC1);
  };
  const unsigned pscr = PACK_OFF + (w & 3) * 8192;
  const int pu0 = (b - 128) * 4 + w;
  unsigned pra[2], prb[2], pn[2], pb[2];
  auto pack_issue = [&]() {
    const u64 po = wo, pd = wd;
#pragma unroll
    for (int r = 0; r < 2; ++r) {
      const int u = pu0 + 512 * r;
      const int ks = u / 10, cg = u - 10 * ks;
      const unsigned r8 = lane >> 3;
      const u32x4 srd = mk_srd(ks < 20 ? po : pd, ks < 20 ? 640 * 1280 : 1536 * 1280);
      const unsigned base = ks < 16 ? 40 * ks : ks < 20 ? 160 * (ks - 16) + 16 : 32 * (ks - 20);
      const unsigned vr = (ks >= 16 && ks < 20) ? 20 * (r8 >> 2) + (r8 & 3) : r8;
      const unsigned vo = vr * 1280 + (lane & 7) * 16;
#pragma unroll
      for (int p = 0; p < 4; ++p) {
        const unsigned rp = ks < 16 ? 20 * (p >> 1) + 8 * (p & 1) : ks < 20 ? 40 * p : 8 * p;
        dma16(pscr, r * 4096 + p * 1024, vo, srd, u < 680 ? (base + rp) * 1280 + cg * 128 : OOB);
      }
      pra[r] = pscr + r * 4096 + (h * (ks < 16 ? 8 : 4) + q4) * 128 + p4 * 8 - 32;
      prb[r] = pra[r] + (ks < 16 ? 512 : 2048);
      pn[r] = 4 * cg - 1; pb[r] = u < 680 ? WS_PW + ks * 5120 + (4 * cg - 1) * 1024 : OOB;
    }
    asm volatile("" : "+v"(pra[0]), "+v"(prb[0]), "+v"(pra[1]), "+v"(prb[1]));
    asm volatile("" : "+s"(pn[0]), "+s"(pb[0]), "+s"(pn[1]), "+s"(pb[1]));
  };
  const unsigned smask = w != 4 ? 0u : idx >= 16 ? 0x1040u : 0x1000u;
  auto side_b = [&](int t) {
    if (__builtin_expect(!((smask >> t) & 1), 1)) return;
    if (t == 6) {
      u32x2 tv; tv.x = (unsigned)tag; tv.y = (unsigned)(tag >> 32);
      __builtin_amdgcn_raw_buffer_store_b64(tv, rws, opaque_lane() == 0 ? WS_PKF + (b - 128) * 8 : OOB, 0, AUX_SC1);
    } else {
      dma16c(PKF_OFF, 0, opaque_lane() * 16, sws, WS_PKF);
    }
  };
  u32x2 pq[4];
  unsigned ps0, ps1;
  auto pack_gap = [&](int i) {
    if (i == 6)  pq[0] = ldstr(pra[0]);
    if (i == 7)  pq[1] = ldstr(prb[0]);
    if (i == 10) pq[2] = ldstr(pra[1]);
    if (i == 11) pq[3] = ldstr(prb[1]);
    if (i == 14) { pra[0] += 32; prb[0] += 32; asm volatile("" : "+v"(pra[0]), "+v"(prb[0])); }
    if (i == 15) { pra[1] += 32; prb[1] += 32; asm volatile("" : "+v"(pra[1]), "+v"(prb[1])); }
    if (i == 18) { ps0 = (pn[0] * 205) >> 10; asm volatile("" : "+s"(ps0)); }
    if (i == 19) { ps0 = ps0 * (68 * 5120 - 5120) + pb[0]; asm volatile("" : "+s"(ps0)); }
    if (i == 22) { ps1 = (pn[1] * 205) >> 10; asm volatile("" : "+s"(ps1)); }
    if (i == 23) { ps1 = ps1 * (68 * 5120 - 5120) + pb[1]; asm volatile("" : "+s"(ps1)); }
    if (i == 26) { pn[0] += 1; pb[0] += 1024; asm volatile("" : "+s"(pn[0]), "+s"(pb[0])); }
    if (i == 27) { pn[1] += 1; pb[1] += 1024; asm volatile("" : "+s"(pn[1]), "+s"(pb[1])); }
  };
  auto side_f = [&](int) {
    __builtin_amdgcn_raw_buffer_store_b128(cat(pq[0], pq[1]), rws, lane * 16, ps0, AUX_SC1);
    STPAD();
    __builtin_amdgcn_raw_buffer_store_b128(cat(pq[2], pq[3]), rws, lane * 16, ps1, AUX_SC1);
    STPAD();
  };
  auto pk_check = [&]() {
    const u32x4 p0 = lds128(PKF_OFF + lane * 16);
    const unsigned lo = (unsigned)tag, hi = (unsigned)(tag >> 32);
    if (!__all(p0.x == lo && p0.z == lo && p0.y == hi && p0.w == hi)) {
      GAS u64* fp = (GAS u64*)((char*)ws + WS_PKF + opaque_lane() * 8);
#pragma unroll 1
      for (unsigned spin = 0; spin < (1u << 18); ++spin) {
        bool ok = true;
#pragma unroll
        for (int j = 0; j < 2; ++j) ok &= __hip_atomic_load(fp + 64 * j, RLX) == tag;
        if (__all(ok)) break;
        __builtin_amdgcn_s_sleep(2);
      }
    }
    asm volatile("" ::: "memory");
  };
  const int rot = (a + 2 * ((idx >> 3) & 1) + (idx >> 4)) & (ROT - 1);

  const int t0 = 2 * w + min(w, 4);
  float sg0, sg1, sg2;
  auto role = [&](auto RBT) {
    constexpr bool RB = decltype(RBT)::value;
    u32x4 wo_[3][5], wg_[2][5];
    auto tail = [&](int i) {
      if (i == 0) pk_check();
      if (RB) {
        if (i == 1) { ldw(wo_[0], pwb + 2 * w * 5120); ldw(wo_[1], pwb + (2 * w + 1) * 5120); }
        if (i >= 2) ldw(wg_[i - 2], pwb + (20 + 6 * w + gstep(i - 2)) * 5120);
        if (i == 2) cell_dma(w - 4, 0);
        if (i == 3) cell_dma(w, 0);
      } else if (i >= 2) {
        ldw(wo_[i - 2], pwb + (2 * w + i - 2) * 5120);
      }
    };
    auto late_w = [&]() {
#pragma unroll
      for (int n = 0; n < 5; ++n) ld128(wo_[2][n], lane * 16, sws, pwb + (16 + w) * 5120 + n * 1024);
#pragma unroll
      for (int j = 0; j < 10; ++j) ld128(wg_[j / 5][j % 5], lane * 16, sws, pwb + (20 + 6 * w + gstep(j / 5)) * 5120 + (j % 5) * 1024);
    };
    if (idx < 16) {
      const int g = gc, hd = w & 3;
      constexpr int par = RB ? 1 : 0;
      using K = P1<true, RB>;
      K k;
      const int q0 = RB ? 12 + 2 * (w - 4) : 3 * w;
      k.xd = K::XB + q0 * 1024; k.wd0 = w * 2048;
      asm volatile("" : "+s"(k.xd), "+s"(k.wd0));
#pragma unroll
      for (int i = 0; i < K::NXP; ++i) {
        const int rho = 8 * (q0 + i) + (lane >> 3);
        const int pp = rho >= 80 ? 1 : 0, u = rho - 80 * pp;
        const int tok = 128 * a - 32 + 2 * u + pp;
        const unsigned v = tok * 1280 + (((lane & 7) ^ ((rho >> 1) & 7)) << 4);
        k.xv[i] = (u >= 9 && tok >= 0) ? v : OOB;
      }
#pragma unroll
      for (int j = 0; j < 2; ++j) {
        const unsigned I = (2 * w + j) * 64 + lane;
        const unsigned kb = (I >= 248) + (I >= 488) + (I >= 736);
        const unsigned i = I - 240 * kb - 8 * ((kb + 1) >> 1);
        const unsigned r = (i * 2185u) >> 16, rem = i - r * 30;
        const unsigned seg = (rem * 6554u) >> 16, ch = rem - seg * 10;
        const unsigned v = (8 * kb + r) * 3840 + seg * 1280 + g * 160 + ch * 16;
        k.wv[j] = i < 240 ? v : OOB;
      }
      asm volatile("" : "+v"(k.xv[0]), "+v"(k.xv[1]), "+v"(k.xv[K::NXP - 1]), "+v"(k.wv[0]), "+v"(k.wv[1]));
      bind();
      STAMP(0);
      k.srdx = mk_srd(x, 2048 * 1280);
      k.srdw = mk_srd(wqkv, 640 * 3840);
      unsigned gv0, gv1;
      asm volatile("global_load_dwordx2 %0, %1, %2 sc1" : "=v"(ev) : "v"(WS_EPOCH), "s"(ws) : "memory");
      asm volatile("buffer_load_ushort %0, %2, %3, 0 offen\n\tbuffer_load_ushort %1, %2, %3, 0 offen offset:1024"
                   : "=&v"(gv0), "=&v"(gv1) : "v"(tid * 2), "s"(mk_srd(gw, 1280)) : "memory");
      STAMP(11);
      k.stage0(rot);
      BAR();
      k.stage1();
      k.xa0 = (K::XB + par * 5 * 2048 + c * 128 + ((h ^ (c >> 1)) << 4)) ^ ((rot & 1) << 6);
      const unsigned kbo = h * 3840 + ((h + 1) >> 1) * 128;
      k.wa0 = kbo + q4 * 480 + (20 * hd + 4 * p4) * 2;
      const bool isgate = p4 == 3;
      k.gmask = isgate ? ~0u : 0u;
      asm volatile("" : "+v"(k.gmask));
      k.wa1 = isgate ? GATE_OFF + (8 * h + q4) * 8 : kbo + q4 * 480 + p4 * 160 + (20 * hd + 16) * 2;
      k.wa2 = k.wa1 + (isgate ? 32 : 1920);
      f32x4 ba, bb, ba1;
#pragma unroll
      for (int r = 0; r < 4; ++r) {
        const int kap = 4 * h + r;
        ba[r] = kap >= c + 9 ? 0.f : -1e30f;
        bb[r] = (kap <= c && kap >= c - 7) ? 0.f : -1e30f;
        ba1[r] = a == 0 ? -1e30f : ba[r];
      }
      unsigned m0mask = h == 0 ? 0xffffffffu : 0u;
      unsigned scw = w * 15360 + 10240 + c * 32 + h * 8, sck = w * 15360 + 10240 + c * 32 + 8,
               sct = w * 15360 + 10240 + (4 * h + q4) * 32 + p4 * 8;
      unsigned ovo = WS_O + (h & 1) * (2 * 20480) + (2 * g + (hd >> 1)) * 1024 + (2 * (hd & 1) + (h >> 1)) * 256 + c * 16;
      unsigned ovt = h == 2 ? WS_O + (32 + g) * 512 + hd * 128 + c * 8 : OOB;
      asm volatile("" : "+v"(ba), "+v"(bb), "+v"(ba1), "+v"(m0mask), "+v"(scw), "+v"(sck), "+v"(sct), "+v"(ovo), "+v"(ovt));
      asm volatile("" : "+v"(k.xa0), "+v"(k.wa0));
      k.zero();
      k.stage2();
      STAMP(12);
      k.template run<0, RB ? 10 : 0>([&]() {
        asm volatile("s_waitcnt vmcnt(%2)" : "+v"(gv0), "+v"(gv1) : "i"(K::VPRO) : "memory");
        u32x2 v; v.x = gv0; v.y = 0;
        *(LAS u32x2*)(GATE_OFF + tid * 8) = v;
        if (!RB && tid < 128) { v.x = gv1; *(LAS u32x2*)(GATE_OFF + 4096 + tid * 8) = v; }
        make_tag(std::integral_constant<int, K::VPRO>{});
        WAITL();
        BAR();
        STAMP(1);
      }, [&](int t) { if (RB) side_b(t); }, tail, [](int) {});
      STAMP(2);
      u32x2 ql[5], kl[5], t2[5], vl[5], tt[5], k4[5];
      unsigned sb16[4];
#pragma unroll
      for (int i = 0; i < 5; ++i) { t2[i] = pk4(k.acc[i][2]); *(LAS u32x2*)(scw + i * 512) = t2[i]; }
#pragma unroll
      for (int i = 0; i < 5; ++i) { if (i) ql[i] = pk4(k.acc[i][0]); kl[i] = pk4(k.acc[i][1]); }
      WAITL();
#pragma unroll
      for (int i = 0; i < 5; ++i) k4[i] = *(const LAS u32x2*)(sck + i * 512);
#pragma unroll
      for (int i = 0; i < 4; ++i) sb16[i] = *(const LAS unsigned*)(sck + (i + 1) * 512 + 16);
#pragma unroll
      for (int i = 0; i < 5; ++i) tt[i] = ldstr(sct + i * 512);
#pragma unroll
      for (int i = 0; i < 5; ++i) vl[i] = pk4(k.acc[i][3]);
      constexpr float SC = 0.22360679774997896f * LOG2E;
      f32x4 sa[4], sb[4];
#pragma unroll
      for (int i = 1; i < 5; ++i) {
        u32x4 qf; qf.x = ql[i].x; qf.y = ql[i].y; qf.z = t2[i].x & m0mask; qf.w = t2[i].y & m0mask;
        sb[i - 1] = mfma(cat(kl[i], k4[i]), qf, bb);
        sa[i - 1] = mfma(cat(kl[i - 1], k4[i - 1]), qf, i == 1 ? ba1 : ba);
      }
      asm volatile("s_nop 7\n\ts_nop 0" : "+v"(sa[0]), "+v"(sa[1]), "+v"(sa[2]), "+v"(sa[3]), "+v"(sb[0]), "+v"(sb[1]), "+v"(sb[2]), "+v"(sb[3]));
      float mx[4], es[4];
#pragma unroll
      for (int i = 0; i < 4; ++i)
        mx[i] = max3(max3(sb[i].x, sb[i].y, sb[i].z), max3(sb[i].w, sa[i].x, sa[i].y), max2(sa[i].z, sa[i].w));
#pragma unroll
      for (int i = 0; i < 4; ++i) {
        auto r = __builtin_amdgcn_permlane16_swap(f2u(mx[i]), f2u(mx[i]), false, false);
        mx[i] = max2(u2f(r[0]), u2f(r[1]));
      }
#pragma unroll
      for (int i = 0; i < 4; ++i) {
        auto r = __builtin_amdgcn_permlane32_swap(f2u(mx[i]), f2u(mx[i]), false, false);
        mx[i] = max2(u2f(r[0]), u2f(r[1])) * SC;
        es[i] = __builtin_amdgcn_exp2f(__builtin_amdgcn_fmed3f(u2f(sb16[i] << 16), -60.f, 60.f) * LOG2E);
      }
      u32x4 pf[4];
#pragma unroll
      for (int i = 0; i < 4; ++i) {
        f32x4 pa, pb;
#pragma unroll
        for (int r = 0; r < 4; ++r) {
          pa[r] = __builtin_amdgcn_exp2f(sa[i][r] * SC - mx[i]);
          pb[r] = __builtin_amdgcn_exp2f(sb[i][r] * SC - mx[i]);
        }
        pf[i] = cat(pk4(pa), pk4(pb));
      }
      const f32x4 z = {0.f, 0.f, 0.f, 0.f};
      const u32x4 ones = {0x3f803f80u, 0x3f803f80u, 0x3f803f80u, 0x3f803f80u};
      f32x4 ls[4], oa[4], ob[4];
#pragma unroll
      for (int i = 0; i < 4; ++i) {
        ls[i] = mfma(ones, pf[i], z);
        oa[i] = mfma(cat(vl[i], vl[i + 1]), pf[i], z);
        ob[i] = mfma(cat(tt[i], tt[i + 1]), pf[i], z);
      }
      const unsigned mt0 = 8 * a + par;
      u32x2 op[4];
#pragma unroll
      for (int i = 0; i < 4; ++i) {
        const float f = es[i] * __builtin_amdgcn_rcpf(ls[i].x);
        op[i] = pk4(oa[i] * f);
        __builtin_amdgcn_raw_buffer_store_b64(pk4(ob[i] * f), rws, ovt, (mt0 + 2 * i) * 20480, AUX_SC1);
        if (i & 1) {
          auto sx = __builtin_amdgcn_permlane16_swap(op[i - 1].x, op[i].x, false, false);
          auto sy = __builtin_amdgcn_permlane16_swap(op[i - 1].y, op[i].y, false, false);
          u32x4 o; o.x = sx[0]; o.y = sy[0]; o.z = sx[1]; o.w = sy[1];
          __builtin_amdgcn_raw_buffer_store_b128(o, rws, ovo, (mt0 + 2 * i - 2) * 20480, AUX_SC1);
          STPAD();
        }
      }
      if (g == 0 && hd == 0) {
        const float e = h == 0 ? es[0] : h == 1 ? es[1] : h == 2 ? es[2] : es[3];
        __builtin_amdgcn_raw_buffer_store_b32(f2u(e), rws, WS_G + h * 128 + c * 4, mt0 * 64, AUX_SC1);
      }
    } else {
      const int cj = gc;
      using K = P1<false, RB>;
      K k;
      const int mh = RB ? (w < 6 ? 1 : 0) : (w >> 1);
      const int up = (w >> 1) & 1, kp = w & 1;
#pragma unroll
      for (int i = 0; i < K::NXP; ++i) {
        const int rho = 8 * (4 * (w & 3) + i) + (lane >> 3);
        const int j = rho >> 4;
        const int tok = 128 * a + 32 * (j >> 1) + 2 * (rho & 15) + (j & 1);
        k.xv[i] = tok * 1280 + (((lane & 7) ^ ((rho >> 1) & 7)) << 4);
      }
      k.xd = K::XB + 4 * (w & 3) * 1024;
      {
        const int r = lane >> 3, p8 = lane & 7;
        const int s = ((r >> 1) & 1) + 2 * kp;
        k.wv[0] = (8 * kp + r) * 3072 + cj * 384 + (((((p8 >> 1) ^ s) << 1) | (p8 & 1)) << 4) + (RB ? 0 : 16 * 3072 + 128);
        k.wv[1] = k.wv[0] + 128; k.wv[2] = k.wv[0] + 256; k.wv[3] = k.wv[0] + 16 * 3072;
      }
      k.wd0 = RB ? (kp * 6 + 3 * up) * 1024 : ((kp + 2) * 6 + 3 * up + 1) * 1024;
      k.wd1 = ((kp + 2) * 6 + 3 * up) * 1024;
      if (RB) asm volatile("" : "+v"(k.xv[0]), "+v"(k.xv[1]), "+v"(k.xv[2]), "+v"(k.xv[3]), "+v"(k.wv[2]), "+v"(k.wv[3]));
      asm volatile("" : "+v"(k.wv[0]), "+v"(k.wv[1]));
      bind();
      STAMP(0);
      const u64 pg = wg, pu = wu;
      k.srdx = mk_srd(x, 2048 * 1280);
      k.srdw = mk_srd(up ? pu : pg, 640 * 3072);
      asm volatile("global_load_dwordx2 %0, %1, %2 sc1" : "=v"(ev) : "v"(WS_EPOCH), "s"(ws) : "memory");
      STAMP(11);
      k.stage0(rot);
      BAR();
      if (!RB) pack_issue();
      k.stage1();
      k.xa0 = (K::XB + mh * 4 * 2048 + c * 128 + ((h ^ (c >> 1)) << 4)) ^ ((rot & 1) << 6);
      {
        const int s = ((q4 >> 1) & 1) + 2 * (h & 1);
        k.wa0 = (h * 6 + (RB ? 2 : (w & 1))) * 1024 + q4 * 128 + (((RB ? 2 * (w & 1) : 0) ^ s) << 5) + p4 * 8;
      }
      asm volatile("" : "+v"(k.xa0), "+v"(k.wa0));
      k.zero();
      k.stage2();
      STAMP(12);
      k.template run<0, RB ? 10 : 0>([&]() {
        make_tag(std::integral_constant<int, RB ? K::VPRO : 8>{});
        BAR();
        STAMP(1);
      }, [&](int t) { if (RB) side_b(t); else side_f(t); }, tail, pack_gap);
      STAMP(2);
      constexpr int NK = RB ? 1 : 2;
      float nl2e = -LOG2E;
      asm volatile("" : "+s"(nl2e));
      const unsigned gso = WS_GU + ((8 * a + 4 * mh) * 48 + 6 * cj + (RB ? 4 + (w & 1) : 2 * (w & 1))) * 1024;
#pragma unroll
      for (int m = 0; m < 4; ++m) {
#pragma unroll
        for (int kk = 0; kk < NK; ++kk) {
          float v[8];
#pragma unroll
          for (int j = 0; j < 8; j += 2) {
            const f32x2 g2 = {k.acc[m][2 * kk + (j >> 2)][j & 3], k.acc[m][2 * kk + (j >> 2)][(j & 3) + 1]};
            const f32x2 u2 = {k.acc[m][2 * NK + 2 * kk + (j >> 2)][j & 3], k.acc[m][2 * NK + 2 * kk + (j >> 2)][(j & 3) + 1]};
            const f32x2 a2 = g2 * nl2e;
            const f32x2 d2 = f32x2{__builtin_amdgcn_exp2f(a2.x), __builtin_amdgcn_exp2f(a2.y)} + 1.f;
            const f32x2 r2 = g2 * u2 * f32x2{__builtin_amdgcn_rcpf(d2.x), __builtin_amdgcn_rcpf(d2.y)};
            v[j] = r2.x; v[j + 1] = r2.y;
          }
          u32x4 o; o.x = pk2(v[0], v[1]); o.y = pk2(v[2], v[3]); o.z = pk2(v[4], v[5]); o.w = pk2(v[6], v[7]);
          __builtin_amdgcn_raw_buffer_store_b128(o, rws, lane * 16, gso + m * (48 * 1024) + kk * 1024, AUX_SC1);
          STPAD();
        }
      }
    }
    SB();
    STAMP(13);
    auto pinw = [&]() {
    asm volatile("" : "+v"(wo_[0][0]), "+v"(wo_[0][1]), "+v"(wo_[0][2]), "+v"(wo_[0][3]), "+v"(wo_[0][4]),
                      "+v"(wo_[1][0]), "+v"(wo_[1][1]), "+v"(wo_[1][2]), "+v"(wo_[1][3]), "+v"(wo_[1][4]));
    if (RB) asm volatile("" : "+v"(wg_[0][0]), "+v"(wg_[0][1]), "+v"(wg_[0][2]), "+v"(wg_[0][3]), "+v"(wg_[0][4]),
                              "+v"(wg_[1][0]), "+v"(wg_[1][1]), "+v"(wg_[1][2]), "+v"(wg_[1][3]), "+v"(wg_[1][4]));
    };
    if (!RB) pinw();
    SB();
    STAMP(14);
    if (RB && idx >= 16) { if (PRE > 0) cell_dma(w - 4, 1); if (PRE > 1) cell_dma(w, 1); if (PRE > 2) cell_dma(w - 4, 2); WAITV(5 * PRE); }
    else if (RB || !PREA) WAITV(0);
    else { late_w(); WAITV(15); }
    STAMP(3);
    WAITL();
    BAR();
    if (!RB && w == 0 && lane < 2) {
      GAS u64* fp = (GAS u64*)((char*)ws + (lane ? WS_DONE + b * 8 : WS_FLAG + (a * 16 + (idx < 16 ? 8 : 0) + gc) * 8));
      __hip_atomic_store(fp, tag, RLX);
    }
    STAMP(4);

    SB();
    const unsigned fo = WS_FLAG + ((rb >> 1) * 16 + c) * 8;
    auto wait_flags = [&](u32x2& v, bool need) {
#pragma unroll 1
      for (unsigned spin = 0; spin < (1u << 18); ++spin) {
        if (__all(!need || (v.x == (unsigned)tag && v.y == (unsigned)(tag >> 32)))) break;
        ld64(v, fo, sws, 0);
        asm volatile("s_waitcnt vmcnt(0)" : "+v"(v) :: "memory");
      }
      asm volatile("" ::: "memory");
    };
    u32x2 fv;
    if (RB) {
      if (idx < 16) { if (PRE > 0) cell_dma(w - 4, 1); if (PRE > 1) cell_dma(w, 1); if (PRE > 2) cell_dma(w - 4, 2); }
      if (PRE < 1) cell_dma(w - 4, 1); if (PRE < 2) cell_dma(w, 1); if (PRE < 3) cell_dma(w - 4, 2);
      cell_dma(w, 2);
    } else {
      if (!PREA) late_w();
      if (w == 0) {
        ld64(fv, fo, sws, 0);
        asm volatile("s_waitcnt vmcnt(0)" : "+v"(fv) :: "memory");
        wait_flags(fv, c >= 8);
      }
    }
    if (RB) __builtin_amdgcn_s_setprio(1);
    BAR();
    STAMP(5);
    f32x4 acc[4][5];
    u32x4 ag[6][4];
    auto tk = [](int) {};
    tk(0);
    const unsigned gb = (EXP & 1) ? pwb : WS_GU + (4 * rb * 48 + 6 * w) * 1024;
    auto ldg = [&](int s, int m) { ld128(ag[s][m], lane * 16, sws, gb + gstep(s) * 1024 + m * (48 * 1024)); };
    unsigned ca0 = CELL0 + w * 5120 + lane * 16, ca1 = w * 15360 + lane * 16;
    asm volatile("" : "+v"(ca0), "+v"(ca1));
    {
      using S = Sched<RB>;
      constexpr int NO = S::NO, OB = S::OB;
      u32x4 af[2][4], at[4];
      u32x2 tl[4], th[4];
      const unsigned ob = WS_O + 4 * rb * 20480;
      auto req = [&](auto IT) {
        constexpr int i = IT.value;
        if constexpr (i < 8) ld128(af[i >> 2][i & 3], lane * 16, sws, ob + (i & 3) * 20480 + (2 * w + (i >> 2)) * 1024);
        else if constexpr (i < OB) ld64((i & 1) ? th[(i - 8) >> 1] : tl[(i - 8) >> 1], lane * 8, sws, ob + ((i - 8) >> 1) * 20480 + (16 + w) * 1024 + (i & 1) * 512);
        else if constexpr (i < OB + 29 && (i - OB) % 5 < 4) ldg((i - OB) / 5, (i - OB) % 5);
        else if constexpr (i < OB + 29) dma16c(w * 15360, 10240 + ((i - OB) / 5) * 1024, lane * 16, sws, pwb + (20 + 6 * w + gstep(5)) * 5120 + ((i - OB) / 5) * 1024);
        else ld32(i == OB + 29 ? sg0 : i == OB + 30 ? sg1 : sg2, c * 4, sws, WS_G + (4 * rb + min(t0 + i - OB - 29, 19) / 5) * 64);
      };
      if (RB || w != GPOLL || !GPOLL) sfor<0, S::upto(PL - 1)>(req);
      if (RB) { __builtin_amdgcn_s_setprio(0); WAITV(S::upto(PL - 1)); pinw(); }
      else if (w == GPOLL) { if (GPOLL) { fv.x = ~(unsigned)tag; fv.y = 0; } wait_flags(fv, c < 8); }
      STAMP(6);
      BAR();
      STAMP(7);
      if (!RB && GPOLL && w == GPOLL) sfor<0, S::upto(PL - 1)>(req);
      tk(1);
      u32x4 wc[5];
      sfor<0, NO + 6>([&](auto ST) {
        constexpr int st = ST.value, s = st - NO;
        if constexpr (st == 2 && !RB) {
          asm volatile("s_waitcnt vmcnt(%8)" : "+v"(tl[0]), "+v"(tl[1]), "+v"(tl[2]), "+v"(tl[3]), "+v"(th[0]), "+v"(th[1]), "+v"(th[2]), "+v"(th[3])
                       : "i"(S::issued(st, 0) - 16) : "memory");
#pragma unroll
          for (int m = 0; m < 4; ++m) at[m] = cat(tl[m], th[m]);
          asm volatile("s_nop 1" : "+v"(at[0]), "+v"(at[1]), "+v"(at[2]), "+v"(at[3]));
        }
        sfor<0, 20>([&](auto IT) {
          constexpr int i = IT.value, m = i / 5, n = i % 5, is = S::issued(st, i);
          if constexpr (WGR == 1 ? n == 0 : i % (5 * WGR) == 0) {
            constexpr int ml = m + WGR - 1;
            if constexpr (WGR == 1) {
              if constexpr (s >= 0)      asm volatile("s_waitcnt vmcnt(%1)" : "+v"(ag[s][m]) : "i"(is - S::gpos(s, m) - 1) : "memory");
              else if constexpr (st < 2) asm volatile("s_waitcnt vmcnt(%1)" : "+v"(af[st][m]) : "i"(is - (4 * st + m) - 1) : "memory");
            } else if constexpr (WGR == 2) {
              if constexpr (s >= 0)      asm volatile("s_waitcnt vmcnt(%2)" : "+v"(ag[s][m]), "+v"(ag[s][ml]) : "i"(is - S::gpos(s, ml) - 1) : "memory");
              else if constexpr (st < 2) asm volatile("s_waitcnt vmcnt(%2)" : "+v"(af[st][m]), "+v"(af[st][ml]) : "i"(is - (4 * st + ml) - 1) : "memory");
            } else {
              if constexpr (s >= 0)      asm volatile("s_waitcnt vmcnt(%4)" : "+v"(ag[s][0]), "+v"(ag[s][1]), "+v"(ag[s][2]), "+v"(ag[s][3]) : "i"(is - S::gpos(s, 3) - 1) : "memory");
              else if constexpr (st < 2) asm volatile("s_waitcnt vmcnt(%4)" : "+v"(af[st][0]), "+v"(af[st][1]), "+v"(af[st][2]), "+v"(af[st][3]) : "i"(is - (4 * st + 3) - 1) : "memory");
            }
          }
          if constexpr (st == 0 && i == 0 && !RB)
            asm volatile("" : "+v"(wo_[2][0]), "+v"(wo_[2][1]), "+v"(wo_[2][2]), "+v"(wo_[2][3]), "+v"(wo_[2][4]),
                              "+v"(wg_[0][0]), "+v"(wg_[0][1]), "+v"(wg_[0][2]), "+v"(wg_[0][3]), "+v"(wg_[0][4]),
                              "+v"(wg_[1][0]), "+v"(wg_[1][1]), "+v"(wg_[1][2]), "+v"(wg_[1][3]), "+v"(wg_[1][4]));
          if constexpr (st == 0)     mul0(acc[m][n], wo_[0][n], af[0][m]);
          else if constexpr (s >= 0) mac(acc[m][n], wg_[s & 1][n], ag[s][m]);
          else if constexpr (st < 2) mac(acc[m][n], wo_[1][n], af[1][m]);
          else                       mac(acc[m][n], wo_[2][n], at[m]);
          SB();
          if constexpr (s == 4 && i == 9) WAITV(is - S::gpos(4, 4) - 1);
          if constexpr (m == (s == 4 ? 2 : 0) && s >= 1 && s < 5) wc[n] = lds128((s > 1 ? ca1 + (s - 2) * 5120 : ca0) + n * 1024);
          if constexpr (S::issued(st, i + 1) > is) req(std::integral_constant<int, is>{});
          SB();
        });
        tk(2 + st);
        if constexpr (s >= 1 && s < 5) {
          asm volatile("" : "+v"(wc[0]), "+v"(wc[1]), "+v"(wc[2]), "+v"(wc[3]), "+v"(wc[4]));
#pragma unroll
          for (int n = 0; n < 5; ++n) wg_[(s + 1) & 1][n] = wc[n];
        }
      });
      asm volatile("s_waitcnt vmcnt(0)" : "+v"(sg0), "+v"(sg1), "+v"(sg2) :: "memory");
    }
    MFMA_DONE();
    STAMP(8);
#pragma unroll
    for (int j = 0; j < 10; ++j) {
      const int ta = j < 4 ? 3 * j : j < 6 ? 6 * j - 22 : 2 * j, tb = j < 4 ? ta + 1 : j < 6 ? ta + 3 : ta + 1;
      *(LAS u32x4*)(ca1 + j * 1024) = cat(pk4(acc[ta / 5][ta % 5]), pk4(acc[tb / 5][tb % 5]));
    }
    tk(12);
  };
  if (w < 4) role(std::false_type{}); else role(std::true_type{});

  if (b == 0 && w == 2) {
    GAS u64* dp = (GAS u64*)((char*)ws_ + WS_DONE + lane * 8);
#pragma unroll 1
    for (unsigned spin = 0; spin < (1u << 18); ++spin) {
      bool ok = true;
#pragma unroll
      for (int j = 0; j < 4; ++j) ok &= __hip_atomic_load(dp + j * 64, RLX) == tag;
      if (__all(ok)) break;
      __builtin_amdgcn_s_sleep(2);
    }
    if (lane == 0) __hip_atomic_store((GAS u64*)((char*)ws_ + WS_EPOCH), tag, RLX);
  }
  sg0 = __builtin_amdgcn_rcpf(1.f + sg0); sg1 = __builtin_amdgcn_rcpf(1.f + sg1); sg2 = __builtin_amdgcn_rcpf(1.f + sg2);
  auto ooff = [&](int tl) {
    const int m = tl / 5, n = tl - 5 * m;
    return (unsigned)((64 * rb + 32 * (m >> 1) + 2 * c + (m & 1)) * 1280 + ct * 160 + n * 32 + h * 8);
  };
  unsigned oo0 = ooff(t0), oo1 = ooff(t0 + 1), oo2 = ooff(t0 + 2);
  asm volatile("" : "+v"(sg0), "+v"(sg1), "+v"(sg2), "+v"(oo0), "+v"(oo1), "+v"(oo2));
  WAITL();
  BAR();
  STAMP(9);
  {
    u32x4 p[8];
    u32x2 p2[8];
    unsigned ra = (w < 4 ? w : w + 2) * 1024 + lane * 16, rb_ = ra + 4 * 15360;
    unsigned rc = (4 + (w >> 1)) * 1024 + (w & 1) * 8 + lane * 16, rd = rc + 4 * 15360;
    asm volatile("" : "+v"(ra), "+v"(rb_), "+v"(rc), "+v"(rd));
#pragma unroll
    for (int v = 0; v < 8; ++v) p[v] = lds128((v < 4 ? ra : rb_) + (v & 3) * 15360);
    if (w < 4) {
#pragma unroll
      for (int v = 0; v < 8; ++v) { p2[v] = *(const LAS u32x2*)((v < 4 ? rc : rd) + (v & 3) * 15360); SB(); }
    }
    SB();
#pragma unroll
    for (int j = 0; j < 3; ++j) {
      if (j < 2 || w < 4) {
        f32x4 s = {0.f, 0.f, 0.f, 0.f};
#pragma unroll
        for (int v = 0; v < 8; ++v) {
          const unsigned px = j == 0 ? p[v].x : j == 1 ? p[v].z : p2[v].x, py = j == 0 ? p[v].y : j == 1 ? p[v].w : p2[v].y;
          const f32x4 q = {u2f(px << 16), u2f(px & 0xffff0000u), u2f(py << 16), u2f(py & 0xffff0000u)};
          s = v ? s + q : q;
        }
        const u32x2 ov = pk4(s * (j == 0 ? sg0 : j == 1 ? sg1 : sg2));
        asm volatile("global_store_dwordx2 %0, %1, %2" OUTFL :: "v"(j == 0 ? oo0 : j == 1 ? oo1 : oo2), "v"(ov), "s"(out_) : "memory");
      }
    }
  }
  STAMP(10);
  if (AMP0 >= 0 && (AMPSEL == 0 || (AMPSEL == 1) == (idx < 16))) {
    const u64 d = amp_t1 - amp_t0, t = __builtin_amdgcn_s_memrealtime();
    while (__builtin_amdgcn_s_memrealtime() - t < d) __builtin_amdgcn_s_sleep(1);
  }
}
}

extern "C" void kernel_launch(void* const* d_in, const int* in_sizes, int n_in, void* d_out, int out_size, void* d_ws,
                              size_t ws_size, hipStream_t stream) {
  fused<<<256, 512, 0, stream>>>(d_in[0], d_in[1], d_in[2], d_in[4], d_in[5], d_in[6], d_in[7], d_out, d_ws);
}
```

```cpp
#include <hip/hip_runtime.h>
#include <hip/hip_bf16.h>
#include <stdint.h>
#include <type_traits>

#pragma clang diagnostic ignored "-Wint-to-pointer-cast"

#define LAS __attribute__((address_space(3)))
#define GAS __attribute__((address_space(1)))
typedef __bf16 bf16x8 __attribute__((ext_vector_type(8)));
typedef __bf16 bf16x2 __attribute__((ext_vector_type(2)));
typedef float f32x4 __attribute__((ext_vector_type(4)));
typedef float f32x2 __attribute__((ext_vector_type(2)));
typedef unsigned u32x4 __attribute__((ext_vector_type(4)));
typedef unsigned u32x2 __attribute__((ext_vector_type(2)));
typedef short s16x4 __attribute__((ext_vector_type(4)));
typedef unsigned long long u64;

namespace {
constexpr int LDS_BYTES = 163840;
constexpr unsigned GATE_OFF = 110592, PACK_OFF = 122880, PKF_OFF = 162816, CELL0 = 122880;
constexpr unsigned OOB = 0x40000000u;
constexpr unsigned CODE_BYTES = 40000;
constexpr int PL = 2;
constexpr unsigned WS_FLAG = 0;
constexpr unsigned WS_DONE = 4096;
constexpr unsigned WS_PKF = 8192;
constexpr unsigned WS_EPOCH = 12288;
constexpr unsigned WS_G = 16384;
constexpr unsigned WS_O = 131072;
constexpr unsigned WS_GU = WS_O + 128 * 40 * 512;
constexpr unsigned WS_PW = WS_GU + 128 * 48 * 1024;
constexpr unsigned WS_END = WS_PW + 8 * 68 * 5120;
constexpr int AUX_SC1 = 16;
constexpr float LOG2E = 1.4426950408889634f;

__device__ __forceinline__ u32x4 mk_srd(u64 a, unsigned bytes) {
  u32x4 r; r.x = (unsigned)a; r.y = (unsigned)(a >> 32) & 0xffffu; r.z = bytes; r.w = 0x00020000u; return r;
}
__device__ __forceinline__ void dma16(unsigned lds, unsigned imm, unsigned voff, u32x4 srd, unsigned soff) {
  asm volatile("s_add_u32 m0, %0, %4\n\ts_nop 0\n\tbuffer_load_dwordx4 %1, %2, %3 offen lds"
               :: "s"(lds), "v"(voff), "s"(srd), "s"(soff), "i"(imm) : "memory", "scc");
}
__device__ __forceinline__ void dma16c(unsigned lds, unsigned imm, unsigned voff, u32x4 srd, unsigned soff) {
  asm volatile("s_add_u32 m0, %0, %4\n\ts_nop 0\n\tbuffer_load_dwordx4 %1, %2, %3 offen sc1 lds"
               :: "s"(lds), "v"(voff), "s"(srd), "s"(soff), "i"(imm) : "memory", "scc");
}
__device__ __forceinline__ void ld128(u32x4& d, unsigned voff, u32x4 srd, unsigned soff) {
  asm volatile("buffer_load_dwordx4 %0, %1, %2, %3 offen sc1" : "=v"(d) : "v"(voff), "s"(srd), "s"(soff) : "memory");
}
__device__ __forceinline__ void ld64(u32x2& d, unsigned voff, u32x4 srd, unsigned soff) {
  asm volatile("buffer_load_dwordx2 %0, %1, %2, %3 offen sc1" : "=v"(d) : "v"(voff), "s"(srd), "s"(soff) : "memory");
}
__device__ __forceinline__ void ld32(float& d, unsigned voff, u32x4 srd, unsigned soff) {
  asm volatile("buffer_load_dword %0, %1, %2, %3 offen sc1" : "=v"(d) : "v"(voff), "s"(srd), "s"(soff) : "memory");
}
__device__ __forceinline__ u32x4 lds128(unsigned a) { return *(const LAS u32x4*)a; }
__device__ __forceinline__ u32x2 ldstr(unsigned a) {
  return __builtin_bit_cast(u32x2, __builtin_amdgcn_ds_read_tr16_b64_v4i16((LAS s16x4*)a));
}
__device__ __forceinline__ u32x4 cat(u32x2 a, u32x2 b) { u32x4 r; r.x = a.x; r.y = a.y; r.z = b.x; r.w = b.y; return r; }
__device__ __forceinline__ f32x4 mfma(u32x4 a, u32x4 b, f32x4 c) {
  return __builtin_amdgcn_mfma_f32_16x16x32_bf16(__builtin_bit_cast(bf16x8, a), __builtin_bit_cast(bf16x8, b), c, 0, 0, 0);
}
__device__ __forceinline__ void mac(f32x4& c, u32x4 a, u32x4 b) {
  asm volatile("v_mfma_f32_16x16x32_bf16 %0, %1, %2, %0" : "+v"(c) : "v"(a), "v"(b));
}
__device__ __forceinline__ void mul0(f32x4& c, u32x4 a, u32x4 b) {
  asm volatile("v_mfma_f32_16x16x32_bf16 %0, %1, %2, 0" : "=&v"(c) : "v"(a), "v"(b));
}
__device__ __forceinline__ unsigned pk2(float a, float b) {
  f32x2 t = {a, b};
  return __builtin_bit_cast(unsigned, __builtin_convertvector(t, bf16x2));
}
__device__ __forceinline__ u32x2 pk4(f32x4 v) { u32x2 r; r.x = pk2(v.x, v.y); r.y = pk2(v.z, v.w); return r; }
__device__ __forceinline__ float u2f(unsigned u) { return __builtin_bit_cast(float, u); }
__device__ __forceinline__ unsigned f2u(float f) { return __builtin_bit_cast(unsigned, f); }
#define SB() __builtin_amdgcn_sched_barrier(0)
#define WAITV(n) asm volatile("s_waitcnt vmcnt(%0)" :: "i"(n) : "memory")
#define WAITVL(n) asm volatile("s_waitcnt vmcnt(%0) lgkmcnt(0)" :: "i"(n) : "memory")
#define WAITL() asm volatile("s_waitcnt lgkmcnt(0)" ::: "memory")
#define BAR() asm volatile("s_barrier" ::: "memory")
#define STPAD() do { SB(); asm volatile("s_nop 1" ::: "memory"); SB(); } while (0)
#define MFMA_DONE() do { SB(); asm volatile("s_nop 7\n\ts_nop 1" ::: "memory"); SB(); } while (0)
#define RLX __ATOMIC_RELAXED, __HIP_MEMORY_SCOPE_AGENT
__device__ __forceinline__ unsigned rfl(unsigned v) { return __builtin_amdgcn_readfirstlane(v); }
__device__ __forceinline__ unsigned opaque_lane() { unsigned l = threadIdx.x & 63; asm volatile("" : "+v"(l)); return l; }

template <int I, int N, class F>
__device__ __forceinline__ void sfor(F&& f) {
  if constexpr (I < N) { f(std::integral_constant<int, I>{}); sfor<I + 1, N>(f); }
}
template <bool RB> struct Sched {
  static constexpr int NO = RB ? 2 : 3, OB = RB ? 8 : 16;
  static constexpr int gpos(int s, int m) { return OB + 5 * s + m; }
  static constexpr int upto(int st) { return st < 2 ? 4 * (st + 1) : st < NO ? 16 : st < NO + 5 ? OB + 5 * (st - NO + 1) : st == NO + 5 ? OB + 29 : OB + 32; }
  static constexpr int issued(int st, int i) {
    const int base = upto(st + PL - 1), goal = upto(st + PL), every = goal - base > 5 ? 2 : goal - base > 4 ? 4 : 5;
    int n = base;
    for (int k = 0; k < i; ++k) if (k % every == 1 && n < goal) ++n;
    return n;
  }
};
__device__ __forceinline__ float max3(float a, float b, float c) { float r; asm("v_max3_f32 %0, %1, %2, %3" : "=v"(r) : "v"(a), "v"(b), "v"(c)); return r; }
__device__ __forceinline__ float max2(float a, float b) { float r; asm("v_max_f32 %0, %1, %2" : "=v"(r) : "v"(a), "v"(b)); return r; }

template <bool QKV, bool RB>
struct P1 {
  static constexpr int NM = QKV ? 5 : 4;
  static constexpr int NN = QKV ? 4 : (RB ? 4 : 8);
  static constexpr int NWS = 3;
  static constexpr unsigned WSL = QKV ? 16384 : 24576, XB = NWS * WSL, XSL = QKV ? 20480 : 16384;
  static constexpr unsigned WROW = QKV ? 3840 : 3072;
  static constexpr int NWP = QKV ? 2 : (RB ? 4 : 2);
  static constexpr int NXE = QKV ? (RB ? 1 : 2) : (RB ? 2 : 0), NXO = QKV ? 1 : NXE;
  static constexpr int NXP = NXO + NXE;
  static constexpr int OO = NWP + NXO, OE = NWP + NXE;
  static constexpr bool HAND = !QKV;
  static constexpr int ht = 4;
  f32x4 acc[NM][NN];
  u32x4 srdx, srdw;
  unsigned xv[4], wv[4];
  unsigned xd;
  unsigned wd0, wd1;
  unsigned xa0, wa0, wa1, wa2;
  unsigned gmask;
  unsigned sow, sox, gof;
  unsigned kg, kw, kx, kr;
  template <bool ODD, bool X = true>
  __device__ __forceinline__ void loads(unsigned wwr, unsigned xwr) {
    if (X && NXP) {
#pragma unroll
      for (int i = ODD ? NXE : 0; i < (ODD ? NXP : NXE); ++i) dma16(xwr + xd, i * 1024, xv[i], srdx, sox);
      if (ODD) { kx += 1; sox = (kx ^ (kg >> 1)) * 128; }
    }
    dma16(wwr + wd0, 0, wv[0], srdw, sow);
    dma16(wwr + wd0, 1024, wv[1], srdw, sow);
    if (!QKV && RB) {
      dma16(wwr + wd0, 2048, wv[2], srdw, sow);
      dma16(wwr + wd1, 0, wv[3], srdw, sow);
    }
  }
  __device__ __forceinline__ void other(unsigned wwr) {
    dma16(wwr + wd1, 1024, wv[3], srdw, sow + 128);
    dma16(wwr + wd1, 2048, wv[3], srdw, sow + 256);
  }
  __device__ __forceinline__ void next() { kw += 1; sow = (kw ^ kg) * (32 * WROW); asm volatile("" : "+s"(sow), "+s"(kw)); if (NXP) asm volatile("" : "+s"(sox), "+s"(kx)); }
  __device__ __forceinline__ void stage0(int rot) {
    kg = rot; kw = 0; kx = 0; kr = 0; sow = kg * (32 * WROW); sox = (kg >> 1) * 128; gof = kg * 256;
    if (HAND && !RB) { kw = 3; sow = (3 ^ kg) * (32 * WROW); return; }
#pragma unroll
    for (int i = 0; i < NXE; ++i) dma16(xd, i * 1024, xv[i], srdx, sox);
    loads<true>(0, 0); if (HAND) other(0); next();
  }
  __device__ __forceinline__ void stage1() { if (HAND && !RB) return; loads<false>(WSL, XSL); if (HAND) other(WSL); next(); }
  __device__ __forceinline__ void stage2() { if (HAND && !RB) return; loads<true>(2 * WSL, XSL); if (HAND) other(2 * WSL); next(); }
  static constexpr int VPRO = OE + OO + (HAND ? 4 : 0);
  struct Ad { unsigned x, w[4]; };
  __device__ __forceinline__ void addr(Ad& a, unsigned xb, unsigned wrd) {
    a.x = xb; a.w[0] = wa0 + wrd;
    if (QKV) {
      const unsigned s2 = (gmask & (gof - wrd)) + wrd;
      kr += 1; gof = (kr ^ kg) * 256;
      a.w[1] = wa1 + s2; a.w[2] = wa2 + s2;
      asm volatile("" : "+v"(a.x), "+v"(a.w[0]), "+v"(a.w[1]), "+v"(a.w[2]));
    } else if (RB) {
      a.w[1] = a.w[0] ^ 32;
      asm volatile("" : "+v"(a.x), "+v"(a.w[0]), "+v"(a.w[1]));
    } else {
      a.w[1] = a.w[0] ^ 32; a.w[2] = a.w[0] ^ 64; a.w[3] = a.w[0] ^ 96;
      asm volatile("" : "+v"(a.x), "+v"(a.w[0]), "+v"(a.w[1]), "+v"(a.w[2]), "+v"(a.w[3]));
    }
  }
  __device__ __forceinline__ unsigned wad(const Ad& a, int n, int hi) {
    if (QKV) return n == 2 ? a.w[1 + hi] : a.w[0] + (n == 0 ? 0 : n == 1 ? 160 : 320) + hi * 1920;
    if (RB) return a.w[n & 1] + (n >> 1) * 3072 + hi * 512;
    return a.w[n & 3] + (n >> 2) * 3072 + hi * 512;
  }
  __device__ __forceinline__ void rd_all(u32x4 (&xf)[NM], u32x4 (&wf)[NN], const Ad& a) {
#pragma unroll
    for (int n = 0; n < NN; ++n) wf[n] = cat(ldstr(wad(a, n, 0)), ldstr(wad(a, n, 1)));
#pragma unroll
    for (int m = 0; m < NM; ++m) xf[m] = lds128(a.x + m * 2048);
  }
  template <bool RD = true, class L>
  __device__ __forceinline__ void mm_rd(const u32x4 (&xc)[NM], const u32x4 (&wc)[NN], u32x4 (&xn)[NM], u32x4 (&wn)[NN], const Ad& a, L&& late) {
    u32x2 lo[NN];
    constexpr bool NMAJ = !QKV && !RB;
    int r = 0;
#pragma unroll
    for (int i = 0; i < NM * NN; ++i) {
      const int m = NMAJ ? i % NM : i / NN, n = NMAJ ? i / NM : i % NN;
      if (QKV && m == 0 && n == 0) continue;
      if (QKV && n == 3) mac(acc[m][3], xc[m], wc[3]);
      else               mac(acc[m][n], wc[n], xc[m]);
      SB();
      if (!RD) continue;
      if (NMAJ) {
        if (i < 4) xn[i] = lds128(a.x + i * 2048);
        else if ((i & 3) < 2) {
          const int k = (i >> 2) - 1;
          if (i & 1) wn[k] = cat(lo[k], ldstr(wad(a, k, 1))); else lo[k] = ldstr(wad(a, k, 0));
        } else late(i);
      } else {
        if (r < 2 * NN) {
          const int k = r >> 1;
          if (r & 1) wn[k] = cat(lo[k], ldstr(wad(a, k, 1))); else lo[k] = ldstr(wad(a, k, 0));
        } else if (r < 2 * NN + NM) {
          xn[r - 2 * NN] = lds128(a.x + (r - 2 * NN) * 2048);
        }
        else late(r - 2 * NN - NM);
        ++r;
      }
      SB();
    }
    if (NMAJ && RD) { wn[NN - 1] = cat(ldstr(wad(a, NN - 1, 0)), ldstr(wad(a, NN - 1, 1))); SB(); }
  }
  __device__ __forceinline__ void pin(u32x4 (&xf)[NM], u32x4 (&wf)[NN]) {
    asm volatile("" : "+v"(xf[0]), "+v"(xf[1]), "+v"(xf[2]), "+v"(xf[3]), "+v"(wf[0]), "+v"(wf[1]), "+v"(wf[2]), "+v"(wf[3]));
    if (NM == 5) asm volatile("" : "+v"(xf[NM - 1]));
    if (NN == 8) asm volatile("" : "+v"(wf[NN - 4]), "+v"(wf[NN - 3]), "+v"(wf[NN - 2]), "+v"(wf[NN - 1]));
  }
  __device__ __forceinline__ void zero() {
#pragma unroll
    for (int m = 0; m < NM; ++m)
#pragma unroll
      for (int n = 0; n < NN; ++n) { acc[m][n] = f32x4{0.f, 0.f, 0.f, 0.f}; asm volatile("" : "+v"(acc[m][n])); }
  }
  template <int TL0, int TL1, class F, class G, class H, class U>
  __device__ __forceinline__ void run(F&& pre, G&& extra, H&& tail, U&& gap) {
    pre();
    u32x4 xA[NM], wA[NN], xB[NM], wB[NN];
    Ad a;
    addr(a, xa0, 0);
    rd_all(xA, wA, a);
    if (!RB) __builtin_amdgcn_s_setprio(1);
    unsigned w0 = 0, w1 = WSL, w2 = 2 * WSL;
    unsigned xp = 2 * XSL, x0 = 0, x1 = XSL;
    int t = 0;
    unsigned nw1, nw2, nx1;
    auto adv0 = [&]() { nw1 = w2 + WSL; if (nw1 == NWS * WSL) nw1 = 0; asm volatile("" : "+s"(nw1)); };
    auto adv1 = [&]() { nw2 = nw1 + WSL; if (nw2 == NWS * WSL) nw2 = 0; asm volatile("" : "+s"(nw2)); };
    auto adv2 = [&]() { nx1 = x1 + XSL; if (nx1 == 3 * XSL) nx1 = 0; asm volatile("" : "+s"(nx1)); };
    auto adv3 = [&]() {
      w0 = w2; w1 = nw1; w2 = nw2; xp = x0; x0 = x1; x1 = nx1; t += 2;
      asm volatile("" : "+s"(w0), "+s"(w1), "+s"(w2), "+s"(xp), "+s"(x0), "+s"(x1), "+s"(t));
    };
    auto adv = [&]() { adv0(); adv1(); adv2(); adv3(); };
    asm volatile("" : "+s"(w0), "+s"(w1), "+s"(w2), "+s"(xp), "+s"(x0), "+s"(x1), "+s"(t));
    if (QKV) asm volatile("" : "+s"(gof), "+s"(kr));
    auto none = [](int) {};
    if (RB) {
      auto help = [&](int tt, unsigned wwr) { if (HAND && __builtin_expect(tt < ht, 0)) other(wwr); extra(tt); };
      pin(xA, wA);
      SB(); WAITVL(OO + (HAND ? 2 : 0)); BAR(); SB();
      loads<false>(w0, xp); help(0, w0); next(); addr(a, (xa0 ^ 64) + x0, w1); SB();
      auto half = [&]() {
        mm_rd(xA, wA, xB, wB, a, [&](int i) {
          if (QKV && i == 0) adv0();
          if (QKV && i == 2) adv1();
          if (QKV && i == 4) adv2();
        });
        pin(xB, wB);
        SB(); WAITVL(OE); BAR(); SB();
        loads<true>(w1, xp); help(t + 1, w1); next();
        addr(a, xa0 + x1, w2);
        if (QKV) adv3(); else adv();
        SB();
        mm_rd(xB, wB, xA, wA, a, none);
      };
#pragma unroll 1
      do {
        half();
        pin(xA, wA);
        SB(); WAITVL(OO); BAR(); SB();
        loads<false>(w0, xp); help(t, w0); next(); addr(a, (xa0 ^ 64) + x0, w1); SB();
        if (QKV) asm volatile("" : "+s"(gof), "+s"(kr));
      } while (t < 14);
      half();
    } else {
      addr(a, (xa0 ^ 64) + x0, w1);
      auto trip = [&](auto EARLY) {
        constexpr bool E = EARLY.value;
        pin(xA, wA);
        SB(); if (E) WAITL(); else WAITVL(OO);
        BAR(); SB();
        mm_rd(xA, wA, xB, wB, a, [&](int i) { if (E) gap(i); });
        if (E) { if (t == 0) WAITV(0); else extra(t); }
        if (!E || t >= ht) loads<false>(w0, xp);
        next(); addr(a, xa0 + x1, w2); adv0(); adv1(); adv2(); SB();
        pin(xB, wB);
        SB(); if (E) WAITL(); else WAITVL(OE);
        BAR(); SB();
        mm_rd(xB, wB, xA, wA, a, [&](int i) { if (E) gap(i); });
        if (E && t < ht) extra(t + 1); else loads<true>(w1, xp);
        next(); adv3(); addr(a, (xa0 ^ 64) + x0, w1); SB();
        if (QKV) asm volatile("" : "+s"(gof), "+s"(kr));
      };
      if (HAND) {
#pragma unroll 1
        do trip(std::true_type{}); while (t < ht + 2);
      }
#pragma unroll 1
      do trip(std::false_type{}); while (t < 16);
    }
    constexpr unsigned S16 = (16 % NWS) * WSL, S17 = (17 % NWS) * WSL, S18 = (18 % NWS) * WSL, S19 = (19 % NWS) * WSL, X8 = 2 * XSL, X9 = 0;
    pin(xA, wA);
    SB(); WAITVL(OO);
    BAR(); SB();
    if (RB) { loads<false, false>(S16, 0); tail(0); addr(a, (xa0 ^ 64) + X8, S17); SB(); mm_rd(xA, wA, xB, wB, a, none); }
    else    { mm_rd(xA, wA, xB, wB, a, none); loads<false, false>(S16, 0); tail(0); addr(a, xa0 + X9, S18); SB(); }
    pin(xB, wB);
    SB(); WAITVL(NWP + TL0); BAR(); SB();
    if (RB) { tail(1); addr(a, xa0 + X9, S18); SB(); mm_rd(xB, wB, xA, wA, a, none); }
    else    { mm_rd(xB, wB, xA, wA, a, none); tail(1); addr(a, (xa0 ^ 64) + X9, S19); SB(); }
    pin(xA, wA);
    SB(); WAITVL(TL0 + TL1); BAR(); SB();
    if (RB) { tail(2); addr(a, (xa0 ^ 64) + X9, S19); SB(); mm_rd(xA, wA, xB, wB, a, none); }
    else    { mm_rd(xA, wA, xB, wB, a, none); tail(2); SB(); }
    pin(xB, wB);
    SB(); WAITL(); BAR(); SB();
    if (RB) { tail(3); SB(); mm_rd<false>(xB, wB, xA, wA, a, none); }
    else    { mm_rd<false>(xB, wB, xA, wA, a, none); tail(3); SB(); }
    if (!RB) __builtin_amdgcn_s_setprio(0);
    MFMA_DONE();
  }
};

__global__ __launch_bounds__(512, 2) void fused(const void* x_, const void* wqkv_, const void* wo_p, const void* wg_p,
                                                const void* wu_p, const void* wd_p, const void* gw_, void* out_, void* ws_) {
  __shared__ __attribute__((aligned(16))) char smem[LDS_BYTES];
  asm volatile("" :: "s"((unsigned)(size_t)(LAS char*)smem));
  const int tid = threadIdx.x, lane = tid & 63;
  const int w = rfl(tid >> 6);
  const int h = lane >> 4, c = lane & 15, q4 = (lane & 15) >> 2, p4 = lane & 3;
  const int b = blockIdx.x, xcd = b & 7, idx = b >> 3;
  const int rh = xcd & 1, cq = xcd >> 1;
  const int a = 8 * rh + (idx & 7);
  const int gc = 2 * cq + ((idx >> 3) & 1);
  const int rb = 4 * xcd + (idx >> 3), ct = idx & 7;
  u64 x, wqkv, wo, wg, wu, wd, gw;
  char* ws;
  __amdgpu_buffer_rsrc_t rws;
  u32x4 sws;
  auto bind = [&]() {
    x = (u64)x_; wqkv = (u64)wqkv_; wo = (u64)wo_p; wg = (u64)wg_p; wu = (u64)wu_p; wd = (u64)wd_p; gw = (u64)gw_;
    u64 pw = (u64)ws_;
    asm volatile("" : "+s"(x), "+s"(wqkv), "+s"(wo), "+s"(wg), "+s"(wu), "+s"(wd), "+s"(gw), "+s"(pw));
    ws = (char*)pw;
    rws = __builtin_amdgcn_make_buffer_rsrc(ws, 0, WS_END, 0x00020000);
    sws = mk_srd(pw, WS_END);
  };

  {
    u64 pc;
    asm volatile("s_getpc_b64 %0" : "=s"(pc));
    asm volatile("s_mov_b32 m0, %0\n\ts_nop 0\n\tbuffer_load_dword %1, %2, 0 offen lds"
                 :: "s"(PKF_OFF), "v"(lane < 6 ? ((idx & 7) * 48 + w * 6 + lane) * 128 : OOB), "s"(mk_srd(pc & ~127ull, CODE_BYTES)) : "memory");
  }
  u32x2 ev;
  u64 tag = 0;
  auto make_tag = [&](auto NV) {
    unsigned lo, hi;
    asm volatile("s_waitcnt vmcnt(%4)\n\tv_readfirstlane_b32 %0, %2\n\tv_readfirstlane_b32 %1, %3"
                 : "=s"(lo), "=s"(hi) : "v"(ev.x), "v"(ev.y), "i"(NV.value) : "memory");
    const u64 e = ((u64)hi << 32) | lo;
    tag = (e ^ 0x9E3779B97F4A7C15ull) * 0xBF58476D1CE4E5B9ull + 0x632BE59BD9B4E019ull;
    if (tag == e) tag += 1;
  };
  const unsigned pwb = WS_PW + ct * (68 * 5120);
  const int grot = 3 * ((idx >> 3) & 1);
  auto gstep = [&](int s) { const int v = s + grot; return v >= 6 ? v - 6 : v; };
  auto cell_dma = [&](int v, int cell) {
    const unsigned so = pwb + (20 + 6 * v + gstep(2 + cell)) * 5120;
#pragma unroll
    for (int j = 0; j < 5; ++j) dma16c(cell ? v * 15360 : CELL0 + v * 5120, (cell ? cell - 1 : 0) * 5120 + j * 1024, lane * 16, sws, so + j * 1024);
  };
  auto ldw = [&](u32x4 (&d)[5], unsigned so) {
#pragma unroll
    for (int n = 0; n < 5; ++n) d[n] = __builtin_amdgcn_raw_buffer_load_b128(rws, lane * 16, so + n * 1024, AUX_SC1);
  };
  const unsigned pscr = PACK_OFF + (w & 3) * 8192;
  const int pu0 = (b - 128) * 4 + w;
  unsigned pra[2], prb[2], pn[2], pb[2];
  auto pack_issue = [&]() {
    const u64 po = wo, pd = wd;
#pragma unroll
    for (int r = 0; r < 2; ++r) {
      const int u = pu0 + 512 * r;
      const int ks = u / 10, cg = u - 10 * ks;
      const unsigned r8 = lane >> 3;
      const u32x4 srd = mk_srd(ks < 20 ? po : pd, ks < 20 ? 640 * 1280 : 1536 * 1280);
      const unsigned base = ks < 16 ? 40 * ks : ks < 20 ? 160 * (ks - 16) + 16 : 32 * (ks - 20);
      const unsigned vr = (ks >= 16 && ks < 20) ? 20 * (r8 >> 2) + (r8 & 3) : r8;
      const unsigned vo = vr * 1280 + (lane & 7) * 16;
#pragma unroll
      for (int p = 0; p < 4; ++p) {
        const unsigned rp = ks < 16 ? 20 * (p >> 1) + 8 * (p & 1) : ks < 20 ? 40 * p : 8 * p;
        dma16(pscr, r * 4096 + p * 1024, vo, srd, u < 680 ? (base + rp) * 1280 + cg * 128 : OOB);
      }
      pra[r] = pscr + r * 4096 + (h * (ks < 16 ? 8 : 4) + q4) * 128 + p4 * 8 - 32;
      prb[r] = pra[r] + (ks < 16 ? 512 : 2048);
      pn[r] = 4 * cg - 1; pb[r] = u < 680 ? WS_PW + ks * 5120 + (4 * cg - 1) * 1024 : OOB;
    }
    asm volatile("" : "+v"(pra[0]), "+v"(prb[0]), "+v"(pra[1]), "+v"(prb[1]));
    asm volatile("" : "+s"(pn[0]), "+s"(pb[0]), "+s"(pn[1]), "+s"(pb[1]));
  };
  const unsigned smask = w != 4 ? 0u : idx >= 16 ? 0x1040u : 0x1000u;
  auto side_b = [&](int t) {
    if (__builtin_expect(!((smask >> t) & 1), 1)) return;
    if (t == 6) {
      u32x2 tv; tv.x = (unsigned)tag; tv.y = (unsigned)(tag >> 32);
      __builtin_amdgcn_raw_buffer_store_b64(tv, rws, opaque_lane() == 0 ? WS_PKF + (b - 128) * 8 : OOB, 0, AUX_SC1);
    } else {
      dma16c(PKF_OFF, 0, opaque_lane() * 16, sws, WS_PKF);
    }
  };
  u32x2 pq[4];
  unsigned ps0, ps1;
  auto pack_gap = [&](int i) {
    if (i == 6)  pq[0] = ldstr(pra[0]);
    if (i == 7)  pq[1] = ldstr(prb[0]);
    if (i == 10) pq[2] = ldstr(pra[1]);
    if (i == 11) pq[3] = ldstr(prb[1]);
    if (i == 14) { pra[0] += 32; prb[0] += 32; asm volatile("" : "+v"(pra[0]), "+v"(prb[0])); }
    if (i == 15) { pra[1] += 32; prb[1] += 32; asm volatile("" : "+v"(pra[1]), "+v"(prb[1])); }
    if (i == 18) { ps0 = (pn[0] * 205) >> 10; asm volatile("" : "+s"(ps0)); }
    if (i == 19) { ps0 = ps0 * (68 * 5120 - 5120) + pb[0]; asm volatile("" : "+s"(ps0)); }
    if (i == 22) { ps1 = (pn[1] * 205) >> 10; asm volatile("" : "+s"(ps1)); }
    if (i == 23) { ps1 = ps1 * (68 * 5120 - 5120) + pb[1]; asm volatile("" : "+s"(ps1)); }
    if (i == 26) { pn[0] += 1; pb[0] += 1024; asm volatile("" : "+s"(pn[0]), "+s"(pb[0])); }
    if (i == 27) { pn[1] += 1; pb[1] += 1024; asm volatile("" : "+s"(pn[1]), "+s"(pb[1])); }
  };
  auto side_f = [&](int) {
    __builtin_amdgcn_raw_buffer_store_b128(cat(pq[0], pq[1]), rws, lane * 16, ps0, AUX_SC1);
    STPAD();
    __builtin_amdgcn_raw_buffer_store_b128(cat(pq[2], pq[3]), rws, lane * 16, ps1, AUX_SC1);
    STPAD();
  };
  auto pk_check = [&]() {
    const u32x4 p0 = lds128(PKF_OFF + lane * 16);
    const unsigned lo = (unsigned)tag, hi = (unsigned)(tag >> 32);
    if (!__all(p0.x == lo && p0.z == lo && p0.y == hi && p0.w == hi)) {
      GAS u64* fp = (GAS u64*)((char*)ws + WS_PKF + opaque_lane() * 8);
#pragma unroll 1
      for (unsigned spin = 0; spin < (1u << 18); ++spin) {
        bool ok = true;
#pragma unroll
        for (int j = 0; j < 2; ++j) ok &= __hip_atomic_load(fp + 64 * j, RLX) == tag;
        if (__all(ok)) break;
        __builtin_amdgcn_s_sleep(2);
      }
    }
    asm volatile("" ::: "memory");
  };
  const int rot = (a + 2 * ((idx >> 3) & 1) + (idx >> 4)) & 3;

  const int t0 = 2 * w + min(w, 4);
  float sg0, sg1, sg2;
  auto role = [&](auto RBT) {
    constexpr bool RB = decltype(RBT)::value;
    u32x4 wo_[3][5], wg_[2][5];
    auto tail = [&](int i) {
      if (i == 0) pk_check();
      if (RB) {
        if (i == 1) { ldw(wo_[0], pwb + 2 * w * 5120); ldw(wo_[1], pwb + (2 * w + 1) * 5120); }
        if (i >= 2) ldw(wg_[i - 2], pwb + (20 + 6 * w + gstep(i - 2)) * 5120);
        if (i == 2) cell_dma(w - 4, 0);
        if (i == 3) cell_dma(w, 0);
      } else if (i >= 2) {
        ldw(wo_[i - 2], pwb + (2 * w + i - 2) * 5120);
      }
    };
    auto late_w = [&]() {
#pragma unroll
      for (int n = 0; n < 5; ++n) ld128(wo_[2][n], lane * 16, sws, pwb + (16 + w) * 5120 + n * 1024);
#pragma unroll
      for (int j = 0; j < 10; ++j) ld128(wg_[j / 5][j % 5], lane * 16, sws, pwb + (20 + 6 * w + gstep(j / 5)) * 5120 + (j % 5) * 1024);
    };
    if (idx < 16) {
      const int g = gc, hd = w & 3;
      constexpr int par = RB ? 1 : 0;
      using K = P1<true, RB>;
      K k;
      const int q0 = RB ? 12 + 2 * (w - 4) : 3 * w;
      k.xd = K::XB + q0 * 1024; k.wd0 = w * 2048;
      asm volatile("" : "+s"(k.xd), "+s"(k.wd0));
#pragma unroll
      for (int i = 0; i < K::NXP; ++i) {
        const int rho = 8 * (q0 + i) + (lane >> 3);
        const int pp = rho >= 80 ? 1 : 0, u = rho - 80 * pp;
        const int tok = 128 * a - 32 + 2 * u + pp;
        const unsigned v = tok * 1280 + (((lane & 7) ^ ((rho >> 1) & 7)) << 4);
        k.xv[i] = (u >= 9 && tok >= 0) ? v : OOB;
      }
#pragma unroll
      for (int j = 0; j < 2; ++j) {
        const unsigned I = (2 * w + j) * 64 + lane;
        const unsigned kb = (I >= 248) + (I >= 488) + (I >= 736);
        const unsigned i = I - 240 * kb - 8 * ((kb + 1) >> 1);
        const unsigned r = (i * 2185u) >> 16, rem = i - r * 30;
        const unsigned seg = (rem * 6554u) >> 16, ch = rem - seg * 10;
        const unsigned v = (8 * kb + r) * 3840 + seg * 1280 + g * 160 + ch * 16;
        k.wv[j] = i < 240 ? v : OOB;
      }
      asm volatile("" : "+v"(k.xv[0]), "+v"(k.xv[1]), "+v"(k.xv[K::NXP - 1]), "+v"(k.wv[0]), "+v"(k.wv[1]));
      bind();
      k.srdx = mk_srd(x, 2048 * 1280);
      k.srdw = mk_srd(wqkv, 640 * 3840);
      unsigned gv0, gv1;
      asm volatile("global_load_dwordx2 %0, %1, %2 sc1" : "=v"(ev) : "v"(WS_EPOCH), "s"(ws) : "memory");
      asm volatile("buffer_load_ushort %0, %2, %3, 0 offen\n\tbuffer_load_ushort %1, %2, %3, 0 offen offset:1024"
                   : "=&v"(gv0), "=&v"(gv1) : "v"(tid * 2), "s"(mk_srd(gw, 1280)) : "memory");
      k.stage0(rot);
      BAR();
      k.stage1();
      k.xa0 = (K::XB + par * 5 * 2048 + c * 128 + ((h ^ (c >> 1)) << 4)) ^ ((rot & 1) << 6);
      const unsigned kbo = h * 3840 + ((h + 1) >> 1) * 128;
      k.wa0 = kbo + q4 * 480 + (20 * hd + 4 * p4) * 2;
      const bool isgate = p4 == 3;
      k.gmask = isgate ? ~0u : 0u;
      asm volatile("" : "+v"(k.gmask));
      k.wa1 = isgate ? GATE_OFF + (8 * h + q4) * 8 : kbo + q4 * 480 + p4 * 160 + (20 * hd + 16) * 2;
      k.wa2 = k.wa1 + (isgate ? 32 : 1920);
      f32x4 ba, bb, ba1;
#pragma unroll
      for (int r = 0; r < 4; ++r) {
        const int kap = 4 * h + r;
        ba[r] = kap >= c + 9 ? 0.f : -1e30f;
        bb[r] = (kap <= c && kap >= c - 7) ? 0.f : -1e30f;
        ba1[r] = a == 0 ? -1e30f : ba[r];
      }
      unsigned m0mask = h == 0 ? 0xffffffffu : 0u;
      unsigned scw = w * 15360 + 10240 + c * 32 + h * 8, sck = w * 15360 + 10240 + c * 32 + 8,
               sct = w * 15360 + 10240 + (4 * h + q4) * 32 + p4 * 8;
      unsigned ovo = WS_O + (h & 1) * (2 * 20480) + (2 * g + (hd >> 1)) * 1024 + (2 * (hd & 1) + (h >> 1)) * 256 + c * 16;
      unsigned ovt = h == 2 ? WS_O + (32 + g) * 512 + hd * 128 + c * 8 : OOB;
      asm volatile("" : "+v"(ba), "+v"(bb), "+v"(ba1), "+v"(m0mask), "+v"(scw), "+v"(sck), "+v"(sct), "+v"(ovo), "+v"(ovt));
      asm volatile("" : "+v"(k.xa0), "+v"(k.wa0));
      k.zero();
      k.stage2();
      k.template run<0, RB ? 10 : 0>([&]() {
        asm volatile("s_waitcnt vmcnt(%2)" : "+v"(gv0), "+v"(gv1) : "i"(K::VPRO) : "memory");
        u32x2 v; v.x = gv0; v.y = 0;
        *(LAS u32x2*)(GATE_OFF + tid * 8) = v;
        if (!RB && tid < 128) { v.x = gv1; *(LAS u32x2*)(GATE_OFF + 4096 + tid * 8) = v; }
        make_tag(std::integral_constant<int, K::VPRO>{});
        WAITL();
        BAR();
      }, [&](int t) { if (RB) side_b(t); }, tail, [](int) {});
      u32x2 ql[5], kl[5], t2[5], vl[5], tt[5], k4[5];
      unsigned sb16[4];
#pragma unroll
      for (int i = 0; i < 5; ++i) { t2[i] = pk4(k.acc[i][2]); *(LAS u32x2*)(scw + i * 512) = t2[i]; }
#pragma unroll
      for (int i = 0; i < 5; ++i) { if (i) ql[i] = pk4(k.acc[i][0]); kl[i] = pk4(k.acc[i][1]); }
      WAITL();
#pragma unroll
      for (int i = 0; i < 5; ++i) k4[i] = *(const LAS u32x2*)(sck + i * 512);
#pragma unroll
      for (int i = 0; i < 4; ++i) sb16[i] = *(const LAS unsigned*)(sck + (i + 1) * 512 + 16);
#pragma unroll
      for (int i = 0; i < 5; ++i) tt[i] = ldstr(sct + i * 512);
#pragma unroll
      for (int i = 0; i < 5; ++i) vl[i] = pk4(k.acc[i][3]);
      constexpr float SC = 0.22360679774997896f * LOG2E;
      f32x4 sa[4], sb[4];
#pragma unroll
      for (int i = 1; i < 5; ++i) {
        u32x4 qf; qf.x = ql[i].x; qf.y = ql[i].y; qf.z = t2[i].x & m0mask; qf.w = t2[i].y & m0mask;
        sb[i - 1] = mfma(cat(kl[i], k4[i]), qf, bb);
        sa[i - 1] = mfma(cat(kl[i - 1], k4[i - 1]), qf, i == 1 ? ba1 : ba);
      }
      asm volatile("s_nop 7\n\ts_nop 0" : "+v"(sa[0]), "+v"(sa[1]), "+v"(sa[2]), "+v"(sa[3]), "+v"(sb[0]), "+v"(sb[1]), "+v"(sb[2]), "+v"(sb[3]));
      float mx[4], es[4];
#pragma unroll
      for (int i = 0; i < 4; ++i)
        mx[i] = max3(max3(sb[i].x, sb[i].y, sb[i].z), max3(sb[i].w, sa[i].x, sa[i].y), max2(sa[i].z, sa[i].w));
#pragma unroll
      for (int i = 0; i < 4; ++i) {
        auto r = __builtin_amdgcn_permlane16_swap(f2u(mx[i]), f2u(mx[i]), false, false);
        mx[i] = max2(u2f(r[0]), u2f(r[1]));
      }
#pragma unroll
      for (int i = 0; i < 4; ++i) {
        auto r = __builtin_amdgcn_permlane32_swap(f2u(mx[i]), f2u(mx[i]), false, false);
        mx[i] = max2(u2f(r[0]), u2f(r[1])) * SC;
        es[i] = __builtin_amdgcn_exp2f(__builtin_amdgcn_fmed3f(u2f(sb16[i] << 16), -60.f, 60.f) * LOG2E);
      }
      u32x4 pf[4];
#pragma unroll
      for (int i = 0; i < 4; ++i) {
        f32x4 pa, pb;
#pragma unroll
        for (int r = 0; r < 4; ++r) {
          pa[r] = __builtin_amdgcn_exp2f(sa[i][r] * SC - mx[i]);
          pb[r] = __builtin_amdgcn_exp2f(sb[i][r] * SC - mx[i]);
        }
        pf[i] = cat(pk4(pa), pk4(pb));
      }
      const f32x4 z = {0.f, 0.f, 0.f, 0.f};
      const u32x4 ones = {0x3f803f80u, 0x3f803f80u, 0x3f803f80u, 0x3f803f80u};
      f32x4 ls[4], oa[4], ob[4];
#pragma unroll
      for (int i = 0; i < 4; ++i) {
        ls[i] = mfma(ones, pf[i], z);
        oa[i] = mfma(cat(vl[i], vl[i + 1]), pf[i], z);
        ob[i] = mfma(cat(tt[i], tt[i + 1]), pf[i], z);
      }
      const unsigned mt0 = 8 * a + par;
      u32x2 op[4];
#pragma unroll
      for (int i = 0; i < 4; ++i) {
        const float f = es[i] * __builtin_amdgcn_rcpf(ls[i].x);
        op[i] = pk4(oa[i] * f);
        __builtin_amdgcn_raw_buffer_store_b64(pk4(ob[i] * f), rws, ovt, (mt0 + 2 * i) * 20480, AUX_SC1);
        if (i & 1) {
          auto sx = __builtin_amdgcn_permlane16_swap(op[i - 1].x, op[i].x, false, false);
          auto sy = __builtin_amdgcn_permlane16_swap(op[i - 1].y, op[i].y, false, false);
          u32x4 o; o.x = sx[0]; o.y = sy[0]; o.z = sx[1]; o.w = sy[1];
          __builtin_amdgcn_raw_buffer_store_b128(o, rws, ovo, (mt0 + 2 * i - 2) * 20480, AUX_SC1);
          STPAD();
        }
      }
      if (g == 0 && hd == 0) {
        const float e = h == 0 ? es[0] : h == 1 ? es[1] : h == 2 ? es[2] : es[3];
        __builtin_amdgcn_raw_buffer_store_b32(f2u(e), rws, WS_G + h * 128 + c * 4, mt0 * 64, AUX_SC1);
      }
    } else {
      const int cj = gc;
      using K = P1<false, RB>;
      K k;
      const int mh = RB ? (w < 6 ? 1 : 0) : (w >> 1);
      const int up = (w >> 1) & 1, kp = w & 1;
#pragma unroll
      for (int i = 0; i < K::NXP; ++i) {
        const int rho = 8 * (4 * (w & 3) + i) + (lane >> 3);
        const int j = rho >> 4;
        const int tok = 128 * a + 32 * (j >> 1) + 2 * (rho & 15) + (j & 1);
        k.xv[i] = tok * 1280 + (((lane & 7) ^ ((rho >> 1) & 7)) << 4);
      }
      k.xd = K::XB + 4 * (w & 3) * 1024;
      {
        const int r = lane >> 3, p8 = lane & 7;
        const int s = ((r >> 1) & 1) + 2 * kp;
        k.wv[0] = (8 * kp + r) * 3072 + cj * 384 + (((((p8 >> 1) ^ s) << 1) | (p8 & 1)) << 4) + (RB ? 0 : 16 * 3072 + 128);
        k.wv[1] = k.wv[0] + 128; k.wv[2] = k.wv[0] + 256; k.wv[3] = k.wv[0] + 16 * 3072;
      }
      k.wd0 = RB ? (kp * 6 + 3 * up) * 1024 : ((kp + 2) * 6 + 3 * up + 1) * 1024;
      k.wd1 = ((kp + 2) * 6 + 3 * up) * 1024;
      if (RB) asm volatile("" : "+v"(k.xv[0]), "+v"(k.xv[1]), "+v"(k.xv[2]), "+v"(k.xv[3]), "+v"(k.wv[2]), "+v"(k.wv[3]));
      asm volatile("" : "+v"(k.wv[0]), "+v"(k.wv[1]));
      bind();
      const u64 pg = wg, pu = wu;
      k.srdx = mk_srd(x, 2048 * 1280);
      k.srdw = mk_srd(up ? pu : pg, 640 * 3072);
      asm volatile("global_load_dwordx2 %0, %1, %2 sc1" : "=v"(ev) : "v"(WS_EPOCH), "s"(ws) : "memory");
      k.stage0(rot);
      BAR();
      if (!RB) pack_issue();
      k.stage1();
      k.xa0 = (K::XB + mh * 4 * 2048 + c * 128 + ((h ^ (c >> 1)) << 4)) ^ ((rot & 1) << 6);
      {
        const int s = ((q4 >> 1) & 1) + 2 * (h & 1);
        k.wa0 = (h * 6 + (RB ? 2 : (w & 1))) * 1024 + q4 * 128 + (((RB ? 2 * (w & 1) : 0) ^ s) << 5) + p4 * 8;
      }
      asm volatile("" : "+v"(k.xa0), "+v"(k.wa0));
      k.zero();
      k.stage2();
      k.template run<0, RB ? 10 : 0>([&]() {
        make_tag(std::integral_constant<int, RB ? K::VPRO : 8>{});
        BAR();
      }, [&](int t) { if (RB) side_b(t); else side_f(t); }, tail, pack_gap);
      constexpr int NK = RB ? 1 : 2;
      float nl2e = -LOG2E;
      asm volatile("" : "+s"(nl2e));
      const unsigned gso = WS_GU + ((8 * a + 4 * mh) * 48 + 6 * cj + (RB ? 4 + (w & 1) : 2 * (w & 1))) * 1024;
#pragma unroll
      for (int m = 0; m < 4; ++m) {
#pragma unroll
        for (int kk = 0; kk < NK; ++kk) {
          float v[8];
#pragma unroll
          for (int j = 0; j < 8; j += 2) {
            const f32x2 g2 = {k.acc[m][2 * kk + (j >> 2)][j & 3], k.acc[m][2 * kk + (j >> 2)][(j & 3) + 1]};
            const f32x2 u2 = {k.acc[m][2 * NK + 2 * kk + (j >> 2)][j & 3], k.acc[m][2 * NK + 2 * kk + (j >> 2)][(j & 3) + 1]};
            const f32x2 a2 = g2 * nl2e;
            const f32x2 d2 = f32x2{__builtin_amdgcn_exp2f(a2.x), __builtin_amdgcn_exp2f(a2.y)} + 1.f;
            const f32x2 r2 = g2 * u2 * f32x2{__builtin_amdgcn_rcpf(d2.x), __builtin_amdgcn_rcpf(d2.y)};
            v[j] = r2.x; v[j + 1] = r2.y;
          }
          u32x4 o; o.x = pk2(v[0], v[1]); o.y = pk2(v[2], v[3]); o.z = pk2(v[4], v[5]); o.w = pk2(v[6], v[7]);
          __builtin_amdgcn_raw_buffer_store_b128(o, rws, lane * 16, gso + m * (48 * 1024) + kk * 1024, AUX_SC1);
          STPAD();
        }
      }
    }
    SB();
    asm volatile("" : "+v"(wo_[0][0]), "+v"(wo_[0][1]), "+v"(wo_[0][2]), "+v"(wo_[0][3]), "+v"(wo_[0][4]),
                      "+v"(wo_[1][0]), "+v"(wo_[1][1]), "+v"(wo_[1][2]), "+v"(wo_[1][3]), "+v"(wo_[1][4]));
    if (RB) asm volatile("" : "+v"(wg_[0][0]), "+v"(wg_[0][1]), "+v"(wg_[0][2]), "+v"(wg_[0][3]), "+v"(wg_[0][4]),
                              "+v"(wg_[1][0]), "+v"(wg_[1][1]), "+v"(wg_[1][2]), "+v"(wg_[1][3]), "+v"(wg_[1][4]));
    SB();
    if (RB && idx >= 16) { cell_dma(w - 4, 1); cell_dma(w, 1); WAITV(10); }
    else WAITV(0);
    WAITL();
    BAR();
    if (!RB && w == 0 && lane < 2) {
      GAS u64* fp = (GAS u64*)((char*)ws + (lane ? WS_DONE + b * 8 : WS_FLAG + (a * 16 + (idx < 16 ? 8 : 0) + gc) * 8));
      __hip_atomic_store(fp, tag, RLX);
    }

    SB();
    const unsigned fo = WS_FLAG + ((rb >> 1) * 16 + c) * 8;
    auto wait_flags = [&](u32x2& v, bool need) {
#pragma unroll 1
      for (unsigned spin = 0; spin < (1u << 18); ++spin) {
        if (__all(!need || (v.x == (unsigned)tag && v.y == (unsigned)(tag >> 32)))) break;
        ld64(v, fo, sws, 0);
        asm volatile("s_waitcnt vmcnt(0)" : "+v"(v) :: "memory");
      }
      asm volatile("" ::: "memory");
    };
    u32x2 fv;
    if (RB) {
      if (idx < 16) { cell_dma(w - 4, 1); cell_dma(w, 1); }
      cell_dma(w - 4, 2);
      cell_dma(w, 2);
    } else {
      late_w();
      if (w == 0) {
        ld64(fv, fo, sws, 0);
        asm volatile("s_waitcnt vmcnt(0)" : "+v"(fv) :: "memory");
        wait_flags(fv, c >= 8);
      }
    }
    if (RB) __builtin_amdgcn_s_setprio(1);
    BAR();
    f32x4 acc[4][5];
    u32x4 ag[6][4];
    const unsigned gb = WS_GU + (4 * rb * 48 + 6 * w) * 1024;
    auto ldg = [&](int s, int m) { ld128(ag[s][m], lane * 16, sws, gb + gstep(s) * 1024 + m * (48 * 1024)); };
    unsigned ca0 = CELL0 + w * 5120 + lane * 16, ca1 = w * 15360 + lane * 16;
    asm volatile("" : "+v"(ca0), "+v"(ca1));
    {
      using S = Sched<RB>;
      constexpr int NO = S::NO, OB = S::OB;
      u32x4 af[2][4], at[4];
      u32x2 tl[4], th[4];
      const unsigned ob = WS_O + 4 * rb * 20480;
      auto req = [&](auto IT) {
        constexpr int i = IT.value;
        if constexpr (i < 8) ld128(af[i >> 2][i & 3], lane * 16, sws, ob + (i & 3) * 20480 + (2 * w + (i >> 2)) * 1024);
        else if constexpr (i < OB) ld64((i & 1) ? th[(i - 8) >> 1] : tl[(i - 8) >> 1], lane * 8, sws, ob + ((i - 8) >> 1) * 20480 + (16 + w) * 1024 + (i & 1) * 512);
        else if constexpr (i < OB + 29 && (i - OB) % 5 < 4) ldg((i - OB) / 5, (i - OB) % 5);
        else if constexpr (i < OB + 29) dma16c(w * 15360, 10240 + ((i - OB) / 5) * 1024, lane * 16, sws, pwb + (20 + 6 * w + gstep(5)) * 5120 + ((i - OB) / 5) * 1024);
        else ld32(i == OB + 29 ? sg0 : i == OB + 30 ? sg1 : sg2, c * 4, sws, WS_G + (4 * rb + min(t0 + i - OB - 29, 19) / 5) * 64);
      };
      if (RB || w != 1) sfor<0, S::upto(PL - 1)>(req);
      if (RB) { __builtin_amdgcn_s_setprio(0); WAITV(S::upto(PL - 1)); }
      else if (w == 1) { fv.x = ~(unsigned)tag; fv.y = 0; wait_flags(fv, c < 8); }
      BAR();
      if (!RB && w == 1) sfor<0, S::upto(PL - 1)>(req);
      u32x4 wc[5];
      sfor<0, NO + 6>([&](auto ST) {
        constexpr int st = ST.value, s = st - NO;
        if constexpr (st == 2 && !RB) {
          asm volatile("s_waitcnt vmcnt(%8)" : "+v"(tl[0]), "+v"(tl[1]), "+v"(tl[2]), "+v"(tl[3]), "+v"(th[0]), "+v"(th[1]), "+v"(th[2]), "+v"(th[3])
                       : "i"(S::issued(st, 0) - 16) : "memory");
#pragma unroll
          for (int m = 0; m < 4; ++m) at[m] = cat(tl[m], th[m]);
          asm volatile("s_nop 1" : "+v"(at[0]), "+v"(at[1]), "+v"(at[2]), "+v"(at[3]));
        }
        sfor<0, 20>([&](auto IT) {
          constexpr int i = IT.value, m = i / 5, n = i % 5, is = S::issued(st, i);
          if constexpr (i == 0) {
            if constexpr (s >= 0)      asm volatile("s_waitcnt vmcnt(%4)" : "+v"(ag[s][0]), "+v"(ag[s][1]), "+v"(ag[s][2]), "+v"(ag[s][3]) : "i"(is - S::gpos(s, 3) - 1) : "memory");
            else if constexpr (st < 2) asm volatile("s_waitcnt vmcnt(%4)" : "+v"(af[st][0]), "+v"(af[st][1]), "+v"(af[st][2]), "+v"(af[st][3]) : "i"(is - (4 * st + 3) - 1) : "memory");
          }
          if constexpr (st == 0 && i == 0 && !RB)
            asm volatile("" : "+v"(wo_[2][0]), "+v"(wo_[2][1]), "+v"(wo_[2][2]), "+v"(wo_[2][3]), "+v"(wo_[2][4]),
                              "+v"(wg_[0][0]), "+v"(wg_[0][1]), "+v"(wg_[0][2]), "+v"(wg_[0][3]), "+v"(wg_[0][4]),
                              "+v"(wg_[1][0]), "+v"(wg_[1][1]), "+v"(wg_[1][2]), "+v"(wg_[1][3]), "+v"(wg_[1][4]));
          if constexpr (st == 0)     mul0(acc[m][n], wo_[0][n], af[0][m]);
          else if constexpr (s >= 0) mac(acc[m][n], wg_[s & 1][n], ag[s][m]);
          else if constexpr (st < 2) mac(acc[m][n], wo_[1][n], af[1][m]);
          else                       mac(acc[m][n], wo_[2][n], at[m]);
          SB();
          if constexpr (s == 4 && i == 9) WAITV(is - S::gpos(4, 4) - 1);
          if constexpr (m == (s == 4 ? 2 : 0) && s >= 1 && s < 5) wc[n] = lds128((s > 1 ? ca1 + (s - 2) * 5120 : ca0) + n * 1024);
          if constexpr (S::issued(st, i + 1) > is) req(std::integral_constant<int, is>{});
          SB();
        });
        if constexpr (s >= 1 && s < 5) {
          asm volatile("" : "+v"(wc[0]), "+v"(wc[1]), "+v"(wc[2]), "+v"(wc[3]), "+v"(wc[4]));
#pragma unroll
          for (int n = 0; n < 5; ++n) wg_[(s + 1) & 1][n] = wc[n];
        }
      });
      asm volatile("s_waitcnt vmcnt(0)" : "+v"(sg0), "+v"(sg1), "+v"(sg2) :: "memory");
    }
    MFMA_DONE();
#pragma unroll
    for (int j = 0; j < 10; ++j) {
      const int ta = j < 4 ? 3 * j : j < 6 ? 6 * j - 22 : 2 * j, tb = j < 4 ? ta + 1 : j < 6 ? ta + 3 : ta + 1;
      *(LAS u32x4*)(ca1 + j * 1024) = cat(pk4(acc[ta / 5][ta % 5]), pk4(acc[tb / 5][tb % 5]));
    }
  };
  if (w < 4) role(std::false_type{}); else role(std::true_type{});

  if (b == 0 && w == 2) {
    GAS u64* dp = (GAS u64*)((char*)ws_ + WS_DONE + lane * 8);
#pragma unroll 1
    for (unsigned spin = 0; spin < (1u << 18); ++spin) {
      bool ok = true;
#pragma unroll
      for (int j = 0; j < 4; ++j) ok &= __hip_atomic_load(dp + j * 64, RLX) == tag;
      if (__all(ok)) break;
      __builtin_amdgcn_s_sleep(2);
    }
    if (lane == 0) __hip_atomic_store((GAS u64*)((char*)ws_ + WS_EPOCH), tag, RLX);
  }
  sg0 = __builtin_amdgcn_rcpf(1.f + sg0); sg1 = __builtin_amdgcn_rcpf(1.f + sg1); sg2 = __builtin_amdgcn_rcpf(1.f + sg2);
  auto ooff = [&](int tl) {
    const int m = tl / 5, n = tl - 5 * m;
    return (unsigned)((64 * rb + 32 * (m >> 1) + 2 * c + (m & 1)) * 1280 + ct * 160 + n * 32 + h * 8);
  };
  unsigned oo0 = ooff(t0), oo1 = ooff(t0 + 1), oo2 = ooff(t0 + 2);
  asm volatile("" : "+v"(sg0), "+v"(sg1), "+v"(sg2), "+v"(oo0), "+v"(oo1), "+v"(oo2));
  WAITL();
  BAR();
  {
    u32x4 p[8];
    u32x2 p2[8];
    unsigned ra = (w < 4 ? w : w + 2) * 1024 + lane * 16, rb_ = ra + 4 * 15360;
    unsigned rc = (4 + (w >> 1)) * 1024 + (w & 1) * 8 + lane * 16, rd = rc + 4 * 15360;
    asm volatile("" : "+v"(ra), "+v"(rb_), "+v"(rc), "+v"(rd));
#pragma unroll
    for (int v = 0; v < 8; ++v) p[v] = lds128((v < 4 ? ra : rb_) + (v & 3) * 15360);
    if (w < 4) {
#pragma unroll
      for (int v = 0; v < 8; ++v) { p2[v] = *(const LAS u32x2*)((v < 4 ? rc : rd) + (v & 3) * 15360); SB(); }
    }
    SB();
#pragma unroll
    for (int j = 0; j < 3; ++j) {
      if (j < 2 || w < 4) {
        f32x4 s = {0.f, 0.f, 0.f, 0.f};
#pragma unroll
        for (int v = 0; v < 8; ++v) {
          const unsigned px = j == 0 ? p[v].x : j == 1 ? p[v].z : p2[v].x, py = j == 0 ? p[v].y : j == 1 ? p[v].w : p2[v].y;
          const f32x4 q = {u2f(px << 16), u2f(px & 0xffff0000u), u2f(py << 16), u2f(py & 0xffff0000u)};
          s = v ? s + q : q;
        }
        const u32x2 ov = pk4(s * (j == 0 ? sg0 : j == 1 ? sg1 : sg2));
        asm volatile("global_store_dwordx2 %0, %1, %2" :: "v"(j == 0 ? oo0 : j == 1 ? oo1 : oo2), "v"(ov), "s"(out_) : "memory");
      }
    }
  }
}
}

extern "C" void kernel_launch(void* const* d_in, const int* in_sizes, int n_in, void* d_out, int out_size, void* d_ws,
                              size_t ws_size, hipStream_t stream) {
  fused<<<256, 512, 0, stream>>>(d_in[0], d_in[1], d_in[2], d_in[4], d_in[5], d_in[6], d_in[7], d_out, d_ws);
}
```
